```python
import torch

def _softmax(x, axis=-1):
    return torch.softmax(x, dim=axis)

def _gelu(x):
    return torch.nn.functional.gelu(x, approximate='tanh')

def _silu(x):
    return torch.nn.functional.silu(x)

def _sigmoid(x):
    return torch.sigmoid(x)

def _layernorm(x, axis=-1, eps=1e-05):
    m = x.mean(dim=axis, keepdim=True)
    v = x.var(dim=axis, keepdim=True, unbiased=False)
    return (x - m) / torch.sqrt(v + eps)

def _scatter_add(tgt, idx, src, axis=0):
    return tgt.clone().index_add_(axis, idx, src)

def _segment_sum(src, seg, n):
    out = torch.zeros((n,) + src.shape[1:], dtype=src.dtype)
    return out.index_add_(0, seg, src)

def _pad(x, pw):
    flat = tuple((v for pair in reversed(pw) for v in pair))
    return torch.nn.functional.pad(x, flat)
kernel_name = 'parallel_sum__grouped_kv_mqa__mixer_gmlp__prompt'

def setup_inputs():

    _w = lambda a: (lambda t: t.to(torch.bfloat16) if t.is_floating_point() else t)(torch.as_tensor(a))

    def _A_setup():
        B, S, D, H = (1, 2048, 576, 32)
        Dh = D // H
        return {'x': _w((lambda *s: torch.randn(*s, dtype=torch.bfloat16) * 0.05)(B, S, D)), 'wq': _w((lambda *s: torch.randn(*s, dtype=torch.bfloat16) * 0.2)(D, H * Dh)), 'wk': _w((lambda *s: torch.randn(*s, dtype=torch.bfloat16) * 0.2)(D, Dh)), 'wv': _w((lambda *s: torch.randn(*s, dtype=torch.bfloat16) * 0.1)(D, Dh)), 'wo': _w((lambda *s: torch.randn(*s, dtype=torch.bfloat16) * 0.1)(H * Dh, D))}
    _A_in = _A_setup()

    def _B_setup():
        B, S, D, F = (1, 2048, 576, 4608)
        Fh = F // 2
        return {'x': _w((lambda *s: torch.randn(*s, dtype=torch.bfloat16) * 0.05)(B, S, D)), 'w_in': _w((lambda *s: torch.randn(*s, dtype=torch.bfloat16) * 0.4)(D, 2 * Fh)), 'w_sp': _w(0.01 * (lambda *s: torch.randn(*s, dtype=torch.bfloat16) * 0.4)(S, S)), 'b_sp': _w(1.0 + 0.01 * (lambda *s: torch.randn(*s, dtype=torch.bfloat16) * 0.1)(S)), 'w_out': _w((lambda *s: torch.randn(*s, dtype=torch.bfloat16) * 0.1)(Fh, D))}
    _B_in = _B_setup()
    return {'A_x': _A_in['x'], 'A_wq': _A_in['wq'], 'A_wk': _A_in['wk'], 'A_wv': _A_in['wv'], 'A_wo': _A_in['wo'], 'B_x': _B_in['x'], 'B_w_in': _B_in['w_in'], 'B_w_sp': _B_in['w_sp'], 'B_b_sp': _B_in['b_sp'], 'B_w_out': _B_in['w_out']}

def reference(A_x, A_wq, A_wk, A_wv, A_wo, B_x, B_w_in, B_w_sp, B_b_sp, B_w_out):

    def _A_ref(x, wq, wk, wv, wo):
        B, S, D = x.shape
        H = 32
        Dh = D // H
        q = (lambda x, *d: x.permute(*d))((x @ wq).reshape(B, S, H, Dh), 0, 2, 1, 3)
        k = x @ wk
        v = x @ wv
        scores = torch.einsum('bhqd,bkd->bhqk', q, k) * Dh ** (-0.5)
        p = _softmax(scores, axis=-1)
        o = torch.einsum('bhqk,bkd->bhqd', p, v)
        return (lambda x, *d: x.permute(*d))(o, 0, 2, 1, 3).reshape(B, S, H * Dh) @ wo

    def _B_ref(x, w_in, w_sp, b_sp, w_out):
        B, S, D = x.shape
        F = w_in.shape[-1]
        Fh = F // 2
        z = _gelu(torch.einsum('bsd,df->bsf', x, w_in))
        z1 = z[..., :Fh].clone()
        z2 = _layernorm(z[..., Fh:], axis=-1)
        g = torch.einsum('ts,bsf->btf', w_sp, z2) + b_sp[None, :, None]
        return torch.einsum('bsf,fd->bsd', z1 * g, w_out)
    x = A_x
    xn = _layernorm(x, axis=-1)
    return x + _A_ref(xn, A_wq, A_wk, A_wv, A_wo) + _B_ref(xn, B_w_in, B_w_sp, B_b_sp, B_w_out)
```

```cpp
#include <hip/hip_runtime.h>
#include <hip/hip_bf16.h>
#include <stdint.h>

typedef unsigned short u16;
typedef short bf16x8 __attribute__((ext_vector_type(8)));
typedef short s16x4 __attribute__((ext_vector_type(4)));
typedef float f32x4 __attribute__((ext_vector_type(4)));
typedef float f32x16 __attribute__((ext_vector_type(16)));
typedef unsigned u32x4 __attribute__((ext_vector_type(4)));
typedef unsigned u32x2 __attribute__((ext_vector_type(2)));
#define LAS __attribute__((address_space(3)))
#define DI __device__ __forceinline__

constexpr int S_ = 2048, D_ = 576, FH = 2304, F_ = 4608, KU = 2880;

constexpr size_t WS_XN = 0;
constexpr size_t WS_Q = WS_XN + (size_t)S_ * D_ * 2;
constexpr size_t WS_KV = WS_Q + (size_t)S_ * D_ * 2;
constexpr size_t WS_Z1 = WS_KV + 64 * 3072;
constexpr size_t WS_Z2 = WS_Z1 + (size_t)S_ * FH * 2;
constexpr size_t WS_UO = WS_Z2 + (size_t)S_ * FH * 2;
constexpr size_t WS_ST = WS_UO + (size_t)S_ * KU * 2;

typedef float f32x2 __attribute__((ext_vector_type(2)));
typedef __bf16 bf16x2_t __attribute__((ext_vector_type(2)));
DI unsigned cvtpk(float lo, float hi) { f32x2 v = {lo, hi}; return __builtin_bit_cast(unsigned, __builtin_convertvector(v, bf16x2_t)); }
DI float bf2f(u16 b) { return __uint_as_float(((unsigned)b) << 16); }
DI float bflo(unsigned w) { return __uint_as_float(w << 16); }
DI float bfhi(unsigned w) { return __uint_as_float(w & 0xffff0000u); }

DI void glds16(const void* g, unsigned lds_base) {
    unsigned sv;
    asm volatile("s_mov_b32 %0, m0\n\ts_mov_b32 m0, %2\n\ts_nop 0\n\tglobal_load_lds_dwordx4 %1, off\n\ts_mov_b32 m0, %0"
                 : "=&s"(sv) : "v"(g), "s"(lds_base) : "memory");
}
DI s16x4 trread(const LAS char* p) {
    return __builtin_bit_cast(s16x4, __builtin_amdgcn_ds_read_tr16_b64_v4i16((LAS s16x4*)p));
}
template <int N> DI void wait_vm_bar() { asm volatile("s_waitcnt vmcnt(%0) lgkmcnt(0)\n\ts_barrier" ::"n"(N) : "memory"); }

DI float gelu_t(float x) {
    const float c0 = -2.0f * 1.4426950408889634f * 0.7978845608028654f;
    const float c1 = c0 * 0.044715f;
    float x2 = x * x;
    float t = __builtin_fmaf(x2, c1, c0);
    float e = __builtin_amdgcn_exp2f(x * t);
    return x * __builtin_amdgcn_rcpf(1.0f + e);
}

__global__ __launch_bounds__(256) void ln_kernel(const u16* __restrict__ x, u16* __restrict__ xn) {
    int row = blockIdx.x * 4 + (threadIdx.x >> 6), lane = threadIdx.x & 63;
    const u32x4* src = (const u32x4*)(x + (size_t)row * D_);
    u32x4 a = src[lane], b = {0, 0, 0, 0};
    if (lane < 8) b = src[64 + lane];
    float v[16];
#pragma unroll
    for (int i = 0; i < 4; ++i) { v[2 * i] = bflo(a[i]); v[2 * i + 1] = bfhi(a[i]); v[8 + 2 * i] = bflo(b[i]); v[8 + 2 * i + 1] = bfhi(b[i]); }
    float s = 0;
#pragma unroll
    for (int i = 0; i < 16; ++i) s += v[i];
#pragma unroll
    for (int o = 32; o >= 1; o >>= 1) s += __shfl_xor(s, o);
    float m = s * (1.0f / D_), q = 0;
#pragma unroll
    for (int i = 0; i < 8; ++i) { float d = v[i] - m; q += d * d; }
    if (lane < 8) {
#pragma unroll
        for (int i = 8; i < 16; ++i) { float d = v[i] - m; q += d * d; }
    }
#pragma unroll
    for (int o = 32; o >= 1; o >>= 1) q += __shfl_xor(q, o);
    float r = 1.0f / sqrtf(q * (1.0f / D_) + 1e-5f);
    u32x4 oa, ob;
#pragma unroll
    for (int i = 0; i < 4; ++i) { oa[i] = cvtpk((v[2 * i] - m) * r, (v[2 * i + 1] - m) * r); ob[i] = cvtpk((v[8 + 2 * i] - m) * r, (v[8 + 2 * i + 1] - m) * r); }
    u32x4* dst = (u32x4*)(xn + (size_t)row * D_);
    dst[lane] = oa;
    if (lane < 8) dst[64 + lane] = ob;
}

struct Cnt { int a, b; };
template <int NK, int NA, int NBS> constexpr Cnt gemm_count(int I) {
    int na = 0, nb = 0; bool found = false; (void)found;
    int lo = -(NBS > 2 * NA ? NBS : 2 * NA) - 2;
    if (I + 1 >= NK) return Cnt{0, 0};
    for (int i = lo; i < (I < 0 ? 0 : I); ++i) {
        int bi = i + NBS;
        if (bi >= 0 && bi < NK) { if (bi == I + 1) { na = 0; nb = 0; } else nb++; }
        if (i & 1) { int ai = (i - 1) / 2 + NA; if (i < 0) ai = -((-i + 1) / 2) + NA;
            if (ai >= 0 && ai < NK / 2) { if (ai == (I + 1) / 2) { na = 0; nb = 0; } else na++; } }
    }
    return Cnt{na, nb};
}

template <int NW, int MF, int NB2, int NK, int NA, int NBS, int WC, int MFW, int NFW, int SMF>
struct Gemm {
    static constexpr int ASLOT = MF * 2048, BSLOT = NB2 * 2048, AI = MF * 2, BI = NB2 * 2;
    static constexpr int AW = (AI + NW - 1) / NW, BW = (BI + NW - 1) / NW;
    static constexpr int LDS_BYTES = NA * ASLOT + NBS * BSLOT;
    static_assert(AI % NW == 0, "A instrs must divide");
    static constexpr int BFULL = BI - (BW - 1) * NW;
    struct Frags { bf16x8 a[MFW]; bf16x8 b[NFW]; bf16x8 sa[SMF > 0 ? 1 : 1]; bf16x8 sb; };

    const char* ap[AW]; const char* bp[BW]; unsigned bstep[BW];
    unsigned lds0; const LAS char* lds; int wid, lane, wr, wc;
    unsigned aoff[2], boff[2];

    DI void issueA(int slot) {
#pragma unroll
        for (int s = 0; s < AW; ++s) { glds16(ap[s], lds0 + slot * ASLOT + (wid + NW * s) * 1024); ap[s] += 128; }
    }
    DI void issueB(int slot) {
#pragma unroll
        for (int s = 0; s < BW; ++s) {
            if (s < BW - 1 || BFULL == NW || wid < BFULL) { glds16(bp[s], lds0 + NA * ASLOT + slot * BSLOT + (wid + NW * s) * 1024); }
            bp[s] += bstep[s];
        }
    }
    template <int I> DI void wait() {
        constexpr Cnt c = gemm_count<NK, NA, NBS>(I);
        if (BFULL == NW || wid < BFULL) wait_vm_bar<c.a * AW + c.b * BW>(); else wait_vm_bar<c.a * AW + c.b * (BW - 1)>();
    }
    DI void read(Frags& f, int aslot, int ks, int bslot) {
        const LAS char* pa = lds + aslot * ASLOT + aoff[ks];
#pragma unroll
        for (int m = 0; m < MFW; ++m) f.a[m] = *(const LAS bf16x8*)(pa + (wr * MFW + m) * 2048);
        const LAS char* pb0 = lds + NA * ASLOT + bslot * BSLOT + boff[0];
        const LAS char* pb1 = lds + NA * ASLOT + bslot * BSLOT + boff[1];
#pragma unroll
        for (int n = 0; n < NFW; ++n) {
            int nf = wc * NFW + n;
            const LAS char* p = ((nf & 1) ? pb1 : pb0) + (nf >> 1) * 512;
            s16x4 lo = trread(p), hi = trread(p + 256);
            f.b[n] = __builtin_shufflevector(lo, hi, 0, 1, 2, 3, 4, 5, 6, 7);
        }
        if constexpr (SMF > 0) {
            constexpr int nf = WC * NFW;
            const LAS char* p = ((nf & 1) ? pb1 : pb0) + (nf >> 1) * 512;
            s16x4 lo = trread(p), hi = trread(p + 256);
            f.sb = __builtin_shufflevector(lo, hi, 0, 1, 2, 3, 4, 5, 6, 7);
        }
    }
    DI void mma(const Frags& f, f32x4 (&acc)[MFW][NFW], f32x4 (&sacc)[SMF > 0 ? SMF : 1]) {
#pragma unroll
        for (int n = 0; n < NFW; ++n)
#pragma unroll
            for (int m = 0; m < MFW; ++m) acc[m][n] = __builtin_amdgcn_mfma_f32_16x16x32_bf16(f.b[n], f.a[m], acc[m][n], 0, 0, 0);
        if constexpr (SMF > 0) {
#pragma unroll
            for (int s = 0; s < SMF; ++s) {
                bf16x8 a = f.a[s];
#pragma unroll
                for (int c = 1; c < WC; ++c) if (wc == c) a = f.a[c * SMF + s];
                sacc[s] = __builtin_amdgcn_mfma_f32_16x16x32_bf16(f.sb, a, sacc[s], 0, 0, 0);
            }
        }
    }
    template <int I> DI void step(int i, int& ca, int& cb, Frags& cur, Frags& nxt, f32x4 (&acc)[MFW][NFW], f32x4 (&sacc)[SMF > 0 ? SMF : 1]) {
        wait<I>();
        if constexpr (I + NBS < NK) issueB(cb);
        if constexpr ((I & 1) && (I / 2 + NA < NK / 2)) issueA(ca);
        int nb = cb + 1 == NBS ? 0 : cb + 1;
        int na = ca; if constexpr (I & 1) na = ca + 1 == NA ? 0 : ca + 1;
        if constexpr (I + 1 < NK) read(nxt, na, (I + 1) & 1, nb);
        mma(cur, acc, sacc);
        ca = na; cb = nb;
    }
    template <int I> DI void tail(int& ca, int& cb, Frags& f0, Frags& f1, f32x4 (&acc)[MFW][NFW], f32x4 (&sacc)[SMF > 0 ? SMF : 1]) {
        if constexpr (I < NK) { step<I>(I, ca, cb, f0, f1, acc, sacc); step<I + 1>(I + 1, ca, cb, f1, f0, acc, sacc); tail<I + 2>(ca, cb, f0, f1, acc, sacc); }
    }
    template <class Hook> DI void run(f32x4 (&acc)[MFW][NFW], f32x4 (&sacc)[SMF > 0 ? SMF : 1], Hook hook) {
        constexpr int lo = -(NBS > 2 * NA ? NBS : 2 * NA) - 2;
#pragma unroll
        for (int i = lo; i < 0; ++i) {
            if (i + NBS >= 0) issueB(i + NBS);
            if (i & 1) { int ai = -((-i + 1) / 2) + NA; if (ai >= 0) issueA(ai); }
        }
        Frags f0, f1;
        wait<-1>();
        read(f0, 0, 0, 0);
        int ca = 0, cb = 0;
        constexpr int TAIL = ((NBS > 2 * NA ? NBS : 2 * NA) + 1) & ~1;
        constexpr int MAIN = NK - TAIL > 0 ? NK - TAIL : 0;
        for (int i = 0; i < MAIN; i += 2) {
            hook(i);
            step<0>(i, ca, cb, f0, f1, acc, sacc);
            step<1>(i + 1, ca, cb, f1, f0, acc, sacc);
        }
        tail<MAIN>(ca, cb, f0, f1, acc, sacc);
    }
    DI void init(char* smem) {
        lds = (const LAS char*)smem; lds0 = (unsigned)(uintptr_t)smem;
        lane = threadIdx.x & 63; wid = __builtin_amdgcn_readfirstlane(threadIdx.x >> 6); wr = wid / WC; wc = wid % WC;
        int r = lane & 15, g = lane >> 4;
        aoff[0] = r * 128 + ((g ^ (r >> 1)) * 16); aoff[1] = r * 128 + (((4 + g) ^ (r >> 1)) * 16);
        int q = (lane & 15) >> 2, p = lane & 3;
        boff[0] = g * NB2 * 512 + q * 64 + ((0 ^ (g & 1)) * 32) + 8 * p;
        boff[1] = g * NB2 * 512 + q * 64 + ((1 ^ (g & 1)) * 32) + 8 * p;
    }
    DI void initA(const u16* A, int lda) {
#pragma unroll
        for (int s = 0; s < AW; ++s) { int ii = wid + NW * s, row = 8 * ii + (lane >> 3), c = (lane & 7) ^ ((row >> 1) & 7);
            ap[s] = (const char*)(A + (size_t)row * lda + 8 * c); }
    }
    DI void bcoord(int s, int& k, int& n) {
        int ii = wid + NW * s; if (ii >= BI) ii = BI - 1;
        int b = 2 * ii + (lane >> 5), row = (lane >> 2) & 7, pc = lane & 3, kb = b / NB2, blk = b % NB2;
        k = 8 * kb + row; n = 32 * blk + 8 * (pc ^ (2 * (kb & 1)));
    }
};

using G1 = Gemm<8, 8, 11, 18, 3, 4, 4, 4, 5, 1>;
constexpr float QSCALE = 0.23570226039551584f * 1.4426950408889634f;

__global__ __launch_bounds__(512, 2) void gemm1_kernel(const u16* __restrict__ xn, const u16* __restrict__ wq, const u16* __restrict__ wk,
                                                       const u16* __restrict__ wv, const u16* __restrict__ w_in, char* __restrict__ ws) {
    extern __shared__ __attribute__((aligned(16))) char smem[];
    int bid = blockIdx.x;
    int xcd = bid & 7, idx = bid >> 3;
    int pm = (xcd & 1) * 8 + (idx & 7), j = (xcd >> 1) * 4 + (idx >> 3);
    G1 g; g.init(smem);
    g.initA(xn + (size_t)pm * 128 * D_, D_);
#pragma unroll
    for (int s = 0; s < G1::BW; ++s) {
        int k, n; g.bcoord(s, k, n);
        if (n >= 336) n -= 16;
        const u16* p; unsigned rs;
        if (n < 144) { p = w_in + 144 * j + n; rs = F_; }
        else if (n < 288) { p = w_in + FH + 144 * j + (n - 144); rs = F_; }
        else if (n < 320) { p = wq + 32 * j + (n - 288); rs = D_; }
        else {
            int h = (n - 320) >> 3;
            if (j < 4) { p = wq + 512 + 16 * j + 8 * h; rs = D_; }
            else if (j == 5) { p = h ? wv : wk + 10; rs = 18; }
            else if (j == 6) { p = h ? wv + 10 : wv + 8; rs = 18; }
            else { p = wk + 8 * h; rs = 18; }
        }
        g.bp[s] = (const char*)(p + (size_t)k * rs); g.bstep[s] = 32 * rs * 2;
    }
    f32x4 acc[4][5] = {}; f32x4 sacc[1] = {};
    g.run(acc, sacc, [](int) {});
    int lane = g.lane, c = lane & 15, gq = lane >> 4, wr = g.wr, wc = g.wc;
    u16* z1 = (u16*)(ws + WS_Z1); u16* z2 = (u16*)(ws + WS_Z2); u16* q = (u16*)(ws + WS_Q);
    float ssum[4] = {0, 0, 0, 0}, ssq[4] = {0, 0, 0, 0};
#pragma unroll
    for (int m = 0; m < 4; ++m) {
        int row = pm * 128 + wr * 64 + m * 16 + c;
#pragma unroll
        for (int n = 0; n < 5; ++n) {
            int nf = wc * 5 + n;
            f32x4 v = acc[m][n];
            if (nf < 18) {
#pragma unroll
                for (int r = 0; r < 4; ++r) v[r] = gelu_t(v[r]);
                u32x2 w = {cvtpk(v[0], v[1]), cvtpk(v[2], v[3])};
                if (nf < 9) *(u32x2*)(z1 + (size_t)row * FH + 144 * j + nf * 16 + 4 * gq) = w;
                else {
                    *(u32x2*)(z2 + (size_t)row * FH + 144 * j + (nf - 9) * 16 + 4 * gq) = w;
                    ssum[m] += (v[0] + v[1]) + (v[2] + v[3]); ssq[m] += (v[0] * v[0] + v[1] * v[1]) + (v[2] * v[2] + v[3] * v[3]);
                }
            } else {
                u32x2 w = {cvtpk(v[0] * QSCALE, v[1] * QSCALE), cvtpk(v[2] * QSCALE, v[3] * QSCALE)};
                *(u32x2*)(q + (size_t)row * D_ + 32 * j + (nf - 18) * 16 + 4 * gq) = w;
            }
        }
    }
    {
        int row = pm * 128 + wr * 64 + wc * 16 + c; f32x4 v = sacc[0];
        if (j < 4) {
            u32x2 w = {cvtpk(v[0] * QSCALE, v[1] * QSCALE), cvtpk(v[2] * QSCALE, v[3] * QSCALE)};
            *(u32x2*)(q + (size_t)row * D_ + 512 + 16 * j + 4 * gq) = w;
        } else if (j < 7) {
            char* kv = ws + WS_KV + (size_t)(row >> 5) * 3072; int key = row & 31;
#pragma unroll
            for (int r = 0; r < 4; ++r) {
                int col = 4 * gq + r, h = col >> 3, e = col & 7; bool isv; int d;
                if (j == 4) { isv = false; d = col; }
                else if (j == 5) { isv = h; d = h ? e : 10 + e; if (!h && e < 6) d = -1; }
                else { isv = true; d = h ? 10 + e : 8 + e; if (h && e < 6) d = -1; }
                u16 b = (u16)(cvtpk(v[r], 0.f) & 0xffff);
                if (d >= 0) {
                    if (!isv) {
                        if (d < 16) *(u16*)(kv + ((d >> 3) * 32 + key) * 16 + (d & 7) * 2) = b;
                        else *(u16*)(kv + 1024 + 24 * 16 + key * 4 + (d - 16) * 2) = b;
                    } else {
                        int s = key >> 4, kk = key & 15, hh = (kk >> 2) & 1, jj = (kk & 3) + 4 * (kk >> 3);
                        *(u16*)(kv + 1024 + s * 1024 + (hh * 32 + d) * 16 + jj * 2) = b;
                    }
                }
            }
            if (j == 4 && gq == 0) {
                int s = key >> 4, kk = key & 15, hh = (kk >> 2) & 1, jj = (kk & 3) + 4 * (kk >> 3);
                *(u16*)(kv + 1024 + s * 1024 + (hh * 32 + 18) * 16 + jj * 2) = 0x3f80;
            }
        }
    }
    __syncthreads();
    float* red = (float*)smem;
#pragma unroll
    for (int m = 0; m < 4; ++m) {
        float a = ssum[m], b = ssq[m];
        a += __shfl_xor(a, 16); a += __shfl_xor(a, 32); b += __shfl_xor(b, 16); b += __shfl_xor(b, 32);
        if (gq == 0) { int r = wr * 64 + m * 16 + c; red[(r * 4 + wc) * 2] = a; red[(r * 4 + wc) * 2 + 1] = b; }
    }
    __syncthreads();
    if (threadIdx.x < 128) {
        int r = threadIdx.x; float a = 0, b = 0;
#pragma unroll
        for (int w = 1; w < 4; ++w) { a += red[(r * 4 + w) * 2]; b += red[(r * 4 + w) * 2 + 1]; }
        float2* st = (float2*)(ws + WS_ST);
        st[(size_t)(pm * 128 + r) * 16 + j] = make_float2(a, b);
    }
}

__global__ __launch_bounds__(256) void norm_kernel(char* __restrict__ ws) {
    int row = blockIdx.x * 4 + (threadIdx.x >> 6), lane = threadIdx.x & 63;
    const float2* st = (const float2*)(ws + WS_ST) + (size_t)row * 16;
    float2 p = st[lane & 15];
    float a = p.x, b = p.y;
#pragma unroll
    for (int o = 8; o >= 1; o >>= 1) { a += __shfl_xor(a, o); b += __shfl_xor(b, o); }
    float m = a * (1.0f / FH), var = b * (1.0f / FH) - m * m; var = var < 0 ? 0 : var;
    float r = 1.0f / sqrtf(var + 1e-5f);
    u32x4* z = (u32x4*)(ws + WS_Z2 + (size_t)row * FH * 2);
    for (int c = lane; c < FH / 8; c += 64) {
        u32x4 v = z[c], o;
#pragma unroll
        for (int i = 0; i < 4; ++i) o[i] = cvtpk((bflo(v[i]) - m) * r, (bfhi(v[i]) - m) * r);
        z[c] = o;
    }
}

#define MFMA32(a, b, c) __builtin_amdgcn_mfma_f32_32x32x16_bf16(a, b, c, 0, 0, 0)
constexpr float ATT_BIAS = 64.0f;
DI float xhalf_max(float m) { auto rr = __builtin_amdgcn_permlane32_swap(__float_as_uint(m), __float_as_uint(m), false, false); return fmaxf(__uint_as_float(rr[0]), __uint_as_float(rr[1])); }
DI float lo_bcast(float m) { auto rr = __builtin_amdgcn_permlane32_swap(__float_as_uint(m), __float_as_uint(m), false, false); return __uint_as_float(rr[0]); }
DI float bf_round(float x) { return __uint_as_float(cvtpk(x, 0.f) << 16); }
DI float max16(const f32x16& s) {
    float a = fmaxf(fmaxf(s[0], s[1]), s[2]), b = fmaxf(fmaxf(s[3], s[4]), s[5]);
    a = fmaxf(fmaxf(a, s[6]), s[7]); b = fmaxf(fmaxf(b, s[8]), s[9]); a = fmaxf(fmaxf(a, s[10]), s[11]); b = fmaxf(fmaxf(b, s[12]), s[13]);
    a = fmaxf(fmaxf(a, s[14]), s[15]); return fmaxf(a, b);
}
DI void exp_pack(const f32x16& s, bf16x8& p0, bf16x8& p1) {
    float e[16];
#pragma unroll
    for (int i = 0; i < 16; ++i) e[i] = __builtin_amdgcn_exp2f(s[i]);
    u32x4 a = {cvtpk(e[0], e[1]), cvtpk(e[2], e[3]), cvtpk(e[4], e[5]), cvtpk(e[6], e[7])};
    u32x4 b = {cvtpk(e[8], e[9]), cvtpk(e[10], e[11]), cvtpk(e[12], e[13]), cvtpk(e[14], e[15])};
    p0 = __builtin_bit_cast(bf16x8, a); p1 = __builtin_bit_cast(bf16x8, b);
}

__global__ __launch_bounds__(256, 2) void attn_kernel(char* __restrict__ ws) {
    extern __shared__ __attribute__((aligned(16))) char smem[];
    const int lane = threadIdx.x & 63, wid = __builtin_amdgcn_readfirstlane(threadIdx.x >> 6), c = lane & 31, h = lane >> 5;
    const unsigned lds0 = (unsigned)(uintptr_t)smem; const LAS char* lds = (const LAS char*)smem;
    const char* kvg = ws + WS_KV;
    constexpr int CH = 8 * 3072;
    auto issue = [&](int ch, int slot) {
#pragma unroll
        for (int s = 0; s < 6; ++s) glds16(kvg + (size_t)ch * CH + (wid * 6 + s) * 1024 + lane * 16, lds0 + slot * CH + (wid * 6 + s) * 1024);
    };
    issue(0, 0);
    const int row0 = blockIdx.x * 8 + wid * 2;
    bf16x8 q1[2], q2[2]; float mref[2];
    const f32x16 zero = {};
#pragma unroll
    for (int t = 0; t < 2; ++t) {
        const u16* qr = (const u16*)(ws + WS_Q) + (size_t)(row0 + t) * D_ + 18 * c;
        u32x4 a; const unsigned* qp = (const unsigned*)(qr + 8 * h);
        a[0] = qp[0]; a[1] = qp[1]; a[2] = qp[2]; a[3] = qp[3];
        q1[t] = __builtin_bit_cast(bf16x8, a);
        unsigned e = *(const unsigned*)(qr + 16);
        u32x4 b = {h ? 0u : e, 0u, 0u, 0u};
        q2[t] = __builtin_bit_cast(bf16x8, b);
    }
    f32x16 o[2] = {zero, zero};
    bool bad = false;
    asm volatile("s_waitcnt vmcnt(0)\n\ts_barrier" ::: "memory");
    {
#pragma unroll
        for (int t = 0; t < 2; ++t) {
            float m = -3.0e38f;
#pragma unroll
            for (int kt = 0; kt < 2; ++kt) {
                bf16x8 k1 = *(const LAS bf16x8*)(lds + kt * 3072 + lane * 16);
                u32x4 k2v = {*(const LAS unsigned*)(lds + kt * 3072 + 1024 + 384 + c * 4), 0x3f80u, 0u, 0u};
                f32x16 s = MFMA32(k1, q1[t], zero); s = MFMA32(__builtin_bit_cast(bf16x8, k2v), q2[t], s);
                m = fmaxf(m, max16(s));
            }
            m = xhalf_max(m);
            mref[t] = bf_round(rintf(m) + ATT_BIAS);
            u32x4 b = __builtin_bit_cast(u32x4, q2[t]); b[1] = h ? 0u : (__float_as_uint(-mref[t]) >> 16); q2[t] = __builtin_bit_cast(bf16x8, b);
        }
    }
    for (int ch = 0; ch < 8; ++ch) {
        if (ch > 0) asm volatile("s_waitcnt vmcnt(0) lgkmcnt(0)\n\ts_barrier" ::: "memory");
        if (ch + 1 < 8) issue(ch + 1, (ch + 1) & 1);
        const LAS char* base = lds + (ch & 1) * CH;
#pragma unroll 2
        for (int kt = 0; kt < 8; ++kt) {
            const LAS char* tb = base + kt * 3072;
            bf16x8 k1 = *(const LAS bf16x8*)(tb + lane * 16);
            u32x4 k2v = {*(const LAS unsigned*)(tb + 1024 + 384 + c * 4), 0x3f80u, 0u, 0u};
            bf16x8 v0 = *(const LAS bf16x8*)(tb + 1024 + lane * 16), v1 = *(const LAS bf16x8*)(tb + 2048 + lane * 16);
#pragma unroll
            for (int t = 0; t < 2; ++t) {
                f32x16 s = MFMA32(k1, q1[t], zero); s = MFMA32(__builtin_bit_cast(bf16x8, k2v), q2[t], s);
                bf16x8 p0, p1; exp_pack(s, p0, p1);
                o[t] = MFMA32(v0, p0, o[t]); o[t] = MFMA32(v1, p1, o[t]);
            }
        }
#pragma unroll
        for (int t = 0; t < 2; ++t) {
            float l = o[t][10];
            bool need = (h == 0) && !(l <= 0x1p-48f);
            if (__builtin_expect(__any(need), 0)) {
                bool nf = false;
#pragma unroll
                for (int i = 0; i < 11; ++i) nf = nf || (!(fabsf(o[t][i]) <= 3.0e38f) && (i < 8 || h == 0));
                if (__any(nf)) bad = true;
                else {
                    float lb = __shfl(l, c);
                    int e = __builtin_amdgcn_frexp_expf(lb);
                    float d = (float)(e + (int)ATT_BIAS); d = d < 0 ? 0 : d;
                    float nm = bf_round(mref[t] + d); float dd = nm - mref[t]; mref[t] = nm;
                    int sh = -(int)dd;
#pragma unroll
                    for (int i = 0; i < 16; ++i) o[t][i] = __builtin_amdgcn_ldexpf(o[t][i], sh);
                    u32x4 b = __builtin_bit_cast(u32x4, q2[t]); b[1] = h ? 0u : (__float_as_uint(-nm) >> 16); q2[t] = __builtin_bit_cast(bf16x8, b);
                }
            }
        }
    }
    if (__builtin_expect(bad, 0)) {
#pragma unroll 1
        for (int t = 0; t < 2; ++t) {
            bf16x8 qa = t ? q1[1] : q1[0]; u32x4 qb = __builtin_bit_cast(u32x4, t ? q2[1] : q2[0]); qb[1] = 0;
            f32x16 oo = zero; float m = -3.0e38f;
            for (int kt = 0; kt < 64; ++kt) {
                const char* tb = kvg + (size_t)kt * 3072;
                bf16x8 k1 = *(const bf16x8*)(tb + lane * 16);
                u32x4 k2v = {*(const unsigned*)(tb + 1024 + 384 + c * 4), 0u, 0u, 0u};
                bf16x8 v0 = *(const bf16x8*)(tb + 1024 + lane * 16), v1 = *(const bf16x8*)(tb + 2048 + lane * 16);
                f32x16 s = MFMA32(k1, qa, zero); s = MFMA32(__builtin_bit_cast(bf16x8, k2v), __builtin_bit_cast(bf16x8, qb), s);
                float nm = fmaxf(m, xhalf_max(max16(s)));
                float sc = __builtin_amdgcn_exp2f(m - nm); m = nm;
#pragma unroll
                for (int i = 0; i < 16; ++i) { oo[i] *= sc; s[i] -= nm; }
                bf16x8 p0, p1; exp_pack(s, p0, p1);
                oo = MFMA32(v0, p0, oo); oo = MFMA32(v1, p1, oo);
            }
            if (t) o[1] = oo; else o[0] = oo;
        }
    }
#pragma unroll
    for (int t = 0; t < 2; ++t) {
        float l = __shfl(o[t][10], c);
        float inv = 1.0f / l;
        u16* dst = (u16*)(ws + WS_UO) + (size_t)(row0 + t) * KU + FH + 18 * c;
        unsigned w0 = cvtpk(o[t][0] * inv, o[t][1] * inv), w1 = cvtpk(o[t][2] * inv, o[t][3] * inv);
        unsigned w2 = cvtpk(o[t][4] * inv, o[t][5] * inv), w3 = cvtpk(o[t][6] * inv, o[t][7] * inv);
        unsigned w4 = cvtpk(o[t][8] * inv, o[t][9] * inv);
        unsigned* p = (unsigned*)(dst + 4 * h);
        p[0] = w0; p[1] = w1; p[4] = w2; p[5] = w3;
        if (h == 0) p[8] = w4;
    }
}

using G2 = Gemm<4, 8, 5, 64, 3, 4, 2, 4, 4, 2>;
__global__ __launch_bounds__(256, 2) void gemm2_kernel(const u16* __restrict__ w_sp, const u16* __restrict__ b_sp, char* __restrict__ ws) {
    extern __shared__ __attribute__((aligned(16))) char smem[];
    int bid = blockIdx.x, xcd = bid & 7, idx = bid >> 3;
    int pm = (xcd & 1) * 8 + (idx & 7), j = (xcd >> 1) * 4 + (idx >> 3);
    G2 g; g.init(smem);
    g.initA(w_sp + (size_t)pm * 128 * S_, S_);
    const u16* z2 = (const u16*)(ws + WS_Z2);
#pragma unroll
    for (int s = 0; s < G2::BW; ++s) {
        int k, n; g.bcoord(s, k, n); if (n >= 144) n -= 16;
        g.bp[s] = (const char*)(z2 + (size_t)k * FH + 144 * j + n); g.bstep[s] = 32 * FH * 2;
    }
    f32x4 acc[4][4] = {}; f32x4 sacc[2] = {};
    g.run(acc, sacc, [](int) {});
    int lane = g.lane, c = lane & 15, gq = lane >> 4, wr = g.wr, wc = g.wc;
    const u16* z1 = (const u16*)(ws + WS_Z1); u16* u = (u16*)(ws + WS_UO);
    auto fin = [&](int row, int col, f32x4 v) {
        float b = bf2f(b_sp[row]);
        u32x2 z = *(const u32x2*)(z1 + (size_t)row * FH + col);
        u32x2 w = {cvtpk(bflo(z[0]) * (v[0] + b), bfhi(z[0]) * (v[1] + b)), cvtpk(bflo(z[1]) * (v[2] + b), bfhi(z[1]) * (v[3] + b))};
        *(u32x2*)(u + (size_t)row * KU + col) = w;
    };
#pragma unroll
    for (int m = 0; m < 4; ++m)
#pragma unroll
        for (int n = 0; n < 4; ++n) fin(pm * 128 + wr * 64 + m * 16 + c, 144 * j + (wc * 4 + n) * 16 + 4 * gq, acc[m][n]);
#pragma unroll
    for (int s = 0; s < 2; ++s) fin(pm * 128 + wr * 64 + (wc * 2 + s) * 16 + c, 144 * j + 128 + 4 * gq, sacc[s]);
}

using G3 = Gemm<4, 4, 3, 90, 3, 4, 2, 2, 3, 0>;
__global__ __launch_bounds__(256, 2) void gemm3_kernel(const u16* __restrict__ x, const u16* __restrict__ w_out, const u16* __restrict__ wo,
                                                       const char* __restrict__ ws, u16* __restrict__ out) {
    extern __shared__ __attribute__((aligned(16))) char smem[];
    int bid = blockIdx.x;
    int xcd = bid & 7, idx = bid >> 3;
    int pm = xcd * 4 + (idx & 3), j = idx >> 2;
    G3 g; g.init(smem);
    g.initA((const u16*)(ws + WS_UO) + (size_t)pm * 64 * KU, KU);
#pragma unroll
    for (int s = 0; s < G3::BW; ++s) {
        int k, n; g.bcoord(s, k, n);
        g.bp[s] = (const char*)(w_out + (size_t)k * D_ + 96 * j + n); g.bstep[s] = 32 * D_ * 2;
    }
    long delta = (const char*)wo - (const char*)(w_out + (size_t)FH * D_);
    f32x4 acc[2][3] = {}; f32x4 sacc[1] = {};
    g.run(acc, sacc, [&](int i) { if (i == 68) {
#pragma unroll
        for (int s = 0; s < G3::BW; ++s) g.bp[s] += delta; } });
    int lane = g.lane, c = lane & 15, gq = lane >> 4, wr = g.wr, wc = g.wc;
#pragma unroll
    for (int m = 0; m < 2; ++m)
#pragma unroll
        for (int n = 0; n < 3; ++n) {
            int row = pm * 64 + wr * 32 + m * 16 + c, col = 96 * j + (wc * 3 + n) * 16 + 4 * gq;
            u32x2 xv = *(const u32x2*)(x + (size_t)row * D_ + col); f32x4 v = acc[m][n];
            u32x2 w = {cvtpk(bflo(xv[0]) + v[0], bfhi(xv[0]) + v[1]), cvtpk(bflo(xv[1]) + v[2], bfhi(xv[1]) + v[3])};
            *(u32x2*)(out + (size_t)row * D_ + col) = w;
        }
}

extern "C" void kernel_launch(void* const* d_in, const int* in_sizes, int n_in, void* d_out, int out_size, void* d_ws, size_t ws_size, hipStream_t stream) {
    const u16* x = (const u16*)d_in[0]; const u16* wq = (const u16*)d_in[1]; const u16* wk = (const u16*)d_in[2]; const u16* wv = (const u16*)d_in[3];
    const u16* wo = (const u16*)d_in[4]; const u16* w_in = (const u16*)d_in[6]; const u16* w_sp = (const u16*)d_in[7]; const u16* b_sp = (const u16*)d_in[8];
    const u16* w_out = (const u16*)d_in[9]; char* ws = (char*)d_ws;
    (void)hipFuncSetAttribute((const void*)gemm1_kernel, hipFuncAttributeMaxDynamicSharedMemorySize, G1::LDS_BYTES);
    (void)hipFuncSetAttribute((const void*)gemm2_kernel, hipFuncAttributeMaxDynamicSharedMemorySize, G2::LDS_BYTES);
    (void)hipFuncSetAttribute((const void*)gemm3_kernel, hipFuncAttributeMaxDynamicSharedMemorySize, G3::LDS_BYTES);
    ln_kernel<<<512, 256, 0, stream>>>(x, (u16*)(ws + WS_XN));
    gemm1_kernel<<<256, 512, G1::LDS_BYTES, stream>>>((const u16*)(ws + WS_XN), wq, wk, wv, w_in, ws);
    norm_kernel<<<512, 256, 0, stream>>>(ws);
    attn_kernel<<<256, 256, 2 * 8 * 3072, stream>>>(ws);
    gemm2_kernel<<<256, 256, G2::LDS_BYTES, stream>>>(w_sp, b_sp, ws);
    gemm3_kernel<<<192, 256, G3::LDS_BYTES, stream>>>(x, w_out, wo, ws, (u16*)d_out);
}
```

```cpp
#include <hip/hip_runtime.h>
#include <hip/hip_bf16.h>
#include <stdint.h>

typedef unsigned short u16;
typedef short bf16x8 __attribute__((ext_vector_type(8)));
typedef short s16x4 __attribute__((ext_vector_type(4)));
typedef float f32x2 __attribute__((ext_vector_type(2)));
typedef float f32x4 __attribute__((ext_vector_type(4)));
typedef float f32x16 __attribute__((ext_vector_type(16)));
typedef unsigned u32x4 __attribute__((ext_vector_type(4)));
typedef unsigned u32x2 __attribute__((ext_vector_type(2)));
typedef __bf16 bf16x2_t __attribute__((ext_vector_type(2)));
#define LAS __attribute__((address_space(3)))
#define DI __device__ __forceinline__

constexpr int S_ = 2048, D_ = 576, FH = 2304, F_ = 4608;

constexpr size_t WS_Q = 0;
constexpr size_t WS_KV = WS_Q + (size_t)S_ * D_ * 2;
constexpr size_t WS_Z1 = WS_KV + 64 * 3072;
constexpr size_t WS_Z2T = WS_Z1 + (size_t)S_ * FH * 2;
constexpr size_t WS_UO = WS_Z2T + (size_t)S_ * FH * 2;
constexpr size_t WS_ST = WS_UO + (size_t)S_ * 2880 * 2;
constexpr size_t WS_XCH = WS_ST + (size_t)S_ * 16 * 8;
constexpr size_t WS_DBG = 128u << 20;
#define STAMP(kid, i)
#define STAMP_DRAIN()

DI unsigned cvtpk(float lo, float hi) { f32x2 v = {lo, hi}; return __builtin_bit_cast(unsigned, __builtin_convertvector(v, bf16x2_t)); }
DI float bf2f(u16 b) { return __uint_as_float(((unsigned)b) << 16); }
DI float bflo(unsigned w) { return __uint_as_float(w << 16); }
DI float bfhi(unsigned w) { return __uint_as_float(w & 0xffff0000u); }
#define SB() __builtin_amdgcn_sched_barrier(0)
#define PIN(x) asm volatile("" : "+v"(x))
#define EX(x) __builtin_amdgcn_exp2f(x)
#define MFMA16(a, b, c) __builtin_amdgcn_mfma_f32_16x16x32_bf16(a, b, c, 0, 0, 0)
#define MFMA32(a, b, c) __builtin_amdgcn_mfma_f32_32x32x16_bf16(a, b, c, 0, 0, 0)

DI s16x4 trread(const LAS char* p) { return __builtin_bit_cast(s16x4, __builtin_amdgcn_ds_read_tr16_b64_v4i16((LAS s16x4*)p)); }
template <int N> DI void wait_vm_bar() { asm volatile("s_waitcnt vmcnt(%0) lgkmcnt(0)\n\ts_barrier" ::"n"(N) : "memory"); }
template <int N> DI void wait_vm() { asm volatile("s_waitcnt vmcnt(%0)" ::"n"(N) : "memory"); }
template <int OFF> DI void glds_s(unsigned voff, const char* sbase) { asm volatile("global_load_lds_dwordx4 %0, %1 offset:%2" ::"v"(voff), "s"(sbase), "n"(OFF)); }
DI void glds_v(const char* p, unsigned m0v) { asm volatile("s_mov_b32 m0, %1\n\ts_nop 0\n\tglobal_load_lds_dwordx4 %0, off" ::"v"(p), "s"(m0v)); }
DI unsigned ld_sc1_async(const void* p) { unsigned v; asm volatile("global_load_dword %0, %1, off sc1" : "=v"(v) : "v"(p) : "memory"); return v; }
DI void st16(void* p, u32x4 v) { *(u32x4*)p = v; }
DI void st8(void* p, u32x2 v) { *(u32x2*)p = v; }
DI void set_m0(unsigned v) { asm volatile("s_mov_b32 m0, %0\n\ts_nop 0" ::"s"(v)); }
DI bf16x8 ldsA(const LAS char* p) { return *(const LAS bf16x8*)p; }
DI bf16x8 ldsB2(const LAS char* p0, const LAS char* p1) { s16x4 lo = trread(p0), hi = trread(p1); return __builtin_shufflevector(lo, hi, 0, 1, 2, 3, 4, 5, 6, 7); }
DI bf16x8 ldsB(const LAS char* p) { return ldsB2(p, p + 256); }

DI float gelu_t(float x) {
    const float c0 = -2.0f * 1.4426950408889634f * 0.7978845608028654f;
    const float c1 = c0 * 0.044715f;
    float x2 = x * x;
    float t = __builtin_fmaf(x2, c1, c0);
    float e = __builtin_amdgcn_exp2f(x * t);
    return x * __builtin_amdgcn_rcpf(1.0f + e);
}
DI int k2slot(int c) { return c < 13 ? 1024 + (19 + c) * 16 : (c < 25 ? 1024 + (51 + c - 13) * 16 : 2048 + (19 + c - 25) * 16); }
constexpr float QSCALE = 0.23570226039551584f * 1.4426950408889634f;

constexpr int K1_SLOT = 40960, K1_TAB = 3 * K1_SLOT, K1_LDS = K1_TAB + 8192;
typedef __attribute__((address_space(1))) unsigned gu32;
typedef __attribute__((address_space(1))) unsigned long long gu64;
#define RLX_AGENT __ATOMIC_RELAXED, __HIP_MEMORY_SCOPE_AGENT
struct K1F { bf16x8 a[4], b[3], x; };
struct K1S {
    const char* gp[5]; long st[5];
    unsigned m0;
    unsigned pa[2], pb[3]; int xo;
    const LAS char* lds;
};
DI void k1_issue(K1S& s, int i, unsigned wo) { glds_v(s.gp[i], s.m0 + wo + i * 1024); s.gp[i] += s.st[i]; }
DI f32x2 gelu_y2(f32x2 y, float k0, float k1, float ir) {
    f32x2 t = (y * y) * k1 + k0;
    f32x2 a = y * t;
    f32x2 e = {__builtin_amdgcn_exp2f(a[0]), __builtin_amdgcn_exp2f(a[1])};
    f32x2 d = e * ir + ir;
    f32x2 rc = {__builtin_amdgcn_rcpf(d[0]), __builtin_amdgcn_rcpf(d[1])};
    return y * rc;
}
template <int KS, int GL, int KBS> DI void k1_p1(K1S& s, const K1F& f, K1F& n, unsigned ro, unsigned wo, f32x4 (&acc)[4][3], f32x4 (&cso)[2], f32x4& rs, f32x4& rq, const bf16x8& ones) {
    const LAS char* aa = s.lds + (s.pa[KS] + ro); const LAS char* bb = s.lds + ro + KS * 4 * KBS;
#define MM(m, nn) acc[m][nn] = MFMA16(f.b[nn], f.a[m], acc[m][nn])
    MM(0, 0); n.b[0] = ldsB(bb + s.pb[0]); SB();
    MM(1, 0); n.a[0] = ldsA(aa); SB();
    MM(2, 0); n.a[1] = ldsA(aa + 2048); SB();
    MM(3, 0); n.a[2] = ldsA(aa + 4096); SB();
    cso[0] = MFMA16(f.b[0], ones, cso[0]); n.a[3] = ldsA(aa + 6144); SB();
    MM(0, 1); n.b[1] = ldsB(bb + s.pb[1]); SB();
    MM(1, 1); n.b[2] = ldsB(bb + s.pb[2]); SB();
    rs = MFMA16(ones, f.x, rs); SB();
    rq = MFMA16(f.x, f.x, rq); n.x = ldsA(aa + s.xo); SB();
    MM(2, 1); k1_issue(s, GL == 1 ? 0 : 3, wo); SB();
    MM(3, 1); SB();
    cso[1] = MFMA16(f.b[1], ones, cso[1]); SB();
    MM(0, 2); k1_issue(s, GL == 1 ? 1 : 4, wo); SB();
    MM(1, 2); SB();
    MM(2, 2); if (GL == 1) k1_issue(s, 2, wo); SB();
    MM(3, 2); SB();
#undef MM
}
template <int KS, int GL, bool RD, class E> DI void k1_p2(K1S& s, const K1F& f, K1F& n, unsigned ro, unsigned wo, f32x4 (&acc)[4][2], f32x4& sacc, f32x4 (&cso)[2], const bf16x8& ones, E epi) {
    const LAS char* aa = s.lds + (s.pa[KS] + ro); const LAS char* bb = s.lds + ro + KS * 4 * 2560;
#define MM(m, nn) acc[m][nn] = MFMA16(f.b[nn], f.a[m], acc[m][nn])
    MM(0, 0); if (RD) n.b[0] = ldsB(bb + s.pb[0]); SB();
    MM(1, 0); if (RD) n.a[0] = ldsA(aa); SB();
    MM(2, 0); if (RD) n.a[1] = ldsA(aa + 2048); epi(0); SB();
    MM(3, 0); if (RD) n.a[2] = ldsA(aa + 4096); SB();
    cso[0] = MFMA16(f.b[0], ones, cso[0]); if (RD) n.a[3] = ldsA(aa + 6144); SB();
    sacc = MFMA16(f.b[2], f.x, sacc); if (RD) n.b[1] = ldsB(bb + s.pb[1]); epi(1); SB();
    cso[1] = MFMA16(f.b[2], ones, cso[1]); if (RD) n.x = ldsA(aa + s.xo); SB();
    MM(0, 1); if (RD) n.b[2] = ldsB(bb + s.pb[2]); SB();
    MM(1, 1); if (GL) k1_issue(s, GL == 1 ? 0 : 3, wo); epi(2); SB();
    MM(2, 1); if (GL) k1_issue(s, GL == 1 ? 1 : 4, wo); SB();
    MM(3, 1); if (GL == 1) k1_issue(s, 2, wo); SB();
#undef MM
}

__global__ __launch_bounds__(512, 2) void k1_kernel(const u16* __restrict__ x, const u16* __restrict__ wq, const u16* __restrict__ wk, const u16* __restrict__ wv,
                                                    const u16* __restrict__ w_in, const u16* __restrict__ w_sp, char* __restrict__ ws) {
    extern __shared__ __attribute__((aligned(16))) char smem[];
    const int bid = blockIdx.x, xcd = bid & 7, idx = bid >> 3;
    const int pm = (xcd & 1) * 8 + (idx & 7), j = (xcd >> 1) * 4 + (idx >> 3);
    const int lane = threadIdx.x & 63, wid = __builtin_amdgcn_readfirstlane(threadIdx.x >> 6), wr = wid >> 2, wc = wid & 3;
    const unsigned lds0 = (unsigned)(uintptr_t)smem;
    STAMP(0, 0);
    gu32* XN = (gu32*)(ws + WS_XCH) + pm * 16;
    gu32* XE = (gu32*)(ws + WS_XCH + 4096) + pm * 256;
    gu64* XP = (gu64*)(ws + WS_ST) + (size_t)pm * 16 * 128;
    unsigned eo = ld_sc1_async(ws + WS_XCH + 4096 + (pm * 256 + j * 16 + (lane & 15)) * 4);
    K1S s; s.lds = (const LAS char*)smem; s.m0 = lds0 + wid * 5120;
#pragma unroll
    for (int i = 0; i < 5; ++i) {
        const int e = wid * 5 + i;
        if (e < 16) {
            int row = 8 * e + (lane >> 3), c = (lane & 7) ^ ((row >> 1) & 7);
            s.gp[i] = (const char*)(x + (size_t)(pm * 128 + row) * D_ + 8 * c); s.st[i] = 128;
        } else {
            int pe = e - 16, kb = pe / 3, blk = 2 * (pe % 3) + (lane >> 5), row = (lane >> 2) & 7, pc = lane & 3;
            int k = 8 * kb + row, n = 32 * blk + 8 * (pc ^ (2 * (kb & 1)));
            const u16* p; int rs;
            if (n < 32) { p = wq + 32 * j + n; rs = D_; }
            else if (n < 176) { p = w_in + FH + 144 * j + (n - 32); rs = F_; }
            else {
                int h = (n - 176) >> 3;
                if (j < 4) { p = wq + 512 + 16 * j + 8 * h; rs = D_; }
                else if (j == 5) { p = h ? wv : wk + 10; rs = 18; }
                else if (j == 6) { p = h ? wv + 10 : wv + 8; rs = 18; }
                else { p = wk + 8 * h; rs = 18; }
            }
            s.gp[i] = (const char*)(p + (size_t)k * rs); s.st[i] = 128 * rs;
        }
    }
#pragma unroll
    for (int t = 0; t < 3; ++t)
#pragma unroll
        for (int i = 0; i < 5; ++i) if (t < 2 || i < 3) k1_issue(s, i, t * K1_SLOT);
    unsigned o2[5];
#pragma unroll
    for (int i = 0; i < 5; ++i) {
        const int e = wid * 5 + i;
        int b = 2 * (e - 16) + (lane >> 5), kb = b / 5, blk = b % 5, row = (lane >> 2) & 7, pc = lane & 3;
        int k = 8 * kb + row, n = 32 * blk + 8 * (pc ^ (2 * (kb & 1))); if (n >= 144) n -= 16;
        o2[i] = e < 36 ? (unsigned)(k * F_ + 144 * j + n) * 2u : (unsigned)(bid * 32768 + (e - 36) * 7680 + lane * 16);
    }
    unsigned nonce = 0;
    const int r16 = lane & 15, g = lane >> 4;
    {
        int q = r16 >> 2, p = lane & 3;
#pragma unroll
        for (int ks = 0; ks < 2; ++ks) s.pa[ks] = r16 * 128 + (((4 * ks + g) ^ (r16 >> 1)) * 16) + wr * 8192;
        s.xo = wc * 2048;
#pragma unroll
        for (int n = 0; n < 3; ++n) { int nf = 3 * wc + (wr ? (n + 2) % 3 : n); s.pb[n] = 16384 + g * 3072 + q * 64 + 8 * p + (nf >> 1) * 512 + (((nf ^ g) & 1) * 32); }
    }
    f32x4 acc1[4][3] = {}; f32x4 cso[2] = {};
    f32x4 rs = {}, rq = {};
    const u32x4 onesv = {0x3f803f80u, 0x3f803f80u, 0x3f803f80u, 0x3f803f80u}; bf16x8 ones = __builtin_bit_cast(bf16x8, onesv); PIN(ones);
    K1F f0, f1;
    wait_vm_bar<8>();
    PIN(eo);
    if (wid == 7) {
        nonce = 1;
        while (__ballot(eo == nonce) != 0ull) ++nonce;
        if (lane == 0) __hip_atomic_store(XN + j, nonce, RLX_AGENT);
    }
    STAMP(0, 1);
    {
        const LAS char* L = s.lds;
        f0.a[0] = ldsA(L + s.pa[0]); f0.a[1] = ldsA(L + s.pa[0] + 2048); f0.a[2] = ldsA(L + s.pa[0] + 4096); f0.a[3] = ldsA(L + s.pa[0] + 6144); f0.x = ldsA(L + s.pa[0] + s.xo);
#pragma unroll
        for (int n = 0; n < 3; ++n) f0.b[n] = ldsB(L + s.pb[n]);
    }
    unsigned cur = 0, nxt = K1_SLOT, prv = 2 * K1_SLOT;
#define K1_ROT() { unsigned t_ = cur; cur = nxt; nxt = prv; prv = t_; }
#pragma unroll 1
    for (int T = 0; T < 8; ++T) {
        k1_p1<1, 2, 3072>(s, f0, f1, cur, prv, acc1, cso, rs, rq, ones);
        wait_vm_bar<5>(); SB();
        if (T == 6) {
#pragma unroll
            for (int i = 0; i < 5; ++i) {
                const int e = wid * 5 + i;
                if (e < 16) s.gp[i] -= 9 * 128;
                else { s.gp[i] = (e < 36 ? (const char*)w_in : (const char*)w_sp) + o2[i]; s.st[i] = e < 36 ? 128 * F_ : 1024; }
            }
        }
        k1_p1<0, 1, 3072>(s, f1, f0, nxt, cur, acc1, cso, rs, rq, ones);
        K1_ROT();
    }
    k1_p1<1, 2, 3072>(s, f0, f1, cur, prv, acc1, cso, rs, rq, ones);
    wait_vm_bar<5>(); SB();
    {
        int q = r16 >> 2, p = lane & 3;
#pragma unroll
        for (int n = 0; n < 3; ++n) { int nf = n == 2 ? 8 : 2 * wc + (n ^ wr); s.pb[n] = 16384 + g * 2560 + q * 64 + 8 * p + (nf >> 1) * 512 + (((nf ^ g) & 1) * 32); }
    }
    k1_p1<0, 1, 2560>(s, f1, f0, nxt, cur, acc1, cso, rs, rq, ones);
    K1_ROT();
    STAMP(3, 0);
    float* CS = (float*)(smem + K1_TAB);
    f32x2* RS = (f32x2*)(smem + K1_TAB + 2048);
    float* red = (float*)(smem + K1_TAB + 3072);
    f32x2* ZS = (f32x2*)(smem + K1_TAB + 7168);
    if (r16 == 0) {
        *(f32x4*)(CS + (3 * wc + (wr ? 2 : 0)) * 16 + 4 * g) = cso[0];
        if (!wr) *(f32x4*)(CS + (3 * wc + 1) * 16 + 4 * g) = cso[1];
    }
    {
        float sq = (r16 & 2) ? ((r16 & 1) ? rq[3] : rq[2]) : ((r16 & 1) ? rq[1] : rq[0]);
        float mean = rs[0] * (1.0f / D_), var = fmaxf(sq * (1.0f / D_) - mean * mean, 0.f);
        if (g == (r16 >> 2)) RS[wr * 64 + wc * 16 + r16] = f32x2{1.0f / sqrtf(var + 1e-5f), mean};
    }
    f32x4 acc2[4][2] = {}; f32x4 sacc = {}; cso[0] = f32x4{}; cso[1] = f32x4{};
    f32x2 ssum[4] = {}, ssq[4] = {};
    u16* qg = (u16*)(ws + WS_Q);
    const float c0 = -2.0f * 1.4426950408889634f * 0.7978845608028654f, c1 = c0 * 0.044715f;
    f32x4 ey; float ek0, ek1, eir;
    auto none = [](int) {};
#define EPI1(m, n) [&](int st) { \
        const int nf = 3 * wc + (wr ? ((n) + 2) % 3 : (n)); \
        if (st == 0) { const f32x2 rr = RS[wr * 64 + (m) * 16 + r16]; const f32x4 cs = *(const f32x4*)(CS + nf * 16 + 4 * g); \
            ey = acc1[m][n] - cs * rr[1]; ek0 = c0 * rr[0]; ek1 = c1 * rr[0] * rr[0] * rr[0]; eir = __builtin_amdgcn_rcpf(rr[0]); \
            if (nf < 2 || nf == 11) acc1[m][n] = ey * rr[0]; } \
        else if (nf >= 2 && nf < 11) { f32x2 v = gelu_y2(f32x2{ey[2 * st - 2], ey[2 * st - 1]}, ek0, ek1, eir); acc1[m][n][2 * st - 2] = v[0]; acc1[m][n][2 * st - 1] = v[1]; \
            ssum[m] += v; ssq[m] += v * v; } \
        PIN(acc1[m][n]); }
#define K1_IT2(GA, GB, VM, RDN, EA, EB) \
    k1_p2<1, GA ? 2 : 0, true>(s, f0, f1, cur, prv, acc2, sacc, cso, ones, EA); \
    wait_vm_bar<VM>(); SB(); \
    k1_p2<0, GB ? 1 : 0, RDN>(s, f1, f0, nxt, cur, acc2, sacc, cso, ones, EB); K1_ROT();
    K1_IT2(true, true, 5, true, none, EPI1(0, 0));
    K1_IT2(true, true, 5, true, EPI1(1, 0), EPI1(2, 0));
    K1_IT2(true, true, 5, true, EPI1(3, 0), EPI1(0, 1));
    K1_IT2(true, true, 5, true, EPI1(1, 1), EPI1(2, 1));
    K1_IT2(true, true, 5, true, EPI1(3, 1), EPI1(0, 2));
    K1_IT2(true, true, 5, true, EPI1(1, 2), EPI1(2, 2));
    k1_p2<1, 2, true>(s, f0, f1, cur, prv, acc2, sacc, cso, ones, EPI1(3, 2));
    STAMP(3, 1);
#pragma unroll
    for (int m = 0; m < 4; ++m) {
        float a = ssum[m][0] + ssum[m][1], b = ssq[m][0] + ssq[m][1];
        a += __shfl_xor(a, 16); a += __shfl_xor(a, 32); b += __shfl_xor(b, 16); b += __shfl_xor(b, 32);
        if (g == 0) { int r = wr * 64 + m * 16 + r16; *(f32x2*)(red + (r * 4 + wc) * 2) = f32x2{a, b}; }
    }
    wait_vm_bar<5>(); SB();
    unsigned echoed = 0;
    if (wid == 7) {
#pragma unroll
        for (int hf = 0; hf < 2; ++hf) {
            int r = hf * 64 + lane; f32x4 u0 = *(const f32x4*)(red + r * 8), u1 = *(const f32x4*)(red + r * 8 + 4);
            float a = (u0[0] + u0[2]) + (u1[0] + u1[2]), b = (u0[1] + u0[3]) + (u1[1] + u1[3]);
            __hip_atomic_store(XP + j * 128 + r, ((unsigned long long)__float_as_uint(b) << 32) | __float_as_uint(a), RLX_AGENT);
        }
    }
    echoed = ld_sc1_async(ws + WS_XCH + (pm * 16 + (lane & 15)) * 4);
    SB();
    k1_p2<0, 0, true>(s, f1, f0, nxt, cur, acc2, sacc, cso, ones, none); K1_ROT();
    k1_p2<1, 0, true>(s, f0, f1, cur, prv, acc2, sacc, cso, ones, none);
    wait_vm_bar<0>(); SB();
    PIN(echoed);
    if (wid == 7 && lane < 16) __hip_atomic_store(XE + lane * 16 + j, echoed, RLX_AGENT);
    SB();
    k1_p2<0, 0, true>(s, f1, f0, nxt, cur, acc2, sacc, cso, ones, none); K1_ROT();
    k1_p2<1, 0, true>(s, f0, f1, cur, prv, acc2, sacc, cso, ones, none);
    k1_p2<0, 0, false>(s, f1, f0, nxt, cur, acc2, sacc, cso, ones, none);
    STAMP(0, 2);
    if (r16 == 0) { *(f32x4*)(CS + 192 + (2 * wc + wr) * 16 + 4 * g) = cso[0]; if (wid == 0) *(f32x4*)(CS + 192 + 128 + 4 * g) = cso[1]; }
#pragma unroll
    for (int n = 0; n < 3; ++n) {
        const int nf = 3 * wc + (wr ? (n + 2) % 3 : n);
        if (nf < 2 || (nf == 11 && j < 4)) {
            const int col = nf < 2 ? 32 * j + nf * 16 : 512 + 16 * j;
#pragma unroll
            for (int m = 0; m < 4; ++m) {
                f32x4 v = acc1[m][n] * QSCALE; u32x2 w = {cvtpk(v[0], v[1]), cvtpk(v[2], v[3])};
                *(u32x2*)(qg + (size_t)(pm * 128 + wr * 64 + m * 16 + r16) * D_ + col + 4 * g) = w;
            }
        } else if (nf == 11 && j < 7) {
#pragma unroll
            for (int m = 0; m < 4; ++m) {
                const int row = pm * 128 + wr * 64 + m * 16 + r16; f32x4 v = acc1[m][n];
                char* kv = ws + WS_KV + (size_t)(row >> 5) * 3072; int key = row & 31;
                const int sv = key >> 4, kk = key & 15, hh = (kk >> 2) & 1, jj = (kk & 3) + 4 * (kk >> 3);
#pragma unroll
                for (int r = 0; r < 4; ++r) {
                    int col = 4 * g + r, h = col >> 3, e = col & 7; bool isv; int d;
                    if (j == 4) { isv = false; d = col; }
                    else if (j == 5) { isv = h; d = h ? e : 10 + e; if (!h && e < 6) d = -1; }
                    else { isv = true; d = h ? 10 + e : 8 + e; if (h && e < 6) d = -1; }
                    u16 b = (u16)(cvtpk(v[r], 0.f) & 0xffff);
                    if (d >= 0) {
                        if (!isv) {
                            if (d < 16) *(u16*)(kv + ((d >> 3) * 32 + key) * 16 + (d & 7) * 2) = b;
                            else *(u16*)(kv + k2slot(key) + (d - 16) * 2) = b;
                        } else *(u16*)(kv + 1024 + sv * 1024 + (hh * 32 + d) * 16 + jj * 2) = b;
                    }
                }
                if (j == 4 && g == 0) {
                    *(u16*)(kv + 1024 + sv * 1024 + (hh * 32 + 18) * 16 + jj * 2) = 0x3f80;
                    unsigned* k2p = (unsigned*)(kv + k2slot(key)); k2p[1] = 0x3f80u; k2p[2] = 0u; k2p[3] = 0u;
                    if (key < 8) *(unsigned*)(kv + 1024 + (key >> 2) * 1024 + 63 * 16 + (key & 3) * 4) = 0u;
                }
            }
        }
    }
    __syncthreads();
    unsigned long long pv[16][2];
    if (wid == 7) {
        const unsigned long long t0 = __builtin_amdgcn_s_memrealtime();
        for (;;) {
            unsigned ev = __hip_atomic_load(XE + j * 16 + (lane & 15), RLX_AGENT);
            asm volatile("s_waitcnt vmcnt(0)" ::: "memory");
            bool done = __all(ev == nonce);
            unsigned nn = __hip_atomic_load(XN + (lane & 15), RLX_AGENT);
            if (done) {
#pragma unroll
                for (int src = 0; src < 16; ++src)
#pragma unroll
                    for (int hf = 0; hf < 2; ++hf) pv[src][hf] = __hip_atomic_load(XP + src * 128 + hf * 64 + lane, RLX_AGENT);
            }
            if (nn != echoed) { echoed = nn; if (lane < 16) __hip_atomic_store(XE + lane * 16 + j, nn, RLX_AGENT); }
            if (done) break;
            if (__builtin_amdgcn_s_memrealtime() - t0 > 5000000ull) {
#pragma unroll
                for (int src = 0; src < 16; ++src) { pv[src][0] = 0x7fc00000ull; pv[src][1] = 0x7fc00000ull; }
                break;
            }
            __builtin_amdgcn_s_sleep(1);
        }
    }
    char* z1b = ws + WS_Z1 + (size_t)(pm * 16 + j) * 36864;
#pragma unroll
    for (int m = 0; m < 5; ++m) {
        const int rl = wr * 64 + (m < 4 ? m : wc) * 16 + r16; const f32x2 rr = RS[rl];
        const float k0 = c0 * rr[0], k1 = c1 * rr[0] * rr[0] * rr[0], ir = __builtin_amdgcn_rcpf(rr[0]);
#pragma unroll
        for (int n = 0; n < 2; ++n) {
            if (m == 4 && n == 1) continue;
            const int nf = m == 4 ? 8 : 2 * wc + (n ^ wr);
            f32x4 y = (m == 4 ? sacc : acc2[m & 3][n]) - *(const f32x4*)(CS + 192 + nf * 16 + 4 * g) * rr[1];
            f32x2 y0 = gelu_y2(f32x2{y[0], y[1]}, k0, k1, ir), y1 = gelu_y2(f32x2{y[2], y[3]}, k0, k1, ir);
            u32x2 w = {cvtpk(y0[0], y0[1]), cvtpk(y1[0], y1[1])};
            st8(z1b + (2 * nf + (g >> 1)) * 2048 + rl * 16 + (g & 1) * 8, w);
        }
    }
    STAMP(3, 2);
    if (wid == 7) {
#pragma unroll
        for (int hf = 0; hf < 2; ++hf) {
            float a = 0, b = 0;
#pragma unroll
            for (int src = 0; src < 16; ++src) { a += __uint_as_float((unsigned)pv[src][hf]); b += __uint_as_float((unsigned)(pv[src][hf] >> 32)); }
            float m = a * (1.0f / FH), var = fmaxf(b * (1.0f / FH) - m * m, 0.f), r = 1.0f / sqrtf(var + 1e-5f);
            ZS[hf * 64 + lane] = f32x2{r, -m * r};
        }
    }
    __syncthreads();
    STAMP(3, 3);
    char* z2b = ws + WS_Z2T + (size_t)(j * 32 + 2 * pm + wr) * 18432;
#pragma unroll
    for (int n = 0; n < 3; ++n) {
        const int nf = 3 * wc + (wr ? (n + 2) % 3 : n);
        if (nf >= 2 && nf < 11) {
#pragma unroll
            for (int m = 0; m < 4; ++m) {
                const int r64 = m * 16 + r16, kb = r64 >> 3; const f32x2 zs = ZS[wr * 64 + r64];
                f32x4 v = acc1[m][n] * zs[0] + zs[1];
                u32x2 w = {cvtpk(v[0], v[1]), cvtpk(v[2], v[3])};
                st8(z2b + (kb * 9 + nf - 2) * 256 + (((r64 & 7) ^ (4 * (kb & 1))) * 32) + g * 8, w);
            }
        }
    }
    STAMP_DRAIN(); STAMP(0, 3);
}

constexpr float ATT_BIAS = 72.0f;
DI float xhalf_max(float m) { auto rr = __builtin_amdgcn_permlane32_swap(__float_as_uint(m), __float_as_uint(m), false, false); return fmaxf(__uint_as_float(rr[0]), __uint_as_float(rr[1])); }
DI float lo_bcast(float m) { auto rr = __builtin_amdgcn_permlane32_swap(__float_as_uint(m), __float_as_uint(m), false, false); return __uint_as_float(rr[0]); }
DI float bf_round(float x) { return __uint_as_float(cvtpk(x, 0.f) << 16); }
DI float max16(const f32x16& s) {
    float a = fmaxf(fmaxf(s[0], s[1]), s[2]), b = fmaxf(fmaxf(s[3], s[4]), s[5]);
    a = fmaxf(fmaxf(a, s[6]), s[7]); b = fmaxf(fmaxf(b, s[8]), s[9]); a = fmaxf(fmaxf(a, s[10]), s[11]); b = fmaxf(fmaxf(b, s[12]), s[13]);
    a = fmaxf(fmaxf(a, s[14]), s[15]); return fmaxf(a, b);
}
DI void exp_pack(const f32x16& s, bf16x8& p0, bf16x8& p1) {
    float e[16];
#pragma unroll
    for (int i = 0; i < 16; ++i) e[i] = __builtin_amdgcn_exp2f(s[i]);
    u32x4 a = {cvtpk(e[0], e[1]), cvtpk(e[2], e[3]), cvtpk(e[4], e[5]), cvtpk(e[6], e[7])};
    u32x4 b = {cvtpk(e[8], e[9]), cvtpk(e[10], e[11]), cvtpk(e[12], e[13]), cvtpk(e[14], e[15])};
    p0 = __builtin_bit_cast(bf16x8, a); p1 = __builtin_bit_cast(bf16x8, b);
}

struct KVF { bf16x8 k1, k2, v0, v1; };
DI void kv_read(KVF& f, const LAS char* tb, int lane, int k2o, int vo) {
    f.k1 = *(const LAS bf16x8*)(tb + lane * 16); f.k2 = *(const LAS bf16x8*)(tb + k2o);
    f.v0 = *(const LAS bf16x8*)(tb + 1024 + vo); f.v1 = *(const LAS bf16x8*)(tb + 2048 + vo);
}
#define k2frag(x) (x)
template <bool RD>
DI void half_step(f32x16& sx, bf16x8& px0, bf16x8& px1, f32x16& sy, const KVF kq, const bf16x8& qy1, const bf16x8& qy2,
                  f32x16& oy, const KVF kv, const bf16x8& py0, const bf16x8& py1, KVF& ld, const LAS char* tb, int lane, int k2o, int vo) {
    const f32x16 zero = {};
    unsigned a0, a1, a2, a3, b0, b1, b2, b3; float e0, e1, e2, e3;
    f32x16 t = MFMA32(kq.k1, qy1, zero); SB();
    if (RD) ld.k1 = *(const LAS bf16x8*)(tb + lane * 16);
    e0 = EX(sx[0]); e1 = EX(sx[1]); e2 = EX(sx[2]); e3 = EX(sx[3]); PIN(e0); PIN(e1); PIN(e2); PIN(e3); SB();
    sy = MFMA32(kq.k2, qy2, t); SB();
    if (RD) ld.k2 = *(const LAS bf16x8*)(tb + k2o);
    a0 = cvtpk(e0, e1); a1 = cvtpk(e2, e3); PIN(a0); PIN(a1);
    e0 = EX(sx[4]); e1 = EX(sx[5]); e2 = EX(sx[6]); e3 = EX(sx[7]); PIN(e0); PIN(e1); PIN(e2); PIN(e3); SB();
    oy = MFMA32(kv.v0, py0, oy); SB();
    if (RD) ld.v0 = *(const LAS bf16x8*)(tb + 1024 + vo);
    a2 = cvtpk(e0, e1); a3 = cvtpk(e2, e3); PIN(a2); PIN(a3);
    e0 = EX(sx[8]); e1 = EX(sx[9]); e2 = EX(sx[10]); e3 = EX(sx[11]); PIN(e0); PIN(e1); PIN(e2); PIN(e3); SB();
    oy = MFMA32(kv.v1, py1, oy); SB();
    if (RD) ld.v1 = *(const LAS bf16x8*)(tb + 2048 + vo);
    b0 = cvtpk(e0, e1); b1 = cvtpk(e2, e3); PIN(b0); PIN(b1);
    e0 = EX(sx[12]); e1 = EX(sx[13]); e2 = EX(sx[14]); e3 = EX(sx[15]);
    b2 = cvtpk(e0, e1); b3 = cvtpk(e2, e3); PIN(b2); PIN(b3); SB();
    u32x4 a = {a0, a1, a2, a3}, b = {b0, b1, b2, b3};
    px0 = __builtin_bit_cast(bf16x8, a); px1 = __builtin_bit_cast(bf16x8, b);
}
DI void att_rescale(f32x16& o, f32x16& s, float& mref, bf16x8& q2, bool& bad, int c, int h) {
    bool nf = false;
#pragma unroll
    for (int i = 0; i < 11; ++i) nf = nf || (!(fabsf(o[i]) <= 3.0e38f) && (i < 8 || h == 0));
    if (__any(nf)) { bad = true; return; }
    float lb = lo_bcast(o[10]);
    int e = __builtin_amdgcn_frexp_expf(lb);
    float d = (float)(e + (int)ATT_BIAS); d = d < 0 ? 0 : d;
    float nm = bf_round(mref + d); float dd = nm - mref; mref = nm;
    int sh = -(int)dd;
#pragma unroll
    for (int i = 0; i < 16; ++i) { o[i] = __builtin_amdgcn_ldexpf(o[i], sh); s[i] -= dd; }
    u32x4 b = __builtin_bit_cast(u32x4, q2); b[1] = h ? 0u : (__float_as_uint(-nm) >> 16); q2 = __builtin_bit_cast(bf16x8, b);
}

constexpr int K2_SLOT = 40960, K2_LDS = 3 * K2_SLOT;
struct G2F { bf16x8 a[4], b[4], sb; };
struct G2S {
    unsigned va[4], vl, vl2;
    const char* sa; const char* sl; unsigned lstep;
    unsigned m0a, m0l; int so;
    const LAS char* pa[2][2]; const LAS char* pb[2];
};
template <int KS, int GL, bool RD> DI void g2_phase(G2S& s, const G2F& f, G2F& n, unsigned ro, unsigned wo, f32x4 (&acc)[4][4], f32x4 (&sacc)[2]) {
    const LAS char* a0 = s.pa[0][KS] + ro; const LAS char* a1 = s.pa[1][KS] + ro;
    const LAS char* b0 = s.pb[0] + ro + KS * 9216; const LAS char* b1 = s.pb[1] + ro + KS * 9216;
#define MM(m, nn) acc[m][nn] = MFMA16(f.b[nn], f.a[m], acc[m][nn])
    MM(0, 0); if (RD) n.b[0] = ldsB2(b0, b1); SB();
    MM(1, 0); if (RD) n.a[0] = ldsA(a0); SB();
    MM(2, 0); if (RD) n.a[1] = ldsA(a0 + 2048); SB();
    MM(3, 0); if (RD) n.a[2] = ldsA(a1); SB();
    MM(0, 1); if (RD) n.a[3] = ldsA(a1 + 2048); SB();
    MM(1, 1); if (RD) n.b[1] = ldsB2(b0 + 256, b1 + 256); SB();
    MM(2, 1); if (RD) n.b[2] = ldsB2(b0 + 512, b1 + 512); SB();
    MM(3, 1); if (RD) n.b[3] = ldsB2(b0 + 768, b1 + 768); SB();
    MM(0, 2); if (RD) n.sb = ldsB2(b0 + s.so, b1 + s.so); SB();
    MM(1, 2); if (GL == 1) { set_m0(s.m0a + wo); glds_s<0>(s.va[0], s.sa); } if (GL == 2) { glds_s<1024>(s.vl, s.sl); } SB();
    MM(2, 2); SB();
    MM(3, 2); if (GL == 1) glds_s<1024>(s.va[1], s.sa); if (GL == 2) glds_s<2048>(s.vl, s.sl); SB();
    MM(0, 3); SB();
    MM(1, 3); if (GL == 1) glds_s<2048>(s.va[2], s.sa); if (GL == 2) glds_s<3072>(s.vl, s.sl); SB();
    MM(2, 3); SB();
    MM(3, 3); if (GL == 1) glds_s<3072>(s.va[3], s.sa); if (GL == 2) { set_m0(s.m0l + wo + 4096); glds_s<0>(s.vl2, s.sl); } SB();
    sacc[0] = MFMA16(f.sb, f.a[0], sacc[0]); if (GL == 1) { set_m0(s.m0l + wo); glds_s<0>(s.vl, s.sl); } if (GL == 2) glds_s<1024>(s.vl2, s.sl); SB();
    sacc[1] = MFMA16(f.sb, f.a[1], sacc[1]); SB();
#undef MM
    if (GL == 1) s.sa += 128;
    if (GL == 2) s.sl += s.lstep;
}
template <bool G2nd, bool G1st, int VM, bool RDN> DI void g2_iter(G2S& s, G2F& f0, G2F& f1, unsigned& cur, unsigned& nxt, unsigned& prv, f32x4 (&acc)[4][4], f32x4 (&sacc)[2]) {
    g2_phase<1, G2nd ? 2 : 0, true>(s, f0, f1, cur, prv, acc, sacc);
    wait_vm_bar<VM>(); SB();
    g2_phase<0, G1st ? 1 : 0, RDN>(s, f1, f0, nxt, cur, acc, sacc);
    unsigned t = cur; cur = nxt; nxt = prv; prv = t;
}

__global__ __launch_bounds__(512, 2) void k2_kernel(const u16* __restrict__ w_sp, const u16* __restrict__ b_sp, char* __restrict__ ws) {
    extern __shared__ __attribute__((aligned(16))) char smem[];
    int bid = blockIdx.x, xcd = bid & 7, idx = bid >> 3;
    int pm = (xcd & 1) * 8 + (idx & 7), j = (xcd >> 1) * 4 + (idx >> 3);
    const int lane = threadIdx.x & 63, wid = __builtin_amdgcn_readfirstlane(threadIdx.x >> 6);
    const unsigned lds0 = (unsigned)(uintptr_t)smem; const LAS char* lds = (const LAS char*)smem;
    constexpr int EP = 148;
    float* et = (float*)smem;
    STAMP(1, 0);
    if (wid < 4) {
        const int wr = wid >> 1, wc = wid & 1;
        G2S s;
#pragma unroll
        for (int i = 0; i < 4; ++i) { int row = 8 * (wid * 4 + i) + (lane >> 3), c = (lane & 7) ^ ((row >> 1) & 7); s.va[i] = row * (S_ * 2) + c * 16 + 4096 - i * 1024; }
        s.vl = lane * 16; s.vl2 = lane * 16 + 4096;
        s.sa = (const char*)(w_sp + (size_t)pm * 128 * S_) - 4096;
        s.sl = wid < 3 ? ws + WS_Z2T + (size_t)j * 32 * 18432 + wid * 6144 : ws + WS_KV;
        s.lstep = wid < 3 ? 18432 : 6144;
        s.m0a = lds0 + wid * 4096; s.m0l = lds0 + 16384 + wid * 6144;
        {
            int r = lane & 15, g = lane >> 4, q = r >> 2, p = lane & 3;
#pragma unroll
            for (int ks = 0; ks < 2; ++ks) {
                int o = r * 128 + (((4 * ks + g) ^ (r >> 1)) * 16) + wr * 8192;
                s.pa[0][ks] = lds + o + wc * 4096; s.pa[1][ks] = lds + o + (1 - wc) * 4096;
            }
            int ob = 16384 + g * 2304 + q * 32 + 8 * p + wc * 1024;
            s.pb[0] = lds + ob + ((g & 1) * 128); s.pb[1] = lds + ob + (((g & 1) ^ 1) * 128);
            s.so = 2048 - wc * 1024;
        }
#pragma unroll
        for (int t = 0; t < 3; ++t) {
            set_m0(s.m0a + t * K2_SLOT); glds_s<0>(s.va[0], s.sa); glds_s<1024>(s.va[1], s.sa); glds_s<2048>(s.va[2], s.sa); glds_s<3072>(s.va[3], s.sa); s.sa += 128;
            set_m0(s.m0l + t * K2_SLOT); glds_s<0>(s.vl, s.sl);
            if (t < 2) {
                glds_s<1024>(s.vl, s.sl); glds_s<2048>(s.vl, s.sl); glds_s<3072>(s.vl, s.sl);
                set_m0(s.m0l + t * K2_SLOT + 4096); glds_s<0>(s.vl2, s.sl); glds_s<1024>(s.vl2, s.sl); s.sl += s.lstep;
            }
        }
        f32x4 acc[4][4] = {}; f32x4 sacc[2] = {};
        G2F f0, f1;
        wait_vm_bar<15>();
        STAMP(1, 1);
        f0.a[0] = ldsA(s.pa[0][0]); f0.a[1] = ldsA(s.pa[0][0] + 2048); f0.a[2] = ldsA(s.pa[1][0]); f0.a[3] = ldsA(s.pa[1][0] + 2048);
        f0.b[0] = ldsB2(s.pb[0], s.pb[1]); f0.b[1] = ldsB2(s.pb[0] + 256, s.pb[1] + 256); f0.b[2] = ldsB2(s.pb[0] + 512, s.pb[1] + 512); f0.b[3] = ldsB2(s.pb[0] + 768, s.pb[1] + 768); f0.sb = ldsB2(s.pb[0] + s.so, s.pb[1] + s.so);
        unsigned cur = 0, nxt = K2_SLOT, prv = 2 * K2_SLOT;
        for (int t = 0; t < 29; ++t) g2_iter<true, true, 10, true>(s, f0, f1, cur, nxt, prv, acc, sacc);
        g2_iter<true, false, 10, true>(s, f0, f1, cur, nxt, prv, acc, sacc);
        g2_iter<false, false, 0, true>(s, f0, f1, cur, nxt, prv, acc, sacc);
        g2_iter<false, false, 0, false>(s, f0, f1, cur, nxt, prv, acc, sacc);
        STAMP(1, 2);
        {
            int c = lane & 15, gq = lane >> 4;
#pragma unroll
            for (int m = 0; m < 4; ++m) {
                int row = wr * 64 + (m ^ (2 * wc)) * 16 + c;
#pragma unroll
                for (int n = 0; n < 4; ++n) *(f32x4*)(et + row * EP + (wc * 4 + n) * 16 + 4 * gq) = acc[m][n];
                if (m < 2) *(f32x4*)(et + row * EP + 128 + 4 * gq) = sacc[m];
            }
        }
    } else {
        __builtin_amdgcn_s_setprio(1);
        const int c = lane & 31, h = lane >> 5, aw = wid - 4;
        const char* kvg = ws + WS_KV;
        const int row0 = blockIdx.x * 8 + aw * 2;
        bf16x8 q1[2], q2[2]; float mref[2];
        const f32x16 zero = {};
#pragma unroll
        for (int t = 0; t < 2; ++t) {
            const u16* qr = (const u16*)(ws + WS_Q) + (size_t)(row0 + t) * D_ + 18 * c;
            u32x4 a; const unsigned* qp = (const unsigned*)(qr + 8 * h);
            a[0] = qp[0]; a[1] = qp[1]; a[2] = qp[2]; a[3] = qp[3];
            q1[t] = __builtin_bit_cast(bf16x8, a);
            unsigned e = *(const unsigned*)(qr + 16);
            u32x4 b = {h ? 0u : e, 0u, 0u, 0u};
            q2[t] = __builtin_bit_cast(bf16x8, b);
        }
        f32x16 oA = zero, oB = zero;
        bool bad = false;
        const int k2o = k2slot(c), vo = (c < 19 ? lane : 63) * 16;
        const LAS char* kvl = lds + 16384 + 18432;
        asm volatile("s_waitcnt vmcnt(0)\n\ts_barrier" ::: "memory");
        KVF X, Y;
        kv_read(X, kvl, lane, k2o, vo); kv_read(Y, kvl + 3072, lane, k2o, vo);
#pragma unroll
        for (int t = 0; t < 2; ++t) {
            f32x16 s0 = MFMA32(X.k1, q1[t], zero); s0 = MFMA32(X.k2, q2[t], s0);
            f32x16 s1 = MFMA32(Y.k1, q1[t], zero); s1 = MFMA32(Y.k2, q2[t], s1);
            float m = xhalf_max(fmaxf(max16(s0), max16(s1)));
            mref[t] = bf_round(rintf(m) + ATT_BIAS);
            u32x4 b = __builtin_bit_cast(u32x4, q2[t]); b[1] = h ? 0u : (__float_as_uint(-mref[t]) >> 16); q2[t] = __builtin_bit_cast(bf16x8, b);
        }
        f32x16 sA = MFMA32(X.k1, q1[0], zero); sA = MFMA32(X.k2, q2[0], sA);
        f32x16 sB = zero;
        bf16x8 pA0, pA1, pB0 = {}, pB1 = {};
        half_step<false>(sA, pA0, pA1, sB, X, q1[1], q2[1], oB, X, pB0, pB1, Y, kvl, lane, k2o, vo);
        half_step<false>(sB, pB0, pB1, sA, Y, q1[0], q2[0], oA, X, pA0, pA1, Y, kvl, lane, k2o, vo);
        asm volatile("s_waitcnt lgkmcnt(0)\n\ts_barrier" ::: "memory");
        unsigned slot = K2_SLOT;
        for (int t = 1; t < 32; ++t) {
            const LAS char* tb = kvl + slot;
            half_step<true>(sA, pA0, pA1, sB, Y, q1[1], q2[1], oB, X, pB0, pB1, X, tb, lane, k2o, vo);
            half_step<false>(sB, pB0, pB1, sA, X, q1[0], q2[0], oA, Y, pA0, pA1, X, tb, lane, k2o, vo);
            bool chkB = (h == 0) && !(oB[10] <= 0x1p20f);
            half_step<true>(sA, pA0, pA1, sB, X, q1[1], q2[1], oB, Y, pB0, pB1, Y, tb + 3072, lane, k2o, vo);
            if (__builtin_expect(__any(chkB), 0)) att_rescale(oB, sB, mref[1], q2[1], bad, c, h);
            bool chkA = (h == 0) && !(oA[10] <= 0x1p20f);
            half_step<false>(sB, pB0, pB1, sA, Y, q1[0], q2[0], oA, X, pA0, pA1, Y, tb, lane, k2o, vo);
            if (__builtin_expect(__any(chkA), 0)) att_rescale(oA, sA, mref[0], q2[0], bad, c, h);
            asm volatile("s_waitcnt lgkmcnt(0)\n\ts_barrier" ::: "memory");
            slot = slot == 2 * K2_SLOT ? 0 : slot + K2_SLOT;
        }
        half_step<false>(sA, pA0, pA1, sB, Y, q1[1], q2[1], oB, X, pB0, pB1, X, kvl, lane, k2o, vo);
        exp_pack(sB, pB0, pB1);
        oA = MFMA32(Y.v0, pA0, oA); oA = MFMA32(Y.v1, pA1, oA);
        oB = MFMA32(Y.v0, pB0, oB); oB = MFMA32(Y.v1, pB1, oB);
        {
            bool nf = false;
#pragma unroll
            for (int i = 0; i < 11; ++i) nf = nf || ((!(fabsf(oA[i]) <= 3.0e38f) || !(fabsf(oB[i]) <= 3.0e38f)) && (i < 8 || h == 0));
            if (__any(nf)) bad = true;
        }
        f32x16 o[2] = {oA, oB};
        if (__builtin_expect(bad, 0)) {
#pragma unroll 1
            for (int t = 0; t < 2; ++t) {
                bf16x8 qa = t ? q1[1] : q1[0]; u32x4 qb = __builtin_bit_cast(u32x4, t ? q2[1] : q2[0]); qb[1] = 0;
                f32x16 oo = zero; float m = -3.0e38f;
                for (int kt = 0; kt < 64; ++kt) {
                    const char* tb = kvg + (size_t)kt * 3072;
                    bf16x8 k1 = *(const bf16x8*)(tb + lane * 16);
                    u32x4 k2v = *(const u32x4*)(tb + k2o);
                    bf16x8 v0 = *(const bf16x8*)(tb + 1024 + vo), v1 = *(const bf16x8*)(tb + 2048 + vo);
                    f32x16 s = MFMA32(k1, qa, zero); s = MFMA32(__builtin_bit_cast(bf16x8, k2v), __builtin_bit_cast(bf16x8, qb), s);
                    float nm = fmaxf(m, xhalf_max(max16(s)));
                    float sc = __builtin_amdgcn_exp2f(m - nm); m = nm;
#pragma unroll
                    for (int i = 0; i < 16; ++i) { oo[i] *= sc; s[i] -= nm; }
                    bf16x8 p0, p1; exp_pack(s, p0, p1);
                    oo = MFMA32(v0, p0, oo); oo = MFMA32(v1, p1, oo);
                }
                if (t) o[1] = oo; else o[0] = oo;
            }
        }
#pragma unroll
        for (int t = 0; t < 2; ++t) {
            float inv = 1.0f / lo_bcast(o[t][10]);
            const int orow = row0 + t;
            char* ob = ws + WS_UO + ((size_t)((orow >> 6) * 90 + 72) * 4 + ((orow >> 4) & 3)) * 1024 + (orow & 15) * 16;
            unsigned w0 = cvtpk(o[t][0] * inv, o[t][1] * inv), w1 = cvtpk(o[t][2] * inv, o[t][3] * inv);
            unsigned w2 = cvtpk(o[t][4] * inv, o[t][5] * inv), w3 = cvtpk(o[t][6] * inv, o[t][7] * inv);
            unsigned w4 = cvtpk(o[t][8] * inv, o[t][9] * inv);
            auto put = [&](int d, unsigned w) { int kk = 18 * c + d; *(unsigned*)(ob + (kk >> 5) * 4096 + ((kk >> 3) & 3) * 256 + (kk & 7) * 2) = w; };
            put(4 * h, w0); put(4 * h + 2, w1); put(8 + 4 * h, w2); put(10 + 4 * h, w3);
            if (h == 0) put(16, w4);
        }
    }
    const char* z1b = ws + WS_Z1 + (size_t)(pm * 16 + j) * 36864;
    u32x4 z1r[5];
    const int erow = threadIdx.x & 127;
    const float bias = bf2f(b_sp[pm * 128 + erow]);
#pragma unroll
    for (int i = 0; i < 5; ++i) { int ci = threadIdx.x + 512 * i; if (ci >= 2304) ci -= 512; z1r[i] = *(const u32x4*)(z1b + ci * 16); }
    asm volatile("s_waitcnt lgkmcnt(0)\n\ts_barrier" ::: "memory");
    {
        const int grow = pm * 128 + erow;
        char* ub = ws + WS_UO + ((size_t)(grow >> 6) * 90 * 4 + ((grow >> 4) & 3)) * 1024 + (grow & 15) * 16;
#pragma unroll
        for (int i = 0; i < 5; ++i) {
            int ci = threadIdx.x + 512 * i;
            if (ci < 2304) {
                int ch = ci >> 7;
                f32x4 g0 = *(const f32x4*)(et + erow * EP + ch * 8) + bias, g1 = *(const f32x4*)(et + erow * EP + ch * 8 + 4) + bias;
                u32x4 z = z1r[i], w;
                w[0] = cvtpk(bflo(z[0]) * g0[0], bfhi(z[0]) * g0[1]); w[1] = cvtpk(bflo(z[1]) * g0[2], bfhi(z[1]) * g0[3]);
                w[2] = cvtpk(bflo(z[2]) * g1[0], bfhi(z[2]) * g1[1]); w[3] = cvtpk(bflo(z[3]) * g1[2], bfhi(z[3]) * g1[3]);
                int cg = 18 * j + ch;
                st16(ub + (cg >> 2) * 4096 + (cg & 3) * 256, w);
            }
        }
    }
    STAMP_DRAIN(); STAMP(1, 3);
}

constexpr int K3_SLOT = 9216, K3_LDS = 4 * 4 * K3_SLOT + 64;
template <bool GL, int VM> DI void k3_step(const LAS char* rd, unsigned wm0, unsigned va, const unsigned (&vb)[5], const char* sa, const char* sb,
                                           unsigned pa, unsigned pb, f32x4 (&acc)[4][5]) {
    wait_vm<VM>();
    bf16x8 a[4], b[5];
#pragma unroll
    for (int m = 0; m < 4; ++m) a[m] = ldsA(rd + pa + m * 1024);
#pragma unroll
    for (int n = 0; n < 5; ++n) b[n] = ldsB2(rd + pb + n * 32, rd + pb + n * 32 + 576);
    asm volatile("s_waitcnt lgkmcnt(0)" ::: "memory"); SB();
    if (GL) {
        set_m0(wm0); glds_s<0>(va, sa); glds_s<1024>(va, sa); glds_s<2048>(va, sa); glds_s<3072>(va, sa);
        set_m0(wm0 + 4096); glds_s<0>(vb[0], sb); glds_s<1024>(vb[1], sb); glds_s<2048>(vb[2], sb); glds_s<3072>(vb[3], sb);
        set_m0(wm0 + 8192); glds_s<0>(vb[4], sb);
    }
    SB();
#pragma unroll
    for (int n = 0; n < 5; ++n)
#pragma unroll
        for (int m = 0; m < 4; ++m) acc[m][n] = MFMA16(b[n], a[m], acc[m][n]);
    SB();
}
__global__ __launch_bounds__(256, 1) void k3_kernel(const u16* __restrict__ x, const u16* __restrict__ w_out, const u16* __restrict__ wo,
                                                    char* __restrict__ ws, u16* __restrict__ out) {
    extern __shared__ __attribute__((aligned(16))) char smem[];
    const int bid = blockIdx.x, xcd = bid & 7, idx = bid >> 3;
    const int rb = (xcd & 3) * 8 + (idx & 7), j = (xcd >> 2) * 4 + (idx >> 3);
    const int lane = threadIdx.x & 63, wid = __builtin_amdgcn_readfirstlane(threadIdx.x >> 6);
    const unsigned lds0 = (unsigned)(uintptr_t)smem; const LAS char* lds = (const LAS char*)smem + wid * 4 * K3_SLOT;
    STAMP(2, 0);
    const unsigned va = lane * 16;
    unsigned vb[5];
#pragma unroll
    for (int i = 0; i < 5; ++i) { int ci = i * 64 + lane; if (ci >= 288) ci -= 32; int row = ci / 9, cc = ci % 9; vb[i] = row * (D_ * 2) + cc * 16 + 4096 - (i & 3) * 1024; }
    const char* ua = ws + WS_UO + (size_t)rb * 90 * 4096;
    const char* wb0 = (const char*)(w_out + 72 * j) - 4096; const char* wb1 = (const char*)(wo + 72 * j) - 4096 - (size_t)72 * 32 * D_ * 2;
    const unsigned wm = lds0 + wid * 4 * K3_SLOT;
    const int g = lane >> 4, q = (lane & 15) >> 2, p = lane & 3;
    const unsigned pa = lane * 16, pb = 4096 + (8 * g + q) * 144 + 8 * p;
    const int n = wid < 2 ? 23 : 22;
#define K3_SA(ks) (ua + (size_t)(ks) * 4096)
#define K3_SB(ks) (((ks) < 72 ? wb0 : wb1) + (size_t)(ks) * (32 * D_ * 2))
#pragma unroll
    for (int i = 0; i < 4; ++i) {
        int ks = wid + 4 * i; const char* sa = K3_SA(ks); const char* sb = K3_SB(ks); unsigned w = wm + i * K3_SLOT;
        set_m0(w); glds_s<0>(va, sa); glds_s<1024>(va, sa); glds_s<2048>(va, sa); glds_s<3072>(va, sa);
        set_m0(w + 4096); glds_s<0>(vb[0], sb); glds_s<1024>(vb[1], sb); glds_s<2048>(vb[2], sb); glds_s<3072>(vb[3], sb);
        set_m0(w + 8192); glds_s<0>(vb[4], sb);
    }
    f32x4 acc[4][5] = {};
    STAMP(2, 1);
    unsigned slot = 0;
    for (int i = 0; i < n - 4; ++i) {
        int ks = wid + 4 * (i + 4);
        k3_step<true, 27>(lds + slot, wm + slot, va, vb, K3_SA(ks), K3_SB(ks), pa, pb, acc);
        slot = slot == 3 * K3_SLOT ? 0 : slot + K3_SLOT;
    }
    k3_step<false, 27>(lds + slot, 0, va, vb, ua, ua, pa, pb, acc); slot = slot == 3 * K3_SLOT ? 0 : slot + K3_SLOT;
    k3_step<false, 18>(lds + slot, 0, va, vb, ua, ua, pa, pb, acc); slot = slot == 3 * K3_SLOT ? 0 : slot + K3_SLOT;
    k3_step<false, 9>(lds + slot, 0, va, vb, ua, ua, pa, pb, acc); slot = slot == 3 * K3_SLOT ? 0 : slot + K3_SLOT;
    k3_step<false, 0>(lds + slot, 0, va, vb, ua, ua, pa, pb, acc);
    STAMP(2, 2);
    constexpr int EP = 84;
    float* et = (float*)(smem + wid * 4 * K3_SLOT);
#pragma unroll
    for (int m = 0; m < 4; ++m)
#pragma unroll
        for (int nn = 0; nn < 5; ++nn) *(f32x4*)(et + (m * 16 + (lane & 15)) * EP + nn * 16 + 4 * g) = acc[m][nn];
    __syncthreads();
#pragma unroll
    for (int i = 0; i < 3; ++i) {
        int ci = threadIdx.x + 256 * i;
        if (ci < 576) {
            int row = ci / 9, ch = ci % 9;
            u32x4 xv = *(const u32x4*)(x + (size_t)(rb * 64 + row) * D_ + 72 * j + ch * 8);
            f32x4 s0 = {bflo(xv[0]), bfhi(xv[0]), bflo(xv[1]), bfhi(xv[1])}, s1 = {bflo(xv[2]), bfhi(xv[2]), bflo(xv[3]), bfhi(xv[3])};
#pragma unroll
            for (int w = 0; w < 4; ++w) {
                const float* e = (const float*)(smem + w * 4 * K3_SLOT) + row * EP + ch * 8;
                s0 += *(const f32x4*)e; s1 += *(const f32x4*)(e + 4);
            }
            u32x4 o = {cvtpk(s0[0], s0[1]), cvtpk(s0[2], s0[3]), cvtpk(s1[0], s1[1]), cvtpk(s1[2], s1[3])};
            *(u32x4*)(out + (size_t)(rb * 64 + row) * D_ + 72 * j + ch * 8) = o;
        }
    }
    STAMP_DRAIN(); STAMP(2, 3);
}

extern "C" void kernel_launch(void* const* d_in, const int* in_sizes, int n_in, void* d_out, int out_size, void* d_ws, size_t ws_size, hipStream_t stream) {
    const u16* x = (const u16*)d_in[0]; const u16* wq = (const u16*)d_in[1]; const u16* wk = (const u16*)d_in[2]; const u16* wv = (const u16*)d_in[3];
    const u16* wo = (const u16*)d_in[4]; const u16* w_in = (const u16*)d_in[6]; const u16* w_sp = (const u16*)d_in[7]; const u16* b_sp = (const u16*)d_in[8];
    const u16* w_out = (const u16*)d_in[9]; char* ws = (char*)d_ws;
    (void)hipFuncSetAttribute((const void*)k1_kernel, hipFuncAttributeMaxDynamicSharedMemorySize, K1_LDS);
    (void)hipFuncSetAttribute((const void*)k2_kernel, hipFuncAttributeMaxDynamicSharedMemorySize, K2_LDS);
    (void)hipFuncSetAttribute((const void*)k3_kernel, hipFuncAttributeMaxDynamicSharedMemorySize, K3_LDS);
    k1_kernel<<<256, 512, K1_LDS, stream>>>(x, wq, wk, wv, w_in, w_sp, ws);
    k2_kernel<<<256, 512, K2_LDS, stream>>>(w_sp, b_sp, ws);
    k3_kernel<<<256, 256, K3_LDS, stream>>>(x, w_out, wo, ws, (u16*)d_out);
}
```

```cpp
#include <hip/hip_runtime.h>
#include <hip/hip_bf16.h>
#include <stdint.h>

typedef unsigned short u16;
typedef short bf16x8 __attribute__((ext_vector_type(8)));
typedef short s16x4 __attribute__((ext_vector_type(4)));
typedef float f32x2 __attribute__((ext_vector_type(2)));
typedef float f32x4 __attribute__((ext_vector_type(4)));
typedef float f32x16 __attribute__((ext_vector_type(16)));
typedef unsigned u32x4 __attribute__((ext_vector_type(4)));
typedef unsigned u32x2 __attribute__((ext_vector_type(2)));
typedef __bf16 bf16x2_t __attribute__((ext_vector_type(2)));
#define LAS __attribute__((address_space(3)))
#define DI __device__ __forceinline__

constexpr int S_ = 2048, D_ = 576, FH = 2304, F_ = 4608;

constexpr size_t WS_Q = 0;
constexpr size_t WS_KV = WS_Q + (size_t)S_ * D_ * 2;
constexpr size_t WS_Z1 = WS_KV + 64 * 3072;
constexpr size_t WS_Z2T = WS_Z1 + (size_t)S_ * FH * 2;
constexpr size_t WS_UO = WS_Z2T + (size_t)S_ * FH * 2;
constexpr size_t WS_ST = WS_UO + (size_t)S_ * 2880 * 2;
constexpr size_t WS_XCH = WS_ST + (size_t)S_ * 16 * 8;
constexpr size_t WS_W3T = WS_XCH + 4096 + 16384;
constexpr size_t WS_DBG = 128u << 20;
#define STAMP(kid, i)
#define STAMP_DRAIN()
#define STAMPT(tid, kid, i)

DI unsigned cvtpk(float lo, float hi) { f32x2 v = {lo, hi}; return __builtin_bit_cast(unsigned, __builtin_convertvector(v, bf16x2_t)); }
DI float bf2f(u16 b) { return __uint_as_float(((unsigned)b) << 16); }
DI float bflo(unsigned w) { return __uint_as_float(w << 16); }
DI float bfhi(unsigned w) { return __uint_as_float(w & 0xffff0000u); }
#define SB() __builtin_amdgcn_sched_barrier(0)
#define PIN(x) asm volatile("" : "+v"(x))
#define EX(x) __builtin_amdgcn_exp2f(x)
#define MFMA16(a, b, c) __builtin_amdgcn_mfma_f32_16x16x32_bf16(a, b, c, 0, 0, 0)
#define MFMA32(a, b, c) __builtin_amdgcn_mfma_f32_32x32x16_bf16(a, b, c, 0, 0, 0)

DI s16x4 trread(const LAS char* p) { return __builtin_bit_cast(s16x4, __builtin_amdgcn_ds_read_tr16_b64_v4i16((LAS s16x4*)p)); }
template <int N> DI void wait_vm_bar() { asm volatile("s_waitcnt vmcnt(%0) lgkmcnt(0)\n\ts_barrier" ::"n"(N) : "memory"); }
template <int N> DI void wait_vm() { asm volatile("s_waitcnt vmcnt(%0)" ::"n"(N) : "memory"); }
template <int OFF> DI void glds_s(unsigned voff, const char* sbase) { asm volatile("global_load_lds_dwordx4 %0, %1 offset:%2" ::"v"(voff), "s"(sbase), "n"(OFF)); }
template <int OFF> DI void glds_sc1(unsigned voff, const char* sbase) { asm volatile("global_load_lds_dwordx4 %0, %1 offset:%2 sc1" ::"v"(voff), "s"(sbase), "n"(OFF)); }
DI void glds_v(const char* p, unsigned m0v) { asm volatile("s_mov_b32 m0, %1\n\ts_nop 0\n\tglobal_load_lds_dwordx4 %0, off" ::"v"(p), "s"(m0v)); }
DI unsigned ld_sc1_async(const char* sb, unsigned vo) { unsigned v; asm volatile("global_load_dword %0, %1, %2 sc1" : "=v"(v) : "v"(vo), "s"(sb) : "memory"); return v; }
#define WT
DI void st16(void* p, u32x4 v) { asm volatile("global_store_dwordx4 %0, %1, off sc1\n\ts_nop 1" :: "v"(p), "v"(v) : "memory"); }
DI void st8(void* p, u32x2 v) { asm volatile("global_store_dwordx2 %0, %1, off sc1" :: "v"(p), "v"(v) : "memory"); }
DI void set_m0(unsigned v) { asm volatile("s_mov_b32 m0, %0\n\ts_nop 0" ::"s"(v)); }
DI bf16x8 ldsA(const LAS char* p) { return *(const LAS bf16x8*)p; }
DI bf16x8 ldsB2(const LAS char* p0, const LAS char* p1) { s16x4 lo = trread(p0), hi = trread(p1); return __builtin_shufflevector(lo, hi, 0, 1, 2, 3, 4, 5, 6, 7); }
DI bf16x8 ldsB(const LAS char* p) { return ldsB2(p, p + 256); }

DI float gelu_t(float x) {
    const float c0 = -2.0f * 1.4426950408889634f * 0.7978845608028654f;
    const float c1 = c0 * 0.044715f;
    float x2 = x * x;
    float t = __builtin_fmaf(x2, c1, c0);
    float e = __builtin_amdgcn_exp2f(x * t);
    return x * __builtin_amdgcn_rcpf(1.0f + e);
}
DI int k2slot(int c) { return c < 13 ? 1024 + (19 + c) * 16 : (c < 25 ? 1024 + (51 + c - 13) * 16 : 2048 + (19 + c - 25) * 16); }
constexpr float QSCALE = 0.23570226039551584f * 1.4426950408889634f;

constexpr int K1_SLOT = 40960, K1_TAB = 3 * K1_SLOT, K1_LDS = K1_TAB + 9216;
typedef __attribute__((address_space(1))) unsigned gu32;
typedef __attribute__((address_space(1))) unsigned long long gu64;
#define RLX_AGENT __ATOMIC_RELAXED, __HIP_MEMORY_SCOPE_AGENT
struct K1F { bf16x8 a[4], b[3], x; };
struct K1S {
    unsigned va[2], vb[2]; const char* sa; const char* sb;
    const char* gp; long st;
    unsigned m0a, m0b;
    unsigned pa[2], pb[3]; int xo;
    const LAS char* lds;
};
DI void k1_issue(K1S& s, int i, unsigned wo) {
    if (i == 0) { set_m0(s.m0a + wo); glds_s<0>(s.va[0], s.sa); }
    if (i == 1) { glds_s<1024>(s.va[1], s.sa); s.sa += 128; }
    if (i == 2) { glds_v(s.gp, s.m0b + wo + 2048); s.gp += s.st; }
    if (i == 3) { set_m0(s.m0b + wo); glds_s<0>(s.vb[0], s.sb); }
    if (i == 4) { glds_s<1024>(s.vb[1], s.sb); s.sb += 128 * F_; }
}
#define k1_issue2 k1_issue
DI f32x2 gelu_y2(f32x2 y, float k0, float k1, float ir) {
    f32x2 t = (y * y) * k1 + k0;
    f32x2 a = y * t;
    f32x2 e = {__builtin_amdgcn_exp2f(a[0]), __builtin_amdgcn_exp2f(a[1])};
    f32x2 d = e * ir + ir;
    f32x2 rc = {__builtin_amdgcn_rcpf(d[0]), __builtin_amdgcn_rcpf(d[1])};
    return y * rc;
}
DI float dpp_x1(float x) { return __int_as_float(__builtin_amdgcn_mov_dpp(__float_as_int(x), 0xB1, 0xf, 0xf, true)); }
DI float dpp_x2(float x) { return __int_as_float(__builtin_amdgcn_mov_dpp(__float_as_int(x), 0x4E, 0xf, 0xf, true)); }
DI void kv_store(char* kv, int sv, int j, int c, int g, f32x4 v) {
    const int key = sv * 16 + c;
    if (j == 4) {
        *(u32x2*)(kv + ((g >> 1) * 32 + key) * 16 + (g & 1) * 8) = u32x2{cvtpk(v[0], v[1]), cvtpk(v[2], v[3])};
        if (g == 1) { unsigned* k2p = (unsigned*)(kv + k2slot(key)); k2p[1] = 0x3f80u; *(u32x2*)(k2p + 2) = u32x2{0u, 0u}; }
        if (g == 2 && c < 2) *(u32x4*)(kv + 1024 + sv * 1024 + (c * 32 + 18) * 16) = u32x4{0x3f803f80u, 0x3f803f80u, 0x3f803f80u, 0x3f803f80u};
        if (g == 3 && c == 0) *(u32x4*)(kv + 1024 + sv * 1024 + 63 * 16) = u32x4{0u, 0u, 0u, 0u};
        return;
    }
    if (j == 5 && g == 1) *(unsigned*)(kv + k2slot(key)) = cvtpk(v[2], v[3]);
    const bool o1 = c & 1, o2 = c & 2;
    float t0 = dpp_x1(o1 ? v[0] : v[1]), t1 = dpp_x1(o1 ? v[2] : v[3]);
    if (o1) { v[0] = t0; v[2] = t1; } else { v[1] = t0; v[3] = t1; }
    t0 = dpp_x2(o2 ? v[0] : v[2]); t1 = dpp_x2(o2 ? v[1] : v[3]);
    if (o2) { v[0] = t0; v[1] = t1; } else { v[2] = t0; v[3] = t1; }
    int d = j == 5 ? 4 * (g - 2) + (c & 3) : (g < 2 ? 8 + 4 * g + (c & 3) : 14 + (c & 3));
    bool ok = j == 5 ? g >= 2 : (g < 2 || (g == 3 && (c & 3) >= 2));
    const int kq = c >> 2;
    if (ok) *(u32x2*)(kv + 1024 + sv * 1024 + ((kq & 1) * 32 + d) * 16 + (kq >> 1) * 8) = u32x2{cvtpk(v[0], v[1]), cvtpk(v[2], v[3])};
}
template <int KS, int GL, int KBS> DI void k1_p1(K1S& s, const K1F& f, K1F& n, unsigned ro, unsigned wo, f32x4 (&acc)[4][3], f32x4 (&cso)[2], f32x4& rs, f32x4& rq, const bf16x8& ones) {
    const LAS char* aa = s.lds + (s.pa[KS] + ro); const LAS char* bb = s.lds + ro + KS * 4 * KBS;
#define MM(m, nn) acc[m][nn] = MFMA16(f.b[nn], f.a[m], acc[m][nn])
    MM(0, 0); n.b[0] = ldsB(bb + s.pb[0]); SB();
    MM(1, 0); n.a[0] = ldsA(aa); SB();
    MM(2, 0); n.a[1] = ldsA(aa + 2048); SB();
    MM(3, 0); n.a[2] = ldsA(aa + 4096); SB();
    cso[0] = MFMA16(f.b[0], ones, cso[0]); n.a[3] = ldsA(aa + 6144); SB();
    MM(0, 1); n.b[1] = ldsB(bb + s.pb[1]); SB();
    MM(1, 1); n.b[2] = ldsB(bb + s.pb[2]); SB();
    rs = MFMA16(ones, f.x, rs); SB();
    rq = MFMA16(f.x, f.x, rq); n.x = ldsA(aa + s.xo); SB();
    MM(2, 1); k1_issue(s, GL == 1 ? 0 : 3, wo); SB();
    MM(3, 1); SB();
    cso[1] = MFMA16(f.b[1], ones, cso[1]); SB();
    MM(0, 2); k1_issue(s, GL == 1 ? 1 : 4, wo); SB();
    MM(1, 2); SB();
    MM(2, 2); if (GL == 1) k1_issue(s, 2, wo); SB();
    MM(3, 2); SB();
#undef MM
}
template <int KS, int GL, bool RD, class E> DI void k1_p2(K1S& s, const K1F& f, K1F& n, unsigned ro, unsigned wo, f32x4 (&acc)[4][2], f32x4& sacc, f32x4 (&cso)[2], const bf16x8& ones, E epi) {
    const LAS char* aa = s.lds + (s.pa[KS] + ro); const LAS char* bb = s.lds + ro + KS * 4 * 3072;
#define MM(m, nn) acc[m][nn] = MFMA16(f.b[nn], f.a[m], acc[m][nn])
    MM(0, 0); if (RD) n.b[0] = ldsB(bb + s.pb[0]); SB();
    MM(1, 0); if (RD) n.a[0] = ldsA(aa); SB();
    MM(2, 0); if (RD) n.a[1] = ldsA(aa + 2048); epi(0); SB();
    MM(3, 0); if (RD) n.a[2] = ldsA(aa + 4096); SB();
    cso[0] = MFMA16(f.b[0], ones, cso[0]); if (RD) n.a[3] = ldsA(aa + 6144); SB();
    sacc = MFMA16(f.b[2], f.x, sacc); if (RD) n.b[1] = ldsB(bb + s.pb[1]); epi(1); SB();
    cso[1] = MFMA16(f.b[2], ones, cso[1]); if (RD) n.x = ldsA(aa + s.xo); SB();
    MM(0, 1); if (RD) n.b[2] = ldsB(bb + s.pb[2]); SB();
    MM(1, 1); if (GL) k1_issue2(s, GL == 1 ? 0 : 3, wo); epi(2); SB();
    MM(2, 1); if (GL) k1_issue2(s, GL == 1 ? 1 : 4, wo); SB();
    MM(3, 1); if (GL == 1) k1_issue2(s, 2, wo); SB();
#undef MM
}

__global__ __launch_bounds__(512, 2) void k1_kernel(const u16* __restrict__ x, const u16* __restrict__ wq, const u16* __restrict__ wk, const u16* __restrict__ wv,
                                                    const u16* __restrict__ w_in, const u16* __restrict__ w_sp, const u16* __restrict__ w_out, const u16* __restrict__ wo, char* __restrict__ ws) {
    extern __shared__ __attribute__((aligned(16))) char smem[];
    const int bid = blockIdx.x, xcd = bid & 7, idx = bid >> 3;
    const int pm = (xcd & 1) * 8 + (idx & 7), j = (xcd >> 1) * 4 + (idx >> 3);
    const int lane = threadIdx.x & 63, wid = __builtin_amdgcn_readfirstlane(threadIdx.x >> 6), wr = wid >> 2, wc = wid & 3;
    const unsigned lds0 = (unsigned)(uintptr_t)smem;
    STAMP(0, 0);
    gu32* XN = (gu32*)(ws + WS_XCH) + pm * 16;
    gu32* XE = (gu32*)(ws + WS_XCH + 4096) + pm * 256;
    gu64* XP = (gu64*)(ws + WS_ST) + (size_t)pm * 16 * 128;
    unsigned eo = ld_sc1_async(ws + WS_XCH + 4096 + (pm * 256 + j * 16) * 4, (lane & 15) * 4);
    K1S s; s.lds = (const LAS char*)smem; s.m0a = lds0 + wid * 2048; s.m0b = lds0 + 16384 + wid * 3072;
    const int brow = (lane >> 2) & 7, bc = (lane & 3) ^ (2 * (wid & 1)), bk = 8 * wid + brow;
    {
        int r8 = lane >> 3;
        s.va[0] = (16 * wid + r8) * (D_ * 2) + (((lane & 7) ^ (r8 >> 1)) * 16);
        s.va[1] = (16 * wid + 8 + r8) * (D_ * 2) + (((lane & 7) ^ (4 + (r8 >> 1))) * 16) - 1024;
        s.sa = (const char*)(x + (size_t)pm * 128 * D_);
        s.vb[0] = bk * (F_ * 2) + (lane >> 5) * 64 + bc * 16 + 1024; s.vb[1] = s.vb[0] + 128 - 1024;
        s.sb = (const char*)(w_in + FH + 144 * j) - 1024;
    }
#pragma unroll
    for (int i = 0; i < 5; ++i) if (i != 2) k1_issue(s, i, 0);
    {
        const u16* p; int rs;
        const u16* sp0 = j < 4 ? wq + 512 + 16 * j : (j == 5 ? wk + 10 : (j == 6 ? wv + 8 : wk));
        const u16* sp1 = j < 4 ? wq + 520 + 16 * j : (j == 5 ? wv : (j == 6 ? wv + 10 : wk + 8));
        const int srs = j < 4 ? D_ : 18;
        if (lane >= 32) { p = wq + 32 * j + 8 * bc; rs = D_; }
        else if (bc < 2) { p = w_in + FH + 144 * j + 128 + 8 * bc; rs = F_; }
        else { p = bc == 2 ? sp0 : sp1; rs = srs; }
        s.gp = (const char*)(p + (size_t)bk * rs); s.st = 128 * rs;
    }
    k1_issue(s, 2, 0);
#pragma unroll
    for (int t = 1; t < 3; ++t)
#pragma unroll
        for (int i = 0; i < 5; ++i) if (t < 2 || i < 3) k1_issue(s, i, t * K1_SLOT);
    unsigned nonce = 0;
    u32x4 w3v[2]; unsigned w3o[2];
#pragma unroll
    for (int i = 0; i < 2; ++i) {
        int li = threadIdx.x + 512 * i; if (li >= 810) li = 809;
        int gi = bid * 810 + li, k = gi / 72, cc = gi % 72;
        const u16* p = (k < FH ? w_out + (size_t)k * D_ : wo + (size_t)(k - FH) * D_) + cc * 8;
        asm volatile("global_load_dwordx4 %0, %1, off" : "=v"(w3v[i]) : "v"(p) : "memory");
        w3o[i] = ((cc / 9) * 90 + (k >> 5)) * 4608 + (k & 31) * 144 + (cc % 9) * 16;
    }
    const int r16 = lane & 15, g = lane >> 4, cg1 = wc ^ wr;
    const int nf3 = cg1 == 3 ? 8 : (cg1 == 2 ? 9 : 10 + cg1);
#define LI1(n) (wr ? ((n) + 2) % 3 : (n))
#define NF1(n) (LI1(n) < 2 ? 2 * cg1 + LI1(n) : nf3)
    {
        int q = r16 >> 2, p = lane & 3;
#pragma unroll
        for (int ks = 0; ks < 2; ++ks) s.pa[ks] = r16 * 128 + (((4 * ks + g) ^ (r16 >> 1)) * 16) + wr * 8192;
        s.xo = wc * 2048;
#pragma unroll
        for (int n = 0; n < 3; ++n) { int nf = NF1(n); s.pb[n] = 16384 + g * 3072 + q * 64 + 8 * p + (nf >> 1) * 512 + (((nf ^ g) & 1) * 32); }
    }
    f32x4 acc1[4][3] = {}; f32x4 cso[2] = {};
    f32x4 rs = {}, rq = {};
    const u32x4 onesv = {0x3f803f80u, 0x3f803f80u, 0x3f803f80u, 0x3f803f80u}; bf16x8 ones = __builtin_bit_cast(bf16x8, onesv); PIN(ones);
    K1F f0, f1;
    wait_vm_bar<10>();
    PIN(eo);
    if (wid == 7) {
        nonce = 1;
        while (__ballot(eo == nonce) != 0ull) ++nonce;
        if (lane == 0) __hip_atomic_store(XN + j, nonce, RLX_AGENT);
        __hip_atomic_store(XP + j * 128 + lane, ~0ull, RLX_AGENT); __hip_atomic_store(XP + j * 128 + 64 + lane, ~0ull, RLX_AGENT);
    }
    STAMP(0, 1);
    {
        const LAS char* L = s.lds;
        f0.a[0] = ldsA(L + s.pa[0]); f0.a[1] = ldsA(L + s.pa[0] + 2048); f0.a[2] = ldsA(L + s.pa[0] + 4096); f0.a[3] = ldsA(L + s.pa[0] + 6144); f0.x = ldsA(L + s.pa[0] + s.xo);
#pragma unroll
        for (int n = 0; n < 3; ++n) f0.b[n] = ldsB(L + s.pb[n]);
    }
    unsigned cur = 0, nxt = K1_SLOT, prv = 2 * K1_SLOT;
#define K1_ROT() { unsigned t_ = cur; cur = nxt; nxt = prv; prv = t_; asm volatile("" : "+s"(cur), "+s"(nxt), "+s"(prv)); }
#pragma unroll 1
    for (int T = 0; T < 8; ++T) {
        k1_p1<1, 2, 3072>(s, f0, f1, cur, prv, acc1, cso, rs, rq, ones);
        wait_vm_bar<5>(); SB();
        if (T == 6) {
            s.sa -= 9 * 128; s.sb -= (size_t)9 * 128 * F_ + FH * 2;
            if (lane >= 32) { s.gp = (const char*)w_sp + (size_t)bid * 32768 + (wid & 7) * 448 + (lane - 32) * 16; s.st = 3584; }
            else { s.gp = (const char*)(w_in + (size_t)bk * F_ + 144 * j + 128 + 8 * (bc & 1)); s.st = 128 * F_; }
        }
        k1_p1<0, 1, 3072>(s, f1, f0, nxt, cur, acc1, cso, rs, rq, ones);
        K1_ROT();
    }
    k1_p1<1, 2, 3072>(s, f0, f1, cur, prv, acc1, cso, rs, rq, ones);
    wait_vm_bar<5>(); SB();
    {
        int q = r16 >> 2, p = lane & 3;
#pragma unroll
        for (int n = 0; n < 3; ++n) { int nf = n == 2 ? 8 : 2 * wc + (n ^ wr); s.pb[n] = 16384 + g * 3072 + q * 64 + 8 * p + (nf >> 1) * 512 + (((nf ^ g) & 1) * 32); }
    }
    k1_p1<0, 1, 3072>(s, f1, f0, nxt, cur, acc1, cso, rs, rq, ones);
    K1_ROT();
    STAMP(3, 0);
    PIN(w3v[0]); PIN(w3v[1]);
    *(u32x4*)(ws + WS_W3T + w3o[0]) = w3v[0]; *(u32x4*)(ws + WS_W3T + w3o[1]) = w3v[1];
    float* CS = (float*)(smem + K1_TAB);
    f32x4* RS = (f32x4*)(smem + K1_TAB + 2048);
    float* red = (float*)(smem + K1_TAB + 4096);
    f32x2* ZS = (f32x2*)(smem + K1_TAB + 8192);
    const float c0 = -2.0f * 1.4426950408889634f * 0.7978845608028654f, c1 = c0 * 0.044715f;
    if (r16 == 0) {
        *(f32x4*)(CS + NF1(0) * 16 + 4 * g) = cso[0];
        if (!wr) *(f32x4*)(CS + NF1(1) * 16 + 4 * g) = cso[1];
    }
    {
        float sq = (r16 & 2) ? ((r16 & 1) ? rq[3] : rq[2]) : ((r16 & 1) ? rq[1] : rq[0]);
        float mean = rs[0] * (1.0f / D_), var = fmaxf(sq * (1.0f / D_) - mean * mean, 0.f);
        float sd = sqrtf(var + 1e-5f), rstd = 1.0f / sd;
        if (g == (r16 >> 2)) RS[wr * 64 + wc * 16 + r16] = f32x4{mean, c0 * rstd, c1 * rstd * rstd * rstd, sd};
    }
    f32x4 acc2[4][2] = {}; f32x4 sacc = {}; cso[0] = f32x4{}; cso[1] = f32x4{};
    f32x2 ssum = {}, ssq = {};
    u16* qg = (u16*)(ws + WS_Q);
    f32x4 er, ey0, ey1, ee0, ee1;
    auto none = [](int) {};
#define ISZ(n) (NF1(n) < 9)
#define E_LN(m, n, Y) { Y = acc1[m][n] - *(const f32x4*)(CS + NF1(n) * 16 + 4 * g) * er[0]; if (!ISZ(n)) acc1[m][n] = Y * (er[1] * (1.0f / c0)); PIN(Y); }
#define E_EX(n, Y, E) if (ISZ(n)) { f32x4 a_ = Y * ((Y * Y) * er[2] + er[1]); E = f32x4{EX(a_[0]), EX(a_[1]), EX(a_[2]), EX(a_[3])}; PIN(E); }
#define E_FIN(m, n, Y, E) if (ISZ(n)) { f32x4 d_ = E * er[3] + er[3]; \
        f32x4 v_ = Y * f32x4{__builtin_amdgcn_rcpf(d_[0]), __builtin_amdgcn_rcpf(d_[1]), __builtin_amdgcn_rcpf(d_[2]), __builtin_amdgcn_rcpf(d_[3])}; \
        acc1[m][n] = v_; ssum += f32x2{v_[0], v_[1]} + f32x2{v_[2], v_[3]}; ssq += f32x2{v_[0], v_[1]} * f32x2{v_[0], v_[1]}; ssq += f32x2{v_[2], v_[3]} * f32x2{v_[2], v_[3]}; } PIN(acc1[m][n]);
#define EPI2(m) [&](int st) { \
        if (st == 0) { er = RS[wr * 64 + (m) * 16 + r16]; E_LN(m, 0, ey0); E_LN(m, 1, ey1); E_EX(0, ey0, ee0); } \
        else if (st == 1) { E_EX(1, ey1, ee1); E_FIN(m, 0, ey0, ee0); } \
        else { E_FIN(m, 1, ey1, ee1); } }
#define EPI1(m) [&](int st) { \
        if (st == 0) { E_LN(m, 2, ey0); } else if (st == 1) { E_EX(2, ey0, ee0); } else { E_FIN(m, 2, ey0, ee0); \
            float a_ = ssum[0] + ssum[1], b_ = ssq[0] + ssq[1]; ssum = f32x2{}; ssq = f32x2{}; \
            a_ += __shfl_xor(a_, 16); b_ += __shfl_xor(b_, 16); a_ += __shfl_xor(a_, 32); b_ += __shfl_xor(b_, 32); \
            if (g == 0) *(f32x2*)(red + ((wr * 64 + (m) * 16 + r16) * 4 + wc) * 2) = f32x2{a_, b_}; } }
#define QKV(m0) [&](int st) { \
        const int nf = NF1(st); \
        if (nf >= 10 || (nf == 9 && j < 4)) { \
            const int col = nf >= 10 ? 32 * j + (nf - 10) * 16 : 512 + 16 * j; \
            _Pragma("unroll") for (int m = (m0); m < (m0) + 2; ++m) { \
                f32x4 v = (st == 0 ? acc1[m][0] : st == 1 ? acc1[m][1] : acc1[m][2]) * QSCALE; u32x2 w = {cvtpk(v[0], v[1]), cvtpk(v[2], v[3])}; \
                *(u32x2*)(qg + (size_t)(pm * 128 + wr * 64 + m * 16 + r16) * D_ + col + 4 * g) = w; } \
        } else if (nf == 9 && j < 7) { \
            _Pragma("unroll") for (int m = (m0); m < (m0) + 2; ++m) \
                kv_store(ws + WS_KV + (size_t)(pm * 4 + wr * 2 + (m >> 1)) * 3072, m & 1, j, r16, g, st == 0 ? acc1[m][0] : st == 1 ? acc1[m][1] : acc1[m][2]); \
        } }
#define K1_IT2(GA, GB, VM, RDN, EA, EB) \
    k1_p2<1, GA ? 2 : 0, true>(s, f0, f1, cur, prv, acc2, sacc, cso, ones, EA); \
    wait_vm_bar<VM>(); SB(); \
    k1_p2<0, GB ? 1 : 0, RDN>(s, f1, f0, nxt, cur, acc2, sacc, cso, ones, EB); K1_ROT();
#define K1_A(GA, EA) k1_p2<1, GA ? 2 : 0, true>(s, f0, f1, cur, prv, acc2, sacc, cso, ones, EA)
#define K1_B(GB, RDN, EB) k1_p2<0, GB ? 1 : 0, RDN>(s, f1, f0, nxt, cur, acc2, sacc, cso, ones, EB); K1_ROT()
    const char* xn_s = ws + WS_XCH + pm * 64; const char* xe_s = ws + WS_XCH + 4096 + (pm * 256 + j * 16) * 4; const unsigned l4 = (lane & 15) * 4;
    K1_A(true, none); wait_vm_bar<5>(); SB();
    unsigned echoed = ld_sc1_async(xn_s, l4); SB();
    K1_B(true, true, EPI2(0));
    K1_A(true, EPI1(0)); wait_vm_bar<5>(); SB();
    PIN(echoed);
    if (wid == 7 && lane < 16) __hip_atomic_store(XE + lane * 16 + j, echoed, RLX_AGENT);
    SB();
    K1_B(true, true, EPI2(1));
    K1_A(true, EPI1(1)); wait_vm_bar<5>(); SB();
    K1_B(true, true, EPI2(2));
    K1_A(true, EPI1(2)); wait_vm_bar<5>(); SB();
    unsigned ev = ld_sc1_async(xe_s, l4); SB();
    K1_B(true, true, EPI2(3));
    K1_A(true, EPI1(3));
    STAMP(3, 1);
    wait_vm_bar<5>(); SB();
    PIN(ev);
    bool hs = __all(ev == nonce);
    if (wid == 7) {
#pragma unroll
        for (int hf = 0; hf < 2; ++hf) {
            int r = hf * 64 + lane; f32x4 u0 = *(const f32x4*)(red + r * 8), u1 = *(const f32x4*)(red + r * 8 + 4);
            float a = (u0[0] + u0[2]) + (u1[0] + u1[2]), b = (u0[1] + u0[3]) + (u1[1] + u1[3]);
            unsigned bb = __float_as_uint(b); if (bb == 0xffffffffu) bb = 0x7fc00000u;
            __hip_atomic_store(XP + j * 128 + r, ((unsigned long long)bb << 32) | __float_as_uint(a), RLX_AGENT);
        }
    }
    unsigned nn = ld_sc1_async(xn_s, l4); SB();
    K1_B(true, true, none);
    K1_A(true, none); wait_vm_bar<5>(); SB();
    PIN(nn);
    if (wid == 7 && nn != echoed) { echoed = nn; if (lane < 16) __hip_atomic_store(XE + lane * 16 + j, nn, RLX_AGENT); }
    SB();
    K1_B(true, true, QKV(0));
    K1_A(true, QKV(2)); wait_vm_bar<5>(); SB();
    K1_B(false, true, none);
    K1_A(false, none); wait_vm_bar<0>(); SB();
    const unsigned gat = prv;
    auto gather = [&]() {
        const char* sp = ws + WS_ST + (size_t)pm * 16384;
#pragma unroll
        for (int q4 = 0; q4 < 4; ++q4) {
            set_m0(lds0 + gat + q4 * 4096);
            glds_sc1<0>(lane * 16, sp + q4 * 4096); glds_sc1<1024>(lane * 16, sp + q4 * 4096); glds_sc1<2048>(lane * 16, sp + q4 * 4096); glds_sc1<3072>(lane * 16, sp + q4 * 4096);
        }
    };
    if (wid == 7 && hs) gather();
    SB();
    K1_B(false, true, none);
    K1_A(false, none);
    K1_B(false, false, none);
    STAMP(0, 2);
    if (r16 == 0) { *(f32x4*)(CS + 192 + (2 * wc + wr) * 16 + 4 * g) = cso[0]; if (wid == 0) *(f32x4*)(CS + 192 + 128 + 4 * g) = cso[1]; }
    STAMPT(448, 3, 3);
    if (wid == 7) {
        const unsigned long long t0 = __builtin_amdgcn_s_memrealtime();
        bool fetched = hs;
        while (!hs) {
            ev = __hip_atomic_load(XE + j * 16 + (lane & 15), RLX_AGENT);
            asm volatile("s_waitcnt vmcnt(0)" ::: "memory");
            hs = __all(ev == nonce);
            nn = __hip_atomic_load(XN + (lane & 15), RLX_AGENT);
            if (nn != echoed) { echoed = nn; if (lane < 16) __hip_atomic_store(XE + lane * 16 + j, nn, RLX_AGENT); }
            if (__builtin_amdgcn_s_memrealtime() - t0 > 5000000ull) break;
        }
        f32x4 t;
        for (;;) {
            if (!fetched) { __builtin_amdgcn_fence(__ATOMIC_ACQUIRE, "agent"); gather(); }
            asm volatile("s_waitcnt vmcnt(0)" ::: "memory");
            t = f32x4{}; bool empty = false;
#pragma unroll
            for (int src = 0; src < 16; ++src) {
                f32x4 v = *(const f32x4*)(smem + gat + src * 1024 + lane * 16);
                empty = empty || __float_as_uint(v[1]) == 0xffffffffu || __float_as_uint(v[3]) == 0xffffffffu; t += v;
            }
            if (!__any(empty)) break;
            if (__builtin_amdgcn_s_memrealtime() - t0 > 5000000ull) break;
            fetched = false;
        }
#pragma unroll
        for (int hf = 0; hf < 2; ++hf) {
            float m = t[2 * hf] * (1.0f / FH), var = fmaxf(t[2 * hf + 1] * (1.0f / FH) - m * m, 0.f), r = 1.0f / sqrtf(var + 1e-5f);
            ZS[2 * lane + hf] = f32x2{r, -m * r};
        }
    }
    asm volatile("s_waitcnt lgkmcnt(0)\n\ts_barrier" ::: "memory");
    STAMP(3, 2);
    char* z2b = ws + WS_Z2T + (size_t)(j * 32 + 2 * pm + wr) * 18432;
#pragma unroll
    for (int n = 0; n < 3; ++n) {
        const int nf = NF1(n);
        if (nf < 9) {
#pragma unroll
            for (int m = 0; m < 4; ++m) {
                const int r64 = m * 16 + r16, kb = r64 >> 3; const f32x2 zs = ZS[wr * 64 + r64];
                f32x4 v = acc1[m][n] * zs[0] + zs[1];
                u32x2 w = {cvtpk(v[0], v[1]), cvtpk(v[2], v[3])};
                st8(z2b + (kb * 9 + nf) * 256 + (((r64 & 7) ^ (4 * (kb & 1))) * 32) + g * 8, w);
            }
        }
    }
    char* z1b = ws + WS_Z1 + (size_t)(pm * 16 + j) * 36864;
#pragma unroll
    for (int m = 0; m < 5; ++m) {
        const int rl = wr * 64 + (m < 4 ? m : wc) * 16 + r16; const f32x4 rr = RS[rl];
#pragma unroll
        for (int n = 0; n < 2; ++n) {
            if (m == 4 && n == 1) continue;
            const int nf = m == 4 ? 8 : 2 * wc + (n ^ wr);
            f32x4 y = (m == 4 ? sacc : acc2[m & 3][n]) - *(const f32x4*)(CS + 192 + nf * 16 + 4 * g) * rr[0];
            f32x2 y0 = gelu_y2(f32x2{y[0], y[1]}, rr[1], rr[2], rr[3]), y1 = gelu_y2(f32x2{y[2], y[3]}, rr[1], rr[2], rr[3]);
            u32x2 w = {cvtpk(y0[0], y0[1]), cvtpk(y1[0], y1[1])};
            st8(z1b + (2 * nf + (g >> 1)) * 2048 + rl * 16 + (g & 1) * 8, w);
        }
    }
    STAMP_DRAIN(); STAMP(0, 3);
}

constexpr float ATT_BIAS = 72.0f;
DI float xhalf_max(float m) { auto rr = __builtin_amdgcn_permlane32_swap(__float_as_uint(m), __float_as_uint(m), false, false); return fmaxf(__uint_as_float(rr[0]), __uint_as_float(rr[1])); }
DI float lo_bcast(float m) { auto rr = __builtin_amdgcn_permlane32_swap(__float_as_uint(m), __float_as_uint(m), false, false); return __uint_as_float(rr[0]); }
DI float bf_round(float x) { return __uint_as_float(cvtpk(x, 0.f) << 16); }
DI float max16(const f32x16& s) {
    float a = fmaxf(fmaxf(s[0], s[1]), s[2]), b = fmaxf(fmaxf(s[3], s[4]), s[5]);
    a = fmaxf(fmaxf(a, s[6]), s[7]); b = fmaxf(fmaxf(b, s[8]), s[9]); a = fmaxf(fmaxf(a, s[10]), s[11]); b = fmaxf(fmaxf(b, s[12]), s[13]);
    a = fmaxf(fmaxf(a, s[14]), s[15]); return fmaxf(a, b);
}
DI void exp_pack(const f32x16& s, bf16x8& p0, bf16x8& p1) {
    float e[16];
#pragma unroll
    for (int i = 0; i < 16; ++i) e[i] = __builtin_amdgcn_exp2f(s[i]);
    u32x4 a = {cvtpk(e[0], e[1]), cvtpk(e[2], e[3]), cvtpk(e[4], e[5]), cvtpk(e[6], e[7])};
    u32x4 b = {cvtpk(e[8], e[9]), cvtpk(e[10], e[11]), cvtpk(e[12], e[13]), cvtpk(e[14], e[15])};
    p0 = __builtin_bit_cast(bf16x8, a); p1 = __builtin_bit_cast(bf16x8, b);
}

struct KVF { bf16x8 k1, k2, v0, v1; };
DI void kv_read(KVF& f, const LAS char* tb, int lane, int k2o, int vo) {
    f.k1 = *(const LAS bf16x8*)(tb + lane * 16); f.k2 = *(const LAS bf16x8*)(tb + k2o);
    f.v0 = *(const LAS bf16x8*)(tb + 1024 + vo); f.v1 = *(const LAS bf16x8*)(tb + 2048 + vo);
}
#define k2frag(x) (x)
template <bool RD>
DI void half_step(f32x16& sx, bf16x8& px0, bf16x8& px1, f32x16& sy, const KVF kq, const bf16x8& qy1, const bf16x8& qy2,
                  f32x16& oy, const KVF kv, const bf16x8& py0, const bf16x8& py1, KVF& ld, const LAS char* tb, int lane, int k2o, int vo) {
    const f32x16 zero = {};
    unsigned a0, a1, a2, a3, b0, b1, b2, b3; float e0, e1, e2, e3;
    f32x16 t = MFMA32(kq.k1, qy1, zero); SB();
    if (RD) ld.k1 = *(const LAS bf16x8*)(tb + lane * 16);
    e0 = EX(sx[0]); e1 = EX(sx[1]); e2 = EX(sx[2]); e3 = EX(sx[3]); PIN(e0); PIN(e1); PIN(e2); PIN(e3); SB();
    sy = MFMA32(kq.k2, qy2, t); SB();
    if (RD) ld.k2 = *(const LAS bf16x8*)(tb + k2o);
    a0 = cvtpk(e0, e1); a1 = cvtpk(e2, e3); PIN(a0); PIN(a1);
    e0 = EX(sx[4]); e1 = EX(sx[5]); e2 = EX(sx[6]); e3 = EX(sx[7]); PIN(e0); PIN(e1); PIN(e2); PIN(e3); SB();
    oy = MFMA32(kv.v0, py0, oy); SB();
    if (RD) ld.v0 = *(const LAS bf16x8*)(tb + 1024 + vo);
    a2 = cvtpk(e0, e1); a3 = cvtpk(e2, e3); PIN(a2); PIN(a3);
    e0 = EX(sx[8]); e1 = EX(sx[9]); e2 = EX(sx[10]); e3 = EX(sx[11]); PIN(e0); PIN(e1); PIN(e2); PIN(e3); SB();
    oy = MFMA32(kv.v1, py1, oy); SB();
    if (RD) ld.v1 = *(const LAS bf16x8*)(tb + 2048 + vo);
    b0 = cvtpk(e0, e1); b1 = cvtpk(e2, e3); PIN(b0); PIN(b1);
    e0 = EX(sx[12]); e1 = EX(sx[13]); e2 = EX(sx[14]); e3 = EX(sx[15]);
    b2 = cvtpk(e0, e1); b3 = cvtpk(e2, e3); PIN(b2); PIN(b3); SB();
    u32x4 a = {a0, a1, a2, a3}, b = {b0, b1, b2, b3};
    px0 = __builtin_bit_cast(bf16x8, a); px1 = __builtin_bit_cast(bf16x8, b);
}
DI void att_rescale(f32x16& o, f32x16& s, float& mref, bf16x8& q2, bool& bad, int c, int h) {
    bool nf = false;
#pragma unroll
    for (int i = 0; i < 11; ++i) nf = nf || (!(fabsf(o[i]) <= 3.0e38f) && (i < 8 || h == 0));
    if (__any(nf)) { bad = true; return; }
    float lb = lo_bcast(o[10]);
    int e = __builtin_amdgcn_frexp_expf(lb);
    float d = (float)(e + (int)ATT_BIAS); d = d < 0 ? 0 : d;
    float nm = bf_round(mref + d); float dd = nm - mref; mref = nm;
    int sh = -(int)dd;
#pragma unroll
    for (int i = 0; i < 16; ++i) { o[i] = __builtin_amdgcn_ldexpf(o[i], sh); s[i] -= dd; }
    u32x4 b = __builtin_bit_cast(u32x4, q2); b[1] = h ? 0u : (__float_as_uint(-nm) >> 16); q2 = __builtin_bit_cast(bf16x8, b);
}

constexpr int K2_SLOT = 40960, K2_LDS = 3 * K2_SLOT;
struct G2F { bf16x8 a[4], b[4], sb; };
struct G2S {
    unsigned va[4], vl, vl2;
    const char* sa; const char* sl; unsigned lstep;
    unsigned m0a, m0l; int so;
    const LAS char* pa[2][2]; const LAS char* pb[2];
};
template <int KS, int GL, bool RD> DI void g2_phase(G2S& s, const G2F& f, G2F& n, unsigned ro, unsigned wo, f32x4 (&acc)[4][4], f32x4 (&sacc)[2]) {
    const LAS char* a0 = s.pa[0][KS] + ro; const LAS char* a1 = s.pa[1][KS] + ro;
    const LAS char* b0 = s.pb[0] + ro + KS * 9216; const LAS char* b1 = s.pb[1] + ro + KS * 9216;
#define MM(m, nn) acc[m][nn] = MFMA16(f.b[nn], f.a[m], acc[m][nn])
    MM(0, 0); if (RD) n.b[0] = ldsB2(b0, b1); SB();
    MM(1, 0); if (RD) n.a[0] = ldsA(a0); SB();
    MM(2, 0); if (RD) n.a[1] = ldsA(a0 + 2048); SB();
    MM(3, 0); if (RD) n.a[2] = ldsA(a1); SB();
    MM(0, 1); if (RD) n.a[3] = ldsA(a1 + 2048); SB();
    MM(1, 1); if (RD) n.b[1] = ldsB2(b0 + 256, b1 + 256); SB();
    MM(2, 1); if (RD) n.b[2] = ldsB2(b0 + 512, b1 + 512); SB();
    MM(3, 1); if (RD) n.b[3] = ldsB2(b0 + 768, b1 + 768); SB();
    MM(0, 2); if (RD) n.sb = ldsB2(b0 + s.so, b1 + s.so); SB();
    MM(1, 2); if (GL == 1) { set_m0(s.m0a + wo); glds_s<0>(s.va[0], s.sa); } if (GL == 2) { glds_s<1024>(s.vl, s.sl); } SB();
    MM(2, 2); SB();
    MM(3, 2); if (GL == 1) glds_s<1024>(s.va[1], s.sa); if (GL == 2) glds_s<2048>(s.vl, s.sl); SB();
    MM(0, 3); SB();
    MM(1, 3); if (GL == 1) glds_s<2048>(s.va[2], s.sa); if (GL == 2) glds_s<3072>(s.vl, s.sl); SB();
    MM(2, 3); SB();
    MM(3, 3); if (GL == 1) glds_s<3072>(s.va[3], s.sa); if (GL == 2) { set_m0(s.m0l + wo + 4096); glds_s<0>(s.vl2, s.sl); } SB();
    sacc[0] = MFMA16(f.sb, f.a[0], sacc[0]); if (GL == 1) { set_m0(s.m0l + wo); glds_s<0>(s.vl, s.sl); } if (GL == 2) glds_s<1024>(s.vl2, s.sl); SB();
    sacc[1] = MFMA16(f.sb, f.a[1], sacc[1]); SB();
#undef MM
    if (GL == 1) s.sa += 128;
    if (GL == 2) s.sl += s.lstep;
}
template <bool G2nd, bool G1st, int VM, bool RDN> DI void g2_iter(G2S& s, G2F& f0, G2F& f1, unsigned& cur, unsigned& nxt, unsigned& prv, f32x4 (&acc)[4][4], f32x4 (&sacc)[2]) {
    g2_phase<1, G2nd ? 2 : 0, true>(s, f0, f1, cur, prv, acc, sacc);
    wait_vm_bar<VM>(); SB();
    g2_phase<0, G1st ? 1 : 0, RDN>(s, f1, f0, nxt, cur, acc, sacc);
    unsigned t = cur; cur = nxt; nxt = prv; prv = t;
}

__global__ __launch_bounds__(512, 2) void k2_kernel(const u16* __restrict__ w_sp, const u16* __restrict__ b_sp, char* __restrict__ ws) {
    extern __shared__ __attribute__((aligned(16))) char smem[];
    int bid = blockIdx.x, xcd = bid & 7, idx = bid >> 3;
    int pm = (xcd & 1) * 8 + (idx & 7), j = (xcd >> 1) * 4 + (idx >> 3);
    const int lane = threadIdx.x & 63, wid = __builtin_amdgcn_readfirstlane(threadIdx.x >> 6);
    const unsigned lds0 = (unsigned)(uintptr_t)smem; const LAS char* lds = (const LAS char*)smem;
    constexpr int EP = 148;
    float* et = (float*)smem;
    STAMP(1, 0);
    if (wid < 4) {
        const int wr = wid >> 1, wc = wid & 1;
        G2S s;
#pragma unroll
        for (int i = 0; i < 4; ++i) { int row = 8 * (wid * 4 + i) + (lane >> 3), c = (lane & 7) ^ ((row >> 1) & 7); s.va[i] = row * (S_ * 2) + c * 16 + 4096 - i * 1024; }
        s.vl = lane * 16; s.vl2 = lane * 16 + 4096;
        s.sa = (const char*)(w_sp + (size_t)pm * 128 * S_) - 4096;
        s.sl = wid < 3 ? ws + WS_Z2T + (size_t)j * 32 * 18432 + wid * 6144 : ws + WS_KV;
        s.lstep = wid < 3 ? 18432 : 6144;
        s.m0a = lds0 + wid * 4096; s.m0l = lds0 + 16384 + wid * 6144;
        {
            int r = lane & 15, g = lane >> 4, q = r >> 2, p = lane & 3;
#pragma unroll
            for (int ks = 0; ks < 2; ++ks) {
                int o = r * 128 + (((4 * ks + g) ^ (r >> 1)) * 16) + wr * 8192;
                s.pa[0][ks] = lds + o + wc * 4096; s.pa[1][ks] = lds + o + (1 - wc) * 4096;
            }
            int ob = 16384 + g * 2304 + q * 32 + 8 * p + wc * 1024;
            s.pb[0] = lds + ob + ((g & 1) * 128); s.pb[1] = lds + ob + (((g & 1) ^ 1) * 128);
            s.so = 2048 - wc * 1024;
        }
#pragma unroll
        for (int t = 0; t < 3; ++t) {
            set_m0(s.m0a + t * K2_SLOT); glds_s<0>(s.va[0], s.sa); glds_s<1024>(s.va[1], s.sa); glds_s<2048>(s.va[2], s.sa); glds_s<3072>(s.va[3], s.sa); s.sa += 128;
            set_m0(s.m0l + t * K2_SLOT); glds_s<0>(s.vl, s.sl);
            if (t < 2) {
                glds_s<1024>(s.vl, s.sl); glds_s<2048>(s.vl, s.sl); glds_s<3072>(s.vl, s.sl);
                set_m0(s.m0l + t * K2_SLOT + 4096); glds_s<0>(s.vl2, s.sl); glds_s<1024>(s.vl2, s.sl); s.sl += s.lstep;
            }
        }
        f32x4 acc[4][4] = {}; f32x4 sacc[2] = {};
        G2F f0, f1;
        wait_vm_bar<15>();
        STAMP(1, 1);
        f0.a[0] = ldsA(s.pa[0][0]); f0.a[1] = ldsA(s.pa[0][0] + 2048); f0.a[2] = ldsA(s.pa[1][0]); f0.a[3] = ldsA(s.pa[1][0] + 2048);
        f0.b[0] = ldsB2(s.pb[0], s.pb[1]); f0.b[1] = ldsB2(s.pb[0] + 256, s.pb[1] + 256); f0.b[2] = ldsB2(s.pb[0] + 512, s.pb[1] + 512); f0.b[3] = ldsB2(s.pb[0] + 768, s.pb[1] + 768); f0.sb = ldsB2(s.pb[0] + s.so, s.pb[1] + s.so);
        unsigned cur = 0, nxt = K2_SLOT, prv = 2 * K2_SLOT;
        for (int t = 0; t < 29; ++t) g2_iter<true, true, 10, true>(s, f0, f1, cur, nxt, prv, acc, sacc);
        g2_iter<true, false, 10, true>(s, f0, f1, cur, nxt, prv, acc, sacc);
        g2_iter<false, false, 0, true>(s, f0, f1, cur, nxt, prv, acc, sacc);
        const int c = lane & 15, g = lane >> 4;
        const char* z1b = ws + WS_Z1 + (size_t)(pm * 16 + j) * 36864 + (g >> 1) * 2048 + (wr * 64 + c) * 16 + (g & 1) * 8;
        u32x2 z1r[4][4], z1s[2]; float bias[4];
#pragma unroll
        for (int m = 0; m < 4; ++m) {
            const int mo = (m ^ (2 * wc)) * 16;
            bias[m] = bf2f(b_sp[pm * 128 + wr * 64 + mo + c]);
#pragma unroll
            for (int n = 0; n < 4; ++n) z1r[m][n] = *(const u32x2*)(z1b + (wc * 4 + n) * 4096 + mo * 16);
            if (m < 2) z1s[m] = *(const u32x2*)(z1b + 8 * 4096 + mo * 16);
        }
        SB();
        g2_iter<false, false, 63, false>(s, f0, f1, cur, nxt, prv, acc, sacc);
        __builtin_amdgcn_s_setprio(2);
        STAMP(1, 2);
        auto fin = [&](f32x4 v, float b, u32x2 z) { v += b; return u32x2{cvtpk(bflo(z[0]) * v[0], bfhi(z[0]) * v[1]), cvtpk(bflo(z[1]) * v[2], bfhi(z[1]) * v[3])}; };
        auto put2 = [&](u32x2 A, u32x2 B, int rowA, int nfA, int rowB, int nfB) {
            auto r0 = __builtin_amdgcn_permlane16_swap(A[0], B[0], false, false); auto r1 = __builtin_amdgcn_permlane16_swap(A[1], B[1], false, false);
            const int row = (g & 1) ? rowB : rowA, cgl = 18 * j + 2 * ((g & 1) ? nfB : nfA) + (g >> 1);
            char* p = ws + WS_UO + ((size_t)((row >> 6) * 90 + (cgl >> 2)) * 4 + ((row >> 4) & 3)) * 1024 + (cgl & 3) * 256 + (row & 15) * 16;
            st16(p, u32x4{r0[0], r1[0], r0[1], r1[1]});
        };
#pragma unroll
        for (int m = 0; m < 4; ++m) {
            const int row = pm * 128 + wr * 64 + (m ^ (2 * wc)) * 16 + c;
#pragma unroll
            for (int n = 0; n < 4; n += 2) put2(fin(acc[m][n], bias[m], z1r[m][n]), fin(acc[m][n + 1], bias[m], z1r[m][n + 1]), row, wc * 4 + n, row, wc * 4 + n + 1);
        }
        { const int row = pm * 128 + wr * 64 + (2 * wc) * 16 + c; put2(fin(sacc[0], bias[0], z1s[0]), fin(sacc[1], bias[1], z1s[1]), row, 8, row + 16, 8); }
        STAMP_DRAIN(); STAMP(1, 3);
    } else {
        __builtin_amdgcn_s_setprio(1);
        const int c = lane & 31, h = lane >> 5, aw = wid - 4;
        const char* kvg = ws + WS_KV;
        const int row0 = blockIdx.x * 8 + aw * 2;
        bf16x8 q1[2], q2[2]; float mref[2];
        const f32x16 zero = {};
#pragma unroll
        for (int t = 0; t < 2; ++t) {
            const u16* qr = (const u16*)(ws + WS_Q) + (size_t)(row0 + t) * D_ + 18 * c;
            u32x4 a; const unsigned* qp = (const unsigned*)(qr + 8 * h);
            a[0] = qp[0]; a[1] = qp[1]; a[2] = qp[2]; a[3] = qp[3];
            q1[t] = __builtin_bit_cast(bf16x8, a);
            unsigned e = *(const unsigned*)(qr + 16);
            u32x4 b = {h ? 0u : e, 0u, 0u, 0u};
            q2[t] = __builtin_bit_cast(bf16x8, b);
        }
        f32x16 oA = zero, oB = zero;
        bool bad = false;
        const int k2o = k2slot(c), vo = (c < 19 ? lane : 63) * 16;
        const LAS char* kvl = lds + 16384 + 18432;
        asm volatile("s_waitcnt vmcnt(0)\n\ts_barrier" ::: "memory");
        KVF X, Y;
        kv_read(X, kvl, lane, k2o, vo); kv_read(Y, kvl + 3072, lane, k2o, vo);
#pragma unroll
        for (int t = 0; t < 2; ++t) {
            f32x16 s0 = MFMA32(X.k1, q1[t], zero); s0 = MFMA32(X.k2, q2[t], s0);
            f32x16 s1 = MFMA32(Y.k1, q1[t], zero); s1 = MFMA32(Y.k2, q2[t], s1);
            float m = xhalf_max(fmaxf(max16(s0), max16(s1)));
            mref[t] = bf_round(rintf(m) + ATT_BIAS);
            u32x4 b = __builtin_bit_cast(u32x4, q2[t]); b[1] = h ? 0u : (__float_as_uint(-mref[t]) >> 16); q2[t] = __builtin_bit_cast(bf16x8, b);
        }
        f32x16 sA = MFMA32(X.k1, q1[0], zero); sA = MFMA32(X.k2, q2[0], sA);
        f32x16 sB = zero;
        bf16x8 pA0, pA1, pB0 = {}, pB1 = {};
        half_step<false>(sA, pA0, pA1, sB, X, q1[1], q2[1], oB, X, pB0, pB1, Y, kvl, lane, k2o, vo);
        half_step<false>(sB, pB0, pB1, sA, Y, q1[0], q2[0], oA, X, pA0, pA1, Y, kvl, lane, k2o, vo);
        asm volatile("s_waitcnt lgkmcnt(0)\n\ts_barrier" ::: "memory");
        unsigned slot = K2_SLOT;
        for (int t = 1; t < 32; ++t) {
            const LAS char* tb = kvl + slot;
            half_step<true>(sA, pA0, pA1, sB, Y, q1[1], q2[1], oB, X, pB0, pB1, X, tb, lane, k2o, vo);
            half_step<false>(sB, pB0, pB1, sA, X, q1[0], q2[0], oA, Y, pA0, pA1, X, tb, lane, k2o, vo);
            bool chkB = (h == 0) && !(oB[10] <= 0x1p20f);
            half_step<true>(sA, pA0, pA1, sB, X, q1[1], q2[1], oB, Y, pB0, pB1, Y, tb + 3072, lane, k2o, vo);
            if (__builtin_expect(__any(chkB), 0)) att_rescale(oB, sB, mref[1], q2[1], bad, c, h);
            bool chkA = (h == 0) && !(oA[10] <= 0x1p20f);
            half_step<false>(sB, pB0, pB1, sA, Y, q1[0], q2[0], oA, X, pA0, pA1, Y, tb, lane, k2o, vo);
            if (__builtin_expect(__any(chkA), 0)) att_rescale(oA, sA, mref[0], q2[0], bad, c, h);
            asm volatile("s_waitcnt lgkmcnt(0)\n\ts_barrier" ::: "memory");
            slot = slot == 2 * K2_SLOT ? 0 : slot + K2_SLOT;
        }
        half_step<false>(sA, pA0, pA1, sB, Y, q1[1], q2[1], oB, X, pB0, pB1, X, kvl, lane, k2o, vo);
        exp_pack(sB, pB0, pB1);
        oA = MFMA32(Y.v0, pA0, oA); oA = MFMA32(Y.v1, pA1, oA);
        oB = MFMA32(Y.v0, pB0, oB); oB = MFMA32(Y.v1, pB1, oB);
        {
            bool nf = false;
#pragma unroll
            for (int i = 0; i < 11; ++i) nf = nf || ((!(fabsf(oA[i]) <= 3.0e38f) || !(fabsf(oB[i]) <= 3.0e38f)) && (i < 8 || h == 0));
            if (__any(nf)) bad = true;
        }
        f32x16 o[2] = {oA, oB};
        if (__builtin_expect(bad, 0)) {
#pragma unroll 1
            for (int t = 0; t < 2; ++t) {
                bf16x8 qa = t ? q1[1] : q1[0]; u32x4 qb = __builtin_bit_cast(u32x4, t ? q2[1] : q2[0]); qb[1] = 0;
                f32x16 oo = zero; float m = -3.0e38f;
                for (int kt = 0; kt < 64; ++kt) {
                    const char* tb = kvg + (size_t)kt * 3072;
                    bf16x8 k1 = *(const bf16x8*)(tb + lane * 16);
                    u32x4 k2v = *(const u32x4*)(tb + k2o);
                    bf16x8 v0 = *(const bf16x8*)(tb + 1024 + vo), v1 = *(const bf16x8*)(tb + 2048 + vo);
                    f32x16 s = MFMA32(k1, qa, zero); s = MFMA32(__builtin_bit_cast(bf16x8, k2v), __builtin_bit_cast(bf16x8, qb), s);
                    float nm = fmaxf(m, xhalf_max(max16(s)));
                    float sc = __builtin_amdgcn_exp2f(m - nm); m = nm;
#pragma unroll
                    for (int i = 0; i < 16; ++i) { oo[i] *= sc; s[i] -= nm; }
                    bf16x8 p0, p1; exp_pack(s, p0, p1);
                    oo = MFMA32(v0, p0, oo); oo = MFMA32(v1, p1, oo);
                }
                if (t) o[1] = oo; else o[0] = oo;
            }
        }
#pragma unroll
        for (int t = 0; t < 2; ++t) {
            float inv = 1.0f / lo_bcast(o[t][10]);
            const int orow = row0 + t;
            char* ob = ws + WS_UO + ((size_t)((orow >> 6) * 90 + 72) * 4 + ((orow >> 4) & 3)) * 1024 + (orow & 15) * 16;
            unsigned w0 = cvtpk(o[t][0] * inv, o[t][1] * inv), w1 = cvtpk(o[t][2] * inv, o[t][3] * inv);
            unsigned w2 = cvtpk(o[t][4] * inv, o[t][5] * inv), w3 = cvtpk(o[t][6] * inv, o[t][7] * inv);
            unsigned w4 = cvtpk(o[t][8] * inv, o[t][9] * inv);
            auto put = [&](int d, unsigned w) { int kk = 18 * c + d; *(unsigned*)(ob + (kk >> 5) * 4096 + ((kk >> 3) & 3) * 256 + (kk & 7) * 2) = w; };
            put(4 * h, w0); put(4 * h + 2, w1); put(8 + 4 * h, w2); put(10 + 4 * h, w3);
            if (h == 0) put(16, w4);
        }
    }
}

constexpr int K3_SLOT = 8704, K3_NS = 4, K3_GW = K3_NS * K3_SLOT, K3_LDS = 4 * K3_GW + 64;
DI void k3_issue(unsigned wm0, unsigned va, const char* sa, const char* sb) {
    set_m0(wm0); glds_s<0>(va, sa); glds_s<1024>(va, sa); glds_s<2048>(va, sa); glds_s<3072>(va, sa);
    set_m0(wm0 + 4096); glds_s<0>(va, sb); glds_s<1024>(va, sb); glds_s<2048>(va, sb); glds_s<3072>(va, sb);
    asm volatile("s_mov_b32 m0, %2\n\ts_mov_b32 exec_hi, 0\n\tglobal_load_lds_dwordx4 %0, %1\n\ts_mov_b32 exec_hi, -1" ::"v"(va + 4096), "s"(sb), "s"(wm0 + 8192));
}
template <bool GL, int VM> DI void k3_step(const LAS char* rd, unsigned wm0, unsigned va, const char* sa, const char* sb, unsigned pb, f32x4 (&acc)[4][5]) {
    wait_vm<VM>();
    bf16x8 a[4], b[5];
#pragma unroll
    for (int m = 0; m < 4; ++m) a[m] = ldsA(rd + va + m * 1024);
#pragma unroll
    for (int n = 0; n < 5; ++n) b[n] = ldsB2(rd + pb + n * 32, rd + pb + n * 32 + 576);
    asm volatile("s_waitcnt lgkmcnt(0)" ::: "memory"); SB();
    if (GL) k3_issue(wm0, va, sa, sb);
    SB();
#pragma unroll
    for (int n = 0; n < 5; ++n)
#pragma unroll
        for (int m = 0; m < 4; ++m) acc[m][n] = MFMA16(b[n], a[m], acc[m][n]);
    SB();
}
__global__ __launch_bounds__(256, 1) void k3_kernel(const u16* __restrict__ x, char* __restrict__ ws, u16* __restrict__ out) {
    extern __shared__ __attribute__((aligned(16))) char smem[];
    const int bid = blockIdx.x, xcd = bid & 7, idx = bid >> 3;
    const int rb = (xcd & 3) * 8 + (idx & 7), j = (xcd >> 2) * 4 + (idx >> 3);
    const int lane = threadIdx.x & 63, wid = __builtin_amdgcn_readfirstlane(threadIdx.x >> 6);
    const unsigned lds0 = (unsigned)(uintptr_t)smem;
    const unsigned va = lane * 16;
    constexpr int EP = 84;
    STAMP(2, 0);
    const LAS char* lds = (const LAS char*)smem + wid * K3_GW;
    const char* ua = ws + WS_UO + (size_t)rb * 90 * 4096 + wid * 4096;
    const char* wb = ws + WS_W3T + (size_t)j * 90 * 4608 + wid * 4608;
    const unsigned wm = lds0 + wid * K3_GW;
#pragma unroll
    for (int i = 0; i < 4; ++i) k3_issue(wm + i * K3_SLOT, va, ua + i * 16384, wb + i * 18432);
    const int g = lane >> 4, q = (lane & 15) >> 2, p = lane & 3;
    const unsigned pb = 4096 + (8 * g + q) * 144 + 8 * p;
    const int n = wid < 2 ? 23 : 22;
    f32x4 acc[4][5] = {};
    STAMP(2, 1);
    unsigned slot = 0;
#define K3_ROT() slot = slot == 3 * K3_SLOT ? 0 : slot + K3_SLOT
    for (int i = 4; i < n; ++i) { k3_step<true, 27>(lds + slot, wm + slot, va, ua + i * 16384, wb + i * 18432, pb, acc); K3_ROT(); }
    u32x4 xv[3];
#pragma unroll
    for (int i = 0; i < 3; ++i) { int ci = threadIdx.x + 256 * i; if (ci >= 576) ci = 575; xv[i] = *(const u32x4*)(x + (size_t)(rb * 64 + ci / 9) * D_ + 72 * j + (ci % 9) * 8); }
    SB();
    k3_step<false, 27>(lds + slot, 0, va, ua, ua, pb, acc); K3_ROT();
    k3_step<false, 18>(lds + slot, 0, va, ua, ua, pb, acc); K3_ROT();
    k3_step<false, 9>(lds + slot, 0, va, ua, ua, pb, acc); K3_ROT();
    k3_step<false, 0>(lds + slot, 0, va, ua, ua, pb, acc);
    STAMP(2, 2);
    float* et = (float*)(smem + wid * K3_GW);
#pragma unroll
    for (int m = 0; m < 4; ++m)
#pragma unroll
        for (int nn = 0; nn < 5; ++nn) *(f32x4*)(et + (m * 16 + (lane & 15)) * EP + nn * 16 + 4 * g) = acc[m][nn];
    asm volatile("s_waitcnt lgkmcnt(0)\n\ts_barrier" ::: "memory");
#pragma unroll
    for (int i = 0; i < 3; ++i) {
        int ci = threadIdx.x + 256 * i;
        if (ci < 576) {
            int row = ci / 9, ch = ci % 9;
            f32x4 s0 = {bflo(xv[i][0]), bfhi(xv[i][0]), bflo(xv[i][1]), bfhi(xv[i][1])}, s1 = {bflo(xv[i][2]), bfhi(xv[i][2]), bflo(xv[i][3]), bfhi(xv[i][3])};
#pragma unroll
            for (int w = 0; w < 4; ++w) {
                const float* e = (const float*)(smem + w * K3_GW) + row * EP + ch * 8;
                s0 += *(const f32x4*)e; s1 += *(const f32x4*)(e + 4);
            }
            u32x4 o = {cvtpk(s0[0], s0[1]), cvtpk(s0[2], s0[3]), cvtpk(s1[0], s1[1]), cvtpk(s1[2], s1[3])};
            *(u32x4*)(out + (size_t)(rb * 64 + row) * D_ + 72 * j + ch * 8) = o;
        }
    }
    STAMP_DRAIN(); STAMP(2, 3);
}

extern "C" void kernel_launch(void* const* d_in, const int* in_sizes, int n_in, void* d_out, int out_size, void* d_ws, size_t ws_size, hipStream_t stream) {
    const u16* x = (const u16*)d_in[0]; const u16* wq = (const u16*)d_in[1]; const u16* wk = (const u16*)d_in[2]; const u16* wv = (const u16*)d_in[3];
    const u16* wo = (const u16*)d_in[4]; const u16* w_in = (const u16*)d_in[6]; const u16* w_sp = (const u16*)d_in[7]; const u16* b_sp = (const u16*)d_in[8];
    const u16* w_out = (const u16*)d_in[9]; char* ws = (char*)d_ws;
    (void)hipFuncSetAttribute((const void*)k1_kernel, hipFuncAttributeMaxDynamicSharedMemorySize, K1_LDS);
    (void)hipFuncSetAttribute((const void*)k2_kernel, hipFuncAttributeMaxDynamicSharedMemorySize, K2_LDS);
    (void)hipFuncSetAttribute((const void*)k3_kernel, hipFuncAttributeMaxDynamicSharedMemorySize, K3_LDS);
    k1_kernel<<<256, 512, K1_LDS, stream>>>(x, wq, wk, wv, w_in, w_sp, w_out, wo, ws);
    k2_kernel<<<256, 512, K2_LDS, stream>>>(w_sp, b_sp, ws);
    k3_kernel<<<256, 256, K3_LDS, stream>>>(x, ws, (u16*)d_out);
}
```

```cpp
#include <hip/hip_runtime.h>
#include <hip/hip_bf16.h>
#include <stdint.h>

typedef unsigned short u16;
typedef short bf16x8 __attribute__((ext_vector_type(8)));
typedef short s16x4 __attribute__((ext_vector_type(4)));
typedef float f32x2 __attribute__((ext_vector_type(2)));
typedef float f32x4 __attribute__((ext_vector_type(4)));
typedef float f32x16 __attribute__((ext_vector_type(16)));
typedef unsigned u32x4 __attribute__((ext_vector_type(4)));
typedef unsigned u32x2 __attribute__((ext_vector_type(2)));
typedef __bf16 bf16x2_t __attribute__((ext_vector_type(2)));
#define LAS __attribute__((address_space(3)))
#define DI __device__ __forceinline__

constexpr int S_ = 2048, D_ = 576, FH = 2304, F_ = 4608;

constexpr size_t WS_Q = 0;
constexpr size_t WS_KV = WS_Q + (size_t)S_ * D_ * 2;
constexpr size_t WS_Z1 = WS_KV + 64 * 3072;
constexpr size_t WS_Z2T = WS_Z1 + (size_t)S_ * FH * 2;
constexpr size_t WS_UO = WS_Z2T + (size_t)S_ * FH * 2;
constexpr size_t WS_ST = WS_UO + (size_t)S_ * 2880 * 2;
constexpr size_t WS_XCH = WS_ST + (size_t)S_ * 16 * 8;
constexpr size_t WS_W3T = WS_XCH + 4096 + 16384;
constexpr size_t WS_DBG = 128u << 20;
#define STAMP(kid, i)
#define STAMP_DRAIN()
#define STAMPT(tid, kid, i)

DI unsigned cvtpk(float lo, float hi) { f32x2 v = {lo, hi}; return __builtin_bit_cast(unsigned, __builtin_convertvector(v, bf16x2_t)); }
DI float bf2f(u16 b) { return __uint_as_float(((unsigned)b) << 16); }
DI float bflo(unsigned w) { return __uint_as_float(w << 16); }
DI float bfhi(unsigned w) { return __uint_as_float(w & 0xffff0000u); }
#define SB() __builtin_amdgcn_sched_barrier(0)
#define PIN(x) asm volatile("" : "+v"(x))
#define EX(x) __builtin_amdgcn_exp2f(x)
#define MFMA16(a, b, c) __builtin_amdgcn_mfma_f32_16x16x32_bf16(a, b, c, 0, 0, 0)
#define MFMA32(a, b, c) __builtin_amdgcn_mfma_f32_32x32x16_bf16(a, b, c, 0, 0, 0)

DI s16x4 trread(const LAS char* p) { return __builtin_bit_cast(s16x4, __builtin_amdgcn_ds_read_tr16_b64_v4i16((LAS s16x4*)p)); }
template <int N> DI void wait_vm_bar() { asm volatile("s_waitcnt vmcnt(%0) lgkmcnt(0)\n\ts_barrier" ::"n"(N) : "memory"); }
template <int N> DI void wait_vm() { asm volatile("s_waitcnt vmcnt(%0)" ::"n"(N) : "memory"); }
template <int OFF> DI void glds_s(unsigned voff, const char* sbase) { asm volatile("global_load_lds_dwordx4 %0, %1 offset:%2" ::"v"(voff), "s"(sbase), "n"(OFF)); }
template <int OFF> DI void glds_sc1(unsigned voff, const char* sbase) { asm volatile("global_load_lds_dwordx4 %0, %1 offset:%2 sc1" ::"v"(voff), "s"(sbase), "n"(OFF)); }
DI void glds_v(const char* p, unsigned m0v) { asm volatile("s_mov_b32 m0, %1\n\ts_nop 0\n\tglobal_load_lds_dwordx4 %0, off" ::"v"(p), "s"(m0v)); }
DI unsigned ld_sc1_async(const char* sb, unsigned vo) { unsigned v; asm volatile("global_load_dword %0, %1, %2 sc1" : "=v"(v) : "v"(vo), "s"(sb) : "memory"); return v; }
#define WT
DI void st16(void* p, u32x4 v) { asm volatile("global_store_dwordx4 %0, %1, off sc1\n\ts_nop 1" :: "v"(p), "v"(v) : "memory"); }
DI void st8(void* p, u32x2 v) { asm volatile("global_store_dwordx2 %0, %1, off sc1" :: "v"(p), "v"(v) : "memory"); }
DI void set_m0(unsigned v) { asm volatile("s_mov_b32 m0, %0\n\ts_nop 0" ::"s"(v)); }
DI bf16x8 ldsA(const LAS char* p) { return *(const LAS bf16x8*)p; }
DI bf16x8 ldsB2(const LAS char* p0, const LAS char* p1) { s16x4 lo = trread(p0), hi = trread(p1); return __builtin_shufflevector(lo, hi, 0, 1, 2, 3, 4, 5, 6, 7); }
DI bf16x8 ldsB(const LAS char* p) { return ldsB2(p, p + 256); }

DI float gelu_t(float x) {
    const float c0 = -2.0f * 1.4426950408889634f * 0.7978845608028654f;
    const float c1 = c0 * 0.044715f;
    float x2 = x * x;
    float t = __builtin_fmaf(x2, c1, c0);
    float e = __builtin_amdgcn_exp2f(x * t);
    return x * __builtin_amdgcn_rcpf(1.0f + e);
}
DI int k2slot(int c) { return c < 13 ? 1024 + (19 + c) * 16 : (c < 25 ? 1024 + (51 + c - 13) * 16 : 2048 + (19 + c - 25) * 16); }
constexpr float QSCALE = 0.23570226039551584f * 1.4426950408889634f;

constexpr int K1_SLOT = 40960, K1_TAB = 3 * K1_SLOT, K1_LDS = K1_TAB + 9216;
typedef __attribute__((address_space(1))) unsigned gu32;
typedef __attribute__((address_space(1))) unsigned long long gu64;
#define RLX_AGENT __ATOMIC_RELAXED, __HIP_MEMORY_SCOPE_AGENT
struct K1F { bf16x8 a[4], b[3], x; };
struct K1S {
    unsigned va[2], vb[2]; const char* sa; const char* sb;
    const char* gp; long st;
    unsigned m0a, m0b;
    unsigned pa[2], pb[3]; int xo;
    const LAS char* lds;
};
DI void k1_issue(K1S& s, int i, unsigned wo) {
    if (i == 0) { set_m0(s.m0a + wo); glds_s<0>(s.va[0], s.sa); }
    if (i == 1) { glds_s<1024>(s.va[1], s.sa); s.sa += 128; }
    if (i == 2) { glds_v(s.gp, s.m0b + wo + 2048); s.gp += s.st; }
    if (i == 3) { set_m0(s.m0b + wo); glds_s<0>(s.vb[0], s.sb); }
    if (i == 4) { glds_s<1024>(s.vb[1], s.sb); s.sb += 128 * F_; }
}
#define k1_issue2 k1_issue
DI f32x2 gelu_y2(f32x2 y, float k0, float k1, float ir) {
    f32x2 t = (y * y) * k1 + k0;
    f32x2 a = y * t;
    f32x2 e = {__builtin_amdgcn_exp2f(a[0]), __builtin_amdgcn_exp2f(a[1])};
    f32x2 d = e * ir + ir;
    f32x2 rc = {__builtin_amdgcn_rcpf(d[0]), __builtin_amdgcn_rcpf(d[1])};
    return y * rc;
}
DI float dpp_x1(float x) { return __int_as_float(__builtin_amdgcn_mov_dpp(__float_as_int(x), 0xB1, 0xf, 0xf, true)); }
DI float dpp_x2(float x) { return __int_as_float(__builtin_amdgcn_mov_dpp(__float_as_int(x), 0x4E, 0xf, 0xf, true)); }
DI void kv_store(char* kv, int sv, int j, int c, int g, f32x4 v) {
    const int key = sv * 16 + c;
    if (j == 4) {
        *(u32x2*)(kv + ((g >> 1) * 32 + key) * 16 + (g & 1) * 8) = u32x2{cvtpk(v[0], v[1]), cvtpk(v[2], v[3])};
        if (g == 1) { unsigned* k2p = (unsigned*)(kv + k2slot(key)); k2p[1] = 0x3f80u; *(u32x2*)(k2p + 2) = u32x2{0u, 0u}; }
        if (g == 2 && c < 2) *(u32x4*)(kv + 1024 + sv * 1024 + (c * 32 + 18) * 16) = u32x4{0x3f803f80u, 0x3f803f80u, 0x3f803f80u, 0x3f803f80u};
        if (g == 3 && c == 0) *(u32x4*)(kv + 1024 + sv * 1024 + 63 * 16) = u32x4{0u, 0u, 0u, 0u};
        return;
    }
    if (j == 5 && g == 1) *(unsigned*)(kv + k2slot(key)) = cvtpk(v[2], v[3]);
    const bool o1 = c & 1, o2 = c & 2;
    float t0 = dpp_x1(o1 ? v[0] : v[1]), t1 = dpp_x1(o1 ? v[2] : v[3]);
    if (o1) { v[0] = t0; v[2] = t1; } else { v[1] = t0; v[3] = t1; }
    t0 = dpp_x2(o2 ? v[0] : v[2]); t1 = dpp_x2(o2 ? v[1] : v[3]);
    if (o2) { v[0] = t0; v[1] = t1; } else { v[2] = t0; v[3] = t1; }
    int d = j == 5 ? 4 * (g - 2) + (c & 3) : (g < 2 ? 8 + 4 * g + (c & 3) : 14 + (c & 3));
    bool ok = j == 5 ? g >= 2 : (g < 2 || (g == 3 && (c & 3) >= 2));
    const int kq = c >> 2;
    if (ok) *(u32x2*)(kv + 1024 + sv * 1024 + ((kq & 1) * 32 + d) * 16 + (kq >> 1) * 8) = u32x2{cvtpk(v[0], v[1]), cvtpk(v[2], v[3])};
}
template <int KS, int GL, int KBS> DI void k1_p1(K1S& s, const K1F& f, K1F& n, unsigned ro, unsigned wo, f32x4 (&acc)[4][3], f32x4 (&cso)[2], f32x4& rs, f32x4& rq, const bf16x8& ones) {
    const LAS char* aa = s.lds + (s.pa[KS] + ro); const LAS char* bb = s.lds + ro + KS * 4 * KBS;
#define MM(m, nn) acc[m][nn] = MFMA16(f.b[nn], f.a[m], acc[m][nn])
    MM(0, 0); n.b[0] = ldsB(bb + s.pb[0]); SB();
    MM(1, 0); n.a[0] = ldsA(aa); SB();
    MM(2, 0); n.a[1] = ldsA(aa + 2048); SB();
    MM(3, 0); n.a[2] = ldsA(aa + 4096); SB();
    cso[0] = MFMA16(f.b[0], ones, cso[0]); n.a[3] = ldsA(aa + 6144); SB();
    MM(0, 1); n.b[1] = ldsB(bb + s.pb[1]); SB();
    MM(1, 1); n.b[2] = ldsB(bb + s.pb[2]); SB();
    rs = MFMA16(ones, f.x, rs); SB();
    rq = MFMA16(f.x, f.x, rq); n.x = ldsA(aa + s.xo); SB();
    MM(2, 1); k1_issue(s, GL == 1 ? 0 : 3, wo); SB();
    MM(3, 1); SB();
    cso[1] = MFMA16(f.b[1], ones, cso[1]); SB();
    MM(0, 2); k1_issue(s, GL == 1 ? 1 : 4, wo); SB();
    MM(1, 2); SB();
    MM(2, 2); if (GL == 1) k1_issue(s, 2, wo); SB();
    MM(3, 2); SB();
#undef MM
}
template <int KS, int GL, bool RD, class E> DI void k1_p2(K1S& s, const K1F& f, K1F& n, unsigned ro, unsigned wo, f32x4 (&acc)[4][2], f32x4& sacc, f32x4 (&cso)[2], const bf16x8& ones, E epi) {
    const LAS char* aa = s.lds + (s.pa[KS] + ro); const LAS char* bb = s.lds + ro + KS * 4 * 3072;
#define MM(m, nn) acc[m][nn] = MFMA16(f.b[nn], f.a[m], acc[m][nn])
    MM(0, 0); if (RD) n.b[0] = ldsB(bb + s.pb[0]); SB();
    MM(1, 0); if (RD) n.a[0] = ldsA(aa); SB();
    MM(2, 0); if (RD) n.a[1] = ldsA(aa + 2048); epi(0); SB();
    MM(3, 0); if (RD) n.a[2] = ldsA(aa + 4096); SB();
    cso[0] = MFMA16(f.b[0], ones, cso[0]); if (RD) n.a[3] = ldsA(aa + 6144); SB();
    sacc = MFMA16(f.b[2], f.x, sacc); if (RD) n.b[1] = ldsB(bb + s.pb[1]); epi(1); SB();
    cso[1] = MFMA16(f.b[2], ones, cso[1]); if (RD) n.x = ldsA(aa + s.xo); SB();
    MM(0, 1); if (RD) n.b[2] = ldsB(bb + s.pb[2]); SB();
    MM(1, 1); if (GL) k1_issue2(s, GL == 1 ? 0 : 3, wo); epi(2); SB();
    MM(2, 1); if (GL) k1_issue2(s, GL == 1 ? 1 : 4, wo); SB();
    MM(3, 1); if (GL == 1) k1_issue2(s, 2, wo); SB();
#undef MM
}

__global__ __launch_bounds__(512, 2) void k1_kernel(const u16* __restrict__ x, const u16* __restrict__ wq, const u16* __restrict__ wk, const u16* __restrict__ wv,
                                                    const u16* __restrict__ w_in, const u16* __restrict__ w_sp, const u16* __restrict__ w_out, const u16* __restrict__ wo, char* __restrict__ ws) {
    extern __shared__ __attribute__((aligned(16))) char smem[];
    const int bid = blockIdx.x, xcd = bid & 7, idx = bid >> 3;
    const int pm = (xcd & 1) * 8 + (idx & 7), j = (xcd >> 1) * 4 + (idx >> 3);
    const int lane = threadIdx.x & 63, wid = __builtin_amdgcn_readfirstlane(threadIdx.x >> 6), wr = wid >> 2, wc = wid & 3;
    const unsigned lds0 = (unsigned)(uintptr_t)smem;
    STAMP(0, 0);
    gu32* XN = (gu32*)(ws + WS_XCH) + pm * 16;
    gu32* XE = (gu32*)(ws + WS_XCH + 4096) + pm * 256;
    gu64* XP = (gu64*)(ws + WS_ST) + (size_t)pm * 16 * 128;
    unsigned eo = ld_sc1_async(ws + WS_XCH + 4096 + (pm * 256 + j * 16) * 4, (lane & 15) * 4);
    K1S s; s.lds = (const LAS char*)smem; s.m0a = lds0 + wid * 2048; s.m0b = lds0 + 16384 + wid * 3072;
    const int brow = (lane >> 2) & 7, bc = (lane & 3) ^ (2 * (wid & 1)), bk = 8 * wid + brow;
    {
        int r8 = lane >> 3;
        s.va[0] = (16 * wid + r8) * (D_ * 2) + (((lane & 7) ^ (r8 >> 1)) * 16);
        s.va[1] = (16 * wid + 8 + r8) * (D_ * 2) + (((lane & 7) ^ (4 + (r8 >> 1))) * 16) - 1024;
        s.sa = (const char*)(x + (size_t)pm * 128 * D_);
        s.vb[0] = bk * (F_ * 2) + (lane >> 5) * 64 + bc * 16 + 1024; s.vb[1] = s.vb[0] + 128 - 1024;
        s.sb = (const char*)(w_in + FH + 144 * j) - 1024;
    }
#pragma unroll
    for (int i = 0; i < 5; ++i) if (i != 2) k1_issue(s, i, 0);
    {
        const u16* p; int rs;
        const u16* sp0 = j < 4 ? wq + 512 + 16 * j : (j == 5 ? wk + 10 : (j == 6 ? wv + 8 : wk));
        const u16* sp1 = j < 4 ? wq + 520 + 16 * j : (j == 5 ? wv : (j == 6 ? wv + 10 : wk + 8));
        const int srs = j < 4 ? D_ : 18;
        if (lane >= 32) { p = wq + 32 * j + 8 * bc; rs = D_; }
        else if (bc < 2) { p = w_in + FH + 144 * j + 128 + 8 * bc; rs = F_; }
        else { p = bc == 2 ? sp0 : sp1; rs = srs; }
        s.gp = (const char*)(p + (size_t)bk * rs); s.st = 128 * rs;
    }
    k1_issue(s, 2, 0);
#pragma unroll
    for (int t = 1; t < 3; ++t)
#pragma unroll
        for (int i = 0; i < 5; ++i) if (t < 2 || i < 3) k1_issue(s, i, t * K1_SLOT);
    unsigned nonce = 0;
    u32x4 w3v[2]; unsigned w3o[2];
#pragma unroll
    for (int i = 0; i < 2; ++i) {
        int li = threadIdx.x + 512 * i; if (li >= 810) li = 809;
        int gi = bid * 810 + li, k = gi / 72, cc = gi % 72;
        const u16* p = (k < FH ? w_out + (size_t)k * D_ : wo + (size_t)(k - FH) * D_) + cc * 8;
        asm volatile("global_load_dwordx4 %0, %1, off" : "=v"(w3v[i]) : "v"(p) : "memory");
        w3o[i] = ((cc / 9) * 90 + (k >> 5)) * 4608 + (k & 31) * 144 + (cc % 9) * 16;
    }
    const int r16 = lane & 15, g = lane >> 4, cg1 = wc ^ wr;
    const int nf3 = cg1 == 3 ? 8 : (cg1 == 2 ? 9 : 10 + cg1);
#define LI1(n) (wr ? ((n) + 2) % 3 : (n))
#define NF1(n) (LI1(n) < 2 ? 2 * cg1 + LI1(n) : nf3)
    {
        int q = r16 >> 2, p = lane & 3;
#pragma unroll
        for (int ks = 0; ks < 2; ++ks) s.pa[ks] = r16 * 128 + (((4 * ks + g) ^ (r16 >> 1)) * 16) + wr * 8192;
        s.xo = wc * 2048;
#pragma unroll
        for (int n = 0; n < 3; ++n) { int nf = NF1(n); s.pb[n] = 16384 + g * 3072 + q * 64 + 8 * p + (nf >> 1) * 512 + (((nf ^ g) & 1) * 32); }
    }
    f32x4 acc1[4][3] = {}; f32x4 cso[2] = {};
    f32x4 rs = {}, rq = {};
    const u32x4 onesv = {0x3f803f80u, 0x3f803f80u, 0x3f803f80u, 0x3f803f80u}; bf16x8 ones = __builtin_bit_cast(bf16x8, onesv); PIN(ones);
    K1F f0, f1;
    wait_vm_bar<10>();
    PIN(eo);
    if (wid == 7) {
        nonce = 1;
        while (__ballot(eo == nonce) != 0ull) ++nonce;
        if (lane == 0) __hip_atomic_store(XN + j, nonce, RLX_AGENT);
        __hip_atomic_store(XP + j * 128 + lane, ~0ull, RLX_AGENT); __hip_atomic_store(XP + j * 128 + 64 + lane, ~0ull, RLX_AGENT);
    }
    STAMP(0, 1);
    {
        const LAS char* L = s.lds;
        f0.a[0] = ldsA(L + s.pa[0]); f0.a[1] = ldsA(L + s.pa[0] + 2048); f0.a[2] = ldsA(L + s.pa[0] + 4096); f0.a[3] = ldsA(L + s.pa[0] + 6144); f0.x = ldsA(L + s.pa[0] + s.xo);
#pragma unroll
        for (int n = 0; n < 3; ++n) f0.b[n] = ldsB(L + s.pb[n]);
    }
    unsigned cur = 0, nxt = K1_SLOT, prv = 2 * K1_SLOT;
#define K1_ROT() { unsigned t_ = cur; cur = nxt; nxt = prv; prv = t_; asm volatile("" : "+s"(cur), "+s"(nxt), "+s"(prv)); }
#pragma unroll 1
    for (int T = 0; T < 8; ++T) {
        k1_p1<1, 2, 3072>(s, f0, f1, cur, prv, acc1, cso, rs, rq, ones);
        wait_vm_bar<5>(); SB();
        if (T == 6) {
            s.sa -= 9 * 128; s.sb -= (size_t)9 * 128 * F_ + FH * 2;
            if (lane >= 32) { s.gp = (const char*)w_sp + (size_t)bid * 32768 + (wid & 7) * 448 + (lane - 32) * 16; s.st = 3584; }
            else { s.gp = (const char*)(w_in + (size_t)bk * F_ + 144 * j + 128 + 8 * (bc & 1)); s.st = 128 * F_; }
        }
        k1_p1<0, 1, 3072>(s, f1, f0, nxt, cur, acc1, cso, rs, rq, ones);
        K1_ROT();
    }
    k1_p1<1, 2, 3072>(s, f0, f1, cur, prv, acc1, cso, rs, rq, ones);
    wait_vm_bar<5>(); SB();
    {
        int q = r16 >> 2, p = lane & 3;
#pragma unroll
        for (int n = 0; n < 3; ++n) { int nf = n == 2 ? 8 : 2 * wc + (n ^ wr); s.pb[n] = 16384 + g * 3072 + q * 64 + 8 * p + (nf >> 1) * 512 + (((nf ^ g) & 1) * 32); }
    }
    k1_p1<0, 1, 3072>(s, f1, f0, nxt, cur, acc1, cso, rs, rq, ones);
    K1_ROT();
    STAMP(3, 0);
    PIN(w3v[0]); PIN(w3v[1]);
    *(u32x4*)(ws + WS_W3T + w3o[0]) = w3v[0]; *(u32x4*)(ws + WS_W3T + w3o[1]) = w3v[1];
    float* CS = (float*)(smem + K1_TAB);
    f32x4* RS = (f32x4*)(smem + K1_TAB + 2048);
    float* red = (float*)(smem + K1_TAB + 4096);
    f32x2* ZS = (f32x2*)(smem + K1_TAB + 8192);
    const float c0 = -2.0f * 1.4426950408889634f * 0.7978845608028654f, c1 = c0 * 0.044715f;
    if (r16 == 0) {
        *(f32x4*)(CS + NF1(0) * 16 + 4 * g) = cso[0];
        if (!wr) *(f32x4*)(CS + NF1(1) * 16 + 4 * g) = cso[1];
    }
    {
        float sq = (r16 & 2) ? ((r16 & 1) ? rq[3] : rq[2]) : ((r16 & 1) ? rq[1] : rq[0]);
        float mean = rs[0] * (1.0f / D_), var = fmaxf(sq * (1.0f / D_) - mean * mean, 0.f);
        float sd = sqrtf(var + 1e-5f), rstd = 1.0f / sd;
        if (g == (r16 >> 2)) RS[wr * 64 + wc * 16 + r16] = f32x4{mean, c0 * rstd, c1 * rstd * rstd * rstd, sd};
    }
    f32x4 acc2[4][2] = {}; f32x4 sacc = {}; cso[0] = f32x4{}; cso[1] = f32x4{};
    u16* qg = (u16*)(ws + WS_Q);
    f32x4 er, ern, ey0, ey1, ee0, ee1, cs1[3];
    float ssum = 0.f, ssq = 0.f;
    auto none = [](int) {};
#define ISZ(n) (NF1(n) < 9)
#define E_LN(m, n, Y) asm volatile("v_fma_f32 %0, -%8, %12, %4\n\tv_fma_f32 %1, -%9, %12, %5\n\tv_fma_f32 %2, -%10, %12, %6\n\tv_fma_f32 %3, -%11, %12, %7" \
        : "=&v"(Y[0]), "=&v"(Y[1]), "=&v"(Y[2]), "=&v"(Y[3]) : "v"(acc1[m][n][0]), "v"(acc1[m][n][1]), "v"(acc1[m][n][2]), "v"(acc1[m][n][3]), \
          "v"(cs1[n][0]), "v"(cs1[n][1]), "v"(cs1[n][2]), "v"(cs1[n][3]), "v"(er[0]));
#define E_EX(n, Y, E) if (ISZ(n)) asm volatile( \
        "v_mul_f32 %0, %4, %4\n\tv_mul_f32 %1, %5, %5\n\tv_mul_f32 %2, %6, %6\n\tv_mul_f32 %3, %7, %7\n\t" \
        "v_fma_f32 %0, %0, %9, %8\n\tv_fma_f32 %1, %1, %9, %8\n\tv_fma_f32 %2, %2, %9, %8\n\tv_fma_f32 %3, %3, %9, %8\n\t" \
        "v_mul_f32 %0, %0, %4\n\tv_mul_f32 %1, %1, %5\n\tv_mul_f32 %2, %2, %6\n\tv_mul_f32 %3, %3, %7\n\t" \
        "v_exp_f32 %0, %0\n\tv_exp_f32 %1, %1\n\tv_exp_f32 %2, %2\n\tv_exp_f32 %3, %3\n\ts_nop 0" \
        : "=&v"(E[0]), "=&v"(E[1]), "=&v"(E[2]), "=&v"(E[3]) : "v"(Y[0]), "v"(Y[1]), "v"(Y[2]), "v"(Y[3]), "v"(er[1]), "v"(er[2]));
#define E_FIN(m, n, Y, E) if (ISZ(n)) { asm volatile( \
        "v_fma_f32 %0, %0, %10, %10\n\tv_fma_f32 %1, %1, %10, %10\n\tv_fma_f32 %2, %2, %10, %10\n\tv_fma_f32 %3, %3, %10, %10\n\t" \
        "v_rcp_f32 %0, %0\n\tv_rcp_f32 %1, %1\n\tv_rcp_f32 %2, %2\n\tv_rcp_f32 %3, %3\n\t" \
        "v_mul_f32 %0, %0, %6\n\tv_mul_f32 %1, %1, %7\n\tv_mul_f32 %2, %2, %8\n\tv_mul_f32 %3, %3, %9\n\t" \
        "v_add_f32 %4, %4, %0\n\tv_fmac_f32 %5, %0, %0\n\tv_add_f32 %4, %4, %1\n\tv_fmac_f32 %5, %1, %1\n\t" \
        "v_add_f32 %4, %4, %2\n\tv_fmac_f32 %5, %2, %2\n\tv_add_f32 %4, %4, %3\n\tv_fmac_f32 %5, %3, %3" \
        : "+v"(E[0]), "+v"(E[1]), "+v"(E[2]), "+v"(E[3]), "+v"(ssum), "+v"(ssq) : "v"(Y[0]), "v"(Y[1]), "v"(Y[2]), "v"(Y[3]), "v"(er[3])); acc1[m][n] = E; } \
        else { float rs_ = er[1] * (1.0f / c0); asm volatile("v_mul_f32 %0, %4, %8\n\tv_mul_f32 %1, %5, %8\n\tv_mul_f32 %2, %6, %8\n\tv_mul_f32 %3, %7, %8" \
        : "=&v"(E[0]), "=&v"(E[1]), "=&v"(E[2]), "=&v"(E[3]) : "v"(Y[0]), "v"(Y[1]), "v"(Y[2]), "v"(Y[3]), "v"(rs_)); acc1[m][n] = E; }
#define EPI2(m) [&](int st) { \
        if (st == 0) { er = ern; E_LN(m, 0, ey0); E_LN(m, 1, ey1); E_EX(0, ey0, ee0); } \
        else if (st == 1) { E_EX(1, ey1, ee1); E_FIN(m, 0, ey0, ee0); } \
        else { E_FIN(m, 1, ey1, ee1); } }
#define EPI1(m) [&](int st) { \
        if (st == 0) { if ((m) < 3) ern = RS[wr * 64 + ((m) + 1) * 16 + r16]; E_LN(m, 2, ey0); E_EX(2, ey0, ee0); } else if (st == 1) { E_FIN(m, 2, ey0, ee0); } else { \
            auto x1 = __builtin_amdgcn_permlane16_swap(__float_as_uint(ssum), __float_as_uint(ssq), false, false);       \
            float t_ = __uint_as_float(x1[0]) + __uint_as_float(x1[1]);                                                   \
            auto x2 = __builtin_amdgcn_permlane32_swap(__float_as_uint(t_), __float_as_uint(t_), false, false); \
            t_ = __uint_as_float(x2[0]) + __uint_as_float(x2[1]);                                                         \
            if (g < 2) red[((wr * 64 + (m) * 16 + r16) * 4 + wc) * 2 + g] = t_; \
            ssum = 0.f; ssq = 0.f; } }
#define QKV(m0) [&](int st) { \
        const int nf = NF1(st); \
        if (nf >= 10 || (nf == 9 && j < 4)) { \
            const int col = nf >= 10 ? 32 * j + (nf - 10) * 16 : 512 + 16 * j; \
            _Pragma("unroll") for (int m = (m0); m < (m0) + 2; ++m) { \
                f32x4 v = (st == 0 ? acc1[m][0] : st == 1 ? acc1[m][1] : acc1[m][2]) * QSCALE; u32x2 w = {cvtpk(v[0], v[1]), cvtpk(v[2], v[3])}; \
                *(u32x2*)(qg + (size_t)(pm * 128 + wr * 64 + m * 16 + r16) * D_ + col + 4 * g) = w; } \
        } else if (nf == 9 && j < 7) { \
            _Pragma("unroll") for (int m = (m0); m < (m0) + 2; ++m) \
                kv_store(ws + WS_KV + (size_t)(pm * 4 + wr * 2 + (m >> 1)) * 3072, m & 1, j, r16, g, st == 0 ? acc1[m][0] : st == 1 ? acc1[m][1] : acc1[m][2]); \
        } }
#define K1_IT2(GA, GB, VM, RDN, EA, EB) \
    k1_p2<1, GA ? 2 : 0, true>(s, f0, f1, cur, prv, acc2, sacc, cso, ones, EA); \
    wait_vm_bar<VM>(); SB(); \
    k1_p2<0, GB ? 1 : 0, RDN>(s, f1, f0, nxt, cur, acc2, sacc, cso, ones, EB); K1_ROT();
#define K1_A(GA, EA) k1_p2<1, GA ? 2 : 0, true>(s, f0, f1, cur, prv, acc2, sacc, cso, ones, EA)
#define K1_B(GB, RDN, EB) k1_p2<0, GB ? 1 : 0, RDN>(s, f1, f0, nxt, cur, acc2, sacc, cso, ones, EB); K1_ROT()
    const char* xn_s = ws + WS_XCH + pm * 64; const char* xe_s = ws + WS_XCH + 4096 + (pm * 256 + j * 16) * 4; const unsigned l4 = (lane & 15) * 4;
    K1_A(true, none); wait_vm_bar<5>(); SB();
    unsigned echoed = ld_sc1_async(xn_s, l4);
    ern = RS[wr * 64 + r16];
#pragma unroll
    for (int n = 0; n < 3; ++n) cs1[n] = *(const f32x4*)(CS + NF1(n) * 16 + 4 * g);
    SB();
    K1_B(true, true, EPI2(0));
    K1_A(true, EPI1(0)); wait_vm_bar<5>(); SB();
    PIN(echoed);
    if (wid == 7 && lane < 16) __hip_atomic_store(XE + lane * 16 + j, echoed, RLX_AGENT);
    SB();
    K1_B(true, true, EPI2(1));
    K1_A(true, EPI1(1)); wait_vm_bar<5>(); SB();
    K1_B(true, true, EPI2(2));
    K1_A(true, EPI1(2)); wait_vm_bar<5>(); SB();
    unsigned ev = ld_sc1_async(xe_s, l4); SB();
    K1_B(true, true, EPI2(3));
    K1_A(true, EPI1(3));
    STAMP(3, 1);
    wait_vm_bar<5>(); SB();
    PIN(ev);
    bool hs = __all(ev == nonce);
    if (wid == 7) {
#pragma unroll
        for (int hf = 0; hf < 2; ++hf) {
            int r = hf * 64 + lane; f32x4 u0 = *(const f32x4*)(red + r * 8), u1 = *(const f32x4*)(red + r * 8 + 4);
            float a = (u0[0] + u0[2]) + (u1[0] + u1[2]), b = (u0[1] + u0[3]) + (u1[1] + u1[3]);
            unsigned bb = __float_as_uint(b); if (bb == 0xffffffffu) bb = 0x7fc00000u;
            __hip_atomic_store(XP + j * 128 + r, ((unsigned long long)bb << 32) | __float_as_uint(a), RLX_AGENT);
        }
    }
    unsigned nn = ld_sc1_async(xn_s, l4); SB();
    K1_B(true, true, none);
    K1_A(true, none); wait_vm_bar<5>(); SB();
    PIN(nn);
    if (wid == 7 && nn != echoed) { echoed = nn; if (lane < 16) __hip_atomic_store(XE + lane * 16 + j, nn, RLX_AGENT); }
    SB();
    K1_B(true, true, QKV(0));
    K1_A(true, QKV(2)); wait_vm_bar<5>(); SB();
    K1_B(false, true, none);
    K1_A(false, none); wait_vm_bar<0>(); SB();
    const unsigned gat = prv;
    auto gather = [&]() {
        const char* sp = ws + WS_ST + (size_t)pm * 16384;
#pragma unroll
        for (int q4 = 0; q4 < 4; ++q4) {
            set_m0(lds0 + gat + q4 * 4096);
            glds_sc1<0>(lane * 16, sp + q4 * 4096); glds_sc1<1024>(lane * 16, sp + q4 * 4096); glds_sc1<2048>(lane * 16, sp + q4 * 4096); glds_sc1<3072>(lane * 16, sp + q4 * 4096);
        }
    };
    if (wid == 7 && hs) gather();
    SB();
    K1_B(false, true, none);
    K1_A(false, none);
    K1_B(false, false, none);
    STAMP(0, 2);
    if (r16 == 0) { *(f32x4*)(CS + 192 + (2 * wc + wr) * 16 + 4 * g) = cso[0]; if (wid == 0) *(f32x4*)(CS + 192 + 128 + 4 * g) = cso[1]; }
    STAMPT(448, 3, 3);
    if (wid == 7) {
        const unsigned long long t0 = __builtin_amdgcn_s_memrealtime();
        bool fetched = hs;
        while (!hs) {
            ev = __hip_atomic_load(XE + j * 16 + (lane & 15), RLX_AGENT);
            asm volatile("s_waitcnt vmcnt(0)" ::: "memory");
            hs = __all(ev == nonce);
            nn = __hip_atomic_load(XN + (lane & 15), RLX_AGENT);
            if (nn != echoed) { echoed = nn; if (lane < 16) __hip_atomic_store(XE + lane * 16 + j, nn, RLX_AGENT); }
            if (__builtin_amdgcn_s_memrealtime() - t0 > 5000000ull) break;
        }
        f32x4 t;
        for (;;) {
            if (!fetched) { __builtin_amdgcn_fence(__ATOMIC_ACQUIRE, "agent"); gather(); }
            asm volatile("s_waitcnt vmcnt(0)" ::: "memory");
            t = f32x4{}; bool empty = false;
#pragma unroll
            for (int src = 0; src < 16; ++src) {
                f32x4 v = *(const f32x4*)(smem + gat + src * 1024 + lane * 16);
                empty = empty || __float_as_uint(v[1]) == 0xffffffffu || __float_as_uint(v[3]) == 0xffffffffu; t += v;
            }
            if (!__any(empty)) break;
            if (__builtin_amdgcn_s_memrealtime() - t0 > 5000000ull) break;
            fetched = false;
        }
#pragma unroll
        for (int hf = 0; hf < 2; ++hf) {
            float m = t[2 * hf] * (1.0f / FH), var = fmaxf(t[2 * hf + 1] * (1.0f / FH) - m * m, 0.f), r = 1.0f / sqrtf(var + 1e-5f);
            ZS[2 * lane + hf] = f32x2{r, -m * r};
        }
    }
    asm volatile("s_waitcnt lgkmcnt(0)\n\ts_barrier" ::: "memory");
    STAMP(3, 2);
    auto merge = [&](u32x2 A, u32x2 B) {
        auto r0 = __builtin_amdgcn_permlane16_swap(A[0], B[0], false, false); auto r1 = __builtin_amdgcn_permlane16_swap(A[1], B[1], false, false);
        return u32x4{r0[0], r1[0], r0[1], r1[1]};
    };
    char* z2b = ws + WS_Z2T + (size_t)(j * 32 + 2 * pm + wr) * 18432;
#pragma unroll
    for (int n = 0; n < 3; ++n) {
        const int nf = NF1(n);
        if (nf < 9) {
#pragma unroll
            for (int m = 0; m < 4; m += 2) {
                const f32x2 za = ZS[wr * 64 + m * 16 + r16], zb = ZS[wr * 64 + m * 16 + 16 + r16];
                f32x4 va = acc1[m][n] * za[0] + za[1], vb = acc1[m + 1][n] * zb[0] + zb[1];
                u32x4 w = merge(u32x2{cvtpk(va[0], va[1]), cvtpk(va[2], va[3])}, u32x2{cvtpk(vb[0], vb[1]), cvtpk(vb[2], vb[3])});
                const int r64 = (m + (g & 1)) * 16 + r16, kb = r64 >> 3;
                st16(z2b + (kb * 9 + nf) * 256 + (((r64 & 7) ^ (4 * (kb & 1))) * 32) + (g >> 1) * 16, w);
            }
        }
    }
    char* z1b = ws + WS_Z1 + (size_t)(pm * 16 + j) * 36864;
#pragma unroll
    for (int m = 0; m < 5; ++m) {
        const int rl = wr * 64 + (m < 4 ? m : wc) * 16 + r16; const f32x4 rr = RS[rl];
        u32x2 w[2];
#pragma unroll
        for (int n = 0; n < 2; ++n) {
            if (m == 4 && n == 1) continue;
            const int nf = m == 4 ? 8 : 2 * wc + (n ^ wr);
            f32x4 y = (m == 4 ? sacc : acc2[m & 3][n]) - *(const f32x4*)(CS + 192 + nf * 16 + 4 * g) * rr[0];
            f32x2 y0 = gelu_y2(f32x2{y[0], y[1]}, rr[1], rr[2], rr[3]), y1 = gelu_y2(f32x2{y[2], y[3]}, rr[1], rr[2], rr[3]);
            w[n] = u32x2{cvtpk(y0[0], y0[1]), cvtpk(y1[0], y1[1])};
        }
        if (m < 4) st16(z1b + (2 * (2 * wc + ((g & 1) ^ wr)) + (g >> 1)) * 2048 + rl * 16, merge(w[0], w[1]));
        else st8(z1b + (16 + (g >> 1)) * 2048 + rl * 16 + (g & 1) * 8, w[0]);
    }
    STAMP_DRAIN(); STAMP(0, 3);
}

constexpr float ATT_BIAS = 72.0f;
DI float xhalf_max(float m) { auto rr = __builtin_amdgcn_permlane32_swap(__float_as_uint(m), __float_as_uint(m), false, false); return fmaxf(__uint_as_float(rr[0]), __uint_as_float(rr[1])); }
DI float lo_bcast(float m) { auto rr = __builtin_amdgcn_permlane32_swap(__float_as_uint(m), __float_as_uint(m), false, false); return __uint_as_float(rr[0]); }
DI float bf_round(float x) { return __uint_as_float(cvtpk(x, 0.f) << 16); }
DI float max16(const f32x16& s) {
    float a = fmaxf(fmaxf(s[0], s[1]), s[2]), b = fmaxf(fmaxf(s[3], s[4]), s[5]);
    a = fmaxf(fmaxf(a, s[6]), s[7]); b = fmaxf(fmaxf(b, s[8]), s[9]); a = fmaxf(fmaxf(a, s[10]), s[11]); b = fmaxf(fmaxf(b, s[12]), s[13]);
    a = fmaxf(fmaxf(a, s[14]), s[15]); return fmaxf(a, b);
}
DI void exp_pack(const f32x16& s, bf16x8& p0, bf16x8& p1) {
    float e[16];
#pragma unroll
    for (int i = 0; i < 16; ++i) e[i] = __builtin_amdgcn_exp2f(s[i]);
    u32x4 a = {cvtpk(e[0], e[1]), cvtpk(e[2], e[3]), cvtpk(e[4], e[5]), cvtpk(e[6], e[7])};
    u32x4 b = {cvtpk(e[8], e[9]), cvtpk(e[10], e[11]), cvtpk(e[12], e[13]), cvtpk(e[14], e[15])};
    p0 = __builtin_bit_cast(bf16x8, a); p1 = __builtin_bit_cast(bf16x8, b);
}

struct KVF { bf16x8 k1, k2, v0, v1; };
DI void kv_read(KVF& f, const LAS char* tb, int lane, int k2o, int vo) {
    f.k1 = *(const LAS bf16x8*)(tb + lane * 16); f.k2 = *(const LAS bf16x8*)(tb + k2o);
    f.v0 = *(const LAS bf16x8*)(tb + 1024 + vo); f.v1 = *(const LAS bf16x8*)(tb + 2048 + vo);
}
#define k2frag(x) (x)
template <bool RD>
DI void half_step(f32x16& sx, bf16x8& px0, bf16x8& px1, f32x16& sy, const KVF kq, const bf16x8& qy1, const bf16x8& qy2,
                  f32x16& oy, const KVF kv, const bf16x8& py0, const bf16x8& py1, KVF& ld, const LAS char* tb, int lane, int k2o, int vo) {
    const f32x16 zero = {};
    unsigned a0, a1, a2, a3, b0, b1, b2, b3; float e0, e1, e2, e3;
    f32x16 t = MFMA32(kq.k1, qy1, zero); SB();
    if (RD) ld.k1 = *(const LAS bf16x8*)(tb + lane * 16);
    e0 = EX(sx[0]); e1 = EX(sx[1]); e2 = EX(sx[2]); e3 = EX(sx[3]); PIN(e0); PIN(e1); PIN(e2); PIN(e3); SB();
    sy = MFMA32(kq.k2, qy2, t); SB();
    if (RD) ld.k2 = *(const LAS bf16x8*)(tb + k2o);
    a0 = cvtpk(e0, e1); a1 = cvtpk(e2, e3); PIN(a0); PIN(a1);
    e0 = EX(sx[4]); e1 = EX(sx[5]); e2 = EX(sx[6]); e3 = EX(sx[7]); PIN(e0); PIN(e1); PIN(e2); PIN(e3); SB();
    oy = MFMA32(kv.v0, py0, oy); SB();
    if (RD) ld.v0 = *(const LAS bf16x8*)(tb + 1024 + vo);
    a2 = cvtpk(e0, e1); a3 = cvtpk(e2, e3); PIN(a2); PIN(a3);
    e0 = EX(sx[8]); e1 = EX(sx[9]); e2 = EX(sx[10]); e3 = EX(sx[11]); PIN(e0); PIN(e1); PIN(e2); PIN(e3); SB();
    oy = MFMA32(kv.v1, py1, oy); SB();
    if (RD) ld.v1 = *(const LAS bf16x8*)(tb + 2048 + vo);
    b0 = cvtpk(e0, e1); b1 = cvtpk(e2, e3); PIN(b0); PIN(b1);
    e0 = EX(sx[12]); e1 = EX(sx[13]); e2 = EX(sx[14]); e3 = EX(sx[15]);
    b2 = cvtpk(e0, e1); b3 = cvtpk(e2, e3); PIN(b2); PIN(b3); SB();
    u32x4 a = {a0, a1, a2, a3}, b = {b0, b1, b2, b3};
    px0 = __builtin_bit_cast(bf16x8, a); px1 = __builtin_bit_cast(bf16x8, b);
}
DI void att_rescale(f32x16& o, f32x16& s, float& mref, bf16x8& q2, bool& bad, int c, int h) {
    bool nf = false;
#pragma unroll
    for (int i = 0; i < 11; ++i) nf = nf || (!(fabsf(o[i]) <= 3.0e38f) && (i < 8 || h == 0));
    if (__any(nf)) { bad = true; return; }
    float lb = lo_bcast(o[10]);
    int e = __builtin_amdgcn_frexp_expf(lb);
    float d = (float)(e + (int)ATT_BIAS); d = d < 0 ? 0 : d;
    float nm = bf_round(mref + d); float dd = nm - mref; mref = nm;
    int sh = -(int)dd;
#pragma unroll
    for (int i = 0; i < 16; ++i) { o[i] = __builtin_amdgcn_ldexpf(o[i], sh); s[i] -= dd; }
    u32x4 b = __builtin_bit_cast(u32x4, q2); b[1] = h ? 0u : (__float_as_uint(-nm) >> 16); q2 = __builtin_bit_cast(bf16x8, b);
}

constexpr int K2_SLOT = 40960, K2_LDS = 3 * K2_SLOT;
struct G2F { bf16x8 a[4], b[4], sb; };
struct G2S {
    unsigned va[4], vl, vl2;
    const char* sa; const char* sl; unsigned lstep;
    unsigned m0a, m0l; int so;
    const LAS char* pa[2][2]; const LAS char* pb[2];
};
template <int KS, int GL, bool RD> DI void g2_phase(G2S& s, const G2F& f, G2F& n, unsigned ro, unsigned wo, f32x4 (&acc)[4][4], f32x4 (&sacc)[2]) {
    const LAS char* a0 = s.pa[0][KS] + ro; const LAS char* a1 = s.pa[1][KS] + ro;
    const LAS char* b0 = s.pb[0] + ro + KS * 9216; const LAS char* b1 = s.pb[1] + ro + KS * 9216;
#define MM(m, nn) acc[m][nn] = MFMA16(f.b[nn], f.a[m], acc[m][nn])
    MM(0, 0); if (RD) n.b[0] = ldsB2(b0, b1); SB();
    MM(1, 0); if (RD) n.a[0] = ldsA(a0); SB();
    MM(2, 0); if (RD) n.a[1] = ldsA(a0 + 2048); SB();
    MM(3, 0); if (RD) n.a[2] = ldsA(a1); SB();
    MM(0, 1); if (RD) n.a[3] = ldsA(a1 + 2048); SB();
    MM(1, 1); if (RD) n.b[1] = ldsB2(b0 + 256, b1 + 256); SB();
    MM(2, 1); if (RD) n.b[2] = ldsB2(b0 + 512, b1 + 512); SB();
    MM(3, 1); if (RD) n.b[3] = ldsB2(b0 + 768, b1 + 768); SB();
    MM(0, 2); if (RD) n.sb = ldsB2(b0 + s.so, b1 + s.so); SB();
    MM(1, 2); if (GL == 1) { set_m0(s.m0a + wo); glds_s<0>(s.va[0], s.sa); } if (GL == 2) { glds_s<1024>(s.vl, s.sl); } SB();
    MM(2, 2); SB();
    MM(3, 2); if (GL == 1) glds_s<1024>(s.va[1], s.sa); if (GL == 2) glds_s<2048>(s.vl, s.sl); SB();
    MM(0, 3); SB();
    MM(1, 3); if (GL == 1) glds_s<2048>(s.va[2], s.sa); if (GL == 2) glds_s<3072>(s.vl, s.sl); SB();
    MM(2, 3); SB();
    MM(3, 3); if (GL == 1) glds_s<3072>(s.va[3], s.sa); if (GL == 2) { set_m0(s.m0l + wo + 4096); glds_s<0>(s.vl2, s.sl); } SB();
    sacc[0] = MFMA16(f.sb, f.a[0], sacc[0]); if (GL == 1) { set_m0(s.m0l + wo); glds_s<0>(s.vl, s.sl); } if (GL == 2) glds_s<1024>(s.vl2, s.sl); SB();
    sacc[1] = MFMA16(f.sb, f.a[1], sacc[1]); SB();
#undef MM
    if (GL == 1) s.sa += 128;
    if (GL == 2) s.sl += s.lstep;
}
template <bool G2nd, bool G1st, int VM, bool RDN> DI void g2_iter(G2S& s, G2F& f0, G2F& f1, unsigned& cur, unsigned& nxt, unsigned& prv, f32x4 (&acc)[4][4], f32x4 (&sacc)[2]) {
    g2_phase<1, G2nd ? 2 : 0, true>(s, f0, f1, cur, prv, acc, sacc);
    wait_vm_bar<VM>(); SB();
    g2_phase<0, G1st ? 1 : 0, RDN>(s, f1, f0, nxt, cur, acc, sacc);
    unsigned t = cur; cur = nxt; nxt = prv; prv = t;
}

__global__ __launch_bounds__(512, 2) void k2_kernel(const u16* __restrict__ w_sp, const u16* __restrict__ b_sp, char* __restrict__ ws) {
    extern __shared__ __attribute__((aligned(16))) char smem[];
    int bid = blockIdx.x, xcd = bid & 7, idx = bid >> 3;
    int pm = (xcd & 1) * 8 + (idx & 7), j = (xcd >> 1) * 4 + (idx >> 3);
    const int lane = threadIdx.x & 63, wid = __builtin_amdgcn_readfirstlane(threadIdx.x >> 6);
    const unsigned lds0 = (unsigned)(uintptr_t)smem; const LAS char* lds = (const LAS char*)smem;
    constexpr int EP = 148;
    float* et = (float*)smem;
    STAMP(1, 0);
    if (wid < 4) {
        const int wr = wid >> 1, wc = wid & 1;
        G2S s;
#pragma unroll
        for (int i = 0; i < 4; ++i) { int row = 8 * (wid * 4 + i) + (lane >> 3), c = (lane & 7) ^ ((row >> 1) & 7); s.va[i] = row * (S_ * 2) + c * 16 + 4096 - i * 1024; }
        s.vl = lane * 16; s.vl2 = lane * 16 + 4096;
        s.sa = (const char*)(w_sp + (size_t)pm * 128 * S_) - 4096;
        s.sl = wid < 3 ? ws + WS_Z2T + (size_t)j * 32 * 18432 + wid * 6144 : ws + WS_KV;
        s.lstep = wid < 3 ? 18432 : 6144;
        s.m0a = lds0 + wid * 4096; s.m0l = lds0 + 16384 + wid * 6144;
        {
            int r = lane & 15, g = lane >> 4, q = r >> 2, p = lane & 3;
#pragma unroll
            for (int ks = 0; ks < 2; ++ks) {
                int o = r * 128 + (((4 * ks + g) ^ (r >> 1)) * 16) + wr * 8192;
                s.pa[0][ks] = lds + o + wc * 4096; s.pa[1][ks] = lds + o + (1 - wc) * 4096;
            }
            int ob = 16384 + g * 2304 + q * 32 + 8 * p + wc * 1024;
            s.pb[0] = lds + ob + ((g & 1) * 128); s.pb[1] = lds + ob + (((g & 1) ^ 1) * 128);
            s.so = 2048 - wc * 1024;
        }
#pragma unroll
        for (int t = 0; t < 3; ++t) {
            set_m0(s.m0a + t * K2_SLOT); glds_s<0>(s.va[0], s.sa); glds_s<1024>(s.va[1], s.sa); glds_s<2048>(s.va[2], s.sa); glds_s<3072>(s.va[3], s.sa); s.sa += 128;
            set_m0(s.m0l + t * K2_SLOT); glds_s<0>(s.vl, s.sl);
            if (t < 2) {
                glds_s<1024>(s.vl, s.sl); glds_s<2048>(s.vl, s.sl); glds_s<3072>(s.vl, s.sl);
                set_m0(s.m0l + t * K2_SLOT + 4096); glds_s<0>(s.vl2, s.sl); glds_s<1024>(s.vl2, s.sl); s.sl += s.lstep;
            }
        }
        f32x4 acc[4][4] = {}; f32x4 sacc[2] = {};
        G2F f0, f1;
        wait_vm_bar<15>();
        STAMP(1, 1);
        f0.a[0] = ldsA(s.pa[0][0]); f0.a[1] = ldsA(s.pa[0][0] + 2048); f0.a[2] = ldsA(s.pa[1][0]); f0.a[3] = ldsA(s.pa[1][0] + 2048);
        f0.b[0] = ldsB2(s.pb[0], s.pb[1]); f0.b[1] = ldsB2(s.pb[0] + 256, s.pb[1] + 256); f0.b[2] = ldsB2(s.pb[0] + 512, s.pb[1] + 512); f0.b[3] = ldsB2(s.pb[0] + 768, s.pb[1] + 768); f0.sb = ldsB2(s.pb[0] + s.so, s.pb[1] + s.so);
        unsigned cur = 0, nxt = K2_SLOT, prv = 2 * K2_SLOT;
        for (int t = 0; t < 29; ++t) g2_iter<true, true, 10, true>(s, f0, f1, cur, nxt, prv, acc, sacc);
        g2_iter<true, false, 10, true>(s, f0, f1, cur, nxt, prv, acc, sacc);
        g2_iter<false, false, 0, true>(s, f0, f1, cur, nxt, prv, acc, sacc);
        const int c = lane & 15, g = lane >> 4;
        const char* z1b = ws + WS_Z1 + (size_t)(pm * 16 + j) * 36864 + (g >> 1) * 2048 + (wr * 64 + c) * 16 + (g & 1) * 8;
        u32x2 z1r[4][4], z1s[2]; float bias[4];
#pragma unroll
        for (int m = 0; m < 4; ++m) {
            const int mo = (m ^ (2 * wc)) * 16;
            bias[m] = bf2f(b_sp[pm * 128 + wr * 64 + mo + c]);
#pragma unroll
            for (int n = 0; n < 4; ++n) z1r[m][n] = *(const u32x2*)(z1b + (wc * 4 + n) * 4096 + mo * 16);
            if (m < 2) z1s[m] = *(const u32x2*)(z1b + 8 * 4096 + mo * 16);
        }
        SB();
        g2_iter<false, false, 63, false>(s, f0, f1, cur, nxt, prv, acc, sacc);
        __builtin_amdgcn_s_setprio(2);
        STAMP(1, 2);
        auto fin = [&](f32x4 v, float b, u32x2 z) { v += b; return u32x2{cvtpk(bflo(z[0]) * v[0], bfhi(z[0]) * v[1]), cvtpk(bflo(z[1]) * v[2], bfhi(z[1]) * v[3])}; };
        auto put2 = [&](u32x2 A, u32x2 B, int rowA, int nfA, int rowB, int nfB) {
            auto r0 = __builtin_amdgcn_permlane16_swap(A[0], B[0], false, false); auto r1 = __builtin_amdgcn_permlane16_swap(A[1], B[1], false, false);
            const int row = (g & 1) ? rowB : rowA, cgl = 18 * j + 2 * ((g & 1) ? nfB : nfA) + (g >> 1);
            char* p = ws + WS_UO + ((size_t)((row >> 6) * 90 + (cgl >> 2)) * 4 + ((row >> 4) & 3)) * 1024 + (cgl & 3) * 256 + (row & 15) * 16;
            st16(p, u32x4{r0[0], r1[0], r0[1], r1[1]});
        };
#pragma unroll
        for (int m = 0; m < 4; ++m) {
            const int row = pm * 128 + wr * 64 + (m ^ (2 * wc)) * 16 + c;
#pragma unroll
            for (int n = 0; n < 4; n += 2) put2(fin(acc[m][n], bias[m], z1r[m][n]), fin(acc[m][n + 1], bias[m], z1r[m][n + 1]), row, wc * 4 + n, row, wc * 4 + n + 1);
        }
        { const int row = pm * 128 + wr * 64 + (2 * wc) * 16 + c; put2(fin(sacc[0], bias[0], z1s[0]), fin(sacc[1], bias[1], z1s[1]), row, 8, row + 16, 8); }
        STAMP_DRAIN(); STAMP(1, 3);
    } else {
        __builtin_amdgcn_s_setprio(1);
        const int c = lane & 31, h = lane >> 5, aw = wid - 4;
        const char* kvg = ws + WS_KV;
        const int row0 = blockIdx.x * 8 + aw * 2;
        bf16x8 q1[2], q2[2]; float mref[2];
        const f32x16 zero = {};
#pragma unroll
        for (int t = 0; t < 2; ++t) {
            const u16* qr = (const u16*)(ws + WS_Q) + (size_t)(row0 + t) * D_ + 18 * c;
            u32x4 a; const unsigned* qp = (const unsigned*)(qr + 8 * h);
            a[0] = qp[0]; a[1] = qp[1]; a[2] = qp[2]; a[3] = qp[3];
            q1[t] = __builtin_bit_cast(bf16x8, a);
            unsigned e = *(const unsigned*)(qr + 16);
            u32x4 b = {h ? 0u : e, 0u, 0u, 0u};
            q2[t] = __builtin_bit_cast(bf16x8, b);
        }
        f32x16 oA = zero, oB = zero;
        bool bad = false;
        const int k2o = k2slot(c), vo = (c < 19 ? lane : 63) * 16;
        const LAS char* kvl = lds + 16384 + 18432;
        asm volatile("s_waitcnt vmcnt(0)\n\ts_barrier" ::: "memory");
        KVF X, Y;
        kv_read(X, kvl, lane, k2o, vo); kv_read(Y, kvl + 3072, lane, k2o, vo);
#pragma unroll
        for (int t = 0; t < 2; ++t) {
            f32x16 s0 = MFMA32(X.k1, q1[t], zero); s0 = MFMA32(X.k2, q2[t], s0);
            f32x16 s1 = MFMA32(Y.k1, q1[t], zero); s1 = MFMA32(Y.k2, q2[t], s1);
            float m = xhalf_max(fmaxf(max16(s0), max16(s1)));
            mref[t] = bf_round(rintf(m) + ATT_BIAS);
            u32x4 b = __builtin_bit_cast(u32x4, q2[t]); b[1] = h ? 0u : (__float_as_uint(-mref[t]) >> 16); q2[t] = __builtin_bit_cast(bf16x8, b);
        }
        f32x16 sA = MFMA32(X.k1, q1[0], zero); sA = MFMA32(X.k2, q2[0], sA);
        f32x16 sB = zero;
        bf16x8 pA0, pA1, pB0 = {}, pB1 = {};
        half_step<false>(sA, pA0, pA1, sB, X, q1[1], q2[1], oB, X, pB0, pB1, Y, kvl, lane, k2o, vo);
        half_step<false>(sB, pB0, pB1, sA, Y, q1[0], q2[0], oA, X, pA0, pA1, Y, kvl, lane, k2o, vo);
        asm volatile("s_waitcnt lgkmcnt(0)\n\ts_barrier" ::: "memory");
        unsigned slot = K2_SLOT;
        for (int t = 1; t < 32; ++t) {
            const LAS char* tb = kvl + slot;
            half_step<true>(sA, pA0, pA1, sB, Y, q1[1], q2[1], oB, X, pB0, pB1, X, tb, lane, k2o, vo);
            half_step<false>(sB, pB0, pB1, sA, X, q1[0], q2[0], oA, Y, pA0, pA1, X, tb, lane, k2o, vo);
            bool chkB = (h == 0) && !(oB[10] <= 0x1p20f);
            half_step<true>(sA, pA0, pA1, sB, X, q1[1], q2[1], oB, Y, pB0, pB1, Y, tb + 3072, lane, k2o, vo);
            if (__builtin_expect(__any(chkB), 0)) att_rescale(oB, sB, mref[1], q2[1], bad, c, h);
            bool chkA = (h == 0) && !(oA[10] <= 0x1p20f);
            half_step<false>(sB, pB0, pB1, sA, Y, q1[0], q2[0], oA, X, pA0, pA1, Y, tb, lane, k2o, vo);
            if (__builtin_expect(__any(chkA), 0)) att_rescale(oA, sA, mref[0], q2[0], bad, c, h);
            asm volatile("s_waitcnt lgkmcnt(0)\n\ts_barrier" ::: "memory");
            slot = slot == 2 * K2_SLOT ? 0 : slot + K2_SLOT;
        }
        half_step<false>(sA, pA0, pA1, sB, Y, q1[1], q2[1], oB, X, pB0, pB1, X, kvl, lane, k2o, vo);
        exp_pack(sB, pB0, pB1);
        oA = MFMA32(Y.v0, pA0, oA); oA = MFMA32(Y.v1, pA1, oA);
        oB = MFMA32(Y.v0, pB0, oB); oB = MFMA32(Y.v1, pB1, oB);
        {
            bool nf = false;
#pragma unroll
            for (int i = 0; i < 11; ++i) nf = nf || ((!(fabsf(oA[i]) <= 3.0e38f) || !(fabsf(oB[i]) <= 3.0e38f)) && (i < 8 || h == 0));
            if (__any(nf)) bad = true;
        }
        f32x16 o[2] = {oA, oB};
        if (__builtin_expect(bad, 0)) {
#pragma unroll 1
            for (int t = 0; t < 2; ++t) {
                bf16x8 qa = t ? q1[1] : q1[0]; u32x4 qb = __builtin_bit_cast(u32x4, t ? q2[1] : q2[0]); qb[1] = 0;
                f32x16 oo = zero; float m = -3.0e38f;
                for (int kt = 0; kt < 64; ++kt) {
                    const char* tb = kvg + (size_t)kt * 3072;
                    bf16x8 k1 = *(const bf16x8*)(tb + lane * 16);
                    u32x4 k2v = *(const u32x4*)(tb + k2o);
                    bf16x8 v0 = *(const bf16x8*)(tb + 1024 + vo), v1 = *(const bf16x8*)(tb + 2048 + vo);
                    f32x16 s = MFMA32(k1, qa, zero); s = MFMA32(__builtin_bit_cast(bf16x8, k2v), __builtin_bit_cast(bf16x8, qb), s);
                    float nm = fmaxf(m, xhalf_max(max16(s)));
                    float sc = __builtin_amdgcn_exp2f(m - nm); m = nm;
#pragma unroll
                    for (int i = 0; i < 16; ++i) { oo[i] *= sc; s[i] -= nm; }
                    bf16x8 p0, p1; exp_pack(s, p0, p1);
                    oo = MFMA32(v0, p0, oo); oo = MFMA32(v1, p1, oo);
                }
                if (t) o[1] = oo; else o[0] = oo;
            }
        }
#pragma unroll
        for (int t = 0; t < 2; ++t) {
            float inv = 1.0f / lo_bcast(o[t][10]);
            const int orow = row0 + t;
            char* ob = ws + WS_UO + ((size_t)((orow >> 6) * 90 + 72) * 4 + ((orow >> 4) & 3)) * 1024 + (orow & 15) * 16;
            unsigned w0 = cvtpk(o[t][0] * inv, o[t][1] * inv), w1 = cvtpk(o[t][2] * inv, o[t][3] * inv);
            unsigned w2 = cvtpk(o[t][4] * inv, o[t][5] * inv), w3 = cvtpk(o[t][6] * inv, o[t][7] * inv);
            unsigned w4 = cvtpk(o[t][8] * inv, o[t][9] * inv);
            auto put = [&](int d, unsigned w) { int kk = 18 * c + d; *(unsigned*)(ob + (kk >> 5) * 4096 + ((kk >> 3) & 3) * 256 + (kk & 7) * 2) = w; };
            put(4 * h, w0); put(4 * h + 2, w1); put(8 + 4 * h, w2); put(10 + 4 * h, w3);
            if (h == 0) put(16, w4);
        }
    }
}

constexpr int K3_SLOT = 8704, K3_NS = 4, K3_GW = K3_NS * K3_SLOT, K3_LDS = 4 * K3_GW + 64;
DI void k3_issue(unsigned wm0, unsigned va, const char* sa, const char* sb) {
    set_m0(wm0); glds_s<0>(va, sa); glds_s<1024>(va, sa); glds_s<2048>(va, sa); glds_s<3072>(va, sa);
    set_m0(wm0 + 4096); glds_s<0>(va, sb); glds_s<1024>(va, sb); glds_s<2048>(va, sb); glds_s<3072>(va, sb);
    asm volatile("s_mov_b32 m0, %2\n\ts_mov_b32 exec_hi, 0\n\tglobal_load_lds_dwordx4 %0, %1\n\ts_mov_b32 exec_hi, -1" ::"v"(va + 4096), "s"(sb), "s"(wm0 + 8192));
}
template <bool GL, int VM> DI void k3_step(const LAS char* rd, unsigned wm0, unsigned va, const char* sa, const char* sb, unsigned pb, f32x4 (&acc)[4][5]) {
    wait_vm<VM>();
    bf16x8 a[4], b[5];
#pragma unroll
    for (int m = 0; m < 4; ++m) a[m] = ldsA(rd + va + m * 1024);
#pragma unroll
    for (int n = 0; n < 5; ++n) b[n] = ldsB2(rd + pb + n * 32, rd + pb + n * 32 + 576);
    asm volatile("s_waitcnt lgkmcnt(0)" ::: "memory"); SB();
    if (GL) k3_issue(wm0, va, sa, sb);
    SB();
#pragma unroll
    for (int n = 0; n < 5; ++n)
#pragma unroll
        for (int m = 0; m < 4; ++m) acc[m][n] = MFMA16(b[n], a[m], acc[m][n]);
    SB();
}
__global__ __launch_bounds__(256, 1) void k3_kernel(const u16* __restrict__ x, char* __restrict__ ws, u16* __restrict__ out) {
    extern __shared__ __attribute__((aligned(16))) char smem[];
    const int bid = blockIdx.x, xcd = bid & 7, idx = bid >> 3;
    const int rb = (xcd & 3) * 8 + (idx & 7), j = (xcd >> 2) * 4 + (idx >> 3);
    const int lane = threadIdx.x & 63, wid = __builtin_amdgcn_readfirstlane(threadIdx.x >> 6);
    const unsigned lds0 = (unsigned)(uintptr_t)smem;
    const unsigned va = lane * 16;
    constexpr int EP = 84;
    STAMP(2, 0);
    const LAS char* lds = (const LAS char*)smem + wid * K3_GW;
    const char* ua = ws + WS_UO + (size_t)rb * 90 * 4096 + wid * 4096;
    const char* wb = ws + WS_W3T + (size_t)j * 90 * 4608 + wid * 4608;
    const unsigned wm = lds0 + wid * K3_GW;
#pragma unroll
    for (int i = 0; i < 4; ++i) k3_issue(wm + i * K3_SLOT, va, ua + i * 16384, wb + i * 18432);
    const int g = lane >> 4, q = (lane & 15) >> 2, p = lane & 3;
    const unsigned pb = 4096 + (8 * g + q) * 144 + 8 * p;
    const int n = wid < 2 ? 23 : 22;
    f32x4 acc[4][5] = {};
    STAMP(2, 1);
    unsigned slot = 0;
#define K3_ROT() slot = slot == 3 * K3_SLOT ? 0 : slot + K3_SLOT
    for (int i = 4; i < n; ++i) { k3_step<true, 27>(lds + slot, wm + slot, va, ua + i * 16384, wb + i * 18432, pb, acc); K3_ROT(); }
    u32x4 xv[3];
#pragma unroll
    for (int i = 0; i < 3; ++i) { int ci = threadIdx.x + 256 * i; if (ci >= 576) ci = 575; xv[i] = *(const u32x4*)(x + (size_t)(rb * 64 + ci / 9) * D_ + 72 * j + (ci % 9) * 8); }
    SB();
    k3_step<false, 27>(lds + slot, 0, va, ua, ua, pb, acc); K3_ROT();
    k3_step<false, 18>(lds + slot, 0, va, ua, ua, pb, acc); K3_ROT();
    k3_step<false, 9>(lds + slot, 0, va, ua, ua, pb, acc); K3_ROT();
    k3_step<false, 0>(lds + slot, 0, va, ua, ua, pb, acc);
    STAMP(2, 2);
    float* et = (float*)(smem + wid * K3_GW);
#pragma unroll
    for (int m = 0; m < 4; ++m)
#pragma unroll
        for (int nn = 0; nn < 5; ++nn) *(f32x4*)(et + (m * 16 + (lane & 15)) * EP + nn * 16 + 4 * g) = acc[m][nn];
    asm volatile("s_waitcnt lgkmcnt(0)\n\ts_barrier" ::: "memory");
#pragma unroll
    for (int i = 0; i < 3; ++i) {
        int ci = threadIdx.x + 256 * i;
        if (ci < 576) {
            int row = ci / 9, ch = ci % 9;
            f32x4 s0 = {bflo(xv[i][0]), bfhi(xv[i][0]), bflo(xv[i][1]), bfhi(xv[i][1])}, s1 = {bflo(xv[i][2]), bfhi(xv[i][2]), bflo(xv[i][3]), bfhi(xv[i][3])};
#pragma unroll
            for (int w = 0; w < 4; ++w) {
                const float* e = (const float*)(smem + w * K3_GW) + row * EP + ch * 8;
                s0 += *(const f32x4*)e; s1 += *(const f32x4*)(e + 4);
            }
            u32x4 o = {cvtpk(s0[0], s0[1]), cvtpk(s0[2], s0[3]), cvtpk(s1[0], s1[1]), cvtpk(s1[2], s1[3])};
            *(u32x4*)(out + (size_t)(rb * 64 + row) * D_ + 72 * j + ch * 8) = o;
        }
    }
    STAMP_DRAIN(); STAMP(2, 3);
}

extern "C" void kernel_launch(void* const* d_in, const int* in_sizes, int n_in, void* d_out, int out_size, void* d_ws, size_t ws_size, hipStream_t stream) {
    const u16* x = (const u16*)d_in[0]; const u16* wq = (const u16*)d_in[1]; const u16* wk = (const u16*)d_in[2]; const u16* wv = (const u16*)d_in[3];
    const u16* wo = (const u16*)d_in[4]; const u16* w_in = (const u16*)d_in[6]; const u16* w_sp = (const u16*)d_in[7]; const u16* b_sp = (const u16*)d_in[8];
    const u16* w_out = (const u16*)d_in[9]; char* ws = (char*)d_ws;
    (void)hipFuncSetAttribute((const void*)k1_kernel, hipFuncAttributeMaxDynamicSharedMemorySize, K1_LDS);
    (void)hipFuncSetAttribute((const void*)k2_kernel, hipFuncAttributeMaxDynamicSharedMemorySize, K2_LDS);
    (void)hipFuncSetAttribute((const void*)k3_kernel, hipFuncAttributeMaxDynamicSharedMemorySize, K3_LDS);
    k1_kernel<<<256, 512, K1_LDS, stream>>>(x, wq, wk, wv, w_in, w_sp, w_out, wo, ws);
    k2_kernel<<<256, 512, K2_LDS, stream>>>(w_sp, b_sp, ws);
    k3_kernel<<<256, 256, K3_LDS, stream>>>(x, ws, (u16*)d_out);
}
```

```cpp
#include <hip/hip_runtime.h>
#include <hip/hip_bf16.h>
#include <stdint.h>

typedef unsigned short u16;
typedef short bf16x8 __attribute__((ext_vector_type(8)));
typedef short s16x4 __attribute__((ext_vector_type(4)));
typedef float f32x2 __attribute__((ext_vector_type(2)));
typedef float f32x4 __attribute__((ext_vector_type(4)));
typedef float f32x16 __attribute__((ext_vector_type(16)));
typedef unsigned u32x4 __attribute__((ext_vector_type(4)));
typedef unsigned u32x2 __attribute__((ext_vector_type(2)));
typedef __bf16 bf16x2_t __attribute__((ext_vector_type(2)));
#define LAS __attribute__((address_space(3)))
#define DI __device__ __forceinline__

constexpr int S_ = 2048, D_ = 576, FH = 2304, F_ = 4608;

constexpr size_t WS_Q = 0;
constexpr size_t WS_KV = WS_Q + (size_t)S_ * D_ * 2;
constexpr size_t WS_Z1 = WS_KV + 64 * 3072;
constexpr size_t WS_Z2T = WS_Z1 + (size_t)S_ * FH * 2;
constexpr size_t WS_UO = WS_Z2T + (size_t)S_ * FH * 2;
constexpr size_t WS_ST = WS_UO + (size_t)S_ * 2880 * 2;
constexpr size_t WS_XCH = WS_ST + (size_t)S_ * 16 * 8;
constexpr size_t WS_W3T = WS_XCH + 4096 + 16384;
constexpr size_t WS_DBG = 128u << 20;
#define STAMP(kid, i)
#define STAMP_DRAIN()
#define STAMPT(tid, kid, i)

DI unsigned cvtpk(float lo, float hi) { f32x2 v = {lo, hi}; return __builtin_bit_cast(unsigned, __builtin_convertvector(v, bf16x2_t)); }
DI float bf2f(u16 b) { return __uint_as_float(((unsigned)b) << 16); }
DI float bflo(unsigned w) { return __uint_as_float(w << 16); }
DI float bfhi(unsigned w) { return __uint_as_float(w & 0xffff0000u); }
#define SB() __builtin_amdgcn_sched_barrier(0)
#define PIN(x) asm volatile("" : "+v"(x))
#define EX(x) __builtin_amdgcn_exp2f(x)
#define MFMA16(a, b, c) __builtin_amdgcn_mfma_f32_16x16x32_bf16(a, b, c, 0, 0, 0)
#define MFMA32(a, b, c) __builtin_amdgcn_mfma_f32_32x32x16_bf16(a, b, c, 0, 0, 0)

DI s16x4 trread(const LAS char* p) { return __builtin_bit_cast(s16x4, __builtin_amdgcn_ds_read_tr16_b64_v4i16((LAS s16x4*)p)); }
template <int N> DI void wait_vm_bar() { asm volatile("s_waitcnt vmcnt(%0) lgkmcnt(0)\n\ts_barrier" ::"n"(N) : "memory"); }
template <int N> DI void wait_vm() { asm volatile("s_waitcnt vmcnt(%0)" ::"n"(N) : "memory"); }
template <int OFF> DI void glds_s(unsigned voff, const char* sbase) { asm volatile("global_load_lds_dwordx4 %0, %1 offset:%2" ::"v"(voff), "s"(sbase), "n"(OFF)); }
template <int OFF> DI void glds_sc1(unsigned voff, const char* sbase) { asm volatile("global_load_lds_dwordx4 %0, %1 offset:%2 sc1" ::"v"(voff), "s"(sbase), "n"(OFF)); }
DI void glds_v(const char* p, unsigned m0v) { asm volatile("s_mov_b32 m0, %1\n\ts_nop 0\n\tglobal_load_lds_dwordx4 %0, off" ::"v"(p), "s"(m0v)); }
DI unsigned ld_sc1_async(const char* sb, unsigned vo) { unsigned v; asm volatile("global_load_dword %0, %1, %2 sc1" : "=v"(v) : "v"(vo), "s"(sb) : "memory"); return v; }
#define WT
DI void st16(void* p, u32x4 v) { asm volatile("global_store_dwordx4 %0, %1, off sc1\n\ts_nop 1" :: "v"(p), "v"(v) : "memory"); }
DI void st8(void* p, u32x2 v) { asm volatile("global_store_dwordx2 %0, %1, off sc1" :: "v"(p), "v"(v) : "memory"); }
DI void set_m0(unsigned v) { asm volatile("s_mov_b32 m0, %0\n\ts_nop 0" ::"s"(v)); }
DI bf16x8 ldsA(const LAS char* p) { return *(const LAS bf16x8*)p; }
DI bf16x8 ldsB2(const LAS char* p0, const LAS char* p1) { s16x4 lo = trread(p0), hi = trread(p1); return __builtin_shufflevector(lo, hi, 0, 1, 2, 3, 4, 5, 6, 7); }
DI bf16x8 ldsB(const LAS char* p) { return ldsB2(p, p + 256); }

DI float gelu_t(float x) {
    const float c0 = -2.0f * 1.4426950408889634f * 0.7978845608028654f;
    const float c1 = c0 * 0.044715f;
    float x2 = x * x;
    float t = __builtin_fmaf(x2, c1, c0);
    float e = __builtin_amdgcn_exp2f(x * t);
    return x * __builtin_amdgcn_rcpf(1.0f + e);
}
DI int k2slot(int c) { return c < 13 ? 1024 + (19 + c) * 16 : (c < 25 ? 1024 + (51 + c - 13) * 16 : 2048 + (19 + c - 25) * 16); }
constexpr float QSCALE = 0.23570226039551584f * 1.4426950408889634f;

#define KO1 (3 * (((idx & 7) + (idx >> 3)) % 3))
#define PLSLOT 0
constexpr int K1_SA = 16384, K1_SB = 24576, K1_B0 = 3 * K1_SA, K1_TAB = K1_B0 + 3 * K1_SB, K1_LDS = K1_TAB + 10240;
typedef __attribute__((address_space(1))) unsigned gu32;
typedef __attribute__((address_space(1))) unsigned long long gu64;
#define RLX_AGENT __ATOMIC_RELAXED, __HIP_MEMORY_SCOPE_AGENT
struct K1F { bf16x8 a[4], b[3], x; };
struct K1S {
    unsigned va[2], vb[2]; const char* sa; const char* sb;
    const char* gp; unsigned st;
    int k1, k2;
    unsigned m0a, m0b;
    const LAS char* pa[2]; const LAS char* px[2]; const LAS char* pb[3];
};
DI void k1_issue(K1S& s, int i, int w) {
    if (i == 0) { set_m0(s.m0a + w * K1_SA); glds_s<0>(s.va[0], s.sa); }
    if (i == 1) { glds_s<1024>(s.va[1], s.sa); s.sa += s.k1 == 8 ? -8 * 128 : 128; asm volatile("" : "+s"(s.sa)); }
    if (i == 2) { glds_v(s.gp + (unsigned long long)s.st * (unsigned)s.k1, s.m0b + w * K1_SB + 2048); s.k1 = s.k1 == 8 ? 0 : s.k1 + 1; asm volatile("" : "+s"(s.k1)); }
    if (i == 3) { set_m0(s.m0b + w * K1_SB); glds_s<0>(s.vb[0], s.sb); }
    if (i == 4) { glds_s<1024>(s.vb[1], s.sb); s.sb += s.k2 == 8 ? -8 * 128 * F_ : 128 * F_; s.k2 = s.k2 == 8 ? 0 : s.k2 + 1; asm volatile("" : "+s"(s.sb), "+s"(s.k2)); }
}
DI f32x2 gelu_y2(f32x2 y, float k0, float k1, float ir) {
    f32x2 t = (y * y) * k1 + k0;
    f32x2 a = y * t;
    f32x2 e = {__builtin_amdgcn_exp2f(a[0]), __builtin_amdgcn_exp2f(a[1])};
    f32x2 d = e * ir + ir;
    f32x2 rc = {__builtin_amdgcn_rcpf(d[0]), __builtin_amdgcn_rcpf(d[1])};
    return y * rc;
}
DI float dpp_x1(float x) { return __int_as_float(__builtin_amdgcn_mov_dpp(__float_as_int(x), 0xB1, 0xf, 0xf, true)); }
DI float dpp_x2(float x) { return __int_as_float(__builtin_amdgcn_mov_dpp(__float_as_int(x), 0x4E, 0xf, 0xf, true)); }
DI void kv_store(char* kv, int sv, int j, int c, int g, f32x4 v) {
    const int key = sv * 16 + c;
    if (j == 4) {
        *(u32x2*)(kv + ((g >> 1) * 32 + key) * 16 + (g & 1) * 8) = u32x2{cvtpk(v[0], v[1]), cvtpk(v[2], v[3])};
        if (g == 1) { unsigned* k2p = (unsigned*)(kv + k2slot(key)); k2p[1] = 0x3f80u; *(u32x2*)(k2p + 2) = u32x2{0u, 0u}; }
        if (g == 2 && c < 2) *(u32x4*)(kv + 1024 + sv * 1024 + (c * 32 + 18) * 16) = u32x4{0x3f803f80u, 0x3f803f80u, 0x3f803f80u, 0x3f803f80u};
        if (g == 3 && c == 0) *(u32x4*)(kv + 1024 + sv * 1024 + 63 * 16) = u32x4{0u, 0u, 0u, 0u};
        return;
    }
    if (j == 5 && g == 1) *(unsigned*)(kv + k2slot(key)) = cvtpk(v[2], v[3]);
    const bool o1 = c & 1, o2 = c & 2;
    float t0 = dpp_x1(o1 ? v[0] : v[1]), t1 = dpp_x1(o1 ? v[2] : v[3]);
    if (o1) { v[0] = t0; v[2] = t1; } else { v[1] = t0; v[3] = t1; }
    t0 = dpp_x2(o2 ? v[0] : v[2]); t1 = dpp_x2(o2 ? v[1] : v[3]);
    if (o2) { v[0] = t0; v[1] = t1; } else { v[2] = t0; v[3] = t1; }
    int d = j == 5 ? 4 * (g - 2) + (c & 3) : (g < 2 ? 8 + 4 * g + (c & 3) : 14 + (c & 3));
    bool ok = j == 5 ? g >= 2 : (g < 2 || (g == 3 && (c & 3) >= 2));
    const int kq = c >> 2;
    if (ok) *(u32x2*)(kv + 1024 + sv * 1024 + ((kq & 1) * 32 + d) * 16 + (kq >> 1) * 8) = u32x2{cvtpk(v[0], v[1]), cvtpk(v[2], v[3])};
}
template <int KS, int GL, int RS, int WS, bool RX> DI void k1_p1(K1S& s, const K1F& f, K1F& n, f32x4 (&acc)[4][2], f32x4& sacc, f32x4 (&cso)[2], f32x4& rs, f32x4& rq, const bf16x8& ones) {
    constexpr int AO = RS * K1_SA, BO = RS * K1_SB + KS * 12288;
#define MX(d, a_, b_) d = MFMA16(a_, b_, d); PIN(d); SB()
#define MM(m, nn) MX(acc[m][nn], f.b[nn], f.a[m])
    MM(0, 0); n.b[0] = ldsB(s.pb[0] + BO); SB();
    MM(1, 0); n.a[0] = ldsA(s.pa[KS] + AO); SB();
    MM(2, 0); n.a[1] = ldsA(s.pa[KS] + AO + 2048); SB();
    MM(3, 0); n.a[2] = ldsA(s.pa[KS] + AO + 4096); SB();
    MX(cso[0], f.b[0], ones); n.a[3] = ldsA(s.pa[KS] + AO + 6144); SB();
    MX(sacc, f.b[2], f.x); n.b[1] = ldsB(s.pb[1] + BO); SB();
    MX(cso[1], f.b[2], ones); n.b[2] = ldsB(s.pb[2] + BO); SB();
    MX(rs, ones, f.x); k1_issue(s, GL == 1 ? 0 : 3, WS); SB();
    MX(rq, f.x, f.x); if (RX) n.x = ldsA(s.px[KS] + AO); SB();
    MM(0, 1); k1_issue(s, GL == 1 ? 1 : 4, WS); SB();
    MM(1, 1); if (GL == 1) k1_issue(s, 2, WS); SB();
    MM(2, 1);
    MM(3, 1);
}
template <int KS, int GL, int RS, int WS, bool RD, class E> DI void k1_p2(K1S& s, const K1F& f, K1F& n, f32x4 (&acc)[4][3], f32x4 (&cso)[2], const bf16x8& ones, E epi) {
    constexpr int AO = RS * K1_SA, BO = RS * K1_SB + KS * 12288;
    MM(0, 0); if (RD) n.b[0] = ldsB(s.pb[0] + BO); epi(0); SB();
    MM(1, 0); if (RD) n.a[0] = ldsA(s.pa[KS] + AO); epi(1); SB();
    MM(2, 0); if (RD) n.a[1] = ldsA(s.pa[KS] + AO + 2048); epi(2); SB();
    MM(3, 0); if (RD) n.a[2] = ldsA(s.pa[KS] + AO + 4096); epi(3); SB();
    MX(cso[0], f.b[0], ones); if (RD) n.a[3] = ldsA(s.pa[KS] + AO + 6144); epi(4); SB();
    MM(0, 1); if (RD) n.b[1] = ldsB(s.pb[1] + BO); epi(5); SB();
    MM(1, 1); if (RD) n.b[2] = ldsB(s.pb[2] + BO); epi(6); SB();
    MM(2, 1); if (GL) k1_issue(s, GL == 1 ? 0 : 3, WS); epi(7); SB();
    MM(3, 1); epi(8); SB();
    MX(cso[1], f.b[1], ones); if (GL) k1_issue(s, GL == 1 ? 1 : 4, WS); epi(9); SB();
    MM(0, 2); epi(10); SB();
    MM(1, 2); if (GL == 1) k1_issue(s, 2, WS); epi(11); SB();
    MM(2, 2); epi(12); SB();
    MM(3, 2); epi(13); SB();
#undef MM
#undef MX
}

__global__ __launch_bounds__(512, 2) void k1_kernel(const u16* __restrict__ x, const u16* __restrict__ wq, const u16* __restrict__ wk, const u16* __restrict__ wv,
                                                    const u16* __restrict__ w_in, const u16* __restrict__ w_sp, const u16* __restrict__ w_out, const u16* __restrict__ wo, char* __restrict__ ws) {
    extern __shared__ __attribute__((aligned(16))) char smem[];
    const int bid = blockIdx.x, xcd = bid & 7, idx = bid >> 3;
    const int pm = (xcd & 1) * 8 + (idx & 7), j = (xcd >> 1) * 4 + (idx >> 3);
    const int lane = threadIdx.x & 63, wid = __builtin_amdgcn_readfirstlane(threadIdx.x >> 6), wr = wid >> 2, wc = wid & 3;
    const unsigned lds0 = (unsigned)(uintptr_t)smem;
    const LAS char* lds = (const LAS char*)smem;
    STAMP(0, 0);
    K1S s; s.m0a = lds0 + wid * 2048; s.m0b = lds0 + K1_B0 + wid * 3072;
    const int brow = (lane >> 2) & 7, bc = (lane & 3) ^ (2 * (wid & 1)), bk = 8 * wid + brow;
    const int ko2 = ((idx & 7) + 2 * (idx >> 3)) % 9, ko1 = KO1;
    const char* sa0 = (const char*)(x + (size_t)pm * 128 * D_);
    const char* sb0 = (const char*)(w_in + 144 * j) - 1024;
    {
        int r8 = lane >> 3;
        s.va[0] = (16 * wid + r8) * (D_ * 2) + (((lane & 7) ^ (r8 >> 1)) * 16);
        s.va[1] = (16 * wid + 8 + r8) * (D_ * 2) + (((lane & 7) ^ (4 + (r8 >> 1))) * 16) - 1024;
        s.k1 = s.k2 = ko1;
        s.sa = sa0 + ko1 * 128;
        s.vb[0] = bk * (F_ * 2) + (lane >> 5) * 64 + bc * 16 + 1024; s.vb[1] = s.vb[0] + 128 - 1024;
        s.sb = sb0 + FH * 2 + (size_t)ko1 * 128 * F_;
        s.gp = (const char*)(w_in + (size_t)bk * F_ + FH + 144 * j + 128 + 8 * (bc & 1)); s.st = 128 * F_;
    }
#pragma unroll
    for (int i = 0; i < 5; ++i) k1_issue(s, i, 0);
    gu32* XN = (gu32*)(ws + WS_XCH) + pm * 16;
    gu32* XE = (gu32*)(ws + WS_XCH + 4096) + pm * 256;
    gu64* XP = (gu64*)(ws + WS_ST) + (size_t)pm * 16 * 128;
    unsigned eo = ld_sc1_async(ws + WS_XCH + 4096 + (pm * 256 + j * 16) * 4, (lane & 15) * 4);
    {
        const char* pc; unsigned len;
        asm volatile("s_getpc_b64 %0\n.Lk1_here:\n\ts_mov_b32 %1, .Lk3_end-.Lk1_here" : "=s"(pc), "=s"(len));
        unsigned off = ((idx * 8 + wid) * 2 + (lane & 1)) * 128; off = off < len ? off : len;
        asm volatile("s_mov_b32 m0, %2\n\ts_nop 0\n\tglobal_load_lds_dword %0, %1" ::"v"(off), "s"(pc), "s"(lds0 + K1_TAB + 9216 + 512));
    }
#pragma unroll
    for (int t = 1; t < 3; ++t)
#pragma unroll
        for (int i = 0; i < 5; ++i) if (t < 2 || i < 3) k1_issue(s, i, t);
    unsigned nonce = 0;
    u32x4 w3v[2]; unsigned w3o[2];
#pragma unroll
    for (int i = 0; i < 2; ++i) {
        int ln = (threadIdx.x >> 3) + 64 * i; ln = bid * 102 + (ln < 102 ? ln : 101); ln = ln < 25920 ? ln : 25919;
        int gi = ln * 8 + (threadIdx.x & 7), kk = gi / 9, c9 = gi - kk * 9, jj = kk / 2880, k = kk - jj * 2880;
        const u16* p = (k < FH ? w_out + (size_t)k * D_ : wo + (size_t)(k - FH) * D_) + jj * 72 + c9 * 8;
        asm volatile("global_load_dwordx4 %0, %1, off" : "=v"(w3v[i]) : "v"(p) : "memory");
        w3o[i] = gi * 16; PIN(w3o[i]);
    }
    const char* gp2; unsigned st2;
    {
        const u16* p; int rs;
        const u16* sp0 = j < 4 ? wq + 512 + 16 * j : (j == 5 ? wk + 10 : (j == 6 ? wv + 8 : wk));
        const u16* sp1 = j < 4 ? wq + 520 + 16 * j : (j == 5 ? wv : (j == 6 ? wv + 10 : wk + 8));
        const int srs = j < 4 ? D_ : 18;
        if (lane >= 32) { p = wq + 32 * j + 8 * bc; rs = D_; }
        else if (bc < 2) { p = w_in + 144 * j + 128 + 8 * bc; rs = F_; }
        else { p = bc == 2 ? sp0 : sp1; rs = srs; }
        gp2 = (const char*)(p + (size_t)bk * rs); st2 = 128 * rs; PIN(gp2); PIN(st2);
    }
    const int r16 = lane & 15, g = lane >> 4, cg = wc ^ (2 * wr);
    const int nf3 = cg == 3 ? 8 : (cg == 2 ? 9 : 10 + cg);
#define NF1(n) ((n) == 2 ? 8 : 2 * wc + ((n) ^ wr))
#define NF2(n) ((n) == 0 ? nf3 : 2 * cg + (((n) - 1) ^ wr))
    const LAS char* pb2[3];
    {
        int q = r16 >> 2, p = lane & 3;
#pragma unroll
        for (int ks = 0; ks < 2; ++ks) { s.pa[ks] = lds + (r16 * 128 + (((4 * ks + g) ^ (r16 >> 1)) * 16) + wr * 8192); s.px[ks] = s.pa[ks] + wc * 2048; PIN(s.pa[ks]); PIN(s.px[ks]); }
#pragma unroll
        for (int n = 0; n < 3; ++n) {
            int nf = NF1(n); s.pb[n] = lds + (K1_B0 + g * 3072 + q * 64 + 8 * p + (nf >> 1) * 512 + (((nf ^ g) & 1) * 32)); PIN(s.pb[n]);
            nf = NF2(n); pb2[n] = lds + (K1_B0 + g * 3072 + q * 64 + 8 * p + (nf >> 1) * 512 + (((nf ^ g) & 1) * 32)); PIN(pb2[n]);
        }
    }
    f32x4 acc1[4][2] = {}; f32x4 sacc = {}; f32x4 cso[2] = {};
    f32x4 rs = {}, rq = {};
#pragma unroll
    for (int m = 0; m < 4; ++m) { PIN(acc1[m][0]); PIN(acc1[m][1]); }
    PIN(sacc); PIN(cso[0]); PIN(cso[1]); PIN(rs); PIN(rq);
    const u32x4 onesv = {0x3f803f80u, 0x3f803f80u, 0x3f803f80u, 0x3f803f80u}; bf16x8 ones = __builtin_bit_cast(bf16x8, onesv); PIN(ones);
    K1F f0, f1;
    SB();
    wait_vm_bar<10>();
    SB();
    f0.b[0] = ldsB(s.pb[0]);
    f0.a[0] = ldsA(s.pa[0]); f0.a[1] = ldsA(s.pa[0] + 2048); f0.a[2] = ldsA(s.pa[0] + 4096); f0.a[3] = ldsA(s.pa[0] + 6144);
    f0.b[2] = ldsB(s.pb[2]); f0.x = ldsA(s.px[0]); f0.b[1] = ldsB(s.pb[1]);
    SB();
    PIN(eo);
    if (wid == 7) {
        nonce = 1;
        while (__ballot(eo == nonce) != 0ull) ++nonce;
        if (lane == 0) __hip_atomic_store(XN + j, nonce, RLX_AGENT);
        __hip_atomic_store(XP + j * 128 + lane, ~0ull, RLX_AGENT); __hip_atomic_store(XP + j * 128 + 64 + lane, ~0ull, RLX_AGENT);
    }
    STAMP(0, 1);
#define P1_A(c) k1_p1<1, 2, (c), ((c) + 2) % 3, true>(s, f0, f1, acc1, sacc, cso, rs, rq, ones); wait_vm_bar<5>(); SB()
#define P1_B(c, RX) k1_p1<0, 1, ((c) + 1) % 3, (c), RX>(s, f1, f0, acc1, sacc, cso, rs, rq, ones)
#pragma unroll 1
    for (int it = 0; it < 2; ++it) { P1_A(0); P1_B(0, true); P1_A(1); P1_B(1, true); P1_A(2); P1_B(2, true); }
    const char* xn_s = ws + WS_XCH + pm * 64; const char* xe_s = ws + WS_XCH + 4096 + (pm * 256 + j * 16) * 4; const unsigned l4 = (lane & 15) * 4;
    P1_A(0);
    unsigned echoed = ld_sc1_async(xn_s, l4);
    s.k1 = s.k2 = ko2; s.sa = sa0 + ko2 * 128; s.sb = sb0 + (size_t)ko2 * 128 * F_; s.gp = gp2; s.st = st2; SB();
    P1_B(0, true);
    P1_A(1);
    PIN(echoed);
    if (wid == 7 && lane < 16) __hip_atomic_store(XE + lane * 16 + j, echoed, RLX_AGENT);
    SB();
    P1_B(1, true);
    P1_A(2);
    s.pb[0] = pb2[0]; s.pb[1] = pb2[1]; s.pb[2] = pb2[2];
    P1_B(2, false);
    STAMP(3, 0);
    float* CS = (float*)(smem + K1_TAB);
    f32x4* RS = (f32x4*)(smem + K1_TAB + 2048);
    float* red = (float*)(smem + K1_TAB + 4096);
    f32x2* ZS = (f32x2*)(smem + K1_TAB + 9216);
    volatile LAS unsigned* HS = (volatile LAS unsigned*)(lds + K1_TAB + 1792);
    const float c0 = -2.0f * 1.4426950408889634f * 0.7978845608028654f, c1 = c0 * 0.044715f;
    f32x4 acc2[4][3] = {}; f32x4 cs2[2] = {};
    auto none = [](int) {};
    auto tabs = [&](int st) {
        if (st == 1) { PIN(w3v[0]); PIN(w3v[1]); st16(ws + WS_W3T + w3o[0], w3v[0]); st16(ws + WS_W3T + w3o[1], w3v[1]); }
        if (st == 2) { if (r16 == 0) { *(f32x4*)(CS + NF1(0) * 16 + 4 * g) = cso[0]; *(f32x4*)(CS + 128 + 4 * g) = cso[1]; } }
        if (st == 3) {
            float sq = (r16 & 2) ? ((r16 & 1) ? rq[3] : rq[2]) : ((r16 & 1) ? rq[1] : rq[0]);
            float mean = rs[0] * (1.0f / D_), var = fmaxf(sq * (1.0f / D_) - mean * mean, 0.f);
            rs[0] = mean; rs[1] = var + 1e-5f; PIN(rs);
        }
        if (st == 5) { rs[1] = sqrtf(rs[1]); PIN(rs); }
        if (st == 7) { rs[2] = 1.0f / rs[1]; PIN(rs); }
        if (st == 9) { float rstd = rs[2]; if (g == (r16 >> 2)) RS[wr * 64 + wc * 16 + r16] = f32x4{rs[0], c0 * rstd, c1 * rstd * rstd * rstd, rs[1]}; }
    };
    f32x4 er[5], cs1[3], e[3];
    f32x2 su[5], sq[5];
#define ZU(u) ((u) == 8 ? sacc : acc1[((u) >> 1) & 3][(u) & 1])
#define UM(u) ((u) == 8 ? 4 : (u) >> 1)
#define UN(u) ((u) == 8 ? 2 : (u) & 1)
#define PRE(i, u) { f32x4 y = ZU(u) - cs1[UN(u)] * er[UM(u)][0]; e[i] = ((y * y) * er[UM(u)][2] + er[UM(u)][1]) * y; ZU(u) = y; PIN(ZU(u)); PIN(e[i]); }
#define FIN(i, u, first) { ZU(u) *= e[i]; PIN(ZU(u)); f32x4 v = ZU(u); f32x2 lo = {v[0], v[1]}, hi = {v[2], v[3]}; \
        if (first) { su[UM(u)] = lo + hi; sq[UM(u)] = lo * lo + hi * hi; } else { su[UM(u)] += lo; su[UM(u)] += hi; sq[UM(u)] += lo * lo; sq[UM(u)] += hi * hi; } PIN(su[UM(u)]); PIN(sq[UM(u)]); }
#define RED(m) { float a_ = su[m][0] + su[m][1], b_ = sq[m][0] + sq[m][1]; \
            auto x1 = __builtin_amdgcn_permlane16_swap(__float_as_uint(a_), __float_as_uint(b_), false, false);       \
            float t_ = __uint_as_float(x1[0]) + __uint_as_float(x1[1]);                                               \
            auto x2 = __builtin_amdgcn_permlane32_swap(__float_as_uint(t_), __float_as_uint(t_), false, false); \
            t_ = __uint_as_float(x2[0]) + __uint_as_float(x2[1]);                                                     \
            if ((m) < 4) redp[(m) * 160] = t_; else reds[0] = t_; }
    float* redp = red + ((wr * 64 + r16) * 5 + wc) * 2 + (g & 1);
    float* reds = red + ((wr * 64 + wc * 16 + r16) * 5 + 4) * 2 + (g & 1);
#define T1(op, v) asm volatile(op " %0, %0" : "+v"(v))
#define DE1(v, sd) asm volatile("v_fma_f32 %0, %0, %1, %1" : "+v"(v) : "v"(sd))
#define EPI_E(u0, u1, u2) [&](int st) { \
        if (st < 12) { float t_ = e[st >> 2][st & 3]; T1("v_exp_f32", t_); e[st >> 2][st & 3] = t_; } \
        if (st >= 2) { const int v_ = st - 2; float t_ = e[v_ >> 2][v_ & 3]; DE1(t_, er[v_ < 4 ? UM(u0) : (v_ < 8 ? UM(u1) : UM(u2))][3]); e[v_ >> 2][v_ & 3] = t_; } }
#define EPI_R(m0, m1) [&](int st) { if (st < 12) { float t_ = e[st >> 2][st & 3]; T1("v_rcp_f32", t_); e[st >> 2][st & 3] = t_; } \
        if (st == 12 && (m0) >= 0) er[(m0) & 3] = RS[wr * 64 + (m0) * 16 + r16]; if (st == 13 && (m1) >= 0) er[(m1) & 3] = RS[wr * 64 + (m1) * 16 + r16]; }
#define P2_A(c, GL, E) k1_p2<1, GL ? 2 : 0, (c), ((c) + 2) % 3, true>(s, f0, f1, acc2, cs2, ones, E)
#define P2_B(c, GL, RD, E) k1_p2<0, GL ? 1 : 0, ((c) + 1) % 3, (c), RD>(s, f1, f0, acc2, cs2, ones, E)
    P2_A(0, true, tabs); wait_vm_bar<5>(); SB();
    er[0] = RS[wr * 64 + r16]; er[4] = RS[wr * 64 + wc * 16 + r16];
#pragma unroll
    for (int n = 0; n < 3; ++n) cs1[n] = *(const f32x4*)(CS + NF1(n) * 16 + 4 * g);
    PRE(0, 0) PRE(1, 1) PRE(2, 8) SB();
    P2_B(0, true, true, EPI_E(0, 1, 8));
    P2_A(1, true, EPI_R(1, 2)); wait_vm_bar<5>(); SB();
    unsigned ev = ld_sc1_async(xe_s, l4);
    FIN(0, 0, true) FIN(1, 1, false) FIN(2, 8, true) PRE(0, 2) PRE(1, 3) PRE(2, 4) RED(0) RED(4) SB();
    P2_B(1, true, true, EPI_E(2, 3, 4));
    P2_A(2, true, EPI_R(3, -1)); wait_vm_bar<5>(); SB();
    PIN(ev);
    bool hs = __all(ev == nonce);
    unsigned nn = ld_sc1_async(xn_s, l4);
    if (wid == 7) *HS = hs;
    FIN(0, 2, true) FIN(1, 3, false) FIN(2, 4, true) PRE(0, 5) PRE(1, 6) PRE(2, 7) RED(1) SB();
    P2_B(2, true, true, EPI_E(5, 6, 7));
    P2_A(0, true, EPI_R(-1, -1)); wait_vm_bar<5>(); SB();
    PIN(nn);
    FIN(0, 5, false) FIN(1, 6, true) FIN(2, 7, false) RED(2) RED(3) SB();
    auto reecho = [&](int st) { if (st == 2) { if (wid == 7 && nn != echoed) { echoed = nn; if (lane < 16) __hip_atomic_store(XE + lane * 16 + j, nn, RLX_AGENT); } } };
    P2_B(0, true, true, reecho);
    P2_A(1, true, none);
    STAMP(3, 1);
    wait_vm_bar<5>(); SB();
    f32x2 pu[5];
    auto pub = [&](int st) {
        if (st == 0) {
#pragma unroll
            for (int i = 0; i < 5; ++i) pu[i] = *(const f32x2*)(red + (wid * 16 + r16) * 10 + 2 * i);
        }
        if (st == 3) {
            float a = ((pu[0][0] + pu[1][0]) + (pu[2][0] + pu[3][0])) + pu[4][0], b = ((pu[0][1] + pu[1][1]) + (pu[2][1] + pu[3][1])) + pu[4][1];
            unsigned bb = __float_as_uint(b); if (bb == 0xffffffffu) bb = 0x7fc00000u;
            if (lane < 16) __hip_atomic_store(XP + j * 128 + wid * 16 + lane, ((unsigned long long)bb << 32) | __float_as_uint(a), RLX_AGENT);
        }
    };
    P2_B(1, true, true, pub);
    P2_A(2, true, none); wait_vm_bar<5>(); SB();
    P2_B(2, true, true, none);
    P2_A(0, true, none); wait_vm_bar<5>(); SB();
    P2_B(0, false, true, none);
    P2_A(1, false, none); wait_vm_bar<0>(); SB();
    const char* xp_s = ws + WS_ST + (size_t)pm * 16384 + wid * 128; const unsigned xp_v = g * 4096 + r16 * 8;
    u32x2 pv[4];
#define PLOAD() asm volatile("global_load_dwordx2 %0, %4, %5 sc1\n\tglobal_load_dwordx2 %1, %4, %5 offset:1024 sc1\n\tglobal_load_dwordx2 %2, %4, %5 offset:2048 sc1\n\tglobal_load_dwordx2 %3, %4, %5 offset:3072 sc1" \
        : "=&v"(pv[0]), "=&v"(pv[1]), "=&v"(pv[2]), "=&v"(pv[3]) : "v"(xp_v), "s"(xp_s) : "memory")
    auto fetch = [&](int st) {
        if (st == PLSLOT) PLOAD();
        if (st == PLSLOT + 1) {
            const char* sp = (const char*)w_sp + (size_t)(pm * 128 + (idx >> 3) * 32 + wid * 4) * (S_ * 2);
            const unsigned vo = (lane >> 4) * (S_ * 2) + (lane & 15) * 16;
            set_m0(s.m0a); glds_s<0>(vo, sp); glds_s<1024>(vo, sp - 768);
        }
    };
    unsigned hsf = *HS;
    P2_B(1, false, true, fetch);
    P2_A(2, false, none);
    P2_B(2, false, false, none);
    STAMP(0, 2);
    if (r16 == 0) { *(f32x4*)(CS + 192 + NF2(0) * 16 + 4 * g) = cs2[0]; *(f32x4*)(CS + 192 + NF2(1) * 16 + 4 * g) = cs2[1]; }
    f32x4* HX = (f32x4*)(smem + K1_B0) + wr * 256 + lane;
    if (cg == 3) { HX[0] = acc2[0][0]; HX[64] = acc2[1][0]; HX[128] = acc2[2][0]; HX[192] = acc2[3][0]; }
    wait_vm<2>();
    PIN(pv[0]); PIN(pv[1]); PIN(pv[2]); PIN(pv[3]);
#define PEMPTY() __any(pv[0][1] == 0xffffffffu || pv[1][1] == 0xffffffffu || pv[2][1] == 0xffffffffu || pv[3][1] == 0xffffffffu)
    if (__builtin_expect(!hsf || PEMPTY(), 0)) {
        const unsigned long long t0 = __builtin_amdgcn_s_memrealtime();
        if (wid == 7) {
            while (!hs) {
                ev = __hip_atomic_load(XE + j * 16 + (lane & 15), RLX_AGENT);
                asm volatile("s_waitcnt vmcnt(0)" ::: "memory");
                hs = __all(ev == nonce);
                nn = __hip_atomic_load(XN + (lane & 15), RLX_AGENT);
                if (nn != echoed) { echoed = nn; if (lane < 16) __hip_atomic_store(XE + lane * 16 + j, nn, RLX_AGENT); }
                if (__builtin_amdgcn_s_memrealtime() - t0 > 5000000ull) break;
            }
            *HS = 1;
        }
        while (*HS == 0 && __builtin_amdgcn_s_memrealtime() - t0 < 6000000ull) __builtin_amdgcn_s_sleep(2);
        do {
            PLOAD(); wait_vm<0>(); PIN(pv[0]); PIN(pv[1]); PIN(pv[2]); PIN(pv[3]);
        } while (PEMPTY() && __builtin_amdgcn_s_memrealtime() - t0 < 6000000ull);
    }
    {
        float a = (__uint_as_float(pv[0][0]) + __uint_as_float(pv[1][0])) + (__uint_as_float(pv[2][0]) + __uint_as_float(pv[3][0]));
        float b = (__uint_as_float(pv[0][1]) + __uint_as_float(pv[1][1])) + (__uint_as_float(pv[2][1]) + __uint_as_float(pv[3][1]));
        auto a1 = __builtin_amdgcn_permlane16_swap(__float_as_uint(a), __float_as_uint(a), false, false); a = __uint_as_float(a1[0]) + __uint_as_float(a1[1]);
        auto b1 = __builtin_amdgcn_permlane16_swap(__float_as_uint(b), __float_as_uint(b), false, false); b = __uint_as_float(b1[0]) + __uint_as_float(b1[1]);
        auto a2 = __builtin_amdgcn_permlane32_swap(__float_as_uint(a), __float_as_uint(a), false, false); a = __uint_as_float(a2[0]) + __uint_as_float(a2[1]);
        auto b2 = __builtin_amdgcn_permlane32_swap(__float_as_uint(b), __float_as_uint(b), false, false); b = __uint_as_float(b2[0]) + __uint_as_float(b2[1]);
        float m = a * (1.0f / FH), var = fmaxf(b * (1.0f / FH) - m * m, 0.f), r = 1.0f / sqrtf(var + 1e-5f);
        if (lane < 16) ZS[wid * 16 + lane] = f32x2{r, -m * r};
    }
    asm volatile("s_waitcnt lgkmcnt(0)\n\ts_barrier" ::: "memory");
    STAMP(3, 2);
    auto merge = [&](u32x2 A, u32x2 B) {
        auto r0 = __builtin_amdgcn_permlane16_swap(A[0], B[0], false, false); auto r1 = __builtin_amdgcn_permlane16_swap(A[1], B[1], false, false);
        return u32x4{r0[0], r1[0], r0[1], r1[1]};
    };
    auto pk4 = [&](f32x4 v) { return u32x2{cvtpk(v[0], v[1]), cvtpk(v[2], v[3])}; };
    {
        char* z2b = ws + WS_Z2T + (size_t)(j * 32 + 2 * pm + wr) * 18432;
        f32x2 zs[5];
#pragma unroll
        for (int m = 0; m < 5; ++m) zs[m] = ZS[wr * 64 + (m < 4 ? m : wc) * 16 + r16];
#pragma unroll
        for (int n = 0; n < 2; ++n)
#pragma unroll
            for (int m = 0; m < 4; m += 2) {
                u32x4 w = merge(pk4(acc1[m][n] * zs[m][0] + zs[m][1]), pk4(acc1[m + 1][n] * zs[m + 1][0] + zs[m + 1][1]));
                const int r64 = (m + (g & 1)) * 16 + r16, kb = r64 >> 3;
                st16(z2b + (kb * 9 + NF1(n)) * 256 + (((r64 & 7) ^ (4 * (kb & 1))) * 32) + (g >> 1) * 16, w);
            }
        const int r64 = wc * 16 + r16, kb = r64 >> 3;
        st8(z2b + (kb * 9 + 8) * 256 + (((r64 & 7) ^ (4 * (kb & 1))) * 32) + g * 8, pk4(sacc * zs[4][0] + zs[4][1]));
    }
    char* z1b = ws + WS_Z1 + (size_t)(pm * 16 + j) * 36864;
    auto gel = [&](f32x4 a, f32x4 c, f32x4 rr) {
        f32x4 y = a - c * rr[0];
        f32x2 y0 = gelu_y2(f32x2{y[0], y[1]}, rr[1], rr[2], rr[3]), y1 = gelu_y2(f32x2{y[2], y[3]}, rr[1], rr[2], rr[3]);
        return u32x2{cvtpk(y0[0], y0[1]), cvtpk(y1[0], y1[1])};
    };
#pragma unroll
    for (int n = 0; n < 3; ++n) cs1[n] = *(const f32x4*)(CS + 192 + NF2(n) * 16 + 4 * g);
#pragma unroll
    for (int m = 0; m < 4; ++m) er[m] = RS[wr * 64 + m * 16 + r16];
#pragma unroll
    for (int m = 0; m < 4; ++m)
        st16(z1b + (2 * (2 * cg + ((g & 1) ^ wr)) + (g >> 1)) * 2048 + (wr * 64 + m * 16 + r16) * 16, merge(gel(acc2[m][1], cs1[1], er[m]), gel(acc2[m][2], cs1[2], er[m])));
    st8(z1b + (16 + (g >> 1)) * 2048 + (wr * 64 + wc * 16 + r16) * 16 + (g & 1) * 8, gel(HX[wc * 64], *(const f32x4*)(CS + 192 + 128 + 4 * g), RS[wr * 64 + wc * 16 + r16]));
    wait_vm<10>();
    if (cg == 3) {
    } else if (nf3 >= 10 || j < 4) {
        u16* qp = (u16*)(ws + WS_Q) + (size_t)(pm * 128 + wr * 64 + r16) * D_ + (nf3 >= 10 ? 32 * j + (nf3 - 10) * 16 : 512 + 16 * j) + 4 * g;
#pragma unroll
        for (int m = 0; m < 4; ++m) *(u32x2*)(qp + m * 16 * D_) = (pk4((acc2[m][0] - cs1[0] * er[m][0]) * (er[m][1] * (QSCALE / c0))));
    } else if (j < 7) {
#pragma unroll
        for (int m = 0; m < 4; ++m)
            kv_store(ws + WS_KV + (size_t)(pm * 4 + wr * 2 + (m >> 1)) * 3072, m & 1, j, r16, g, (acc2[m][0] - cs1[0] * er[m][0]) * (er[m][1] * (1.0f / c0)));
    }
    STAMP_DRAIN(); STAMP(0, 3);
}

constexpr float ATT_BIAS = 72.0f;
DI float xhalf_max(float m) { auto rr = __builtin_amdgcn_permlane32_swap(__float_as_uint(m), __float_as_uint(m), false, false); return fmaxf(__uint_as_float(rr[0]), __uint_as_float(rr[1])); }
DI float lo_bcast(float m) { auto rr = __builtin_amdgcn_permlane32_swap(__float_as_uint(m), __float_as_uint(m), false, false); return __uint_as_float(rr[0]); }
DI float bf_round(float x) { return __uint_as_float(cvtpk(x, 0.f) << 16); }
DI float max16(const f32x16& s) {
    float a = fmaxf(fmaxf(s[0], s[1]), s[2]), b = fmaxf(fmaxf(s[3], s[4]), s[5]);
    a = fmaxf(fmaxf(a, s[6]), s[7]); b = fmaxf(fmaxf(b, s[8]), s[9]); a = fmaxf(fmaxf(a, s[10]), s[11]); b = fmaxf(fmaxf(b, s[12]), s[13]);
    a = fmaxf(fmaxf(a, s[14]), s[15]); return fmaxf(a, b);
}
DI void exp_pack(const f32x16& s, bf16x8& p0, bf16x8& p1) {
    float e[16];
#pragma unroll
    for (int i = 0; i < 16; ++i) e[i] = __builtin_amdgcn_exp2f(s[i]);
    u32x4 a = {cvtpk(e[0], e[1]), cvtpk(e[2], e[3]), cvtpk(e[4], e[5]), cvtpk(e[6], e[7])};
    u32x4 b = {cvtpk(e[8], e[9]), cvtpk(e[10], e[11]), cvtpk(e[12], e[13]), cvtpk(e[14], e[15])};
    p0 = __builtin_bit_cast(bf16x8, a); p1 = __builtin_bit_cast(bf16x8, b);
}

struct KVF { bf16x8 k1, k2, v0, v1; };
DI void kv_read(KVF& f, const LAS char* tb, int lane, int k2o, int vo) {
    f.k1 = *(const LAS bf16x8*)(tb + lane * 16); f.k2 = *(const LAS bf16x8*)(tb + k2o);
    f.v0 = *(const LAS bf16x8*)(tb + 1024 + vo); f.v1 = *(const LAS bf16x8*)(tb + 2048 + vo);
}
#define k2frag(x) (x)
template <bool RD>
DI void half_step(f32x16& sx, bf16x8& px0, bf16x8& px1, f32x16& sy, const KVF kq, const bf16x8& qy1, const bf16x8& qy2,
                  f32x16& oy, const KVF kv, const bf16x8& py0, const bf16x8& py1, KVF& ld, const LAS char* tb, int lane, int k2o, int vo) {
    const f32x16 zero = {};
    unsigned a0, a1, a2, a3, b0, b1, b2, b3; float e0, e1, e2, e3;
    f32x16 t = MFMA32(kq.k1, qy1, zero); SB();
    if (RD) ld.k1 = *(const LAS bf16x8*)(tb + lane * 16);
    e0 = EX(sx[0]); e1 = EX(sx[1]); e2 = EX(sx[2]); e3 = EX(sx[3]); PIN(e0); PIN(e1); PIN(e2); PIN(e3); SB();
    sy = MFMA32(kq.k2, qy2, t); SB();
    if (RD) ld.k2 = *(const LAS bf16x8*)(tb + k2o);
    a0 = cvtpk(e0, e1); a1 = cvtpk(e2, e3); PIN(a0); PIN(a1);
    e0 = EX(sx[4]); e1 = EX(sx[5]); e2 = EX(sx[6]); e3 = EX(sx[7]); PIN(e0); PIN(e1); PIN(e2); PIN(e3); SB();
    oy = MFMA32(kv.v0, py0, oy); SB();
    if (RD) ld.v0 = *(const LAS bf16x8*)(tb + 1024 + vo);
    a2 = cvtpk(e0, e1); a3 = cvtpk(e2, e3); PIN(a2); PIN(a3);
    e0 = EX(sx[8]); e1 = EX(sx[9]); e2 = EX(sx[10]); e3 = EX(sx[11]); PIN(e0); PIN(e1); PIN(e2); PIN(e3); SB();
    oy = MFMA32(kv.v1, py1, oy); SB();
    if (RD) ld.v1 = *(const LAS bf16x8*)(tb + 2048 + vo);
    b0 = cvtpk(e0, e1); b1 = cvtpk(e2, e3); PIN(b0); PIN(b1);
    e0 = EX(sx[12]); e1 = EX(sx[13]); e2 = EX(sx[14]); e3 = EX(sx[15]);
    b2 = cvtpk(e0, e1); b3 = cvtpk(e2, e3); PIN(b2); PIN(b3); SB();
    u32x4 a = {a0, a1, a2, a3}, b = {b0, b1, b2, b3};
    px0 = __builtin_bit_cast(bf16x8, a); px1 = __builtin_bit_cast(bf16x8, b);
}
DI void att_rescale(f32x16& o, f32x16& s, float& mref, bf16x8& q2, bool& bad, int c, int h) {
    bool nf = false;
#pragma unroll
    for (int i = 0; i < 11; ++i) nf = nf || (!(fabsf(o[i]) <= 3.0e38f) && (i < 8 || h == 0));
    if (__any(nf)) { bad = true; return; }
    float lb = lo_bcast(o[10]);
    int e = __builtin_amdgcn_frexp_expf(lb);
    float d = (float)(e + (int)ATT_BIAS); d = d < 0 ? 0 : d;
    float nm = bf_round(mref + d); float dd = nm - mref; mref = nm;
    int sh = -(int)dd;
#pragma unroll
    for (int i = 0; i < 16; ++i) { o[i] = __builtin_amdgcn_ldexpf(o[i], sh); s[i] -= dd; }
    u32x4 b = __builtin_bit_cast(u32x4, q2); b[1] = h ? 0u : (__float_as_uint(-nm) >> 16); q2 = __builtin_bit_cast(bf16x8, b);
}

constexpr int K2_SLOT = 40960, K2_LDS = 3 * K2_SLOT;
struct G2F { bf16x8 a[4], b[4], sb; };
struct G2S {
    unsigned va[4], vl, vl2;
    const char* sa; const char* sl; unsigned lstep;
    unsigned m0a, m0l; int so;
    const LAS char* pa[2][2]; const LAS char* pb[2];
};
template <int KS, int GL, bool RD> DI void g2_phase(G2S& s, const G2F& f, G2F& n, unsigned ro, unsigned wo, f32x4 (&acc)[4][4], f32x4 (&sacc)[2]) {
    const LAS char* a0 = s.pa[0][KS] + ro; const LAS char* a1 = s.pa[1][KS] + ro;
    const LAS char* b0 = s.pb[0] + ro + KS * 9216; const LAS char* b1 = s.pb[1] + ro + KS * 9216;
#define MM(m, nn) acc[m][nn] = MFMA16(f.b[nn], f.a[m], acc[m][nn])
    MM(0, 0); if (RD) n.b[0] = ldsB2(b0, b1); SB();
    MM(1, 0); if (RD) n.a[0] = ldsA(a0); SB();
    MM(2, 0); if (RD) n.a[1] = ldsA(a0 + 2048); SB();
    MM(3, 0); if (RD) n.a[2] = ldsA(a1); SB();
    MM(0, 1); if (RD) n.a[3] = ldsA(a1 + 2048); SB();
    MM(1, 1); if (RD) n.b[1] = ldsB2(b0 + 256, b1 + 256); SB();
    MM(2, 1); if (RD) n.b[2] = ldsB2(b0 + 512, b1 + 512); SB();
    MM(3, 1); if (RD) n.b[3] = ldsB2(b0 + 768, b1 + 768); SB();
    MM(0, 2); if (RD) n.sb = ldsB2(b0 + s.so, b1 + s.so); SB();
    MM(1, 2); if (GL == 1) { set_m0(s.m0a + wo); glds_s<0>(s.va[0], s.sa); } if (GL == 2) { glds_s<1024>(s.vl, s.sl); } SB();
    MM(2, 2); SB();
    MM(3, 2); if (GL == 1) glds_s<1024>(s.va[1], s.sa); if (GL == 2) glds_s<2048>(s.vl, s.sl); SB();
    MM(0, 3); SB();
    MM(1, 3); if (GL == 1) glds_s<2048>(s.va[2], s.sa); if (GL == 2) glds_s<3072>(s.vl, s.sl); SB();
    MM(2, 3); SB();
    MM(3, 3); if (GL == 1) glds_s<3072>(s.va[3], s.sa); if (GL == 2) { set_m0(s.m0l + wo + 4096); glds_s<0>(s.vl2, s.sl); } SB();
    sacc[0] = MFMA16(f.sb, f.a[0], sacc[0]); if (GL == 1) { set_m0(s.m0l + wo); glds_s<0>(s.vl, s.sl); } if (GL == 2) glds_s<1024>(s.vl2, s.sl); SB();
    sacc[1] = MFMA16(f.sb, f.a[1], sacc[1]); SB();
#undef MM
    if (GL == 1) s.sa += 128;
    if (GL == 2) s.sl += s.lstep;
}
template <bool G2nd, bool G1st, int VM, bool RDN> DI void g2_iter(G2S& s, G2F& f0, G2F& f1, unsigned& cur, unsigned& nxt, unsigned& prv, f32x4 (&acc)[4][4], f32x4 (&sacc)[2]) {
    g2_phase<1, G2nd ? 2 : 0, true>(s, f0, f1, cur, prv, acc, sacc);
    wait_vm_bar<VM>(); SB();
    g2_phase<0, G1st ? 1 : 0, RDN>(s, f1, f0, nxt, cur, acc, sacc);
    unsigned t = cur; cur = nxt; nxt = prv; prv = t;
}

__global__ __launch_bounds__(512, 2) void k2_kernel(const u16* __restrict__ w_sp, const u16* __restrict__ b_sp, char* __restrict__ ws) {
    extern __shared__ __attribute__((aligned(16))) char smem[];
    int bid = blockIdx.x, xcd = bid & 7, idx = bid >> 3;
    int pm = (xcd & 1) * 8 + (idx & 7), j = (xcd >> 1) * 4 + (idx >> 3);
    const int lane = threadIdx.x & 63, wid = __builtin_amdgcn_readfirstlane(threadIdx.x >> 6);
    const unsigned lds0 = (unsigned)(uintptr_t)smem; const LAS char* lds = (const LAS char*)smem;
    constexpr int EP = 148;
    float* et = (float*)smem;
    STAMP(1, 0);
    if (wid < 4) {
        const int wr = wid >> 1, wc = wid & 1;
        G2S s;
#pragma unroll
        for (int i = 0; i < 4; ++i) { int row = 8 * (wid * 4 + i) + (lane >> 3), c = (lane & 7) ^ ((row >> 1) & 7); s.va[i] = row * (S_ * 2) + c * 16 + 4096 - i * 1024; }
        s.vl = lane * 16; s.vl2 = lane * 16 + 4096;
        s.sa = (const char*)(w_sp + (size_t)pm * 128 * S_) - 4096;
        s.sl = wid < 3 ? ws + WS_Z2T + (size_t)j * 32 * 18432 + wid * 6144 : ws + WS_KV;
        s.lstep = wid < 3 ? 18432 : 6144;
        s.m0a = lds0 + wid * 4096; s.m0l = lds0 + 16384 + wid * 6144;
        {
            int r = lane & 15, g = lane >> 4, q = r >> 2, p = lane & 3;
#pragma unroll
            for (int ks = 0; ks < 2; ++ks) {
                int o = r * 128 + (((4 * ks + g) ^ (r >> 1)) * 16) + wr * 8192;
                s.pa[0][ks] = lds + o + wc * 4096; s.pa[1][ks] = lds + o + (1 - wc) * 4096;
            }
            int ob = 16384 + g * 2304 + q * 32 + 8 * p + wc * 1024;
            s.pb[0] = lds + ob + ((g & 1) * 128); s.pb[1] = lds + ob + (((g & 1) ^ 1) * 128);
            s.so = 2048 - wc * 1024;
        }
#pragma unroll
        for (int t = 0; t < 3; ++t) {
            set_m0(s.m0a + t * K2_SLOT); glds_s<0>(s.va[0], s.sa); glds_s<1024>(s.va[1], s.sa); glds_s<2048>(s.va[2], s.sa); glds_s<3072>(s.va[3], s.sa); s.sa += 128;
            set_m0(s.m0l + t * K2_SLOT); glds_s<0>(s.vl, s.sl);
            if (t < 2) {
                glds_s<1024>(s.vl, s.sl); glds_s<2048>(s.vl, s.sl); glds_s<3072>(s.vl, s.sl);
                set_m0(s.m0l + t * K2_SLOT + 4096); glds_s<0>(s.vl2, s.sl); glds_s<1024>(s.vl2, s.sl); s.sl += s.lstep;
            }
        }
        f32x4 acc[4][4] = {}; f32x4 sacc[2] = {};
        G2F f0, f1;
        wait_vm_bar<15>();
        STAMP(1, 1);
        f0.a[0] = ldsA(s.pa[0][0]); f0.a[1] = ldsA(s.pa[0][0] + 2048); f0.a[2] = ldsA(s.pa[1][0]); f0.a[3] = ldsA(s.pa[1][0] + 2048);
        f0.b[0] = ldsB2(s.pb[0], s.pb[1]); f0.b[1] = ldsB2(s.pb[0] + 256, s.pb[1] + 256); f0.b[2] = ldsB2(s.pb[0] + 512, s.pb[1] + 512); f0.b[3] = ldsB2(s.pb[0] + 768, s.pb[1] + 768); f0.sb = ldsB2(s.pb[0] + s.so, s.pb[1] + s.so);
        unsigned cur = 0, nxt = K2_SLOT, prv = 2 * K2_SLOT;
        for (int t = 0; t < 29; ++t) g2_iter<true, true, 10, true>(s, f0, f1, cur, nxt, prv, acc, sacc);
        g2_iter<true, false, 10, true>(s, f0, f1, cur, nxt, prv, acc, sacc);
        g2_iter<false, false, 0, true>(s, f0, f1, cur, nxt, prv, acc, sacc);
        const int c = lane & 15, g = lane >> 4;
        const char* z1b = ws + WS_Z1 + (size_t)(pm * 16 + j) * 36864 + (g >> 1) * 2048 + (wr * 64 + c) * 16 + (g & 1) * 8;
        u32x2 z1r[4][4], z1s[2]; float bias[4];
#pragma unroll
        for (int m = 0; m < 4; ++m) {
            const int mo = (m ^ (2 * wc)) * 16;
            bias[m] = bf2f(b_sp[pm * 128 + wr * 64 + mo + c]);
#pragma unroll
            for (int n = 0; n < 4; ++n) z1r[m][n] = *(const u32x2*)(z1b + (wc * 4 + n) * 4096 + mo * 16);
            if (m < 2) z1s[m] = *(const u32x2*)(z1b + 8 * 4096 + mo * 16);
        }
        SB();
        g2_iter<false, false, 63, false>(s, f0, f1, cur, nxt, prv, acc, sacc);
        __builtin_amdgcn_s_setprio(2);
        STAMP(1, 2);
        auto fin = [&](f32x4 v, float b, u32x2 z) { v += b; return u32x2{cvtpk(bflo(z[0]) * v[0], bfhi(z[0]) * v[1]), cvtpk(bflo(z[1]) * v[2], bfhi(z[1]) * v[3])}; };
        auto put2 = [&](u32x2 A, u32x2 B, int rowA, int nfA, int rowB, int nfB) {
            auto r0 = __builtin_amdgcn_permlane16_swap(A[0], B[0], false, false); auto r1 = __builtin_amdgcn_permlane16_swap(A[1], B[1], false, false);
            const int row = (g & 1) ? rowB : rowA, cgl = 18 * j + 2 * ((g & 1) ? nfB : nfA) + (g >> 1);
            char* p = ws + WS_UO + ((size_t)((row >> 6) * 90 + (cgl >> 2)) * 4 + ((row >> 4) & 3)) * 1024 + (cgl & 3) * 256 + (row & 15) * 16;
            st16(p, u32x4{r0[0], r1[0], r0[1], r1[1]});
        };
#pragma unroll
        for (int m = 0; m < 4; ++m) {
            const int row = pm * 128 + wr * 64 + (m ^ (2 * wc)) * 16 + c;
#pragma unroll
            for (int n = 0; n < 4; n += 2) put2(fin(acc[m][n], bias[m], z1r[m][n]), fin(acc[m][n + 1], bias[m], z1r[m][n + 1]), row, wc * 4 + n, row, wc * 4 + n + 1);
        }
        { const int row = pm * 128 + wr * 64 + (2 * wc) * 16 + c; put2(fin(sacc[0], bias[0], z1s[0]), fin(sacc[1], bias[1], z1s[1]), row, 8, row + 16, 8); }
        STAMP_DRAIN(); STAMP(1, 3);
    } else {
        __builtin_amdgcn_s_setprio(1);
        const int c = lane & 31, h = lane >> 5, aw = wid - 4;
        const char* kvg = ws + WS_KV;
        const int row0 = blockIdx.x * 8 + aw * 2;
        bf16x8 q1[2], q2[2]; float mref[2];
        const f32x16 zero = {};
#pragma unroll
        for (int t = 0; t < 2; ++t) {
            const u16* qr = (const u16*)(ws + WS_Q) + (size_t)(row0 + t) * D_ + 18 * c;
            u32x4 a; const unsigned* qp = (const unsigned*)(qr + 8 * h);
            a[0] = qp[0]; a[1] = qp[1]; a[2] = qp[2]; a[3] = qp[3];
            q1[t] = __builtin_bit_cast(bf16x8, a);
            unsigned e = *(const unsigned*)(qr + 16);
            u32x4 b = {h ? 0u : e, 0u, 0u, 0u};
            q2[t] = __builtin_bit_cast(bf16x8, b);
        }
        f32x16 oA = zero, oB = zero;
        bool bad = false;
        const int k2o = k2slot(c), vo = (c < 19 ? lane : 63) * 16;
        const LAS char* kvl = lds + 16384 + 18432;
        asm volatile("s_waitcnt vmcnt(0)\n\ts_barrier" ::: "memory");
        KVF X, Y;
        kv_read(X, kvl, lane, k2o, vo); kv_read(Y, kvl + 3072, lane, k2o, vo);
#pragma unroll
        for (int t = 0; t < 2; ++t) {
            f32x16 s0 = MFMA32(X.k1, q1[t], zero); s0 = MFMA32(X.k2, q2[t], s0);
            f32x16 s1 = MFMA32(Y.k1, q1[t], zero); s1 = MFMA32(Y.k2, q2[t], s1);
            float m = xhalf_max(fmaxf(max16(s0), max16(s1)));
            mref[t] = bf_round(rintf(m) + ATT_BIAS);
            u32x4 b = __builtin_bit_cast(u32x4, q2[t]); b[1] = h ? 0u : (__float_as_uint(-mref[t]) >> 16); q2[t] = __builtin_bit_cast(bf16x8, b);
        }
        f32x16 sA = MFMA32(X.k1, q1[0], zero); sA = MFMA32(X.k2, q2[0], sA);
        f32x16 sB = zero;
        bf16x8 pA0, pA1, pB0 = {}, pB1 = {};
        half_step<false>(sA, pA0, pA1, sB, X, q1[1], q2[1], oB, X, pB0, pB1, Y, kvl, lane, k2o, vo);
        half_step<false>(sB, pB0, pB1, sA, Y, q1[0], q2[0], oA, X, pA0, pA1, Y, kvl, lane, k2o, vo);
        asm volatile("s_waitcnt lgkmcnt(0)\n\ts_barrier" ::: "memory");
        unsigned slot = K2_SLOT;
        for (int t = 1; t < 32; ++t) {
            const LAS char* tb = kvl + slot;
            half_step<true>(sA, pA0, pA1, sB, Y, q1[1], q2[1], oB, X, pB0, pB1, X, tb, lane, k2o, vo);
            half_step<false>(sB, pB0, pB1, sA, X, q1[0], q2[0], oA, Y, pA0, pA1, X, tb, lane, k2o, vo);
            bool chkB = (h == 0) && !(oB[10] <= 0x1p20f);
            half_step<true>(sA, pA0, pA1, sB, X, q1[1], q2[1], oB, Y, pB0, pB1, Y, tb + 3072, lane, k2o, vo);
            if (__builtin_expect(__any(chkB), 0)) att_rescale(oB, sB, mref[1], q2[1], bad, c, h);
            bool chkA = (h == 0) && !(oA[10] <= 0x1p20f);
            half_step<false>(sB, pB0, pB1, sA, Y, q1[0], q2[0], oA, X, pA0, pA1, Y, tb, lane, k2o, vo);
            if (__builtin_expect(__any(chkA), 0)) att_rescale(oA, sA, mref[0], q2[0], bad, c, h);
            asm volatile("s_waitcnt lgkmcnt(0)\n\ts_barrier" ::: "memory");
            slot = slot == 2 * K2_SLOT ? 0 : slot + K2_SLOT;
        }
        half_step<false>(sA, pA0, pA1, sB, Y, q1[1], q2[1], oB, X, pB0, pB1, X, kvl, lane, k2o, vo);
        exp_pack(sB, pB0, pB1);
        oA = MFMA32(Y.v0, pA0, oA); oA = MFMA32(Y.v1, pA1, oA);
        oB = MFMA32(Y.v0, pB0, oB); oB = MFMA32(Y.v1, pB1, oB);
        {
            bool nf = false;
#pragma unroll
            for (int i = 0; i < 11; ++i) nf = nf || ((!(fabsf(oA[i]) <= 3.0e38f) || !(fabsf(oB[i]) <= 3.0e38f)) && (i < 8 || h == 0));
            if (__any(nf)) bad = true;
        }
        f32x16 o[2] = {oA, oB};
        if (__builtin_expect(bad, 0)) {
#pragma unroll 1
            for (int t = 0; t < 2; ++t) {
                bf16x8 qa = t ? q1[1] : q1[0]; u32x4 qb = __builtin_bit_cast(u32x4, t ? q2[1] : q2[0]); qb[1] = 0;
                f32x16 oo = zero; float m = -3.0e38f;
                for (int kt = 0; kt < 64; ++kt) {
                    const char* tb = kvg + (size_t)kt * 3072;
                    bf16x8 k1 = *(const bf16x8*)(tb + lane * 16);
                    u32x4 k2v = *(const u32x4*)(tb + k2o);
                    bf16x8 v0 = *(const bf16x8*)(tb + 1024 + vo), v1 = *(const bf16x8*)(tb + 2048 + vo);
                    f32x16 s = MFMA32(k1, qa, zero); s = MFMA32(__builtin_bit_cast(bf16x8, k2v), __builtin_bit_cast(bf16x8, qb), s);
                    float nm = fmaxf(m, xhalf_max(max16(s)));
                    float sc = __builtin_amdgcn_exp2f(m - nm); m = nm;
#pragma unroll
                    for (int i = 0; i < 16; ++i) { oo[i] *= sc; s[i] -= nm; }
                    bf16x8 p0, p1; exp_pack(s, p0, p1);
                    oo = MFMA32(v0, p0, oo); oo = MFMA32(v1, p1, oo);
                }
                if (t) o[1] = oo; else o[0] = oo;
            }
        }
#pragma unroll
        for (int t = 0; t < 2; ++t) {
            float inv = 1.0f / lo_bcast(o[t][10]);
            const int orow = row0 + t;
            char* ob = ws + WS_UO + ((size_t)((orow >> 6) * 90 + 72) * 4 + ((orow >> 4) & 3)) * 1024 + (orow & 15) * 16;
            unsigned w0 = cvtpk(o[t][0] * inv, o[t][1] * inv), w1 = cvtpk(o[t][2] * inv, o[t][3] * inv);
            unsigned w2 = cvtpk(o[t][4] * inv, o[t][5] * inv), w3 = cvtpk(o[t][6] * inv, o[t][7] * inv);
            unsigned w4 = cvtpk(o[t][8] * inv, o[t][9] * inv);
            auto put = [&](int d, unsigned w) { int kk = 18 * c + d; *(unsigned*)(ob + (kk >> 5) * 4096 + ((kk >> 3) & 3) * 256 + (kk & 7) * 2) = w; };
            put(4 * h, w0); put(4 * h + 2, w1); put(8 + 4 * h, w2); put(10 + 4 * h, w3);
            if (h == 0) put(16, w4);
        }
    }
}

constexpr int K3_SLOT = 8704, K3_NS = 4, K3_GW = K3_NS * K3_SLOT, K3_LDS = 4 * K3_GW + 64;
DI void k3_issue(unsigned wm0, unsigned va, const char* sa, const char* sb) {
    set_m0(wm0); glds_s<0>(va, sa); glds_s<1024>(va, sa); glds_s<2048>(va, sa); glds_s<3072>(va, sa);
    set_m0(wm0 + 4096); glds_s<0>(va, sb); glds_s<1024>(va, sb); glds_s<2048>(va, sb); glds_s<3072>(va, sb);
    asm volatile("s_mov_b32 m0, %2\n\ts_mov_b32 exec_hi, 0\n\tglobal_load_lds_dwordx4 %0, %1\n\ts_mov_b32 exec_hi, -1" ::"v"(va + 4096), "s"(sb), "s"(wm0 + 8192));
}
template <bool GL, int VM> DI void k3_step(const LAS char* rd, unsigned wm0, unsigned va, const char* sa, const char* sb, unsigned pb, f32x4 (&acc)[4][5]) {
    wait_vm<VM>();
    bf16x8 a[4], b[5];
#pragma unroll
    for (int m = 0; m < 4; ++m) a[m] = ldsA(rd + va + m * 1024);
#pragma unroll
    for (int n = 0; n < 5; ++n) b[n] = ldsB2(rd + pb + n * 32, rd + pb + n * 32 + 576);
    asm volatile("s_waitcnt lgkmcnt(0)" ::: "memory"); SB();
    if (GL) k3_issue(wm0, va, sa, sb);
    SB();
#pragma unroll
    for (int n = 0; n < 5; ++n)
#pragma unroll
        for (int m = 0; m < 4; ++m) acc[m][n] = MFMA16(b[n], a[m], acc[m][n]);
    SB();
}
__global__ __launch_bounds__(256, 1) void k3_kernel(const u16* __restrict__ x, char* __restrict__ ws, u16* __restrict__ out) {
    extern __shared__ __attribute__((aligned(16))) char smem[];
    const int bid = blockIdx.x, xcd = bid & 7, idx = bid >> 3;
    const int rb = (xcd & 3) * 8 + (idx & 7), j = (xcd >> 2) * 4 + (idx >> 3);
    const int lane = threadIdx.x & 63, wid = __builtin_amdgcn_readfirstlane(threadIdx.x >> 6);
    const unsigned lds0 = (unsigned)(uintptr_t)smem;
    const unsigned va = lane * 16;
    constexpr int EP = 84;
    STAMP(2, 0);
    const LAS char* lds = (const LAS char*)smem + wid * K3_GW;
    const char* ua = ws + WS_UO + (size_t)rb * 90 * 4096 + wid * 4096;
    const char* wb = ws + WS_W3T + (size_t)j * 90 * 4608 + wid * 4608;
    const unsigned wm = lds0 + wid * K3_GW;
#pragma unroll
    for (int i = 0; i < 4; ++i) k3_issue(wm + i * K3_SLOT, va, ua + i * 16384, wb + i * 18432);
    const int g = lane >> 4, q = (lane & 15) >> 2, p = lane & 3;
    const unsigned pb = 4096 + (8 * g + q) * 144 + 8 * p;
    const int n = wid < 2 ? 23 : 22;
    f32x4 acc[4][5] = {};
    STAMP(2, 1);
    unsigned slot = 0;
#define K3_ROT() slot = slot == 3 * K3_SLOT ? 0 : slot + K3_SLOT
    for (int i = 4; i < n; ++i) { k3_step<true, 27>(lds + slot, wm + slot, va, ua + i * 16384, wb + i * 18432, pb, acc); K3_ROT(); }
    u32x4 xv[3];
#pragma unroll
    for (int i = 0; i < 3; ++i) { int ci = threadIdx.x + 256 * i; if (ci >= 576) ci = 575; xv[i] = *(const u32x4*)(x + (size_t)(rb * 64 + ci / 9) * D_ + 72 * j + (ci % 9) * 8); }
    SB();
    k3_step<false, 27>(lds + slot, 0, va, ua, ua, pb, acc); K3_ROT();
    k3_step<false, 18>(lds + slot, 0, va, ua, ua, pb, acc); K3_ROT();
    k3_step<false, 9>(lds + slot, 0, va, ua, ua, pb, acc); K3_ROT();
    k3_step<false, 0>(lds + slot, 0, va, ua, ua, pb, acc);
    STAMP(2, 2);
    float* et = (float*)(smem + wid * K3_GW);
#pragma unroll
    for (int m = 0; m < 4; ++m)
#pragma unroll
        for (int nn = 0; nn < 5; ++nn) *(f32x4*)(et + (m * 16 + (lane & 15)) * EP + nn * 16 + 4 * g) = acc[m][nn];
    asm volatile("s_waitcnt lgkmcnt(0)\n\ts_barrier" ::: "memory");
#pragma unroll
    for (int i = 0; i < 3; ++i) {
        int ci = threadIdx.x + 256 * i;
        if (ci < 576) {
            int row = ci / 9, ch = ci % 9;
            f32x4 s0 = {bflo(xv[i][0]), bfhi(xv[i][0]), bflo(xv[i][1]), bfhi(xv[i][1])}, s1 = {bflo(xv[i][2]), bfhi(xv[i][2]), bflo(xv[i][3]), bfhi(xv[i][3])};
#pragma unroll
            for (int w = 0; w < 4; ++w) {
                const float* e = (const float*)(smem + w * K3_GW) + row * EP + ch * 8;
                s0 += *(const f32x4*)e; s1 += *(const f32x4*)(e + 4);
            }
            u32x4 o = {cvtpk(s0[0], s0[1]), cvtpk(s0[2], s0[3]), cvtpk(s1[0], s1[1]), cvtpk(s1[2], s1[3])};
            *(u32x4*)(out + (size_t)(rb * 64 + row) * D_ + 72 * j + ch * 8) = o;
        }
    }
    STAMP_DRAIN(); STAMP(2, 3);
    asm volatile(".Lk3_end:");
}

extern "C" void kernel_launch(void* const* d_in, const int* in_sizes, int n_in, void* d_out, int out_size, void* d_ws, size_t ws_size, hipStream_t stream) {
    const u16* x = (const u16*)d_in[0]; const u16* wq = (const u16*)d_in[1]; const u16* wk = (const u16*)d_in[2]; const u16* wv = (const u16*)d_in[3];
    const u16* wo = (const u16*)d_in[4]; const u16* w_in = (const u16*)d_in[6]; const u16* w_sp = (const u16*)d_in[7]; const u16* b_sp = (const u16*)d_in[8];
    const u16* w_out = (const u16*)d_in[9]; char* ws = (char*)d_ws;
    (void)hipFuncSetAttribute((const void*)k1_kernel, hipFuncAttributeMaxDynamicSharedMemorySize, K1_LDS);
    (void)hipFuncSetAttribute((const void*)k2_kernel, hipFuncAttributeMaxDynamicSharedMemorySize, K2_LDS);
    (void)hipFuncSetAttribute((const void*)k3_kernel, hipFuncAttributeMaxDynamicSharedMemorySize, K3_LDS);
    k1_kernel<<<256, 512, K1_LDS, stream>>>(x, wq, wk, wv, w_in, w_sp, w_out, wo, ws);
    k2_kernel<<<256, 512, K2_LDS, stream>>>(w_sp, b_sp, ws);
    k3_kernel<<<256, 256, K3_LDS, stream>>>(x, ws, (u16*)d_out);
}
```

```cpp
#include <hip/hip_runtime.h>
#include <hip/hip_bf16.h>
#include <stdint.h>

typedef unsigned short u16;
typedef short bf16x8 __attribute__((ext_vector_type(8)));
typedef short s16x4 __attribute__((ext_vector_type(4)));
typedef float f32x2 __attribute__((ext_vector_type(2)));
typedef float f32x4 __attribute__((ext_vector_type(4)));
typedef float f32x16 __attribute__((ext_vector_type(16)));
typedef unsigned u32x4 __attribute__((ext_vector_type(4)));
typedef unsigned u32x2 __attribute__((ext_vector_type(2)));
typedef __bf16 bf16x2_t __attribute__((ext_vector_type(2)));
#define LAS __attribute__((address_space(3)))
#define DI __device__ __forceinline__

constexpr int S_ = 2048, D_ = 576, FH = 2304, F_ = 4608;

constexpr size_t WS_Q = 0;
constexpr size_t WS_KV = WS_Q + (size_t)S_ * D_ * 2;
constexpr size_t WS_Z1 = WS_KV + 64 * 3072;
constexpr size_t WS_Z2T = WS_Z1 + (size_t)S_ * FH * 2;
constexpr size_t WS_UO = WS_Z2T + (size_t)S_ * FH * 2;
constexpr size_t WS_ST = WS_UO + (size_t)S_ * 2880 * 2;
constexpr size_t WS_XCH = WS_ST + (size_t)S_ * 16 * 8;
constexpr size_t WS_W3T = WS_XCH + 4096 + 16384;
constexpr size_t WS_DBG = 128u << 20;
#define STAMP(kid, i)
#define STAMP_DRAIN()
#define STAMPT(tid, kid, i)

DI unsigned cvtpk(float lo, float hi) { f32x2 v = {lo, hi}; return __builtin_bit_cast(unsigned, __builtin_convertvector(v, bf16x2_t)); }
DI float bf2f(u16 b) { return __uint_as_float(((unsigned)b) << 16); }
DI float bflo(unsigned w) { return __uint_as_float(w << 16); }
DI float bfhi(unsigned w) { return __uint_as_float(w & 0xffff0000u); }
#define SB() __builtin_amdgcn_sched_barrier(0)
#define PIN(x) asm volatile("" : "+v"(x))
#define EX(x) __builtin_amdgcn_exp2f(x)
#define MFMA16(a, b, c) __builtin_amdgcn_mfma_f32_16x16x32_bf16(a, b, c, 0, 0, 0)
#define MFMA32(a, b, c) __builtin_amdgcn_mfma_f32_32x32x16_bf16(a, b, c, 0, 0, 0)

DI s16x4 trread(const LAS char* p) { return __builtin_bit_cast(s16x4, __builtin_amdgcn_ds_read_tr16_b64_v4i16((LAS s16x4*)p)); }
template <int N> DI void wait_vm_bar() { asm volatile("s_waitcnt vmcnt(%0) lgkmcnt(0)\n\ts_barrier" ::"n"(N) : "memory"); }
template <int N> DI void wait_vm() { asm volatile("s_waitcnt vmcnt(%0)" ::"n"(N) : "memory"); }
template <int OFF> DI void glds_s(unsigned voff, const char* sbase) { asm volatile("global_load_lds_dwordx4 %0, %1 offset:%2" ::"v"(voff), "s"(sbase), "n"(OFF)); }
template <int OFF> DI void glds_sc1(unsigned voff, const char* sbase) { asm volatile("global_load_lds_dwordx4 %0, %1 offset:%2 sc1" ::"v"(voff), "s"(sbase), "n"(OFF)); }
DI void glds_v(const char* p, unsigned m0v) { asm volatile("s_mov_b32 m0, %1\n\ts_nop 0\n\tglobal_load_lds_dwordx4 %0, off" ::"v"(p), "s"(m0v)); }
DI unsigned ld_sc1_async(const char* sb, unsigned vo) { unsigned v; asm volatile("global_load_dword %0, %1, %2 sc1" : "=v"(v) : "v"(vo), "s"(sb) : "memory"); return v; }
#define WT
DI void st16(void* p, u32x4 v) { asm volatile("global_store_dwordx4 %0, %1, off sc1\n\ts_nop 1" :: "v"(p), "v"(v) : "memory"); }
DI void st8(void* p, u32x2 v) { asm volatile("global_store_dwordx2 %0, %1, off sc1" :: "v"(p), "v"(v) : "memory"); }
DI void set_m0(unsigned v) { asm volatile("s_mov_b32 m0, %0\n\ts_nop 0" ::"s"(v)); }
DI bf16x8 ldsA(const LAS char* p) { return *(const LAS bf16x8*)p; }
DI bf16x8 ldsB2(const LAS char* p0, const LAS char* p1) { s16x4 lo = trread(p0), hi = trread(p1); return __builtin_shufflevector(lo, hi, 0, 1, 2, 3, 4, 5, 6, 7); }
DI bf16x8 ldsB(const LAS char* p) { return ldsB2(p, p + 256); }

DI float gelu_t(float x) {
    const float c0 = -2.0f * 1.4426950408889634f * 0.7978845608028654f;
    const float c1 = c0 * 0.044715f;
    float x2 = x * x;
    float t = __builtin_fmaf(x2, c1, c0);
    float e = __builtin_amdgcn_exp2f(x * t);
    return x * __builtin_amdgcn_rcpf(1.0f + e);
}
DI int k2slot(int c) { return c < 13 ? 1024 + (19 + c) * 16 : (c < 25 ? 1024 + (51 + c - 13) * 16 : 2048 + (19 + c - 25) * 16); }
constexpr float QSCALE = 0.23570226039551584f * 1.4426950408889634f;

#define KO1 (3 * (((idx & 7) + (idx >> 3)) % 3))
#define KO2 (((idx & 7) + 2 * (idx >> 3) + 4 * (xcd & 1)) % 9)
#define PLSLOT 0
constexpr int K1_SA = 16384, K1_SB = 24576, K1_B0 = 3 * K1_SA, K1_TAB = K1_B0 + 3 * K1_SB, K1_LDS = K1_TAB + 10240;
typedef __attribute__((address_space(1))) unsigned gu32;
typedef __attribute__((address_space(1))) unsigned long long gu64;
#define RLX_AGENT __ATOMIC_RELAXED, __HIP_MEMORY_SCOPE_AGENT
struct K1F { bf16x8 a[4], b[3], x; };
struct K1S {
    unsigned va[2], vb[2]; const char* sa; const char* sb;
    const char* gp; unsigned st;
    int k1, k2;
    unsigned m0a, m0b;
    const LAS char* pa[2]; const LAS char* px[2]; const LAS char* pb[3];
};
DI void k1_issue(K1S& s, int i, int w) {
    if (i == 0) { set_m0(s.m0a + w * K1_SA); glds_s<0>(s.va[0], s.sa); }
    if (i == 1) { glds_s<1024>(s.va[1], s.sa); s.sa += s.k1 == 8 ? -8 * 128 : 128; asm volatile("" : "+s"(s.sa)); }
    if (i == 2) { glds_v(s.gp + (unsigned long long)s.st * (unsigned)s.k1, s.m0b + w * K1_SB + 2048); s.k1 = s.k1 == 8 ? 0 : s.k1 + 1; asm volatile("" : "+s"(s.k1)); }
    if (i == 3) { set_m0(s.m0b + w * K1_SB); glds_s<0>(s.vb[0], s.sb); }
    if (i == 4) { glds_s<1024>(s.vb[1], s.sb); s.sb += s.k2 == 8 ? -8 * 128 * F_ : 128 * F_; s.k2 = s.k2 == 8 ? 0 : s.k2 + 1; asm volatile("" : "+s"(s.sb), "+s"(s.k2)); }
}
DI f32x2 gelu_y2(f32x2 y, float k0, float k1, float ir) {
    f32x2 t = (y * y) * k1 + k0;
    f32x2 a = y * t;
    f32x2 e = {__builtin_amdgcn_exp2f(a[0]), __builtin_amdgcn_exp2f(a[1])};
    f32x2 d = e * ir + ir;
    f32x2 rc = {__builtin_amdgcn_rcpf(d[0]), __builtin_amdgcn_rcpf(d[1])};
    return y * rc;
}
DI float dpp_x1(float x) { return __int_as_float(__builtin_amdgcn_mov_dpp(__float_as_int(x), 0xB1, 0xf, 0xf, true)); }
DI float dpp_x2(float x) { return __int_as_float(__builtin_amdgcn_mov_dpp(__float_as_int(x), 0x4E, 0xf, 0xf, true)); }
DI void kv_store(char* kv, int sv, int j, int c, int g, f32x4 v) {
    const int key = sv * 16 + c;
    if (j == 4) {
        *(u32x2*)(kv + ((g >> 1) * 32 + key) * 16 + (g & 1) * 8) = u32x2{cvtpk(v[0], v[1]), cvtpk(v[2], v[3])};
        if (g == 1) { unsigned* k2p = (unsigned*)(kv + k2slot(key)); k2p[1] = 0x3f80u; *(u32x2*)(k2p + 2) = u32x2{0u, 0u}; }
        if (g == 2 && c < 2) *(u32x4*)(kv + 1024 + sv * 1024 + (c * 32 + 18) * 16) = u32x4{0x3f803f80u, 0x3f803f80u, 0x3f803f80u, 0x3f803f80u};
        if (g == 3 && c == 0) *(u32x4*)(kv + 1024 + sv * 1024 + 63 * 16) = u32x4{0u, 0u, 0u, 0u};
        return;
    }
    if (j == 5 && g == 1) *(unsigned*)(kv + k2slot(key)) = cvtpk(v[2], v[3]);
    const bool o1 = c & 1, o2 = c & 2;
    float t0 = dpp_x1(o1 ? v[0] : v[1]), t1 = dpp_x1(o1 ? v[2] : v[3]);
    if (o1) { v[0] = t0; v[2] = t1; } else { v[1] = t0; v[3] = t1; }
    t0 = dpp_x2(o2 ? v[0] : v[2]); t1 = dpp_x2(o2 ? v[1] : v[3]);
    if (o2) { v[0] = t0; v[1] = t1; } else { v[2] = t0; v[3] = t1; }
    int d = j == 5 ? 4 * (g - 2) + (c & 3) : (g < 2 ? 8 + 4 * g + (c & 3) : 14 + (c & 3));
    bool ok = j == 5 ? g >= 2 : (g < 2 || (g == 3 && (c & 3) >= 2));
    const int kq = c >> 2;
    if (ok) *(u32x2*)(kv + 1024 + sv * 1024 + ((kq & 1) * 32 + d) * 16 + (kq >> 1) * 8) = u32x2{cvtpk(v[0], v[1]), cvtpk(v[2], v[3])};
}
template <int KS, int GL, int RS, int WS, bool RX> DI void k1_p1(K1S& s, const K1F& f, K1F& n, f32x4 (&acc)[4][2], f32x4& sacc, f32x4 (&cso)[2], f32x4& rs, f32x4& rq, const bf16x8& ones) {
    constexpr int AO = RS * K1_SA, BO = RS * K1_SB + KS * 12288;
#define MX(d, a_, b_) d = MFMA16(a_, b_, d); PIN(d); SB()
#define MM(m, nn) MX(acc[m][nn], f.b[nn], f.a[m])
    MM(0, 0); n.b[0] = ldsB(s.pb[0] + BO); SB();
    MM(1, 0); n.a[0] = ldsA(s.pa[KS] + AO); SB();
    MM(2, 0); n.a[1] = ldsA(s.pa[KS] + AO + 2048); SB();
    MM(3, 0); n.a[2] = ldsA(s.pa[KS] + AO + 4096); SB();
    MX(cso[0], f.b[0], ones); n.a[3] = ldsA(s.pa[KS] + AO + 6144); SB();
    MX(sacc, f.b[2], f.x); n.b[1] = ldsB(s.pb[1] + BO); SB();
    MX(cso[1], f.b[2], ones); n.b[2] = ldsB(s.pb[2] + BO); SB();
    MX(rs, ones, f.x); k1_issue(s, GL == 1 ? 0 : 3, WS); SB();
    MX(rq, f.x, f.x); if (RX) n.x = ldsA(s.px[KS] + AO); SB();
    MM(0, 1); k1_issue(s, GL == 1 ? 1 : 4, WS); SB();
    MM(1, 1); if (GL == 1) k1_issue(s, 2, WS); SB();
    MM(2, 1);
    MM(3, 1);
}
template <int KS, int GL, int RS, int WS, bool RD, class E> DI void k1_p2(K1S& s, const K1F& f, K1F& n, f32x4 (&acc)[4][3], f32x4 (&cso)[2], const bf16x8& ones, E epi) {
    constexpr int AO = RS * K1_SA, BO = RS * K1_SB + KS * 12288;
    MM(0, 0); if (RD) n.b[0] = ldsB(s.pb[0] + BO); epi(0); SB();
    MM(1, 0); if (RD) n.a[0] = ldsA(s.pa[KS] + AO); epi(1); SB();
    MM(2, 0); if (RD) n.a[1] = ldsA(s.pa[KS] + AO + 2048); epi(2); SB();
    MM(3, 0); if (RD) n.a[2] = ldsA(s.pa[KS] + AO + 4096); epi(3); SB();
    MX(cso[0], f.b[0], ones); if (RD) n.a[3] = ldsA(s.pa[KS] + AO + 6144); epi(4); SB();
    MM(0, 1); if (RD) n.b[1] = ldsB(s.pb[1] + BO); epi(5); SB();
    MM(1, 1); if (RD) n.b[2] = ldsB(s.pb[2] + BO); epi(6); SB();
    MM(2, 1); if (GL) k1_issue(s, GL == 1 ? 0 : 3, WS); epi(7); SB();
    MM(3, 1); epi(8); SB();
    MX(cso[1], f.b[1], ones); if (GL) k1_issue(s, GL == 1 ? 1 : 4, WS); epi(9); SB();
    MM(0, 2); epi(10); SB();
    MM(1, 2); if (GL == 1) k1_issue(s, 2, WS); epi(11); SB();
    MM(2, 2); epi(12); SB();
    MM(3, 2); epi(13); SB();
#undef MM
#undef MX
}

__global__ __launch_bounds__(512, 2) void k1_kernel(const u16* __restrict__ x, const u16* __restrict__ wq, const u16* __restrict__ wk, const u16* __restrict__ wv,
                                                    const u16* __restrict__ w_in, const u16* __restrict__ w_sp, const u16* __restrict__ b_sp, const u16* __restrict__ w_out, const u16* __restrict__ wo, char* __restrict__ ws) {
    extern __shared__ __attribute__((aligned(16))) char smem[];
    const int bid = blockIdx.x, xcd = bid & 7, idx = bid >> 3;
    const int pm = (xcd & 1) * 8 + (idx & 7), j = (xcd >> 1) * 4 + (idx >> 3);
    const int lane = threadIdx.x & 63, wid = __builtin_amdgcn_readfirstlane(threadIdx.x >> 6), wr = wid >> 2, wc = wid & 3;
    const unsigned lds0 = (unsigned)(uintptr_t)smem;
    const LAS char* lds = (const LAS char*)smem;
    {
        const char* pc; unsigned len;
        asm volatile("s_getpc_b64 %0\n.Lk1_here:\n\ts_mov_b32 %1, .Lk3_end-.Lk1_here" : "=s"(pc), "=s"(len));
        unsigned off = ((idx * 8 + wid) * 2 + (lane & 1)) * 128; off = off < len ? off : len;
        asm volatile("s_mov_b32 m0, %2\n\ts_nop 0\n\tglobal_load_lds_dword %0, %1" ::"v"(off), "s"(pc), "s"(lds0 + K1_TAB + 9216 + 512));
    }
    K1S s; s.m0a = lds0 + wid * 2048; s.m0b = lds0 + K1_B0 + wid * 3072;
    const int brow = (lane >> 2) & 7, bc = (lane & 3) ^ (2 * (wid & 1)), bk = 8 * wid + brow;
    const int ko2 = KO2, ko1 = KO1;
    unsigned oa = pm * 128 * D_ * 2, ob = 288 * j, oa1 = oa + ko1 * 128, ob1 = ob + FH * 2 + ko1 * 128 * F_, vg = bk * (F_ * 2) + 256 + 16 * (bc & 1);
    {
        int r8 = lane >> 3;
        s.va[0] = (16 * wid + r8) * (D_ * 2) + (((lane & 7) ^ (r8 >> 1)) * 16);
        s.va[1] = (16 * wid + 8 + r8) * (D_ * 2) + (((lane & 7) ^ (4 + (r8 >> 1))) * 16) - 1024;
        s.vb[0] = bk * (F_ * 2) + (lane >> 5) * 64 + bc * 16 + 1024; s.vb[1] = s.vb[0] + 128 - 1024;
        s.k1 = s.k2 = ko1; s.st = 128 * F_;
        PIN(s.va[0]); PIN(s.va[1]); PIN(s.vb[0]); PIN(s.vb[1]); PIN(vg);
        asm volatile("" : "+s"(oa1), "+s"(ob1), "+s"(s.m0a), "+s"(s.m0b));
    }
    SB();
    s.sa = (const char*)x + oa1; s.sb = (const char*)w_in - 1024 + ob1;
    k1_issue(s, 0, 0); k1_issue(s, 1, 0); k1_issue(s, 3, 0); k1_issue(s, 4, 0);
    SB();
    STAMP(0, 0);
    s.gp = (const char*)w_in + (ob + FH * 2) + vg;
    k1_issue(s, 2, 0);
    const char* sa0 = (const char*)x + oa;
    const char* sb0 = (const char*)w_in - 1024 + ob;
    gu32* XN = (gu32*)(ws + WS_XCH) + pm * 16;
    gu32* XE = (gu32*)(ws + WS_XCH + 4096) + pm * 256;
    gu64* XP = (gu64*)(ws + WS_ST) + (size_t)pm * 16 * 128;
    unsigned eo = ld_sc1_async(ws + WS_XCH + 4096 + (pm * 256 + j * 16) * 4, (lane & 15) * 4);
#pragma unroll
    for (int t = 1; t < 3; ++t)
#pragma unroll
        for (int i = 0; i < 5; ++i) if (t < 2 || i < 3) k1_issue(s, i, t);
    unsigned nonce = 0;
    const char* gp2; unsigned st2;
    {
        const u16* p; int rs;
        const u16* sp0 = j < 4 ? wq + 512 + 16 * j : (j == 5 ? wk + 10 : (j == 6 ? wv + 8 : wk));
        const u16* sp1 = j < 4 ? wq + 520 + 16 * j : (j == 5 ? wv : (j == 6 ? wv + 10 : wk + 8));
        const int srs = j < 4 ? D_ : 18;
        if (lane >= 32) { p = wq + 32 * j + 8 * bc; rs = D_; }
        else if (bc < 2) { p = w_in + 144 * j + 128 + 8 * bc; rs = F_; }
        else { p = bc == 2 ? sp0 : sp1; rs = srs; }
        gp2 = (const char*)(p + (size_t)bk * rs); st2 = 128 * rs; PIN(gp2); PIN(st2);
    }
    const int r16 = lane & 15, g = lane >> 4, cg = wc ^ (2 * wr);
    const int nf3 = cg == 3 ? 8 : (cg == 2 ? 9 : 10 + cg);
#define NF1(n) ((n) == 2 ? 8 : 2 * wc + ((n) ^ wr))
#define NF2(n) ((n) == 0 ? nf3 : 2 * cg + (((n) - 1) ^ wr))
    const LAS char* pb2[3];
    {
        int q = r16 >> 2, p = lane & 3;
#pragma unroll
        for (int ks = 0; ks < 2; ++ks) { s.pa[ks] = lds + (r16 * 128 + (((4 * ks + g) ^ (r16 >> 1)) * 16) + wr * 8192); s.px[ks] = s.pa[ks] + wc * 2048; PIN(s.pa[ks]); PIN(s.px[ks]); }
#pragma unroll
        for (int n = 0; n < 3; ++n) {
            int nf = NF1(n); s.pb[n] = lds + (K1_B0 + g * 3072 + q * 64 + 8 * p + (nf >> 1) * 512 + (((nf ^ g) & 1) * 32)); PIN(s.pb[n]);
            nf = NF2(n); pb2[n] = lds + (K1_B0 + g * 3072 + q * 64 + 8 * p + (nf >> 1) * 512 + (((nf ^ g) & 1) * 32)); PIN(pb2[n]);
        }
    }
    f32x4 acc1[4][2] = {}; f32x4 sacc = {}; f32x4 cso[2] = {};
    f32x4 rs = {}, rq = {};
#pragma unroll
    for (int m = 0; m < 4; ++m) { PIN(acc1[m][0]); PIN(acc1[m][1]); }
    PIN(sacc); PIN(cso[0]); PIN(cso[1]); PIN(rs); PIN(rq);
    const u32x4 onesv = {0x3f803f80u, 0x3f803f80u, 0x3f803f80u, 0x3f803f80u}; bf16x8 ones = __builtin_bit_cast(bf16x8, onesv); PIN(ones);
    K1F f0, f1;
    SB();
    wait_vm_bar<8>();
    SB();
    f0.b[0] = ldsB(s.pb[0]);
    f0.a[0] = ldsA(s.pa[0]); f0.a[1] = ldsA(s.pa[0] + 2048); f0.a[2] = ldsA(s.pa[0] + 4096); f0.a[3] = ldsA(s.pa[0] + 6144);
    f0.b[2] = ldsB(s.pb[2]); f0.x = ldsA(s.px[0]); f0.b[1] = ldsB(s.pb[1]);
    SB();
    PIN(eo);
    if (wid == 7) {
        nonce = 1;
        while (__ballot(eo == nonce) != 0ull) ++nonce;
        if (lane == 0) __hip_atomic_store(XN + j, nonce, RLX_AGENT);
        __hip_atomic_store(XP + j * 128 + lane, ~0ull, RLX_AGENT); __hip_atomic_store(XP + j * 128 + 64 + lane, ~0ull, RLX_AGENT);
    }
    STAMP(0, 1);
#define P1_A(c) k1_p1<1, 2, (c), ((c) + 2) % 3, true>(s, f0, f1, acc1, sacc, cso, rs, rq, ones); wait_vm_bar<5>(); SB()
#define P1_B(c, RX) k1_p1<0, 1, ((c) + 1) % 3, (c), RX>(s, f1, f0, acc1, sacc, cso, rs, rq, ones)
#pragma unroll 1
    for (int it = 0; it < 2; ++it) { P1_A(0); P1_B(0, true); P1_A(1); P1_B(1, true); P1_A(2); P1_B(2, true); }
    const char* xn_s = ws + WS_XCH + pm * 64; const char* xe_s = ws + WS_XCH + 4096 + (pm * 256 + j * 16) * 4; const unsigned l4 = (lane & 15) * 4;
    P1_A(0);
    unsigned echoed = ld_sc1_async(xn_s, l4);
    s.k1 = s.k2 = ko2; s.sa = sa0 + ko2 * 128; s.sb = sb0 + (size_t)ko2 * 128 * F_; s.gp = gp2; s.st = st2; SB();
    P1_B(0, true);
    P1_A(1);
    PIN(echoed);
    if (wid == 7 && lane < 16) __hip_atomic_store(XE + lane * 16 + j, echoed, RLX_AGENT);
    SB();
    P1_B(1, true);
    P1_A(2);
    s.pb[0] = pb2[0]; s.pb[1] = pb2[1]; s.pb[2] = pb2[2];
    P1_B(2, false);
    STAMP(3, 0);
    float* CS = (float*)(smem + K1_TAB);
    f32x4* RS = (f32x4*)(smem + K1_TAB + 2048);
    float* red = (float*)(smem + K1_TAB + 4096);
    f32x2* ZS = (f32x2*)(smem + K1_TAB + 9216);
    volatile LAS unsigned* HS = (volatile LAS unsigned*)(lds + K1_TAB + 1792);
    const float c0 = -2.0f * 1.4426950408889634f * 0.7978845608028654f, c1 = c0 * 0.044715f;
    f32x4 acc2[4][3] = {}; f32x4 cs2[2] = {};
    auto none = [](int) {};
    auto tabs = [&](int st) {
        if (st == 2) { if (r16 == 0) { *(f32x4*)(CS + NF1(0) * 16 + 4 * g) = cso[0]; *(f32x4*)(CS + 128 + 4 * g) = cso[1]; } }
        if (st == 3) {
            float sq = (r16 & 2) ? ((r16 & 1) ? rq[3] : rq[2]) : ((r16 & 1) ? rq[1] : rq[0]);
            float mean = rs[0] * (1.0f / D_), var = fmaxf(sq * (1.0f / D_) - mean * mean, 0.f);
            rs[0] = mean; rs[1] = var + 1e-5f; PIN(rs);
        }
        if (st == 5) { rs[2] = __builtin_amdgcn_rsqf(rs[1]); PIN(rs); }
        if (st == 7) { float rstd = rs[2]; if (g == (r16 >> 2)) RS[wr * 64 + wc * 16 + r16] = f32x4{rs[0], c0 * rstd, c1 * rstd * rstd * rstd, rs[1] * rstd}; }
    };
    f32x4 er[5], cs1[3], e[3];
    f32x2 su[5], sq[5];
#define ZU(u) ((u) == 8 ? sacc : acc1[((u) >> 1) & 3][(u) & 1])
#define UM(u) ((u) == 8 ? 4 : (u) >> 1)
#define UN(u) ((u) == 8 ? 2 : (u) & 1)
#define PRE(i, u) { f32x4 y = ZU(u) - cs1[UN(u)] * er[UM(u)][0]; e[i] = ((y * y) * er[UM(u)][2] + er[UM(u)][1]) * y; ZU(u) = y; PIN(ZU(u)); PIN(e[i]); }
#define FIN(i, u, first) { ZU(u) *= e[i]; PIN(ZU(u)); f32x4 v = ZU(u); f32x2 lo = {v[0], v[1]}, hi = {v[2], v[3]}; \
        if (first) { su[UM(u)] = lo + hi; sq[UM(u)] = lo * lo + hi * hi; } else { su[UM(u)] += lo; su[UM(u)] += hi; sq[UM(u)] += lo * lo; sq[UM(u)] += hi * hi; } PIN(su[UM(u)]); PIN(sq[UM(u)]); }
#define RED(m) { float a_ = su[m][0] + su[m][1], b_ = sq[m][0] + sq[m][1]; \
            auto x1 = __builtin_amdgcn_permlane16_swap(__float_as_uint(a_), __float_as_uint(b_), false, false);       \
            float t_ = __uint_as_float(x1[0]) + __uint_as_float(x1[1]);                                               \
            auto x2 = __builtin_amdgcn_permlane32_swap(__float_as_uint(t_), __float_as_uint(t_), false, false); \
            t_ = __uint_as_float(x2[0]) + __uint_as_float(x2[1]);                                                     \
            if ((m) < 4) redp[(m) * 160] = t_; else reds[0] = t_; }
    float* redp = red + ((wr * 64 + r16) * 5 + wc) * 2 + (g & 1);
    float* reds = red + ((wr * 64 + wc * 16 + r16) * 5 + 4) * 2 + (g & 1);
#define T1(op, v) asm volatile(op " %0, %0" : "+v"(v))
#define DE1(v, sd) asm volatile("v_fma_f32 %0, %0, %1, %1" : "+v"(v) : "v"(sd))
#define EPI_E(u0, u1, u2) [&](int st) { \
        if (st < 12) { float t_ = e[st >> 2][st & 3]; T1("v_exp_f32", t_); e[st >> 2][st & 3] = t_; } \
        if (st >= 2) { const int v_ = st - 2; float t_ = e[v_ >> 2][v_ & 3]; DE1(t_, er[v_ < 4 ? UM(u0) : (v_ < 8 ? UM(u1) : UM(u2))][3]); e[v_ >> 2][v_ & 3] = t_; } }
#define EPI_R(m0, m1) [&](int st) { if (st < 12) { float t_ = e[st >> 2][st & 3]; T1("v_rcp_f32", t_); e[st >> 2][st & 3] = t_; } \
        if (st == 12 && (m0) >= 0) er[(m0) & 3] = RS[wr * 64 + (m0) * 16 + r16]; if (st == 13 && (m1) >= 0) er[(m1) & 3] = RS[wr * 64 + (m1) * 16 + r16]; }
#define P2_A(c, GL, E) k1_p2<1, GL ? 2 : 0, (c), ((c) + 2) % 3, true>(s, f0, f1, acc2, cs2, ones, E)
#define P2_B(c, GL, RD, E) k1_p2<0, GL ? 1 : 0, ((c) + 1) % 3, (c), RD>(s, f1, f0, acc2, cs2, ones, E)
    P2_A(0, true, tabs); wait_vm_bar<5>(); SB();
    er[0] = RS[wr * 64 + r16]; er[4] = RS[wr * 64 + wc * 16 + r16];
#pragma unroll
    for (int n = 0; n < 3; ++n) cs1[n] = *(const f32x4*)(CS + NF1(n) * 16 + 4 * g);
    PRE(0, 0) PRE(1, 1) PRE(2, 8) SB();
    P2_B(0, true, true, EPI_E(0, 1, 8));
    P2_A(1, true, EPI_R(1, 2)); wait_vm_bar<5>(); SB();
    unsigned ev = ld_sc1_async(xe_s, l4);
    FIN(0, 0, true) FIN(1, 1, false) FIN(2, 8, true) PRE(0, 2) PRE(1, 3) PRE(2, 4) RED(0) RED(4) SB();
    P2_B(1, true, true, EPI_E(2, 3, 4));
    P2_A(2, true, EPI_R(3, -1)); wait_vm_bar<5>(); SB();
    PIN(ev);
    bool hs = __all(ev == nonce);
    unsigned nn = ld_sc1_async(xn_s, l4);
    if (wid == 7) *HS = hs;
    FIN(0, 2, true) FIN(1, 3, false) FIN(2, 4, true) PRE(0, 5) PRE(1, 6) PRE(2, 7) RED(1) SB();
    P2_B(2, true, true, EPI_E(5, 6, 7));
    P2_A(0, true, EPI_R(-1, -1)); wait_vm_bar<5>(); SB();
    PIN(nn);
    FIN(0, 5, false) FIN(1, 6, true) FIN(2, 7, false) RED(2) RED(3) SB();
    auto reecho = [&](int st) { if (st == 2) { if (wid == 7 && nn != echoed) { echoed = nn; if (lane < 16) __hip_atomic_store(XE + lane * 16 + j, nn, RLX_AGENT); } } };
    P2_B(0, true, true, reecho);
    P2_A(1, true, none);
    STAMP(3, 1);
    wait_vm_bar<5>(); SB();
    f32x2 pu[5];
    auto pub = [&](int st) {
        if (st == 0) {
#pragma unroll
            for (int i = 0; i < 5; ++i) pu[i] = *(const f32x2*)(red + (wid * 16 + r16) * 10 + 2 * i);
        }
        if (st == 3) {
            float a = ((pu[0][0] + pu[1][0]) + (pu[2][0] + pu[3][0])) + pu[4][0], b = ((pu[0][1] + pu[1][1]) + (pu[2][1] + pu[3][1])) + pu[4][1];
            unsigned bb = __float_as_uint(b); if (bb == 0xffffffffu) bb = 0x7fc00000u;
            if (lane < 16) __hip_atomic_store(XP + j * 128 + wid * 16 + lane, ((unsigned long long)bb << 32) | __float_as_uint(a), RLX_AGENT);
        }
    };
    P2_B(1, true, true, pub);
    P2_A(2, true, none); wait_vm_bar<5>(); SB();
    P2_B(2, true, true, none);
    P2_A(0, true, none); wait_vm_bar<5>(); SB();
    P2_B(0, false, true, none);
    P2_A(1, false, none); wait_vm_bar<0>(); SB();
    const char* xp_s = ws + WS_ST + (size_t)pm * 16384 + wid * 128; const unsigned xp_v = g * 4096 + r16 * 8;
    u32x2 pv[4];
#define PLOAD() asm volatile("global_load_dwordx2 %0, %4, %5 sc1\n\tglobal_load_dwordx2 %1, %4, %5 offset:1024 sc1\n\tglobal_load_dwordx2 %2, %4, %5 offset:2048 sc1\n\tglobal_load_dwordx2 %3, %4, %5 offset:3072 sc1" \
        : "=&v"(pv[0]), "=&v"(pv[1]), "=&v"(pv[2]), "=&v"(pv[3]) : "v"(xp_v), "s"(xp_s) : "memory")
    auto fetch = [&](int st) {
        if (st == PLSLOT) PLOAD();
        if (st == PLSLOT + 1) {
            const char* sp = (const char*)w_sp + (size_t)(pm * 128 + (idx >> 3) * 32 + wid * 4) * (S_ * 2);
            const unsigned vo = (lane >> 4) * (S_ * 2) + (lane & 15) * 16;
            set_m0(s.m0a); glds_s<0>(vo, sp); glds_s<1024>(vo, sp - 768);
        }
        if (st == PLSLOT + 2) {
            int L = wid * 13 + (lane < 13 ? lane : 12);
            const char* p = L < 81 ? (const char*)w_out + (bid * 10368 + L * 128) : (L < 102 ? (const char*)wo + (bid * 2592 + (L - 81) * 128) : (const char*)b_sp + (pm * 256 + (L - 102) * 128));
            asm volatile("global_load_lds_dword %0, off" ::"v"(p));
        }
    };
    unsigned hsf = *HS;
    P2_B(1, false, true, fetch);
    P2_A(2, false, none);
    P2_B(2, false, false, none);
    STAMP(0, 2);
    if (r16 == 0) { *(f32x4*)(CS + 192 + NF2(0) * 16 + 4 * g) = cs2[0]; *(f32x4*)(CS + 192 + NF2(1) * 16 + 4 * g) = cs2[1]; }
    f32x4* HX = (f32x4*)(smem + K1_B0) + wr * 256 + lane;
    if (cg == 3) { HX[0] = acc2[0][0]; HX[64] = acc2[1][0]; HX[128] = acc2[2][0]; HX[192] = acc2[3][0]; }
    wait_vm<3>();
    PIN(pv[0]); PIN(pv[1]); PIN(pv[2]); PIN(pv[3]);
#define PEMPTY() __any(pv[0][1] == 0xffffffffu || pv[1][1] == 0xffffffffu || pv[2][1] == 0xffffffffu || pv[3][1] == 0xffffffffu)
    if (__builtin_expect(!hsf || PEMPTY(), 0)) {
        const unsigned long long t0 = __builtin_amdgcn_s_memrealtime();
        if (wid == 7) {
            while (!hs) {
                ev = __hip_atomic_load(XE + j * 16 + (lane & 15), RLX_AGENT);
                asm volatile("s_waitcnt vmcnt(0)" ::: "memory");
                hs = __all(ev == nonce);
                nn = __hip_atomic_load(XN + (lane & 15), RLX_AGENT);
                if (nn != echoed) { echoed = nn; if (lane < 16) __hip_atomic_store(XE + lane * 16 + j, nn, RLX_AGENT); }
                if (__builtin_amdgcn_s_memrealtime() - t0 > 5000000ull) break;
            }
            *HS = 1;
        }
        while (*HS == 0 && __builtin_amdgcn_s_memrealtime() - t0 < 6000000ull) __builtin_amdgcn_s_sleep(2);
        do {
            PLOAD(); wait_vm<0>(); PIN(pv[0]); PIN(pv[1]); PIN(pv[2]); PIN(pv[3]);
        } while (PEMPTY() && __builtin_amdgcn_s_memrealtime() - t0 < 6000000ull);
    }
    {
        float a = (__uint_as_float(pv[0][0]) + __uint_as_float(pv[1][0])) + (__uint_as_float(pv[2][0]) + __uint_as_float(pv[3][0]));
        float b = (__uint_as_float(pv[0][1]) + __uint_as_float(pv[1][1])) + (__uint_as_float(pv[2][1]) + __uint_as_float(pv[3][1]));
        auto a1 = __builtin_amdgcn_permlane16_swap(__float_as_uint(a), __float_as_uint(a), false, false); a = __uint_as_float(a1[0]) + __uint_as_float(a1[1]);
        auto b1 = __builtin_amdgcn_permlane16_swap(__float_as_uint(b), __float_as_uint(b), false, false); b = __uint_as_float(b1[0]) + __uint_as_float(b1[1]);
        auto a2 = __builtin_amdgcn_permlane32_swap(__float_as_uint(a), __float_as_uint(a), false, false); a = __uint_as_float(a2[0]) + __uint_as_float(a2[1]);
        auto b2 = __builtin_amdgcn_permlane32_swap(__float_as_uint(b), __float_as_uint(b), false, false); b = __uint_as_float(b2[0]) + __uint_as_float(b2[1]);
        float m = a * (1.0f / FH), var = fmaxf(b * (1.0f / FH) - m * m, 0.f), r = __builtin_amdgcn_rsqf(var + 1e-5f);
        if (lane < 16) ZS[wid * 16 + lane] = f32x2{r, -m * r};
    }
    asm volatile("s_waitcnt lgkmcnt(0)\n\ts_barrier" ::: "memory");
    STAMP(3, 2);
    auto merge = [&](u32x2 A, u32x2 B) {
        auto r0 = __builtin_amdgcn_permlane16_swap(A[0], B[0], false, false); auto r1 = __builtin_amdgcn_permlane16_swap(A[1], B[1], false, false);
        return u32x4{r0[0], r1[0], r0[1], r1[1]};
    };
    auto pk4 = [&](f32x4 v) { return u32x2{cvtpk(v[0], v[1]), cvtpk(v[2], v[3])}; };
    {
        char* z2b = ws + WS_Z2T + (size_t)(j * 32 + 2 * pm + wr) * 18432;
        f32x2 zs[5];
#pragma unroll
        for (int m = 0; m < 5; ++m) zs[m] = ZS[wr * 64 + (m < 4 ? m : wc) * 16 + r16];
#pragma unroll
        for (int n = 0; n < 2; ++n)
#pragma unroll
            for (int m = 0; m < 4; m += 2) {
                u32x4 w = merge(pk4(acc1[m][n] * zs[m][0] + zs[m][1]), pk4(acc1[m + 1][n] * zs[m + 1][0] + zs[m + 1][1]));
                const int r64 = (m + (g & 1)) * 16 + r16, kb = r64 >> 3;
                st16(z2b + (kb * 9 + NF1(n)) * 256 + (((r64 & 7) ^ (4 * (kb & 1))) * 32) + (g >> 1) * 16, w);
            }
        const int r64 = wc * 16 + r16, kb = r64 >> 3;
        st8(z2b + (kb * 9 + 8) * 256 + (((r64 & 7) ^ (4 * (kb & 1))) * 32) + g * 8, pk4(sacc * zs[4][0] + zs[4][1]));
    }
    char* z1b = ws + WS_Z1 + (size_t)(pm * 16 + j) * 36864;
    auto gel = [&](f32x4 a, f32x4 c, f32x4 rr) {
        f32x4 y = a - c * rr[0];
        f32x2 y0 = gelu_y2(f32x2{y[0], y[1]}, rr[1], rr[2], rr[3]), y1 = gelu_y2(f32x2{y[2], y[3]}, rr[1], rr[2], rr[3]);
        return u32x2{cvtpk(y0[0], y0[1]), cvtpk(y1[0], y1[1])};
    };
#pragma unroll
    for (int n = 0; n < 3; ++n) cs1[n] = *(const f32x4*)(CS + 192 + NF2(n) * 16 + 4 * g);
#pragma unroll
    for (int m = 0; m < 4; ++m) er[m] = RS[wr * 64 + m * 16 + r16];
#pragma unroll
    for (int m = 0; m < 4; ++m)
        st16(z1b + (2 * (2 * cg + ((g & 1) ^ wr)) + (g >> 1)) * 2048 + (wr * 64 + m * 16 + r16) * 16, merge(gel(acc2[m][1], cs1[1], er[m]), gel(acc2[m][2], cs1[2], er[m])));
    st8(z1b + (16 + (g >> 1)) * 2048 + (wr * 64 + wc * 16 + r16) * 16 + (g & 1) * 8, gel(HX[wc * 64], *(const f32x4*)(CS + 192 + 128 + 4 * g), RS[wr * 64 + wc * 16 + r16]));
    wait_vm<10>();
    if (cg == 3) {
    } else if (nf3 >= 10 || j < 4) {
        u16* qp = (u16*)(ws + WS_Q) + (size_t)(pm * 128 + wr * 64 + r16) * D_ + (nf3 >= 10 ? 32 * j + (nf3 - 10) * 16 : 512 + 16 * j) + 4 * g;
#pragma unroll
        for (int m = 0; m < 4; ++m) *(u32x2*)(qp + m * 16 * D_) = (pk4((acc2[m][0] - cs1[0] * er[m][0]) * (er[m][1] * (QSCALE / c0))));
    } else if (j < 7) {
#pragma unroll
        for (int m = 0; m < 4; ++m)
            kv_store(ws + WS_KV + (size_t)(pm * 4 + wr * 2 + (m >> 1)) * 3072, m & 1, j, r16, g, (acc2[m][0] - cs1[0] * er[m][0]) * (er[m][1] * (1.0f / c0)));
    }
    STAMP_DRAIN(); STAMP(0, 3);
}

constexpr float ATT_BIAS = 72.0f;
DI float xhalf_max(float m) { auto rr = __builtin_amdgcn_permlane32_swap(__float_as_uint(m), __float_as_uint(m), false, false); return fmaxf(__uint_as_float(rr[0]), __uint_as_float(rr[1])); }
DI float lo_bcast(float m) { auto rr = __builtin_amdgcn_permlane32_swap(__float_as_uint(m), __float_as_uint(m), false, false); return __uint_as_float(rr[0]); }
DI float bf_round(float x) { return __uint_as_float(cvtpk(x, 0.f) << 16); }
DI float max16(const f32x16& s) {
    float a = fmaxf(fmaxf(s[0], s[1]), s[2]), b = fmaxf(fmaxf(s[3], s[4]), s[5]);
    a = fmaxf(fmaxf(a, s[6]), s[7]); b = fmaxf(fmaxf(b, s[8]), s[9]); a = fmaxf(fmaxf(a, s[10]), s[11]); b = fmaxf(fmaxf(b, s[12]), s[13]);
    a = fmaxf(fmaxf(a, s[14]), s[15]); return fmaxf(a, b);
}
DI void exp_pack(const f32x16& s, bf16x8& p0, bf16x8& p1) {
    float e[16];
#pragma unroll
    for (int i = 0; i < 16; ++i) e[i] = __builtin_amdgcn_exp2f(s[i]);
    u32x4 a = {cvtpk(e[0], e[1]), cvtpk(e[2], e[3]), cvtpk(e[4], e[5]), cvtpk(e[6], e[7])};
    u32x4 b = {cvtpk(e[8], e[9]), cvtpk(e[10], e[11]), cvtpk(e[12], e[13]), cvtpk(e[14], e[15])};
    p0 = __builtin_bit_cast(bf16x8, a); p1 = __builtin_bit_cast(bf16x8, b);
}

struct KVF { bf16x8 k1, k2, v0, v1; };
DI void kv_read(KVF& f, const LAS char* tb, int lane, int k2o, int vo) {
    f.k1 = *(const LAS bf16x8*)(tb + lane * 16); f.k2 = *(const LAS bf16x8*)(tb + k2o);
    f.v0 = *(const LAS bf16x8*)(tb + 1024 + vo); f.v1 = *(const LAS bf16x8*)(tb + 2048 + vo);
}
#define k2frag(x) (x)
template <bool RD>
DI void half_step(f32x16& sx, bf16x8& px0, bf16x8& px1, f32x16& sy, const KVF kq, const bf16x8& qy1, const bf16x8& qy2,
                  f32x16& oy, const KVF kv, const bf16x8& py0, const bf16x8& py1, KVF& ld, const LAS char* tb, int lane, int k2o, int vo) {
    const f32x16 zero = {};
    unsigned a0, a1, a2, a3, b0, b1, b2, b3; float e0, e1, e2, e3;
    f32x16 t = MFMA32(kq.k1, qy1, zero); SB();
    if (RD) ld.k1 = *(const LAS bf16x8*)(tb + lane * 16);
    e0 = EX(sx[0]); e1 = EX(sx[1]); e2 = EX(sx[2]); e3 = EX(sx[3]); PIN(e0); PIN(e1); PIN(e2); PIN(e3); SB();
    sy = MFMA32(kq.k2, qy2, t); SB();
    if (RD) ld.k2 = *(const LAS bf16x8*)(tb + k2o);
    a0 = cvtpk(e0, e1); a1 = cvtpk(e2, e3); PIN(a0); PIN(a1);
    e0 = EX(sx[4]); e1 = EX(sx[5]); e2 = EX(sx[6]); e3 = EX(sx[7]); PIN(e0); PIN(e1); PIN(e2); PIN(e3); SB();
    oy = MFMA32(kv.v0, py0, oy); SB();
    if (RD) ld.v0 = *(const LAS bf16x8*)(tb + 1024 + vo);
    a2 = cvtpk(e0, e1); a3 = cvtpk(e2, e3); PIN(a2); PIN(a3);
    e0 = EX(sx[8]); e1 = EX(sx[9]); e2 = EX(sx[10]); e3 = EX(sx[11]); PIN(e0); PIN(e1); PIN(e2); PIN(e3); SB();
    oy = MFMA32(kv.v1, py1, oy); SB();
    if (RD) ld.v1 = *(const LAS bf16x8*)(tb + 2048 + vo);
    b0 = cvtpk(e0, e1); b1 = cvtpk(e2, e3); PIN(b0); PIN(b1);
    e0 = EX(sx[12]); e1 = EX(sx[13]); e2 = EX(sx[14]); e3 = EX(sx[15]);
    b2 = cvtpk(e0, e1); b3 = cvtpk(e2, e3); PIN(b2); PIN(b3); SB();
    u32x4 a = {a0, a1, a2, a3}, b = {b0, b1, b2, b3};
    px0 = __builtin_bit_cast(bf16x8, a); px1 = __builtin_bit_cast(bf16x8, b);
}
DI bf16x8 p_scale(bf16x8 p, int sh) {
    u32x4 w = __builtin_bit_cast(u32x4, p);
#pragma unroll
    for (int i = 0; i < 4; ++i) w[i] = cvtpk(__builtin_amdgcn_ldexpf(bflo(w[i]), sh), __builtin_amdgcn_ldexpf(bfhi(w[i]), sh));
    return __builtin_bit_cast(bf16x8, w);
}
template <bool PP> DI void att_rescale(f32x16& o, f32x16& s, bf16x8& p0, bf16x8& p1, float& mref, bf16x8& q2, bool& bad, int c, int h) {
    bool nf = false;
#pragma unroll
    for (int i = 0; i < 11; ++i) nf = nf || (!(fabsf(o[i]) <= 3.0e38f) && (i < 8 || h == 0));
    if (__any(nf)) { bad = true; return; }
    float lb = lo_bcast(o[10]);
    int e = __builtin_amdgcn_frexp_expf(lb);
    float d = (float)(e + (int)ATT_BIAS); d = d < 0 ? 0 : d;
    float nm = bf_round(mref + d); float dd = nm - mref; mref = nm;
    int sh = -(int)dd;
#pragma unroll
    for (int i = 0; i < 16; ++i) { o[i] = __builtin_amdgcn_ldexpf(o[i], sh); if (!PP) s[i] -= dd; }
    if (PP) { p0 = p_scale(p0, sh); p1 = p_scale(p1, sh); }
    u32x4 b = __builtin_bit_cast(u32x4, q2); b[1] = h ? 0u : (__float_as_uint(-nm) >> 16); q2 = __builtin_bit_cast(bf16x8, b);
}

#define W3LD 1
#define W3ST 3
constexpr int K2_SA = 16384, K2_SB = 18432, K2_SK = 6144, K2_B0 = 3 * K2_SA, K2_K0 = K2_B0 + 3 * K2_SB, K2_X0 = K2_K0 + 3 * K2_SK, K2_LDS = K2_X0 + 16384;
struct G2F { bf16x8 a[4], b[4], sb; };
struct G2S {
    unsigned va[4], vl, vl2;
    const char* sa; const char* sl; unsigned lstep;
    unsigned m0a, m0l[3];
    const LAS char* pa[2][2]; const LAS char* pb[2]; const LAS char* ps[2];
};
template <int KS, int GL, bool RD, int RS, int WS> DI void g2_phase(G2S& s, const G2F& f, G2F& n, f32x4 (&acc)[4][4], f32x4 (&sacc)[2]) {
    const LAS char* a0 = s.pa[0][KS] + RS * K2_SA; const LAS char* a1 = s.pa[1][KS] + RS * K2_SA;
    constexpr int BO = RS * K2_SB + KS * 9216;
    const LAS char* b0 = s.pb[0] + BO; const LAS char* b1 = s.pb[1] + BO;
#define MM(m, nn) acc[m][nn] = MFMA16(f.b[nn], f.a[m], acc[m][nn]); PIN(acc[m][nn]); SB()
    MM(0, 0); if (RD) n.b[0] = ldsB2(b0, b1); SB();
    MM(1, 0); if (RD) n.a[0] = ldsA(a0); SB();
    MM(2, 0); if (RD) n.a[1] = ldsA(a0 + 2048); SB();
    MM(3, 0); if (RD) n.a[2] = ldsA(a1); SB();
    MM(0, 1); if (RD) n.a[3] = ldsA(a1 + 2048); SB();
    MM(1, 1); if (RD) n.b[1] = ldsB2(b0 + 256, b1 + 256); SB();
    MM(2, 1); if (RD) n.b[2] = ldsB2(b0 + 512, b1 + 512); SB();
    MM(3, 1); if (RD) n.b[3] = ldsB2(b0 + 768, b1 + 768); SB();
    MM(0, 2); if (RD) n.sb = ldsB2(s.ps[0] + BO, s.ps[1] + BO); SB();
    MM(1, 2); if (GL == 1) { set_m0(s.m0a + WS * K2_SA); glds_s<0>(s.va[0], s.sa); } if (GL == 2) { glds_s<1024>(s.vl, s.sl); } SB();
    MM(2, 2);
    MM(3, 2); if (GL == 1) glds_s<1024>(s.va[1], s.sa); if (GL == 2) glds_s<2048>(s.vl, s.sl); SB();
    MM(0, 3);
    MM(1, 3); if (GL == 1) glds_s<2048>(s.va[2], s.sa); if (GL == 2) glds_s<3072>(s.vl, s.sl); SB();
    MM(2, 3);
    MM(3, 3); if (GL == 1) { glds_s<3072>(s.va[3], s.sa); s.sa += 128; asm volatile("" : "+s"(s.sa)); } if (GL == 2) { set_m0(s.m0l[WS] + 4096); glds_s<0>(s.vl2, s.sl); } SB();
    sacc[0] = MFMA16(f.sb, f.a[0], sacc[0]); PIN(sacc[0]); SB();
    if (GL == 1) { set_m0(s.m0l[WS]); glds_s<0>(s.vl, s.sl); } if (GL == 2) { glds_s<1024>(s.vl2, s.sl); s.sl += s.lstep; asm volatile("" : "+s"(s.sl)); } SB();
    sacc[1] = MFMA16(f.sb, f.a[1], sacc[1]); PIN(sacc[1]); SB();
#undef MM
}
template <int C, bool G2nd, bool G1st, int VM, bool RDN> DI void g2_iter(G2S& s, G2F& f0, G2F& f1, f32x4 (&acc)[4][4], f32x4 (&sacc)[2]) {
    g2_phase<1, G2nd ? 2 : 0, true, C, (C + 2) % 3>(s, f0, f1, acc, sacc);
    wait_vm_bar<VM>(); SB();
    g2_phase<0, G1st ? 1 : 0, RDN, (C + 1) % 3, C>(s, f1, f0, acc, sacc);
}

__global__ __launch_bounds__(512, 2) void k2_kernel(const u16* __restrict__ w_sp, const u16* __restrict__ b_sp, const u16* __restrict__ w_out, const u16* __restrict__ wo, char* __restrict__ ws) {
    extern __shared__ __attribute__((aligned(16))) char smem[];
    int bid = blockIdx.x, xcd = bid & 7, idx = bid >> 3;
    int pm = (xcd & 1) * 8 + (idx & 7), j = (xcd >> 1) * 4 + (idx >> 3);
    const int lane = threadIdx.x & 63, wid = __builtin_amdgcn_readfirstlane(threadIdx.x >> 6);
    const unsigned lds0 = (unsigned)(uintptr_t)smem; const LAS char* lds = (const LAS char*)smem;
    STAMP(1, 0);
    if (__builtin_expect(wid < 4, 1)) {
        const int wr = wid >> 1, wc = wid & 1;
        G2S s;
#pragma unroll
        for (int i = 0; i < 4; ++i) { int row = 8 * (wid * 4 + i) + (lane >> 3), c = (lane & 7) ^ ((row >> 1) & 7); s.va[i] = row * (S_ * 2) + c * 16 + 4096 - i * 1024; PIN(s.va[i]); }
        s.vl = lane * 16; s.vl2 = lane * 16 + 4096;
        unsigned ol = wid < 3 ? (unsigned)WS_Z2T + j * 32 * 18432 + wid * 6144 : (unsigned)WS_KV, oa = pm * 128 * S_ * 2;
        s.lstep = wid < 3 ? 18432 : 6144;
        s.m0a = lds0 + wid * 4096; s.m0l[0] = lds0 + (wid < 3 ? K2_B0 + wid * 6144 : K2_K0); s.m0l[1] = s.m0l[0] + s.lstep; s.m0l[2] = s.m0l[1] + s.lstep;
        unsigned vbias = (pm * 128 + wr * 64 + (lane & 15)) * 2 + wc * 64, vbias2 = vbias ^ 64;
        PIN(s.vl); PIN(s.vl2); PIN(vbias); PIN(vbias2);
        asm volatile("" : "+s"(ol), "+s"(oa), "+s"(s.m0a), "+s"(s.m0l[0]));
        SB();
        s.sa = (const char*)w_sp - 4096 + oa; s.sl = ws + ol;
        unsigned bias[4];
#pragma unroll
        for (int t = 0; t < 3; ++t) {
            set_m0(s.m0a + t * K2_SA); glds_s<0>(s.va[0], s.sa); glds_s<1024>(s.va[1], s.sa); glds_s<2048>(s.va[2], s.sa); glds_s<3072>(s.va[3], s.sa); s.sa += 128;
            set_m0(s.m0l[t]); glds_s<0>(s.vl, s.sl);
            if (t < 2) {
                glds_s<1024>(s.vl, s.sl); glds_s<2048>(s.vl, s.sl); glds_s<3072>(s.vl, s.sl);
                set_m0(s.m0l[t] + 4096); glds_s<0>(s.vl2, s.sl); glds_s<1024>(s.vl2, s.sl); s.sl += s.lstep;
            }
            if (t == 0) asm volatile("global_load_ushort %0, %4, %6\n\tglobal_load_ushort %1, %4, %6 offset:32\n\tglobal_load_ushort %2, %5, %6\n\tglobal_load_ushort %3, %5, %6 offset:32"
                                     : "=&v"(bias[0]), "=&v"(bias[1]), "=&v"(bias[2]), "=&v"(bias[3]) : "v"(vbias), "v"(vbias2), "s"(b_sp) : "memory");
        }
        {
            int r = lane & 15, g = lane >> 4, q = r >> 2, p = lane & 3;
#pragma unroll
            for (int ks = 0; ks < 2; ++ks) {
                int o = r * 128 + (((4 * ks + g) ^ (r >> 1)) * 16) + wr * 8192;
                s.pa[0][ks] = lds + o + wc * 4096; s.pa[1][ks] = lds + o + (1 - wc) * 4096; PIN(s.pa[0][ks]); PIN(s.pa[1][ks]);
            }
            int ob = K2_B0 + g * 2304 + q * 32 + 8 * p;
            s.pb[0] = lds + ob + wc * 1024 + ((g & 1) * 128); s.pb[1] = lds + ob + wc * 1024 + (((g & 1) ^ 1) * 128);
            s.ps[0] = lds + ob + 2048 + ((g & 1) * 128); s.ps[1] = lds + ob + 2048 + (((g & 1) ^ 1) * 128);
            PIN(s.pb[0]); PIN(s.pb[1]); PIN(s.ps[0]); PIN(s.ps[1]);
        }
        G2F f0, f1;
        SB();
        wait_vm_bar<15>();
        STAMP(1, 1);
        f0.b[0] = ldsB2(s.pb[0], s.pb[1]);
        f0.a[0] = ldsA(s.pa[0][0]); f0.a[1] = ldsA(s.pa[0][0] + 2048); f0.a[2] = ldsA(s.pa[1][0]); f0.a[3] = ldsA(s.pa[1][0] + 2048);
        f0.b[1] = ldsB2(s.pb[0] + 256, s.pb[1] + 256); f0.b[2] = ldsB2(s.pb[0] + 512, s.pb[1] + 512); f0.b[3] = ldsB2(s.pb[0] + 768, s.pb[1] + 768); f0.sb = ldsB2(s.ps[0], s.ps[1]);
        f32x4 acc[4][4], sacc[2];
#pragma unroll
        for (int m = 0; m < 4; ++m) {
            PIN(bias[m]); const float b = __uint_as_float(bias[m] << 16);
#pragma unroll
            for (int n = 0; n < 4; ++n) acc[m][n] = f32x4{b, b, b, b};
            if (m < 2) sacc[m] = f32x4{b, b, b, b};
        }
#pragma unroll 1
        for (int t = 0; t < 9; ++t) {
            g2_iter<0, true, true, 10, true>(s, f0, f1, acc, sacc); g2_iter<1, true, true, 10, true>(s, f0, f1, acc, sacc); g2_iter<2, true, true, 10, true>(s, f0, f1, acc, sacc);
        }
        g2_iter<0, true, true, 10, true>(s, f0, f1, acc, sacc); g2_iter<1, true, true, 10, true>(s, f0, f1, acc, sacc);
        g2_iter<2, true, false, 10, true>(s, f0, f1, acc, sacc);
        const int c = lane & 15, g = lane >> 4;
        const char* z1b = ws + WS_Z1 + (size_t)(pm * 16 + j) * 36864 + (g >> 1) * 2048 + (wr * 64 + c) * 16 + (g & 1) * 8;
        u32x2 z1r[4][4], z1s[2];
#pragma unroll
        for (int m = 0; m < 4; ++m) {
            const int mo = (m ^ (2 * wc)) * 16;
#pragma unroll
            for (int n = 0; n < 4; ++n) z1r[m][n] = *(const u32x2*)(z1b + (wc * 4 + n) * 4096 + mo * 16);
            if (m < 2) z1s[m] = *(const u32x2*)(z1b + 8 * 4096 + mo * 16);
        }
        SB();
        g2_iter<0, false, false, 18, true>(s, f0, f1, acc, sacc);
#pragma unroll
        for (int m = 0; m < 4; ++m) { PIN(z1r[m][0]); PIN(z1r[m][1]); PIN(z1r[m][2]); PIN(z1r[m][3]); }
        PIN(z1s[0]); PIN(z1s[1]); SB();
        g2_iter<1, false, false, 63, false>(s, f0, f1, acc, sacc);
        __builtin_amdgcn_s_setprio(2);
        STAMP(1, 2);
        auto fin = [&](f32x4 v, u32x2 z) { return u32x2{cvtpk(bflo(z[0]) * v[0], bfhi(z[0]) * v[1]), cvtpk(bflo(z[1]) * v[2], bfhi(z[1]) * v[3])}; };
        auto put2 = [&](u32x2 A, u32x2 B, int rowA, int nfA, int rowB, int nfB) {
            auto r0 = __builtin_amdgcn_permlane16_swap(A[0], B[0], false, false); auto r1 = __builtin_amdgcn_permlane16_swap(A[1], B[1], false, false);
            const int row = (g & 1) ? rowB : rowA, cgl = 18 * j + 2 * ((g & 1) ? nfB : nfA) + (g >> 1);
            char* p = ws + WS_UO + ((size_t)((row >> 6) * 90 + (cgl >> 2)) * 4 + ((row >> 4) & 3)) * 1024 + (cgl & 3) * 256 + (row & 15) * 16;
            st16(p, u32x4{r0[0], r1[0], r0[1], r1[1]});
        };
#pragma unroll
        for (int m = 0; m < 4; ++m) {
            const int row = pm * 128 + wr * 64 + (m ^ (2 * wc)) * 16 + c;
#pragma unroll
            for (int n = 0; n < 4; n += 2) put2(fin(acc[m][n], z1r[m][n]), fin(acc[m][n + 1], z1r[m][n + 1]), row, wc * 4 + n, row, wc * 4 + n + 1);
        }
        { const int row = pm * 128 + wr * 64 + (2 * wc) * 16 + c; put2(fin(sacc[0], z1s[0]), fin(sacc[1], z1s[1]), row, 8, row + 16, 8); }
    } else {
        __builtin_amdgcn_s_setprio(1);
        const int c = lane & 31, h = lane >> 5, aw = wid - 4;
        const char* kvg = ws + WS_KV;
        const int row0 = blockIdx.x * 8 + aw * 2;
        bf16x8 q1[2], q2[2]; float mref[2]; unsigned qe[2];
        const f32x16 zero = {};
#pragma unroll
        for (int t = 0; t < 2; ++t) {
            const u16* qr = (const u16*)(ws + WS_Q) + (size_t)(row0 + t) * D_ + 18 * c;
            u32x4 a; const unsigned* qp = (const unsigned*)(qr + 8 * h);
            a[0] = qp[0]; a[1] = qp[1]; a[2] = qp[2]; a[3] = qp[3];
            q1[t] = __builtin_bit_cast(bf16x8, a);
            qe[t] = *(const unsigned*)(qr + 16);
        }
        SB();
        f32x16 oA = zero, oB = zero;
        bool bad = false;
        const int k2o = k2slot(c), vo = (c < 19 ? lane : 63) * 16;
        const LAS char* kvl = lds + K2_K0;
        unsigned w3o[4]; const u16* w3p[4];
#pragma unroll
        for (int i = 0; i < 4; ++i) {
            int ln = ((threadIdx.x - 256) >> 3) + 32 * i; ln = bid * 102 + (ln < 102 ? ln : 101); ln = ln < 25920 ? ln : 25919;
            int gi = ln * 8 + (threadIdx.x & 7), kk = gi / 9, c9 = gi - kk * 9, jj = kk / 2880, k = kk - jj * 2880;
            w3p[i] = (k < FH ? w_out + (size_t)k * D_ : wo + (size_t)(k - FH) * D_) + jj * 72 + c9 * 8;
            w3o[i] = gi * 16; PIN(w3o[i]); PIN(w3p[i]);
        }
        SB();
#pragma unroll
        for (int t = 0; t < 2; ++t) { PIN(qe[t]); u32x4 b = {h ? 0u : qe[t], 0u, 0u, 0u}; q2[t] = __builtin_bit_cast(bf16x8, b); }
        asm volatile("s_waitcnt vmcnt(0)\n\ts_barrier" ::: "memory");
        KVF X, Y;
        kv_read(X, kvl, lane, k2o, vo); kv_read(Y, kvl + 3072, lane, k2o, vo);
#pragma unroll
        for (int t = 0; t < 2; ++t) {
            f32x16 s0 = MFMA32(X.k1, q1[t], zero); s0 = MFMA32(X.k2, q2[t], s0);
            f32x16 s1 = MFMA32(Y.k1, q1[t], zero); s1 = MFMA32(Y.k2, q2[t], s1);
            float m = xhalf_max(fmaxf(max16(s0), max16(s1)));
            mref[t] = bf_round(rintf(m) + ATT_BIAS);
            u32x4 b = __builtin_bit_cast(u32x4, q2[t]); b[1] = h ? 0u : (__float_as_uint(-mref[t]) >> 16); q2[t] = __builtin_bit_cast(bf16x8, b);
        }
        f32x16 sA = MFMA32(X.k1, q1[0], zero); sA = MFMA32(X.k2, q2[0], sA);
        f32x16 sB = zero;
        bf16x8 pA0, pA1, pB0 = {}, pB1 = {};
        half_step<false>(sA, pA0, pA1, sB, X, q1[1], q2[1], oB, X, pB0, pB1, Y, kvl, lane, k2o, vo);
        half_step<false>(sB, pB0, pB1, sA, Y, q1[0], q2[0], oA, X, pA0, pA1, Y, kvl, lane, k2o, vo);
        asm volatile("s_waitcnt lgkmcnt(0)\n\ts_barrier" ::: "memory");
#define A_IV(sl, CHK) { const LAS char* tb = kvl + (sl) * K2_SK; \
            half_step<true>(sA, pA0, pA1, sB, Y, q1[1], q2[1], oB, X, pB0, pB1, X, tb, lane, k2o, vo); \
            half_step<false>(sB, pB0, pB1, sA, X, q1[0], q2[0], oA, Y, pA0, pA1, X, tb, lane, k2o, vo); \
            if (CHK) { mB = __builtin_amdgcn_ballot_w64(!(oB[10] <= 0x1p20f)); job >>= 1; }                  \
            half_step<true>(sA, pA0, pA1, sB, X, q1[1], q2[1], oB, Y, pB0, pB1, Y, tb + 3072, lane, k2o, vo); \
            if (CHK) { mA = __builtin_amdgcn_ballot_w64(!(oA[10] <= 0x1p20f)); any = mA | mB | (job & 1); asm volatile("" : "+s"(any)); } \
            half_step<false>(sB, pB0, pB1, sA, Y, q1[0], q2[0], oA, X, pA0, pA1, Y, tb, lane, k2o, vo); }
#define A_BAR() asm volatile("s_waitcnt lgkmcnt(0)\n\ts_barrier" ::: "memory")
        unsigned long long job = (2ull << W3LD) | (2ull << W3ST), mA, mB, any;
#pragma unroll 1
        for (int t = 0; t < 10; ++t) {
            A_IV(1, false); A_BAR();
            A_IV(2, false); A_BAR();
            A_IV(0, true);
            if (__builtin_expect(any != 0, 0)) {
                if (mA) att_rescale<false>(oA, sA, pA0, pA1, mref[0], q2[0], bad, c, h);
                if (mB) att_rescale<true>(oB, sB, pB0, pB1, mref[1], q2[1], bad, c, h);
                if (t == W3LD) {
#pragma unroll
                    for (int i = 0; i < 4; ++i) glds_v((const char*)w3p[i], lds0 + K2_X0 + aw * 4096 + i * 1024);
                }
                if (t == W3ST) {
                    wait_vm<0>();
                    u32x4 w3v[4];
#pragma unroll
                    for (int i = 0; i < 4; ++i) w3v[i] = *(const LAS u32x4*)(lds + K2_X0 + aw * 4096 + i * 1024 + lane * 16);
#pragma unroll
                    for (int i = 0; i < 4; ++i) st16(ws + WS_W3T + w3o[i], w3v[i]);
                }
            }
            A_BAR();
        }
        A_IV(1, false); A_BAR();
        half_step<false>(sA, pA0, pA1, sB, Y, q1[1], q2[1], oB, X, pB0, pB1, X, kvl, lane, k2o, vo);
        exp_pack(sB, pB0, pB1);
        oA = MFMA32(Y.v0, pA0, oA); oA = MFMA32(Y.v1, pA1, oA);
        oB = MFMA32(Y.v0, pB0, oB); oB = MFMA32(Y.v1, pB1, oB);
        {
            float sa_ = fabsf(oA[10]) * 0x1p27f, sb_ = fabsf(oB[10]) * 0x1p27f;
#pragma unroll
            for (int i = 0; i < 10; ++i) { sa_ += fabsf(oA[i]); sb_ += fabsf(oB[i]); }
            if (__builtin_amdgcn_ballot_w64(!(sa_ + sb_ <= 3.0e38f))) bad = true;
        }
        f32x16 o[2] = {oA, oB};
        if (__builtin_expect(bad, 0)) {
#pragma unroll 1
            for (int t = 0; t < 2; ++t) {
                bf16x8 qa = t ? q1[1] : q1[0]; u32x4 qb = __builtin_bit_cast(u32x4, t ? q2[1] : q2[0]); qb[1] = 0;
                f32x16 oo = zero; float m = -3.0e38f;
                for (int kt = 0; kt < 64; ++kt) {
                    const char* tb = kvg + (size_t)kt * 3072;
                    bf16x8 k1 = *(const bf16x8*)(tb + lane * 16);
                    u32x4 k2v = *(const u32x4*)(tb + k2o);
                    bf16x8 v0 = *(const bf16x8*)(tb + 1024 + vo), v1 = *(const bf16x8*)(tb + 2048 + vo);
                    f32x16 s = MFMA32(k1, qa, zero); s = MFMA32(__builtin_bit_cast(bf16x8, k2v), __builtin_bit_cast(bf16x8, qb), s);
                    float nm = fmaxf(m, xhalf_max(max16(s)));
                    float sc = __builtin_amdgcn_exp2f(m - nm); m = nm;
#pragma unroll
                    for (int i = 0; i < 16; ++i) { oo[i] *= sc; s[i] -= nm; }
                    bf16x8 p0, p1; exp_pack(s, p0, p1);
                    oo = MFMA32(v0, p0, oo); oo = MFMA32(v1, p1, oo);
                }
                if (t) o[1] = oo; else o[0] = oo;
            }
        }
#pragma unroll
        for (int t = 0; t < 2; ++t) {
            float inv = __builtin_amdgcn_rcpf(lo_bcast(o[t][10]));
            LAS unsigned* ob = (LAS unsigned*)(lds + K2_X0 + (aw * 2 + t) * 1168 + 36 * c + 8 * h);
            ob[0] = cvtpk(o[t][0] * inv, o[t][1] * inv); ob[1] = cvtpk(o[t][2] * inv, o[t][3] * inv);
            ob[4] = cvtpk(o[t][4] * inv, o[t][5] * inv); ob[5] = cvtpk(o[t][6] * inv, o[t][7] * inv);
            if (h == 0) ob[8] = cvtpk(o[t][8] * inv, o[t][9] * inv);
        }
    }
    asm volatile("s_waitcnt lgkmcnt(0)\n\ts_barrier" ::: "memory");
    {
        const int orow = blockIdx.x * 8;
        char* ob = ws + WS_UO + ((size_t)((orow >> 6) * 90 + 72) * 4 + ((orow >> 4) & 3)) * 1024 + (orow & 15) * 16;
#pragma unroll
        for (int r = 0; r < 2; ++r) {
            const int i = threadIdx.x + 512 * r, ck = i >> 3, row = i & 7;
            if (r == 0 || wid == 0) st16(ob + (ck >> 2) * 4096 + (ck & 3) * 256 + row * 16, *(const LAS u32x4*)(lds + K2_X0 + row * 1168 + ck * 16));
        }
    }
    STAMP_DRAIN(); STAMP(1, 3);
}

constexpr int K3_SLOT = 8704, K3_NS = 4, K3_GW = K3_NS * K3_SLOT, K3_LDS = 4 * K3_GW + 64;
DI void k3_issue(unsigned wm0, unsigned va, const char* sa, const char* sb) {
    set_m0(wm0); glds_s<0>(va, sa); glds_s<1024>(va, sa); glds_s<2048>(va, sa); glds_s<3072>(va, sa);
    set_m0(wm0 + 4096); glds_s<0>(va, sb); glds_s<1024>(va, sb); glds_s<2048>(va, sb); glds_s<3072>(va, sb);
    asm volatile("s_mov_b32 m0, %2\n\ts_mov_b32 exec_hi, 0\n\tglobal_load_lds_dwordx4 %0, %1\n\ts_mov_b32 exec_hi, -1" ::"v"(va + 4096), "s"(sb), "s"(wm0 + 8192));
}
template <bool GL, int VM> DI void k3_step(const LAS char* rd, unsigned wm0, unsigned va, const char* sa, const char* sb, unsigned pb, f32x4 (&acc)[4][5]) {
    wait_vm<VM>();
    bf16x8 a[4], b[5];
#pragma unroll
    for (int m = 0; m < 4; ++m) a[m] = ldsA(rd + va + m * 1024);
#pragma unroll
    for (int n = 0; n < 5; ++n) b[n] = ldsB2(rd + pb + n * 32, rd + pb + n * 32 + 576);
    asm volatile("s_waitcnt lgkmcnt(0)" ::: "memory"); SB();
    if (GL) k3_issue(wm0, va, sa, sb);
    SB();
#pragma unroll
    for (int n = 0; n < 5; ++n)
#pragma unroll
        for (int m = 0; m < 4; ++m) acc[m][n] = MFMA16(b[n], a[m], acc[m][n]);
    SB();
}
__global__ __launch_bounds__(256, 1) void k3_kernel(const u16* __restrict__ x, char* __restrict__ ws, u16* __restrict__ out) {
    extern __shared__ __attribute__((aligned(16))) char smem[];
    const int bid = blockIdx.x, xcd = bid & 7, idx = bid >> 3;
    const int rb = (xcd & 3) * 8 + (idx & 7), j = (xcd >> 2) * 4 + (idx >> 3);
    const int lane = threadIdx.x & 63, wid = __builtin_amdgcn_readfirstlane(threadIdx.x >> 6);
    const unsigned lds0 = (unsigned)(uintptr_t)smem;
    const unsigned va = lane * 16;
    constexpr int EP = 84;
    STAMP(2, 0);
    const LAS char* lds = (const LAS char*)smem + wid * K3_GW;
    const char* ua = ws + WS_UO + (size_t)rb * 90 * 4096 + wid * 4096;
    const char* wb = ws + WS_W3T + (size_t)j * 90 * 4608 + wid * 4608;
    const unsigned wm = lds0 + wid * K3_GW;
#pragma unroll
    for (int i = 0; i < 4; ++i) k3_issue(wm + i * K3_SLOT, va, ua + i * 16384, wb + i * 18432);
    const int g = lane >> 4, q = (lane & 15) >> 2, p = lane & 3;
    const unsigned pb = 4096 + (8 * g + q) * 144 + 8 * p;
    const int n = wid < 2 ? 23 : 22;
    f32x4 acc[4][5] = {};
    STAMP(2, 1);
    unsigned slot = 0;
#define K3_ROT() slot = slot == 3 * K3_SLOT ? 0 : slot + K3_SLOT
    for (int i = 4; i < n; ++i) { k3_step<true, 27>(lds + slot, wm + slot, va, ua + i * 16384, wb + i * 18432, pb, acc); K3_ROT(); }
    u32x4 xv[3];
#pragma unroll
    for (int i = 0; i < 3; ++i) { int ci = threadIdx.x + 256 * i; if (ci >= 576) ci = 575; xv[i] = *(const u32x4*)(x + (size_t)(rb * 64 + ci / 9) * D_ + 72 * j + (ci % 9) * 8); }
    SB();
    k3_step<false, 27>(lds + slot, 0, va, ua, ua, pb, acc); K3_ROT();
    k3_step<false, 18>(lds + slot, 0, va, ua, ua, pb, acc); K3_ROT();
    k3_step<false, 9>(lds + slot, 0, va, ua, ua, pb, acc); K3_ROT();
    k3_step<false, 0>(lds + slot, 0, va, ua, ua, pb, acc);
    STAMP(2, 2);
    float* et = (float*)(smem + wid * K3_GW);
#pragma unroll
    for (int m = 0; m < 4; ++m)
#pragma unroll
        for (int nn = 0; nn < 5; ++nn) *(f32x4*)(et + (m * 16 + (lane & 15)) * EP + nn * 16 + 4 * g) = acc[m][nn];
    asm volatile("s_waitcnt lgkmcnt(0)\n\ts_barrier" ::: "memory");
#pragma unroll
    for (int i = 0; i < 3; ++i) {
        int ci = threadIdx.x + 256 * i;
        if (ci < 576) {
            int row = ci / 9, ch = ci % 9;
            f32x4 s0 = {bflo(xv[i][0]), bfhi(xv[i][0]), bflo(xv[i][1]), bfhi(xv[i][1])}, s1 = {bflo(xv[i][2]), bfhi(xv[i][2]), bflo(xv[i][3]), bfhi(xv[i][3])};
#pragma unroll
            for (int w = 0; w < 4; ++w) {
                const float* e = (const float*)(smem + w * K3_GW) + row * EP + ch * 8;
                s0 += *(const f32x4*)e; s1 += *(const f32x4*)(e + 4);
            }
            u32x4 o = {cvtpk(s0[0], s0[1]), cvtpk(s0[2], s0[3]), cvtpk(s1[0], s1[1]), cvtpk(s1[2], s1[3])};
            *(u32x4*)(out + (size_t)(rb * 64 + row) * D_ + 72 * j + ch * 8) = o;
        }
    }
    STAMP_DRAIN(); STAMP(2, 3);
    asm volatile(".Lk3_end:");
}

extern "C" void kernel_launch(void* const* d_in, const int* in_sizes, int n_in, void* d_out, int out_size, void* d_ws, size_t ws_size, hipStream_t stream) {
    const u16* x = (const u16*)d_in[0]; const u16* wq = (const u16*)d_in[1]; const u16* wk = (const u16*)d_in[2]; const u16* wv = (const u16*)d_in[3];
    const u16* wo = (const u16*)d_in[4]; const u16* w_in = (const u16*)d_in[6]; const u16* w_sp = (const u16*)d_in[7]; const u16* b_sp = (const u16*)d_in[8];
    const u16* w_out = (const u16*)d_in[9]; char* ws = (char*)d_ws;
    (void)hipFuncSetAttribute((const void*)k1_kernel, hipFuncAttributeMaxDynamicSharedMemorySize, K1_LDS);
    (void)hipFuncSetAttribute((const void*)k2_kernel, hipFuncAttributeMaxDynamicSharedMemorySize, K2_LDS);
    (void)hipFuncSetAttribute((const void*)k3_kernel, hipFuncAttributeMaxDynamicSharedMemorySize, K3_LDS);
    k1_kernel<<<256, 512, K1_LDS, stream>>>(x, wq, wk, wv, w_in, w_sp, b_sp, w_out, wo, ws);
    k2_kernel<<<256, 512, K2_LDS, stream>>>(w_sp, b_sp, w_out, wo, ws);
    k3_kernel<<<256, 256, K3_LDS, stream>>>(x, ws, (u16*)d_out);
}
```

```cpp
#include <hip/hip_runtime.h>
#include <hip/hip_bf16.h>
#include <stdint.h>

typedef unsigned short u16;
typedef unsigned long long u64;
typedef short bf16x8 __attribute__((ext_vector_type(8)));
typedef short s16x4 __attribute__((ext_vector_type(4)));
typedef float f32x2 __attribute__((ext_vector_type(2)));
typedef float f32x4 __attribute__((ext_vector_type(4)));
typedef float f32x16 __attribute__((ext_vector_type(16)));
typedef unsigned u32x4 __attribute__((ext_vector_type(4)));
typedef unsigned u32x2 __attribute__((ext_vector_type(2)));
typedef __bf16 bf16x2_t __attribute__((ext_vector_type(2)));
#define LAS __attribute__((address_space(3)))
#define DI __device__ __forceinline__

constexpr int Z1L = 122880, TOPL = 159744, LDS_ALL = 163840;
constexpr int K2_SA = 16384, K2_SB = 18432, K2_SK = 6144, K2_B0 = 3 * K2_SA, K2_K0 = K2_B0 + 3 * K2_SB, K2_X0 = 147520, K2_LDS = K2_X0 + 9344 + 64 + 2048;
constexpr int S_ = 2048, D_ = 576, FH = 2304, F_ = 4608;

constexpr size_t WS_Q = 0;
constexpr size_t WS_KV = WS_Q + (size_t)S_ * D_ * 2;
constexpr size_t WS_Z1 = WS_KV + 64 * 3072;
constexpr size_t WS_Z2T = WS_Z1 + (size_t)S_ * FH * 2;
constexpr size_t WS_UO = WS_Z2T + (size_t)S_ * FH * 2;
constexpr size_t WS_ST = WS_UO + (size_t)S_ * 2880 * 2;
constexpr size_t WS_XCH = WS_ST + (size_t)S_ * 16 * 8;
constexpr size_t WS_XR = WS_XCH + 4096 + 16384;
constexpr size_t WS_FL = WS_XR + 4096;
constexpr size_t WS_FA = WS_FL + 16384;
constexpr size_t WS_FJ = WS_FA + 2048;
constexpr size_t WS_FP = WS_FJ + 8192;
constexpr size_t WS_W3T = WS_FP + 2048;
constexpr size_t WS_DBG = 128u << 20;
#define STAMP(kid, i)
#define STAMP_DRAIN()
#define STAMPT(tid, kid, i)

DI unsigned cvtpk(float lo, float hi) { f32x2 v = {lo, hi}; return __builtin_bit_cast(unsigned, __builtin_convertvector(v, bf16x2_t)); }
DI float bf2f(u16 b) { return __uint_as_float(((unsigned)b) << 16); }
DI float bflo(unsigned w) { return __uint_as_float(w << 16); }
DI float bfhi(unsigned w) { return __uint_as_float(w & 0xffff0000u); }
#define SB() __builtin_amdgcn_sched_barrier(0)
#define PIN(x) asm volatile("" : "+v"(x))
#define EX(x) __builtin_amdgcn_exp2f(x)
#define MFMA16(a, b, c) __builtin_amdgcn_mfma_f32_16x16x32_bf16(a, b, c, 0, 0, 0)
#define MFMA32(a, b, c) __builtin_amdgcn_mfma_f32_32x32x16_bf16(a, b, c, 0, 0, 0)

DI s16x4 trread(const LAS char* p) { return __builtin_bit_cast(s16x4, __builtin_amdgcn_ds_read_tr16_b64_v4i16((LAS s16x4*)p)); }
template <int N> DI void wait_vm_bar() { asm volatile("s_waitcnt vmcnt(%0) lgkmcnt(0)\n\ts_barrier" ::"n"(N) : "memory"); }
template <int N> DI void wait_vm() { asm volatile("s_waitcnt vmcnt(%0)" ::"n"(N) : "memory"); }
template <int OFF> DI void glds_s(unsigned voff, const char* sbase) { asm volatile("global_load_lds_dwordx4 %0, %1 offset:%2" ::"v"(voff), "s"(sbase), "n"(OFF)); }
template <int OFF> DI void glds_sc1(unsigned voff, const char* sbase) { asm volatile("global_load_lds_dwordx4 %0, %1 offset:%2 sc1" ::"v"(voff), "s"(sbase), "n"(OFF)); }
DI void glds_v(const char* p, unsigned m0v) { asm volatile("s_mov_b32 m0, %1\n\ts_nop 0\n\tglobal_load_lds_dwordx4 %0, off" ::"v"(p), "s"(m0v)); }
DI unsigned ld_sc1_async(const char* sb, unsigned vo) { unsigned v; asm volatile("global_load_dword %0, %1, %2 sc1" : "=v"(v) : "v"(vo), "s"(sb) : "memory"); return v; }
#define WT
DI void st16(void* p, u32x4 v) { asm volatile("global_store_dwordx4 %0, %1, off sc1\n\ts_nop 1" :: "v"(p), "v"(v) : "memory"); }
DI void st8(void* p, u32x2 v) { asm volatile("global_store_dwordx2 %0, %1, off sc1" :: "v"(p), "v"(v) : "memory"); }
template <int OFF> DI void st16s(const char* sb, unsigned vo, u32x4 v) { asm volatile("global_store_dwordx4 %0, %1, %2 offset:%3 sc1\n\ts_nop 1" :: "v"(vo), "v"(v), "s"(sb), "n"(OFF) : "memory"); }
template <int OFF> DI void st8s(const char* sb, unsigned vo, u32x2 v) { asm volatile("global_store_dwordx2 %0, %1, %2 offset:%3 sc1" :: "v"(vo), "v"(v), "s"(sb), "n"(OFF) : "memory"); }
DI void set_m0(unsigned v) { asm volatile("s_mov_b32 m0, %0\n\ts_nop 0" ::"s"(v)); }
DI bf16x8 ldsA(const LAS char* p) { return *(const LAS bf16x8*)p; }
DI bf16x8 ldsB2(const LAS char* p0, const LAS char* p1) { s16x4 lo = trread(p0), hi = trread(p1); return __builtin_shufflevector(lo, hi, 0, 1, 2, 3, 4, 5, 6, 7); }
DI bf16x8 ldsB(const LAS char* p) { return ldsB2(p, p + 256); }

DI float gelu_t(float x) {
    const float c0 = -2.0f * 1.4426950408889634f * 0.7978845608028654f;
    const float c1 = c0 * 0.044715f;
    float x2 = x * x;
    float t = __builtin_fmaf(x2, c1, c0);
    float e = __builtin_amdgcn_exp2f(x * t);
    return x * __builtin_amdgcn_rcpf(1.0f + e);
}
constexpr float QSCALE = 0.23570226039551584f * 1.4426950408889634f;

#define KO1 (3 * (((idx & 7) + (idx >> 3)) % 3))
#define KO2 (((idx & 7) + 2 * (idx >> 3) + 4 * (xcd & 1)) % 9)
#define K2_KO (8 * (xcd >> 1))
#define POLLSLEEP 2
#define APRIO 1
#define K3MAP 2
#define K3ROT 0
#define APRE 0
#define AGSLOT 6
#define ZHALF 2
#define POLLW 0
#define POLLSLEEP1 1
#define PLSLOT 0
constexpr int K1_SA = 16384, K1_SB = 24576, K1_B0 = 3 * K1_SA, K1_TAB = K1_B0 + 3 * K1_SB, K1_X0 = K1_TAB + 10240, K1_LDS = K1_X0 + 16384 + 2048;
typedef __attribute__((address_space(1))) unsigned gu32;
typedef __attribute__((address_space(1))) unsigned long long gu64;
#define RLX_AGENT __ATOMIC_RELAXED, __HIP_MEMORY_SCOPE_AGENT
struct K1F { bf16x8 a[4], b[3], x; };
struct K1S {
    unsigned va[2], vb[2]; const char* sa; const char* sb;
    const char* gp; unsigned st;
    int k1, k2;
    unsigned m0a, m0b;
    const LAS char* pa[2]; const LAS char* px[2]; const LAS char* pb[3];
};
DI void k1_issue(K1S& s, int i, int w) {
    if (i == 0) { set_m0(s.m0a + w * K1_SA); glds_s<0>(s.va[0], s.sa); }
    if (i == 1) { glds_s<1024>(s.va[1], s.sa); s.sa += s.k1 == 8 ? -8 * 128 : 128; asm volatile("" : "+s"(s.sa)); }
    if (i == 2) { glds_v(s.gp + (unsigned long long)s.st * (unsigned)s.k1, s.m0b + w * K1_SB + 2048); s.k1 = s.k1 == 8 ? 0 : s.k1 + 1; asm volatile("" : "+s"(s.k1)); }
    if (i == 3) { set_m0(s.m0b + w * K1_SB); glds_s<0>(s.vb[0], s.sb); }
    if (i == 4) { glds_s<1024>(s.vb[1], s.sb); s.sb += s.k2 == 8 ? -8 * 128 * F_ : 128 * F_; s.k2 = s.k2 == 8 ? 0 : s.k2 + 1; asm volatile("" : "+s"(s.sb), "+s"(s.k2)); }
}
DI f32x2 gelu_y2(f32x2 y, float k0, float k1, float ir) {
    f32x2 t = (y * y) * k1 + k0;
    f32x2 a = y * t;
    f32x2 e = {__builtin_amdgcn_exp2f(a[0]), __builtin_amdgcn_exp2f(a[1])};
    f32x2 d = e * ir + ir;
    f32x2 rc = {__builtin_amdgcn_rcpf(d[0]), __builtin_amdgcn_rcpf(d[1])};
    return y * rc;
}
DI float dpp_x1(float x) { return __int_as_float(__builtin_amdgcn_mov_dpp(__float_as_int(x), 0xB1, 0xf, 0xf, true)); }
DI float dpp_x2(float x) { return __int_as_float(__builtin_amdgcn_mov_dpp(__float_as_int(x), 0x4E, 0xf, 0xf, true)); }
DI void kv_store(const char* kv, int sv, int j, int c, int g, f32x4 v) {
    if (j == 4) { st8s<0>(kv + sv * 256, ((g >> 1) * 32 + c) * 16 + (g & 1) * 8, u32x2{cvtpk(v[0], v[1]), cvtpk(v[2], v[3])}); return; }
    const unsigned kk = cvtpk(v[2], v[3]);
    const bool o1 = c & 1, o2 = c & 2;
    float t0 = dpp_x1(o1 ? v[0] : v[1]), t1 = dpp_x1(o1 ? v[2] : v[3]);
    if (o1) { v[0] = t0; v[2] = t1; } else { v[1] = t0; v[3] = t1; }
    t0 = dpp_x2(o2 ? v[0] : v[2]); t1 = dpp_x2(o2 ? v[1] : v[3]);
    if (o2) { v[0] = t0; v[1] = t1; } else { v[2] = t0; v[3] = t1; }
    u32x2 w = {cvtpk(v[0], v[1]), cvtpk(v[2], v[3])};
    const int kq = c >> 2; unsigned off;
    if (j == 5) {
        off = g < 2 ? c * 16 + (g ^ 1) * 8 : 256 + ((kq & 1) * 8 + 4 * (g - 2) + (c & 3)) * 16 + (kq >> 1) * 8;
        if (g < 2) w = g ? u32x2{kk, 0x3f80u} : u32x2{0u, 0u};
    } else {
        off = 512 + ((kq & 1) * 16 + 4 * g + (c & 3)) * 16 + (kq >> 1) * 8;
        if (g == 3 && !o2) { const unsigned cv = o1 ? 0u : 0x3f803f80u; w = u32x2{cv, cv}; }
    }
    st8s<1024>(kv + sv * 1024, off, w);
}
template <int KS, int GL, int RS, int WS, bool RX> DI void k1_p1(K1S& s, const K1F& f, K1F& n, f32x4 (&acc)[4][2], f32x4& sacc, f32x4 (&cso)[2], f32x4& rs, f32x4& rq, const bf16x8& ones) {
    constexpr int AO = RS * K1_SA, BO = RS * K1_SB + KS * 12288;
#define MX(d, a_, b_) d = MFMA16(a_, b_, d); PIN(d); SB()
#define MM(m, nn) MX(acc[m][nn], f.b[nn], f.a[m])
    MM(0, 0); n.b[0] = ldsB(s.pb[0] + BO); SB();
    MM(1, 0); n.a[0] = ldsA(s.pa[KS] + AO); SB();
    MM(2, 0); n.a[1] = ldsA(s.pa[KS] + AO + 2048); SB();
    MM(3, 0); n.a[2] = ldsA(s.pa[KS] + AO + 4096); SB();
    MX(cso[0], f.b[0], ones); n.a[3] = ldsA(s.pa[KS] + AO + 6144); SB();
    MX(sacc, f.b[2], f.x); n.b[1] = ldsB(s.pb[1] + BO); SB();
    MX(cso[1], f.b[2], ones); n.b[2] = ldsB(s.pb[2] + BO); SB();
    MX(rs, ones, f.x); k1_issue(s, GL == 1 ? 0 : 3, WS); SB();
    MX(rq, f.x, f.x); if (RX) n.x = ldsA(s.px[KS] + AO); SB();
    MM(0, 1); k1_issue(s, GL == 1 ? 1 : 4, WS); SB();
    MM(1, 1); if (GL == 1) k1_issue(s, 2, WS); SB();
    MM(2, 1);
    MM(3, 1);
}
template <int KS, int GL, int RS, int WS, bool RD, class E> DI void k1_p2(K1S& s, const K1F& f, K1F& n, f32x4 (&acc)[4][3], f32x4 (&cso)[2], const bf16x8& ones, E epi) {
    constexpr int AO = RS * K1_SA, BO = RS * K1_SB + KS * 12288;
    MM(0, 0); if (RD) n.b[0] = ldsB(s.pb[0] + BO); epi(0); SB();
    MM(1, 0); if (RD) n.a[0] = ldsA(s.pa[KS] + AO); epi(1); SB();
    MM(2, 0); if (RD) n.a[1] = ldsA(s.pa[KS] + AO + 2048); epi(2); SB();
    MM(3, 0); if (RD) n.a[2] = ldsA(s.pa[KS] + AO + 4096); epi(3); SB();
    MX(cso[0], f.b[0], ones); if (RD) n.a[3] = ldsA(s.pa[KS] + AO + 6144); epi(4); SB();
    MM(0, 1); if (RD) n.b[1] = ldsB(s.pb[1] + BO); epi(5); SB();
    MM(1, 1); if (RD) n.b[2] = ldsB(s.pb[2] + BO); epi(6); SB();
    MM(2, 1); if (GL) k1_issue(s, GL == 1 ? 0 : 3, WS); epi(7); SB();
    MM(3, 1); epi(8); SB();
    MX(cso[1], f.b[1], ones); if (GL) k1_issue(s, GL == 1 ? 1 : 4, WS); epi(9); SB();
    MM(0, 2); epi(10); SB();
    MM(1, 2); if (GL == 1) k1_issue(s, 2, WS); epi(11); SB();
    MM(2, 2); epi(12); SB();
    MM(3, 2); epi(13); SB();
#undef MM
#undef MX
}

constexpr unsigned XSALT = 0x9e3779b9u;
DI void epoch_reply(char* ws, int lane, const unsigned (&nv)[4], u64 epoch) {
    const unsigned lo = (unsigned)epoch, hi = (unsigned)(epoch >> 32);
    st16s<0>(ws + WS_XR, lane * 16, u32x4{nv[0], lo, hi, nv[0] ^ lo ^ hi ^ XSALT}); st16s<1024>(ws + WS_XR, lane * 16, u32x4{nv[1], lo, hi, nv[1] ^ lo ^ hi ^ XSALT});
    st16s<2048>(ws + WS_XR, lane * 16, u32x4{nv[2], lo, hi, nv[2] ^ lo ^ hi ^ XSALT}); st16s<3072>(ws + WS_XR, lane * 16, u32x4{nv[3], lo, hi, nv[3] ^ lo ^ hi ^ XSALT});
}
#define XN_LOADS "global_load_dword %0, %4, %5 sc1\n\tglobal_load_dword %1, %4, %5 offset:256 sc1\n\tglobal_load_dword %2, %4, %5 offset:512 sc1\n\tglobal_load_dword %3, %4, %5 offset:768 sc1\n"
DI void epoch_service(char* ws, int lane, u64 epoch) {
    unsigned nv[4];
    asm volatile(XN_LOADS "\ts_waitcnt vmcnt(0)" : "=&v"(nv[0]), "=&v"(nv[1]), "=&v"(nv[2]), "=&v"(nv[3]) : "v"(lane * 4), "s"(ws + WS_XCH) : "memory");
    epoch_reply(ws, lane, nv, epoch);
}
DI unsigned g2_va(int wid, int lane, int i) { int row = 8 * (wid * 4 + i) + (lane >> 3), c = (lane & 7) ^ ((row >> 1) & 7); return row * (S_ * 2) + c * 16 + 4096 - i * 1024; }
DI u64 phase1(const u16* __restrict__ x, const u16* __restrict__ wq, const u16* __restrict__ wk, const u16* __restrict__ wv,
                                                    const u16* __restrict__ w_in, const u16* __restrict__ w_sp, const u16* __restrict__ b_sp, const u16* __restrict__ w_out, const u16* __restrict__ wo, char* __restrict__ ws) {
    extern __shared__ __attribute__((aligned(16))) char smem[];
    const int bid = blockIdx.x, xcd = bid & 7, idx = bid >> 3;
    const int pm = (xcd & 1) * 8 + (idx & 7), j = (xcd >> 1) * 4 + (idx >> 3);
    const int lane = threadIdx.x & 63, wid = __builtin_amdgcn_readfirstlane(threadIdx.x >> 6), wr = wid >> 2, wc = wid & 3;
    const unsigned lds0 = (unsigned)(uintptr_t)smem;
    const LAS char* lds = (const LAS char*)smem;
    {
        const char* pc; unsigned len;
        asm volatile("s_getpc_b64 %0\n.Lk1_here:\n\ts_mov_b32 %1, .Lk3_end-.Lk1_here" : "=s"(pc), "=s"(len));
        unsigned off = ((idx * 8 + wid) * 2 + (lane & 1)) * 128; off = off < len ? off : len;
        asm volatile("s_mov_b32 m0, %2\n\ts_nop 0\n\tglobal_load_lds_dword %0, %1" ::"v"(off), "s"(pc), "s"(lds0 + K1_TAB + 9216 + 512));
    }
    K1S s; s.m0a = lds0 + wid * 2048; s.m0b = lds0 + K1_B0 + wid * 3072;
    const int brow = (lane >> 2) & 7, bc = (lane & 3) ^ (2 * (wid & 1)), bk = 8 * wid + brow;
    const int ko2 = KO2, ko1 = KO1;
    unsigned oa = pm * 128 * D_ * 2, ob = 288 * j, oa1 = oa + ko1 * 128, ob1 = ob + FH * 2 + ko1 * 128 * F_, vg = bk * (F_ * 2) + 256 + 16 * (bc & 1);
    {
        int r8 = lane >> 3;
        s.va[0] = (16 * wid + r8) * (D_ * 2) + (((lane & 7) ^ (r8 >> 1)) * 16);
        s.va[1] = (16 * wid + 8 + r8) * (D_ * 2) + (((lane & 7) ^ (4 + (r8 >> 1))) * 16) - 1024;
        s.vb[0] = bk * (F_ * 2) + (lane >> 5) * 64 + bc * 16 + 1024; s.vb[1] = s.vb[0] + 128 - 1024;
        s.k1 = s.k2 = ko1; s.st = 128 * F_;
        PIN(s.va[0]); PIN(s.va[1]); PIN(s.vb[0]); PIN(s.vb[1]); PIN(vg);
        asm volatile("" : "+s"(oa1), "+s"(ob1), "+s"(s.m0a), "+s"(s.m0b));
    }
    SB();
    s.sa = (const char*)x + oa1; s.sb = (const char*)w_in - 1024 + ob1;
    k1_issue(s, 0, 0); k1_issue(s, 1, 0); k1_issue(s, 3, 0); k1_issue(s, 4, 0);
    SB();
    STAMP(0, 0);
    s.gp = (const char*)w_in + (ob + FH * 2) + vg;
    k1_issue(s, 2, 0);
    const char* sa0 = (const char*)x + oa;
    const char* sb0 = (const char*)w_in - 1024 + ob;
    gu32* XN = (gu32*)(ws + WS_XCH) + pm * 16;
    gu32* XE = (gu32*)(ws + WS_XCH + 4096) + pm * 256;
    gu64* XP = (gu64*)(ws + WS_ST) + (size_t)pm * 16 * 128;
    const char* xe_s = ws + WS_XCH + 4096 + (pm * 256 + j * 16) * 4;
    const unsigned le = (lane & 16) ? 16384 - (pm * 16 + j) * 48 + (lane & 3) * 4 : (lane & 15) * 4;
    unsigned eo = ld_sc1_async(xe_s, le);
#pragma unroll
    for (int t = 1; t < 3; ++t)
#pragma unroll
        for (int i = 0; i < 5; ++i) if (t < 2 || i < 3) k1_issue(s, i, t);
    unsigned nonce = 0;
    const char* gp2; unsigned st2;
    {
        const u16* p; int rs;
        const u16* sp0 = j < 4 ? wq + 512 + 16 * j : (j == 5 ? wk + 10 : (j == 6 ? wv + 8 : wk));
        const u16* sp1 = j < 4 ? wq + 520 + 16 * j : (j == 5 ? wv : (j == 6 ? wv + 10 : wk + 8));
        const int srs = j < 4 ? D_ : 18;
        if (lane >= 32) { p = wq + 32 * j + 8 * bc; rs = D_; }
        else if (bc < 2) { p = w_in + 144 * j + 128 + 8 * bc; rs = F_; }
        else { p = bc == 2 ? sp0 : sp1; rs = srs; }
        gp2 = (const char*)(p + (size_t)bk * rs); st2 = 128 * rs; PIN(gp2); PIN(st2);
    }
    const int r16 = lane & 15, g = lane >> 4, cg = wc ^ (2 * wr);
    const int nf3 = cg == 3 ? 8 : (cg == 2 ? 9 : 10 + cg);
#define NF1(n) ((n) == 2 ? 8 : 2 * wc + ((n) ^ wr))
#define NF2(n) ((n) == 0 ? nf3 : 2 * cg + (((n) - 1) ^ wr))
    const LAS char* pb2[3];
    {
        int q = r16 >> 2, p = lane & 3;
#pragma unroll
        for (int ks = 0; ks < 2; ++ks) { s.pa[ks] = lds + (r16 * 128 + (((4 * ks + g) ^ (r16 >> 1)) * 16) + wr * 8192); s.px[ks] = s.pa[ks] + wc * 2048; PIN(s.pa[ks]); PIN(s.px[ks]); }
#pragma unroll
        for (int n = 0; n < 3; ++n) {
            int nf = NF1(n); s.pb[n] = lds + (K1_B0 + g * 3072 + q * 64 + 8 * p + (nf >> 1) * 512 + (((nf ^ g) & 1) * 32)); PIN(s.pb[n]);
            nf = NF2(n); pb2[n] = lds + (K1_B0 + g * 3072 + q * 64 + 8 * p + (nf >> 1) * 512 + (((nf ^ g) & 1) * 32)); PIN(pb2[n]);
        }
    }
    f32x4 acc1[4][2] = {}; f32x4 sacc = {}; f32x4 cso[2] = {};
    f32x4 rs = {}, rq = {};
#pragma unroll
    for (int m = 0; m < 4; ++m) { PIN(acc1[m][0]); PIN(acc1[m][1]); }
    PIN(sacc); PIN(cso[0]); PIN(cso[1]); PIN(rs); PIN(rq);
    const u32x4 onesv = {0x3f803f80u, 0x3f803f80u, 0x3f803f80u, 0x3f803f80u}; bf16x8 ones = __builtin_bit_cast(bf16x8, onesv); PIN(ones);
    K1F f0, f1;
    SB();
    wait_vm_bar<8>();
    SB();
    f0.b[0] = ldsB(s.pb[0]);
    f0.a[0] = ldsA(s.pa[0]); f0.a[1] = ldsA(s.pa[0] + 2048); f0.a[2] = ldsA(s.pa[0] + 4096); f0.a[3] = ldsA(s.pa[0] + 6144);
    f0.b[2] = ldsB(s.pb[2]); f0.x = ldsA(s.px[0]); f0.b[1] = ldsB(s.pb[1]);
    SB();
    PIN(eo);
    if (wid == 7) {
        nonce = 1;
        while (__ballot(eo == nonce) != 0ull) ++nonce;
        if (lane == 0) __hip_atomic_store(XN + j, nonce, RLX_AGENT);
        __hip_atomic_store(XP + j * 128 + lane, ~0ull, RLX_AGENT); __hip_atomic_store(XP + j * 128 + 64 + lane, ~0ull, RLX_AGENT);
    }
    STAMP(0, 1);
#define P1_A(c) k1_p1<1, 2, (c), ((c) + 2) % 3, true>(s, f0, f1, acc1, sacc, cso, rs, rq, ones); wait_vm_bar<5>(); SB()
#define P1_B(c, RX) k1_p1<0, 1, ((c) + 1) % 3, (c), RX>(s, f1, f0, acc1, sacc, cso, rs, rq, ones)
#pragma unroll 1
    for (int it = 0; it < 2; ++it) { P1_A(0); P1_B(0, true); P1_A(1); P1_B(1, true); P1_A(2); P1_B(2, true); }
    const char* xn_s = ws + WS_XCH + pm * 64; const unsigned l4 = (lane & 15) * 4;
    P1_A(0);
    unsigned echoed = ld_sc1_async(xn_s, l4);
    unsigned alln[4];
    asm volatile("s_cmp_lg_u32 %6, 0\n\ts_cbranch_scc1 .Lld%=\n\t" XN_LOADS ".Lld%=:" : "=&v"(alln[0]), "=&v"(alln[1]), "=&v"(alln[2]), "=&v"(alln[3])
                 : "v"(lane * 4), "s"(ws + WS_XCH), "s"(bid) : "memory", "scc");
    s.k1 = s.k2 = ko2; s.sa = sa0 + ko2 * 128; s.sb = sb0 + (size_t)ko2 * 128 * F_; s.gp = gp2; s.st = st2; SB();
    P1_B(0, true);
    P1_A(1);
    PIN(echoed);
    if (wid == 7 && lane < 16) __hip_atomic_store(XE + lane * 16 + j, echoed, RLX_AGENT);
    PIN(alln[0]); PIN(alln[1]); PIN(alln[2]); PIN(alln[3]);
    if (bid == 0 && wid == 6) epoch_reply(ws, lane, alln, __builtin_amdgcn_s_memrealtime() | (1ull << 63));
    SB();
    P1_B(1, true);
    P1_A(2);
    s.pb[0] = pb2[0]; s.pb[1] = pb2[1]; s.pb[2] = pb2[2];
    P1_B(2, false);
    STAMP(3, 0);
    float* CS = (float*)(smem + K1_TAB);
    f32x4* RS = (f32x4*)(smem + K1_TAB + 2048);
    float* red = (float*)(smem + K1_TAB + 4096);
    f32x2* ZS = (f32x2*)(smem + K1_TAB + 9216);
    volatile LAS unsigned* HS = (volatile LAS unsigned*)(lds + K1_TAB + 1792);
    const float c0 = -2.0f * 1.4426950408889634f * 0.7978845608028654f, c1 = c0 * 0.044715f;
    f32x4 acc2[4][3] = {}; f32x4 cs2[2] = {};
    auto none = [](int) {};
    auto tabs = [&](int st) {
        if (st == 2) { if (r16 == 0) { *(f32x4*)(CS + NF1(0) * 16 + 4 * g) = cso[0]; *(f32x4*)(CS + 128 + 4 * g) = cso[1]; } }
        if (st == 3) {
            float sq = (r16 & 2) ? ((r16 & 1) ? rq[3] : rq[2]) : ((r16 & 1) ? rq[1] : rq[0]);
            float mean = rs[0] * (1.0f / D_), var = fmaxf(sq * (1.0f / D_) - mean * mean, 0.f);
            rs[0] = mean; rs[1] = var + 1e-5f; PIN(rs);
        }
        if (st == 5) { rs[2] = __builtin_amdgcn_rsqf(rs[1]); PIN(rs); }
        if (st == 7) { float rstd = rs[2]; if (g == (r16 >> 2)) RS[wr * 64 + wc * 16 + r16] = f32x4{rs[0], c0 * rstd, c1 * rstd * rstd * rstd, rs[1] * rstd}; }
    };
    f32x4 er[5], cs1[3], e[3];
    f32x2 su[5], sq[5];
#define ZU(u) ((u) == 8 ? sacc : acc1[((u) >> 1) & 3][(u) & 1])
#define UM(u) ((u) == 8 ? 4 : (u) >> 1)
#define UN(u) ((u) == 8 ? 2 : (u) & 1)
#define PRE(i, u) { f32x4 y = ZU(u) - cs1[UN(u)] * er[UM(u)][0]; e[i] = ((y * y) * er[UM(u)][2] + er[UM(u)][1]) * y; ZU(u) = y; PIN(ZU(u)); PIN(e[i]); }
#define FIN(i, u, first) { ZU(u) *= e[i]; PIN(ZU(u)); f32x4 v = ZU(u); f32x2 lo = {v[0], v[1]}, hi = {v[2], v[3]}; \
        if (first) { su[UM(u)] = lo + hi; sq[UM(u)] = lo * lo + hi * hi; } else { su[UM(u)] += lo; su[UM(u)] += hi; sq[UM(u)] += lo * lo; sq[UM(u)] += hi * hi; } PIN(su[UM(u)]); PIN(sq[UM(u)]); }
#define RED(m) { float a_ = su[m][0] + su[m][1], b_ = sq[m][0] + sq[m][1]; \
            auto x1 = __builtin_amdgcn_permlane16_swap(__float_as_uint(a_), __float_as_uint(b_), false, false);       \
            float t_ = __uint_as_float(x1[0]) + __uint_as_float(x1[1]);                                               \
            auto x2 = __builtin_amdgcn_permlane32_swap(__float_as_uint(t_), __float_as_uint(t_), false, false); \
            t_ = __uint_as_float(x2[0]) + __uint_as_float(x2[1]);                                                     \
            if ((m) < 4) redp[(m) * 160] = t_; else reds[0] = t_; }
    float* redp = red + ((wr * 64 + r16) * 5 + wc) * 2 + (g & 1);
    float* reds = red + ((wr * 64 + wc * 16 + r16) * 5 + 4) * 2 + (g & 1);
#define T1(op, v) asm volatile(op " %0, %0" : "+v"(v))
#define DE1(v, sd) asm volatile("v_fma_f32 %0, %0, %1, %1" : "+v"(v) : "v"(sd))
#define EPI_E(u0, u1, u2) [&](int st) { \
        if (st < 12) { float t_ = e[st >> 2][st & 3]; T1("v_exp_f32", t_); e[st >> 2][st & 3] = t_; } \
        if (st >= 2) { const int v_ = st - 2; float t_ = e[v_ >> 2][v_ & 3]; DE1(t_, er[v_ < 4 ? UM(u0) : (v_ < 8 ? UM(u1) : UM(u2))][3]); e[v_ >> 2][v_ & 3] = t_; } }
#define EPI_R(m0, m1) [&](int st) { if (st < 12) { float t_ = e[st >> 2][st & 3]; T1("v_rcp_f32", t_); e[st >> 2][st & 3] = t_; } \
        if (st == 12 && (m0) >= 0) er[(m0) & 3] = RS[wr * 64 + (m0) * 16 + r16]; if (st == 13 && (m1) >= 0) er[(m1) & 3] = RS[wr * 64 + (m1) * 16 + r16]; }
#define P2_A(c, GL, E) k1_p2<1, GL ? 2 : 0, (c), ((c) + 2) % 3, true>(s, f0, f1, acc2, cs2, ones, E)
#define P2_B(c, GL, RD, E) k1_p2<0, GL ? 1 : 0, ((c) + 1) % 3, (c), RD>(s, f1, f0, acc2, cs2, ones, E)
    P2_A(0, true, tabs); wait_vm_bar<5>(); SB();
    er[0] = RS[wr * 64 + r16]; er[4] = RS[wr * 64 + wc * 16 + r16];
#pragma unroll
    for (int n = 0; n < 3; ++n) cs1[n] = *(const f32x4*)(CS + NF1(n) * 16 + 4 * g);
    PRE(0, 0) PRE(1, 1) PRE(2, 8) SB();
    P2_B(0, true, true, EPI_E(0, 1, 8));
    P2_A(1, true, EPI_R(1, 2)); wait_vm_bar<5>(); SB();
    unsigned ev = ld_sc1_async(xe_s, le);
    FIN(0, 0, true) FIN(1, 1, false) FIN(2, 8, true) PRE(0, 2) PRE(1, 3) PRE(2, 4) RED(0) RED(4) SB();
    P2_B(1, true, true, EPI_E(2, 3, 4));
    const int w3p = bid < 240 ? bid : 239;
    auto epi_r11 = EPI_R(3, -1);
    auto cold = [&](int st) {
        epi_r11(st);
        if (st == 12) {
            const char* sp = (const char*)w_sp + (size_t)(pm * 128 + (idx >> 3) * 32 + wid * 4) * (S_ * 2);
            const unsigned vo = (lane >> 4) * (S_ * 2) + (lane & 15) * 16;
            set_m0(lds0 + K1_X0 + 16384); glds_s<0>(vo, sp); glds_s<1024>(vo, sp - 768);
            asm volatile("s_mov_b32 m0, %2\n\ts_nop 0\n\tglobal_load_lds_dword %0, %1" ::"v"(lane * 4), "s"(b_sp + pm * 128), "s"(lds0 + TOPL + 2304));
        }
        if (st == 13) {
            const int jj = w3p / 30, pc = w3p - jj * 30;
            const char* sp = (pc < 24 ? (const char*)w_out + pc * 96 * 1152 : (const char*)wo + (pc - 24) * 96 * 1152) + jj * 144;
            set_m0(lds0 + K1_X0 + wid * 2048);
#pragma unroll
            for (int i = 0; i < 2; ++i) {
                unsigned c_ = threadIdx.x + 512 * i; c_ = c_ < 864 ? c_ : 863;
                const unsigned row = (c_ * 7282) >> 16, vo = row * (1152 - 144) + c_ * 16;
                if (i == 0) glds_s<0>(vo, sp); else glds_s<1024>(vo, sp - 1024);
            }
        }
    };
    P2_A(2, true, cold); wait_vm_bar<10>(); SB();
    PIN(ev);
    bool hs = false; u64 epoch = 0;
    auto xcheck = [&]() {
        hs = hs || __all((lane & 16) || ev == nonce);
        const unsigned w0 = __builtin_amdgcn_readlane(ev, 16), w1 = __builtin_amdgcn_readlane(ev, 17), w2 = __builtin_amdgcn_readlane(ev, 18), w3 = __builtin_amdgcn_readlane(ev, 19);
        if (w0 == nonce && w3 == (w0 ^ w1 ^ w2 ^ XSALT)) epoch = ((u64)w2 << 32) | w1;
    };
    xcheck();
    unsigned nn = ld_sc1_async(xn_s, l4);
    FIN(0, 2, true) FIN(1, 3, false) FIN(2, 4, true) PRE(0, 5) PRE(1, 6) PRE(2, 7) RED(1) SB();
    P2_B(2, true, true, EPI_E(5, 6, 7));
    P2_A(0, true, EPI_R(-1, -1)); wait_vm_bar<11>(); SB();
    FIN(0, 5, false) FIN(1, 6, true) FIN(2, 7, false) RED(2) RED(3) SB();
    P2_B(0, true, true, none);
    P2_A(1, true, none);
    STAMP(3, 1);
    wait_vm_bar<5>(); SB();
    ev = ld_sc1_async(xe_s, le);
    f32x2 pu[5];
    auto pub = [&](int st) {
        if (st == 0) {
#pragma unroll
            for (int i = 0; i < 5; ++i) pu[i] = *(const f32x2*)(red + (wid * 16 + r16) * 10 + 2 * i);
        }
        if (st == 3) {
            float a = ((pu[0][0] + pu[1][0]) + (pu[2][0] + pu[3][0])) + pu[4][0], b = ((pu[0][1] + pu[1][1]) + (pu[2][1] + pu[3][1])) + pu[4][1];
            unsigned bb = __float_as_uint(b); if (bb == 0xffffffffu) bb = 0x7fc00000u;
            if (lane < 16) __hip_atomic_store(XP + j * 128 + wid * 16 + lane, ((unsigned long long)bb << 32) | __float_as_uint(a), RLX_AGENT);
        }
        if (st == 5) { PIN(nn); if (wid == 7 && nn != echoed) { echoed = nn; if (lane < 16) __hip_atomic_store(XE + lane * 16 + j, nn, RLX_AGENT); } }
    };
    P2_B(1, true, true, pub);
    P2_A(2, true, none); wait_vm_bar<5>(); SB();
    PIN(ev); xcheck();
    if (wid == 7) *HS = hs;
    u32x4 w3v[2];
    auto w3st = [&](int st) {
        if (st == 0) { w3v[0] = *(const LAS u32x4*)(lds + K1_X0 + wid * 2048 + lane * 16); w3v[1] = *(const LAS u32x4*)(lds + K1_X0 + wid * 2048 + 1024 + lane * 16); }
        if (st == 4) st16s<0>(ws + WS_W3T + w3p * 13824, threadIdx.x * 16, w3v[0]);
        if (st == 5) { if (threadIdx.x < 352) st16s<0>(ws + WS_W3T + w3p * 13824 + 8192, threadIdx.x * 16, w3v[1]); }
    };
    P2_B(2, true, true, w3st);
    P2_A(0, true, none); wait_vm_bar<5>(); SB();
    const char* xp_s = ws + WS_ST + (size_t)pm * 16384 + wid * 128; const unsigned xp_v = g * 4096 + r16 * 8;
    u32x2 pv[4];
#define PLOAD() asm volatile("global_load_dwordx2 %0, %4, %5 sc1\n\tglobal_load_dwordx2 %1, %4, %5 offset:1024 sc1\n\tglobal_load_dwordx2 %2, %4, %5 offset:2048 sc1\n\tglobal_load_dwordx2 %3, %4, %5 offset:3072 sc1" \
        : "=&v"(pv[0]), "=&v"(pv[1]), "=&v"(pv[2]), "=&v"(pv[3]) : "v"(xp_v), "s"(xp_s) : "memory")
    auto pload = [&](int st) { if (st == PLSLOT) PLOAD(); };
    P2_B(0, false, true, pload);
    P2_A(1, false, none); wait_vm_bar<4>(); SB();
#define PEMPTY() __any(pv[0][1] == 0xffffffffu || pv[1][1] == 0xffffffffu || pv[2][1] == 0xffffffffu || pv[3][1] == 0xffffffffu)
    auto again = [&](int st) {
        if (st == AGSLOT) {
            wait_vm<0>();
            PIN(pv[0]); PIN(pv[1]); PIN(pv[2]); PIN(pv[3]);
            const unsigned long long em = __builtin_amdgcn_ballot_w64(pv[0][1] == 0xffffffffu || pv[1][1] == 0xffffffffu || pv[2][1] == 0xffffffffu || pv[3][1] == 0xffffffffu);
            asm volatile("s_cmp_eq_u64 %6, 0\n\ts_cbranch_scc1 .Lpl%=\n\tglobal_load_dwordx2 %0, %4, %5 sc1\n\tglobal_load_dwordx2 %1, %4, %5 offset:1024 sc1\n\t"
                         "global_load_dwordx2 %2, %4, %5 offset:2048 sc1\n\tglobal_load_dwordx2 %3, %4, %5 offset:3072 sc1\n.Lpl%=:"
                         : "+v"(pv[0]), "+v"(pv[1]), "+v"(pv[2]), "+v"(pv[3]) : "v"(xp_v), "s"(xp_s), "s"(em) : "memory", "scc");
        }
    };
    unsigned hsf = *HS;
    P2_B(1, false, true, again);
    float za, zb;
    auto zstat = [&](int st) {
        if (st == 1) {
            wait_vm<0>();
            PIN(pv[0]); PIN(pv[1]); PIN(pv[2]); PIN(pv[3]);
            if (__builtin_expect(!hsf || PEMPTY(), 0)) {
                const unsigned long long t0 = __builtin_amdgcn_s_memrealtime();
                if (wid == 7) {
                    while (!hs) {
                        ev = ld_sc1_async(xe_s, le); wait_vm<0>(); PIN(ev);
                        xcheck();
                        nn = __hip_atomic_load(XN + (lane & 15), RLX_AGENT);
                        if (nn != echoed) { echoed = nn; if (lane < 16) __hip_atomic_store(XE + lane * 16 + j, nn, RLX_AGENT); }
                        if (__builtin_amdgcn_s_memrealtime() - t0 > 5000000ull) break;
                    }
                    *HS = 1;
                }
                while (*HS == 0 && __builtin_amdgcn_s_memrealtime() - t0 < 6000000ull) __builtin_amdgcn_s_sleep(2);
                do {
                    PLOAD(); wait_vm<0>(); PIN(pv[0]); PIN(pv[1]); PIN(pv[2]); PIN(pv[3]);
                } while (PEMPTY() && __builtin_amdgcn_s_memrealtime() - t0 < 6000000ull);
            }
        }
        if (st == 3) {
            za = (__uint_as_float(pv[0][0]) + __uint_as_float(pv[1][0])); PIN(za); zb = (__uint_as_float(pv[0][1]) + __uint_as_float(pv[1][1])); PIN(zb);
            float c_ = __uint_as_float(pv[2][0]) + __uint_as_float(pv[3][0]); PIN(c_); float d_ = __uint_as_float(pv[2][1]) + __uint_as_float(pv[3][1]); PIN(d_);
            za += c_; PIN(za); zb += d_; PIN(zb);
        }
        if (st == 5) {
            auto a1 = __builtin_amdgcn_permlane16_swap(__float_as_uint(za), __float_as_uint(za), false, false); za = __uint_as_float(a1[0]) + __uint_as_float(a1[1]); PIN(za);
            auto b1 = __builtin_amdgcn_permlane16_swap(__float_as_uint(zb), __float_as_uint(zb), false, false); zb = __uint_as_float(b1[0]) + __uint_as_float(b1[1]); PIN(zb);
        }
        if (st == 7) {
            auto a2 = __builtin_amdgcn_permlane32_swap(__float_as_uint(za), __float_as_uint(za), false, false); za = __uint_as_float(a2[0]) + __uint_as_float(a2[1]); PIN(za);
            auto b2 = __builtin_amdgcn_permlane32_swap(__float_as_uint(zb), __float_as_uint(zb), false, false); zb = __uint_as_float(b2[0]) + __uint_as_float(b2[1]); PIN(zb);
        }
        if (st == 9) {
            float m = za * (1.0f / FH); PIN(m); float var = fmaxf(zb * (1.0f / FH) - m * m, 0.f), r = __builtin_amdgcn_rsqf(var + 1e-5f);
            ZS[wid * 16 + r16] = f32x2{r, -m * r};
        }
    };
    P2_A(2, false, none);
    P2_B(2, false, false, zstat);
    if (r16 == 0) { *(f32x4*)(CS + 192 + NF2(0) * 16 + 4 * g) = cs2[0]; *(f32x4*)(CS + 192 + NF2(1) * 16 + 4 * g) = cs2[1]; }
    f32x4* HX = (f32x4*)(smem + K1_B0) + wr * 256 + lane;
    if (cg == 3) { HX[0] = acc2[0][0]; HX[64] = acc2[1][0]; HX[128] = acc2[2][0]; HX[192] = acc2[3][0]; }
    asm volatile("s_waitcnt lgkmcnt(0)\n\ts_barrier" ::: "memory");
    STAMP(3, 2);
    auto merge = [&](u32x2 A, u32x2 B) {
        auto r0 = __builtin_amdgcn_permlane16_swap(A[0], B[0], false, false); auto r1 = __builtin_amdgcn_permlane16_swap(A[1], B[1], false, false);
        return u32x4{r0[0], r1[0], r0[1], r1[1]};
    };
    auto pk4 = [&](f32x4 v) { return u32x2{cvtpk(v[0], v[1]), cvtpk(v[2], v[3])}; };
    {
        const char* z2b = ws + WS_Z2T + (size_t)(j * 32 + 2 * pm + wr) * 18432;
        f32x2 zs[5];
#pragma unroll
        for (int m = 0; m < 5; ++m) zs[m] = ZS[wr * 64 + (m < 4 ? m : wc) * 16 + r16];
        const unsigned vs = (r16 >> 3) * 2304 + (((r16 & 7) ^ (4 * (r16 >> 3))) * 32), v2 = vs + (g & 1) * 4608 + (g >> 1) * 16;
#pragma unroll
        for (int n = 0; n < 2; ++n)
#pragma unroll
            for (int m = 0; m < 4; m += 2)
                st16s<0>(z2b + NF1(n) * 256 + m * 4608, v2, merge(pk4(acc1[m][n] * zs[m][0] + zs[m][1]), pk4(acc1[m + 1][n] * zs[m + 1][0] + zs[m + 1][1])));
        st8s<2048>(z2b + wc * 4608, vs + g * 8, pk4(sacc * zs[4][0] + zs[4][1]));
    }
    auto gel = [&](f32x4 a, f32x4 c, f32x4 rr) {
        f32x4 y = a - c * rr[0];
        f32x2 y0 = gelu_y2(f32x2{y[0], y[1]}, rr[1], rr[2], rr[3]), y1 = gelu_y2(f32x2{y[2], y[3]}, rr[1], rr[2], rr[3]);
        return u32x2{cvtpk(y0[0], y0[1]), cvtpk(y1[0], y1[1])};
    };
#pragma unroll
    for (int n = 0; n < 3; ++n) cs1[n] = *(const f32x4*)(CS + 192 + NF2(n) * 16 + 4 * g);
#pragma unroll
    for (int m = 0; m < 4; ++m) er[m] = RS[wr * 64 + m * 16 + r16];
    if (cg == 3) {
    } else if (nf3 >= 10 || j < 4) {
        const char* qb = ws + WS_Q + (pm * 16 + wr * 8) * 9216 + (nf3 >= 10 ? 4 * j + (nf3 - 10) * 2 : 64 + 2 * j) * 128;
        const unsigned vq = ((g & 1) * 2 + (r16 >> 3)) * 9216 + (g >> 1) * 128 + (r16 & 7) * 16;
#define QV(m) pk4((acc2[m][0] - cs1[0] * er[m][0]) * (er[m][1] * (QSCALE / c0)))
        st16s<0>(qb, vq, merge(QV(0), QV(1))); st16s<0>(qb + 4 * 9216, vq, merge(QV(2), QV(3)));
    } else if (j < 7) {
#pragma unroll
        for (int m = 0; m < 4; ++m)
            kv_store(ws + WS_KV + (size_t)(pm * 4 + wr * 2 + (m >> 1)) * 3072, m & 1, j, r16, g, (acc2[m][0] - cs1[0] * er[m][0]) * (er[m][1] * (1.0f / c0)));
    }
    SB();
    f32x4 h8 = HX[wc * 64], c8 = *(const f32x4*)(CS + 192 + 128 + 4 * g); er[4] = RS[wr * 64 + wc * 16 + r16];
    volatile LAS u64* EW = (volatile LAS u64*)(lds + TOPL + 8);
    if (wid == 7) {
        if (__builtin_expect(epoch == 0, 0)) {
            const u64 t0 = __builtin_amdgcn_s_memrealtime();
            do { ev = ld_sc1_async(xe_s, le); wait_vm<0>(); PIN(ev); xcheck(); } while (epoch == 0 && __builtin_amdgcn_s_memrealtime() - t0 < 5000000ull);
        }
        *EW = epoch;
    }
    SB();
    wait_vm_bar<0>();
    STAMP(0, 3);
    if (wid == 7) {
        const bool kvp = j >= 4 && j < 7;
        const unsigned fo = lane == 0 ? (unsigned)(WS_FJ - WS_FA) + (j * 64 + pm) * 8 : (lane == 1 ? (unsigned)(WS_FP - WS_FA) + (pm * 16 + j) * 8 : (lane == 2 ? bid * 8
                            : (unsigned)(WS_FJ - WS_FA) + ((lane & 15) * 64 + 16 + pm * 3 + (j - 4)) * 8));
        if (lane < 3 || (kvp && g == 1)) __hip_atomic_store((gu64*)(ws + WS_FA + fo), epoch, RLX_AGENT);
    } else if (wid < 4) {
        const char* sa = (const char*)w_sp - 4096 + pm * 128 * S_ * 2;
#pragma unroll
        for (int t = 0; t < 2; ++t) {
            set_m0(lds0 + wid * 4096 + t * K2_SA);
            glds_s<0>(g2_va(wid, lane, 0), sa + t * 128); glds_s<1024>(g2_va(wid, lane, 1), sa + t * 128); glds_s<2048>(g2_va(wid, lane, 2), sa + t * 128); glds_s<3072>(g2_va(wid, lane, 3), sa + t * 128);
        }
    }
    SB();
    u32x4 zw[4];
#pragma unroll
    for (int m = 0; m < 4; ++m) zw[m] = merge(gel(acc2[m][1], cs1[1], er[m]), gel(acc2[m][2], cs1[2], er[m]));
    u32x2 zs8 = gel(h8, c8, er[4]);
    {
        LAS char* zl = (LAS char*)lds + Z1L + wr * 1024 + r16 * 16;
#pragma unroll
        for (int m = 0; m < 4; ++m) *(LAS u32x4*)(zl + cg * 8192 + (2 * ((g & 1) ^ wr) + (g >> 1)) * 2048 + m * 256) = zw[m];
        *(LAS u32x2*)(zl + 32768 + wc * 256 + (g >> 1) * 2048 + (g & 1) * 8) = zs8;
    }
    const u64 e_ = *EW;
    const unsigned eh = __builtin_amdgcn_readfirstlane((unsigned)(e_ >> 32)), el = __builtin_amdgcn_readfirstlane((unsigned)e_);
    return ((u64)eh << 32) | el;
}

DI void wait_in(char* ws, u64 epoch, int lane, int wid, int pm, int j) {
    if (wid == POLLW) {
        const u64 t0 = __builtin_amdgcn_s_memrealtime();
        for (;;) {
            u64 f0_, f1_;
            asm volatile("global_load_dwordx2 %0, %2, %3 sc1\n\tglobal_load_dwordx2 %1, %4, %5 sc1\n\ts_waitcnt vmcnt(0)" : "=&v"(f0_), "=&v"(f1_)
                         : "v"(lane * 8), "s"(ws + WS_FJ + j * 512), "v"((lane & 15) * 8), "s"(ws + WS_FP + pm * 128) : "memory");
            if (__all(f0_ == epoch && f1_ == epoch)) break;
            const u64 dt = __builtin_amdgcn_s_memrealtime() - t0;
            if (dt > 5000000ull) break;
            if (dt > 1000 && blockIdx.x == 0) epoch_service(ws, lane, epoch);
            __builtin_amdgcn_s_sleep(POLLSLEEP1);
        }
    }
    asm volatile("s_barrier" ::: "memory");
}
DI void wait_fa(char* ws, u64 epoch, int lane) {
    const u64 t0 = __builtin_amdgcn_s_memrealtime();
    for (;;) {
        u32x4 a, b;
        asm volatile("global_load_dwordx4 %0, %2, %3 sc1\n\tglobal_load_dwordx4 %1, %2, %3 offset:1024 sc1\n\ts_waitcnt vmcnt(0)" : "=&v"(a), "=&v"(b) : "v"(lane * 16), "s"(ws + WS_FA) : "memory");
        const unsigned lo = (unsigned)epoch, hi = (unsigned)(epoch >> 32);
        if (__all(a[0] == lo && a[1] == hi && a[2] == lo && a[3] == hi && b[0] == lo && b[1] == hi && b[2] == lo && b[3] == hi)) break;
        if (__builtin_amdgcn_s_memrealtime() - t0 > 5000000ull) break;
        if (blockIdx.x == 0) epoch_service(ws, lane, epoch);
        __builtin_amdgcn_s_sleep(8);
    }
}

constexpr float ATT_BIAS = 72.0f;
DI float xhalf_max(float m) { auto rr = __builtin_amdgcn_permlane32_swap(__float_as_uint(m), __float_as_uint(m), false, false); return fmaxf(__uint_as_float(rr[0]), __uint_as_float(rr[1])); }
DI float lo_bcast(float m) { auto rr = __builtin_amdgcn_permlane32_swap(__float_as_uint(m), __float_as_uint(m), false, false); return __uint_as_float(rr[0]); }
DI float bf_round(float x) { return __uint_as_float(cvtpk(x, 0.f) << 16); }
DI float max16(const f32x16& s) {
    float a = fmaxf(fmaxf(s[0], s[1]), s[2]), b = fmaxf(fmaxf(s[3], s[4]), s[5]);
    a = fmaxf(fmaxf(a, s[6]), s[7]); b = fmaxf(fmaxf(b, s[8]), s[9]); a = fmaxf(fmaxf(a, s[10]), s[11]); b = fmaxf(fmaxf(b, s[12]), s[13]);
    a = fmaxf(fmaxf(a, s[14]), s[15]); return fmaxf(a, b);
}
DI void exp_pack(const f32x16& s, bf16x8& p0, bf16x8& p1) {
    float e[16];
#pragma unroll
    for (int i = 0; i < 16; ++i) e[i] = __builtin_amdgcn_exp2f(s[i]);
    u32x4 a = {cvtpk(e[0], e[1]), cvtpk(e[2], e[3]), cvtpk(e[4], e[5]), cvtpk(e[6], e[7])};
    u32x4 b = {cvtpk(e[8], e[9]), cvtpk(e[10], e[11]), cvtpk(e[12], e[13]), cvtpk(e[14], e[15])};
    p0 = __builtin_bit_cast(bf16x8, a); p1 = __builtin_bit_cast(bf16x8, b);
}

struct KVF { bf16x8 k1, k2, v0, v1; };
DI void kv_read(KVF& f, const LAS char* tb, int lane, int k2o, int vo) {
    f.k1 = *(const LAS bf16x8*)(tb + lane * 16); f.k2 = *(const LAS bf16x8*)(tb + k2o);
    f.v0 = *(const LAS bf16x8*)(tb + 1024 + vo); f.v1 = *(const LAS bf16x8*)(tb + 2048 + vo);
}
#define k2frag(x) (x)
template <bool RD>
DI void half_step(f32x16& sx, bf16x8& px0, bf16x8& px1, f32x16& sy, const KVF kq, const bf16x8& qy1, const bf16x8& qy2,
                  f32x16& oy, const KVF kv, const bf16x8& py0, const bf16x8& py1, KVF& ld, const LAS char* tb, int lane, int k2o, int vo) {
    const f32x16 zero = {};
    unsigned a0, a1, a2, a3, b0, b1, b2, b3; float e0, e1, e2, e3;
    f32x16 t = MFMA32(kq.k1, qy1, zero); SB();
    if (RD) ld.k1 = *(const LAS bf16x8*)(tb + lane * 16);
    e0 = EX(sx[0]); e1 = EX(sx[1]); e2 = EX(sx[2]); e3 = EX(sx[3]); PIN(e0); PIN(e1); PIN(e2); PIN(e3); SB();
    sy = MFMA32(kq.k2, qy2, t); SB();
    if (RD) ld.k2 = *(const LAS bf16x8*)(tb + k2o);
    a0 = cvtpk(e0, e1); a1 = cvtpk(e2, e3); PIN(a0); PIN(a1);
    e0 = EX(sx[4]); e1 = EX(sx[5]); e2 = EX(sx[6]); e3 = EX(sx[7]); PIN(e0); PIN(e1); PIN(e2); PIN(e3); SB();
    oy = MFMA32(kv.v0, py0, oy); SB();
    if (RD) ld.v0 = *(const LAS bf16x8*)(tb + 1024 + vo);
    a2 = cvtpk(e0, e1); a3 = cvtpk(e2, e3); PIN(a2); PIN(a3);
    e0 = EX(sx[8]); e1 = EX(sx[9]); e2 = EX(sx[10]); e3 = EX(sx[11]); PIN(e0); PIN(e1); PIN(e2); PIN(e3); SB();
    oy = MFMA32(kv.v1, py1, oy); SB();
    if (RD) ld.v1 = *(const LAS bf16x8*)(tb + 2048 + vo);
    b0 = cvtpk(e0, e1); b1 = cvtpk(e2, e3); PIN(b0); PIN(b1);
    e0 = EX(sx[12]); e1 = EX(sx[13]); e2 = EX(sx[14]); e3 = EX(sx[15]);
    b2 = cvtpk(e0, e1); b3 = cvtpk(e2, e3); PIN(b2); PIN(b3); SB();
    u32x4 a = {a0, a1, a2, a3}, b = {b0, b1, b2, b3};
    px0 = __builtin_bit_cast(bf16x8, a); px1 = __builtin_bit_cast(bf16x8, b);
}
DI bf16x8 p_scale(bf16x8 p, int sh) {
    u32x4 w = __builtin_bit_cast(u32x4, p);
#pragma unroll
    for (int i = 0; i < 4; ++i) w[i] = cvtpk(__builtin_amdgcn_ldexpf(bflo(w[i]), sh), __builtin_amdgcn_ldexpf(bfhi(w[i]), sh));
    return __builtin_bit_cast(bf16x8, w);
}
template <bool PP> DI void att_rescale(f32x16& o, f32x16& s, bf16x8& p0, bf16x8& p1, float& mref, bf16x8& q2, bool& bad, int c, int h) {
    bool nf = false;
#pragma unroll
    for (int i = 0; i < 11; ++i) nf = nf || (!(fabsf(o[i]) <= 3.0e38f) && (i < 8 || h == 0));
    if (__any(nf)) { bad = true; return; }
    float lb = lo_bcast(o[10]);
    int e = __builtin_amdgcn_frexp_expf(lb);
    float d = (float)(e + (int)ATT_BIAS); d = d < 0 ? 0 : d;
    float nm = bf_round(mref + d); float dd = nm - mref; mref = nm;
    int sh = -(int)dd;
#pragma unroll
    for (int i = 0; i < 16; ++i) { o[i] = __builtin_amdgcn_ldexpf(o[i], sh); if (!PP) s[i] -= dd; }
    if (PP) { p0 = p_scale(p0, sh); p1 = p_scale(p1, sh); }
    u32x4 b = __builtin_bit_cast(u32x4, q2); b[1] = h ? 0u : (__float_as_uint(-nm) >> 16); q2 = __builtin_bit_cast(bf16x8, b);
}

#define W3LD 1
#define W3ST 3
struct G2F { bf16x8 a[4], b[4], sb; };
struct G2S {
    unsigned va[4], vl, vl2;
    const char* sa; const char* sl; unsigned lstep;
    unsigned m0a, m0l[3];
    const LAS char* pa[2][2]; const LAS char* pb[2]; const LAS char* ps[2];
};
template <int KS, int GL, bool RD, int RS, int WS> DI void g2_phase(G2S& s, const G2F& f, G2F& n, f32x4 (&acc)[4][4], f32x4 (&sacc)[2]) {
    const LAS char* a0 = s.pa[0][KS] + RS * K2_SA; const LAS char* a1 = s.pa[1][KS] + RS * K2_SA;
    constexpr int BO = RS * K2_SB + KS * 9216;
    const LAS char* b0 = s.pb[0] + BO; const LAS char* b1 = s.pb[1] + BO;
#define MM(m, nn) acc[m][nn] = MFMA16(f.b[nn], f.a[m], acc[m][nn]); PIN(acc[m][nn]); SB()
    MM(0, 0); if (RD) n.b[0] = ldsB2(b0, b1); SB();
    MM(1, 0); if (RD) n.a[0] = ldsA(a0); SB();
    MM(2, 0); if (RD) n.a[1] = ldsA(a0 + 2048); SB();
    MM(3, 0); if (RD) n.a[2] = ldsA(a1); SB();
    MM(0, 1); if (RD) n.a[3] = ldsA(a1 + 2048); SB();
    MM(1, 1); if (RD) n.b[1] = ldsB2(b0 + 256, b1 + 256); SB();
    MM(2, 1); if (RD) n.b[2] = ldsB2(b0 + 512, b1 + 512); SB();
    MM(3, 1); if (RD) n.b[3] = ldsB2(b0 + 768, b1 + 768); SB();
    MM(0, 2); if (RD) n.sb = ldsB2(s.ps[0] + BO, s.ps[1] + BO); SB();
    MM(1, 2); if (GL == 1) { set_m0(s.m0a + WS * K2_SA); glds_s<0>(s.va[0], s.sa); } if (GL == 2) { glds_sc1<1024>(s.vl, s.sl); } SB();
    MM(2, 2);
    MM(3, 2); if (GL == 1) glds_s<1024>(s.va[1], s.sa); if (GL == 2) glds_sc1<2048>(s.vl, s.sl); SB();
    MM(0, 3);
    MM(1, 3); if (GL == 1) glds_s<2048>(s.va[2], s.sa); if (GL == 2) glds_sc1<3072>(s.vl, s.sl); SB();
    MM(2, 3);
    MM(3, 3); if (GL == 1) { glds_s<3072>(s.va[3], s.sa); s.sa += 128; asm volatile("" : "+s"(s.sa)); } if (GL == 2) { set_m0(s.m0l[WS] + 4096); glds_sc1<0>(s.vl2, s.sl); } SB();
    sacc[0] = MFMA16(f.sb, f.a[0], sacc[0]); PIN(sacc[0]); SB();
    if (GL == 1) { set_m0(s.m0l[WS]); glds_sc1<0>(s.vl, s.sl); } if (GL == 2) { glds_sc1<1024>(s.vl2, s.sl); s.sl += s.lstep; asm volatile("" : "+s"(s.sl)); } SB();
    sacc[1] = MFMA16(f.sb, f.a[1], sacc[1]); PIN(sacc[1]); SB();
#undef MM
}
template <int C, bool G2nd, bool G1st, int VM, bool RDN> DI void g2_iter(G2S& s, G2F& f0, G2F& f1, f32x4 (&acc)[4][4], f32x4 (&sacc)[2]) {
    g2_phase<1, G2nd ? 2 : 0, true, C, (C + 2) % 3>(s, f0, f1, acc, sacc);
    wait_vm_bar<VM>(); SB();
    g2_phase<0, G1st ? 1 : 0, RDN, (C + 1) % 3, C>(s, f1, f0, acc, sacc);
}

constexpr int K3_S = 4608, K3_RW = 4 * K3_S;
struct B5 { bf16x8 b[5]; };
DI void k3_a(unsigned m0v, unsigned va, const char* sa) {
    set_m0(m0v); glds_sc1<0>(va, sa); glds_sc1<1024>(va, sa); glds_sc1<2048>(va, sa); glds_sc1<3072>(va, sa);
}
DI void k3_b(unsigned m0v, unsigned va, const char* sb) {
    set_m0(m0v); glds_sc1<0>(va, sb); glds_sc1<1024>(va, sb); glds_sc1<2048>(va, sb); glds_sc1<3072>(va, sb);
    asm volatile("s_mov_b32 m0, %2\n\ts_mov_b32 exec_hi, 0\n\tglobal_load_lds_dwordx4 %0, %1 sc1\n\ts_mov_b32 exec_hi, -1" ::"v"(va + 4096), "s"(sb), "s"(m0v + 4096));
}
DI void k3_rdb(B5& r, const LAS char* p) {
#pragma unroll
    for (int n = 0; n < 5; ++n) r.b[n] = ldsB2(p + n * 32, p + n * 32 + 576);
}
template <int N0, int N1> DI void k3_mm(f32x4 (&acc)[4][5], const bf16x8 (&a)[4], const B5& r) {
#pragma unroll
    for (int n = N0; n < N1; ++n)
#pragma unroll
        for (int m = 0; m < 4; ++m) { acc[m][n] = MFMA16(r.b[n], a[m], acc[m][n]); PIN(acc[m][n]); }
    SB();
}

DI void phase2(const u16* __restrict__ w_sp, const u16* __restrict__ b_sp, const u16* __restrict__ x, char* __restrict__ ws, u16* __restrict__ out, const u64 epoch, const int wid) {
    extern __shared__ __attribute__((aligned(16))) char smem[];
    int bid = blockIdx.x, xcd = bid & 7, idx = bid >> 3;
    int pm = (xcd & 1) * 8 + (idx & 7), j = (xcd >> 1) * 4 + (idx >> 3);
    unsigned zz = 0; asm volatile("" : "+s"(zz));
    const int lane = __builtin_amdgcn_mbcnt_hi(~0u, __builtin_amdgcn_mbcnt_lo(~0u, zz)) & 63;
    const int tid = wid * 64 + lane;
    const unsigned lds0 = (unsigned)(uintptr_t)smem; const LAS char* lds = (const LAS char*)smem;
    STAMP(1, 0);
    const int rb = ((xcd & 1) ^ 1) * 16 + (xcd >> 1) * 4 + (idx & 3), j3 = idx >> 2;
    volatile LAS unsigned* SY = (volatile LAS unsigned*)(lds + TOPL);
    gu64* FLr = (gu64*)(ws + WS_FL) + rb * 64;
    const unsigned va = lane * 16, wm = lds0 + wid * K3_RW;
    const char* wb = ws + WS_W3T + (size_t)j3 * 90 * 4608 + wid * 4608;
#define K3_P(i) ((i) < 9 ? ((i) + K3ROT >= 9 ? (i) + K3ROT - 9 : (i) + K3ROT) : (i))
#define K3_PRE() { _Pragma("unroll") for (int i = 0; i < 4; ++i) k3_b(wm + i * K3_S, va, wb + K3_P(i) * 36864); SB(); }
    if (__builtin_expect(wid < 4, 1)) {
        const int wr = wid >> 1, wc = wid & 1;
        G2S s;
#pragma unroll
        for (int i = 0; i < 4; ++i) { s.va[i] = g2_va(wid, lane, i); PIN(s.va[i]); }
        s.vl = lane * 16; s.vl2 = lane * 16 + 4096;
        s.lstep = wid < 3 ? 18432 : 6144;
        unsigned ol = (wid < 3 ? (unsigned)WS_Z2T + j * 32 * 18432 + wid * 6144 : (unsigned)WS_KV), oa = pm * 128 * S_ * 2;
        s.m0a = lds0 + wid * 4096; s.m0l[0] = lds0 + (wid < 3 ? K2_B0 + wid * 6144 : K2_K0); s.m0l[1] = s.m0l[0] + s.lstep; s.m0l[2] = s.m0l[1] + s.lstep;
        PIN(s.vl); PIN(s.vl2);
        asm volatile("" : "+s"(ol), "+s"(oa), "+s"(s.m0a), "+s"(s.m0l[0]));
        SB();
        s.sa = (const char*)w_sp - 4096 + oa; s.sl = ws + ol;
        s.sa += 256;
        {
            int r = lane & 15, g = lane >> 4, q = r >> 2, p = lane & 3;
#pragma unroll
            for (int ks = 0; ks < 2; ++ks) {
                int o = r * 128 + (((4 * ks + g) ^ (r >> 1)) * 16) + wr * 8192;
                s.pa[0][ks] = lds + o + wc * 4096; s.pa[1][ks] = lds + o + (1 - wc) * 4096; PIN(s.pa[0][ks]); PIN(s.pa[1][ks]);
            }
            int ob = K2_B0 + g * 2304 + q * 32 + 8 * p;
            s.pb[0] = lds + ob + wc * 1024 + ((g & 1) * 128); s.pb[1] = lds + ob + wc * 1024 + (((g & 1) ^ 1) * 128);
            s.ps[0] = lds + ob + 2048 + ((g & 1) * 128); s.ps[1] = lds + ob + 2048 + (((g & 1) ^ 1) * 128);
            PIN(s.pb[0]); PIN(s.pb[1]); PIN(s.ps[0]); PIN(s.ps[1]);
        }
        G2F f0, f1;
        SB();
        f32x4 acc[4][4], sacc[2];
#pragma unroll
        for (int m = 0; m < 4; ++m) {
            const float b = bf2f(*(const LAS u16*)(lds + TOPL + 2304 + (wr * 64 + (m ^ (2 * wc)) * 16 + (lane & 15)) * 2));
#pragma unroll
            for (int n = 0; n < 4; ++n) { acc[m][n] = f32x4{b, b, b, b}; PIN(acc[m][n]); }
            if (m < 2) { sacc[m] = f32x4{b, b, b, b}; PIN(sacc[m]); }
        }
        SB();
        wait_in(ws, epoch, lane, wid, pm, j);
        STAMPT(0, 3, 3);
#pragma unroll
        for (int t = 0; t < 2; ++t) {
            set_m0(s.m0l[t]); glds_sc1<0>(s.vl, s.sl); glds_sc1<1024>(s.vl, s.sl); glds_sc1<2048>(s.vl, s.sl); glds_sc1<3072>(s.vl, s.sl);
            set_m0(s.m0l[t] + 4096); glds_sc1<0>(s.vl2, s.sl); glds_sc1<1024>(s.vl2, s.sl); s.sl += s.lstep;
        }
        set_m0(s.m0a + 2 * K2_SA); glds_s<0>(s.va[0], s.sa); glds_s<1024>(s.va[1], s.sa); glds_s<2048>(s.va[2], s.sa); glds_s<3072>(s.va[3], s.sa); s.sa += 128;
        set_m0(s.m0l[2]); glds_sc1<0>(s.vl, s.sl);
        wait_vm_bar<11>();
        STAMP(1, 1);
        f0.b[0] = ldsB2(s.pb[0], s.pb[1]);
        f0.a[0] = ldsA(s.pa[0][0]); f0.a[1] = ldsA(s.pa[0][0] + 2048); f0.a[2] = ldsA(s.pa[1][0]); f0.a[3] = ldsA(s.pa[1][0] + 2048);
        f0.b[1] = ldsB2(s.pb[0] + 256, s.pb[1] + 256); f0.b[2] = ldsB2(s.pb[0] + 512, s.pb[1] + 512); f0.b[3] = ldsB2(s.pb[0] + 768, s.pb[1] + 768); f0.sb = ldsB2(s.ps[0], s.ps[1]);
#pragma unroll 1
        for (int t = 0; t < 9; ++t) {
            g2_iter<0, true, true, 10, true>(s, f0, f1, acc, sacc); g2_iter<1, true, true, 10, true>(s, f0, f1, acc, sacc); g2_iter<2, true, true, 10, true>(s, f0, f1, acc, sacc);
        }
        g2_iter<0, true, true, 10, true>(s, f0, f1, acc, sacc); g2_iter<1, true, true, 10, true>(s, f0, f1, acc, sacc);
        g2_iter<2, true, false, 10, true>(s, f0, f1, acc, sacc);
        const int c = lane & 15, g = lane >> 4;
        const LAS char* z1b = lds + Z1L + (g >> 1) * 2048 + (wr * 64 + c) * 16 + (g & 1) * 8;
        u32x2 z1r[4][4], z1s[2];
#pragma unroll
        for (int m = 0; m < 4; ++m) {
            const int mo = (m ^ (2 * wc)) * 16;
#pragma unroll
            for (int n = 0; n < 4; ++n) z1r[m][n] = *(const LAS u32x2*)(z1b + (wc * 4 + n) * 4096 + mo * 16);
            if (m < 2) z1s[m] = *(const LAS u32x2*)(z1b + 8 * 4096 + mo * 16);
        }
        u64 fa;
        asm volatile("global_load_dwordx2 %0, %1, %2 sc1" : "=v"(fa) : "v"(lane * 8), "s"(ws + WS_FA + wid * 512) : "memory");
        SB();
        g2_iter<0, false, false, 0, true>(s, f0, f1, acc, sacc);
        PIN(fa);
        if (__builtin_expect(!__all(fa == epoch), 0)) wait_fa(ws, epoch, lane);
#pragma unroll
        for (int m = 0; m < 4; ++m) { PIN(z1r[m][0]); PIN(z1r[m][1]); PIN(z1r[m][2]); PIN(z1r[m][3]); }
        PIN(z1s[0]); PIN(z1s[1]);
        unsigned vu[3];
#pragma unroll
        for (int i = 0; i < 3; ++i) {
            const int cgl = 18 * j + (i < 2 ? 2 * (wc * 4 + 2 * i + (g & 1)) : 16) + (g >> 1);
            vu[i] = (cgl >> 2) * 4096 + (cgl & 3) * 256 + c * 16 + (i == 2 ? (g & 1) * 1024 : 0); PIN(vu[i]);
        }
        const char* ub0 = ws + WS_UO + (size_t)(pm * 2 + wr) * 90 * 4096 + wc * 2048; const char* ub1 = ub0 + 2048 - wc * 4096;
        SB();
        g2_phase<1, 0, true, 1, 0>(s, f0, f1, acc, sacc);
        wait_vm_bar<63>(); SB();
        g2_phase<0, 0, false, 2, 1>(s, f1, f0, acc, sacc);
        __builtin_amdgcn_s_setprio(2);
        STAMP(1, 2);
        auto fin = [&](f32x4 v, u32x2 z) { return u32x2{cvtpk(bflo(z[0]) * v[0], bfhi(z[0]) * v[1]), cvtpk(bflo(z[1]) * v[2], bfhi(z[1]) * v[3])}; };
        auto mrg = [&](u32x2 A, u32x2 B) {
            auto r0 = __builtin_amdgcn_permlane16_swap(A[0], B[0], false, false); auto r1 = __builtin_amdgcn_permlane16_swap(A[1], B[1], false, false);
            return u32x4{r0[0], r1[0], r0[1], r1[1]};
        };
#define PUT(m, OFF, ub_) st16s<OFF>(ub_, vu[0], mrg(fin(acc[m][0], z1r[m][0]), fin(acc[m][1], z1r[m][1]))); st16s<OFF>(ub_, vu[1], mrg(fin(acc[m][2], z1r[m][2]), fin(acc[m][3], z1r[m][3])))
        PUT(0, 0, ub0); PUT(1, 1024, ub0); PUT(2, 0, ub1); PUT(3, 1024, ub1);
        st16s<0>(ub0, vu[2], mrg(fin(sacc[0], z1s[0]), fin(sacc[1], z1s[1])));
        SB(); K3_PRE();
        wait_vm<20>();
        if (lane == 0) __hip_atomic_store((gu64*)(ws + WS_FL) + (pm * 2 + wr) * 64 + j * 2 + wc, epoch, RLX_AGENT);
        STAMP(1, 3);
        wait_vm<0>();
        __builtin_amdgcn_s_setprio(0);
    } else {
        __builtin_amdgcn_s_setprio(1);
        const int c = lane & 31, h = lane >> 5, aw = wid - 4;
        const char* kvg = ws + WS_KV;
        bf16x8 q1[2], q2[2]; float mref[2];
        const f32x16 zero = {};
        {
            const int qbase = (aw < 3 ? K2_B0 + 2 * K2_SB + aw * 6144 : K2_K0 + 2 * K2_SK) + 1024;
            const LAS char* ql = lds + qbase;
            unsigned qo[5];
#pragma unroll
            for (int i = 0; i < 5; ++i) { const int k = 18 * c + (i < 4 ? 8 * h + 2 * i : 16); qo[i] = (k >> 3) * 32 + (k & 7) * 2; PIN(qo[i]); }
            const char* qs = ws + WS_Q + (pm * 16 + j) * 9216 + aw * 32;
            unsigned vq = (lane >> 1) * 128 + (lane & 1) * 16; PIN(vq);
            wait_in(ws, epoch, lane, wid, pm, j);
            set_m0(lds0 + qbase); glds_sc1<0>(vq, qs); glds_sc1<1024>(vq, qs + 3072);
            if (lane < 16) glds_sc1<2048>(vq, qs + 6144);
            wait_vm<0>();
#pragma unroll
            for (int t = 0; t < 2; ++t) {
                u32x4 a;
#pragma unroll
                for (int i = 0; i < 4; ++i) a[i] = *(const LAS unsigned*)(ql + qo[i] + t * 16);
                q1[t] = __builtin_bit_cast(bf16x8, a);
                const unsigned e = *(const LAS unsigned*)(ql + qo[4] + t * 16);
                u32x4 b = {h ? 0u : e, 0u, 0u, 0u}; q2[t] = __builtin_bit_cast(bf16x8, b);
                PIN(q1[t]); PIN(q2[t]);
            }
        }
        f32x16 oA = zero, oB = zero;
        bool bad = false;
        const int k2o = 1024 + (c >> 4) * 1024 + (c & 15) * 16;
        const int vo = c < 8 ? 256 + (h * 8 + c) * 16 : 512 + (h * 16 + (c < 16 ? c - 8 : (c < 18 ? c - 2 : (c == 18 ? 12 : 13)))) * 16;
        const LAS char* kvl = lds + K2_K0;
        if (tid == 256) SY[0] = 0;
        asm volatile("s_waitcnt vmcnt(0) lgkmcnt(0)\n\ts_barrier" ::: "memory");
        KVF X, Y;
        kv_read(X, kvl, lane, k2o, vo); kv_read(Y, kvl + 3072, lane, k2o, vo);
#pragma unroll
        for (int t = 0; t < 2; ++t) {
            f32x16 s0 = MFMA32(X.k1, q1[t], zero); s0 = MFMA32(X.k2, q2[t], s0);
            f32x16 s1 = MFMA32(Y.k1, q1[t], zero); s1 = MFMA32(Y.k2, q2[t], s1);
            float m = xhalf_max(fmaxf(max16(s0), max16(s1)));
            mref[t] = bf_round(rintf(m) + ATT_BIAS);
            u32x4 b = __builtin_bit_cast(u32x4, q2[t]); b[1] = h ? 0u : (__float_as_uint(-mref[t]) >> 16); q2[t] = __builtin_bit_cast(bf16x8, b);
        }
        f32x16 sA = MFMA32(X.k1, q1[0], zero); sA = MFMA32(X.k2, q2[0], sA);
        f32x16 sB = zero;
        bf16x8 pA0, pA1, pB0 = {}, pB1 = {};
        half_step<false>(sA, pA0, pA1, sB, X, q1[1], q2[1], oB, X, pB0, pB1, Y, kvl, lane, k2o, vo);
        half_step<false>(sB, pB0, pB1, sA, Y, q1[0], q2[0], oA, X, pA0, pA1, Y, kvl, lane, k2o, vo);
        asm volatile("s_waitcnt lgkmcnt(0)\n\ts_barrier" ::: "memory");
#define A_IV(sl, CHK) { const LAS char* tb = kvl + (sl) * K2_SK; \
            half_step<true>(sA, pA0, pA1, sB, Y, q1[1], q2[1], oB, X, pB0, pB1, X, tb, lane, k2o, vo); \
            half_step<false>(sB, pB0, pB1, sA, X, q1[0], q2[0], oA, Y, pA0, pA1, X, tb, lane, k2o, vo); \
            if (CHK) { mB = __builtin_amdgcn_ballot_w64(!(oB[10] <= 0x1p20f)); }                  \
            half_step<true>(sA, pA0, pA1, sB, X, q1[1], q2[1], oB, Y, pB0, pB1, Y, tb + 3072, lane, k2o, vo); \
            if (CHK) { mA = __builtin_amdgcn_ballot_w64(!(oA[10] <= 0x1p20f)); any = mA | mB; asm volatile("" : "+s"(any)); } \
            half_step<false>(sB, pB0, pB1, sA, Y, q1[0], q2[0], oA, X, pA0, pA1, Y, tb, lane, k2o, vo); }
#define A_BAR() asm volatile("s_waitcnt lgkmcnt(0)\n\ts_barrier" ::: "memory")
        unsigned long long mA, mB, any;
#pragma unroll 1
        for (int t = 0; t < 10; ++t) {
            A_IV(1, false); A_BAR();
            A_IV(2, false); A_BAR();
            A_IV(0, true);
            if (__builtin_expect(any != 0, 0)) {
                if (mA) att_rescale<false>(oA, sA, pA0, pA1, mref[0], q2[0], bad, c, h);
                if (mB) att_rescale<true>(oB, sB, pB0, pB1, mref[1], q2[1], bad, c, h);
            }
            A_BAR();
        }
        A_IV(1, false); A_BAR();
        K3_PRE();
        half_step<false>(sA, pA0, pA1, sB, Y, q1[1], q2[1], oB, X, pB0, pB1, X, kvl, lane, k2o, vo);
        exp_pack(sB, pB0, pB1);
        oA = MFMA32(Y.v0, pA0, oA); oA = MFMA32(Y.v1, pA1, oA);
        oB = MFMA32(Y.v0, pB0, oB); oB = MFMA32(Y.v1, pB1, oB);
        {
            float sa_ = fabsf(oA[10]) * 0x1p27f, sb_ = fabsf(oB[10]) * 0x1p27f;
#pragma unroll
            for (int i = 0; i < 10; ++i) { sa_ += fabsf(oA[i]); sb_ += fabsf(oB[i]); }
            if (__builtin_amdgcn_ballot_w64(!(sa_ + sb_ <= 3.0e38f))) bad = true;
        }
        f32x16 o[2] = {oA, oB};
        if (__builtin_expect(bad, 0)) {
#pragma unroll 1
            for (int t = 0; t < 2; ++t) {
                bf16x8 qa = t ? q1[1] : q1[0]; u32x4 qb = __builtin_bit_cast(u32x4, t ? q2[1] : q2[0]); qb[1] = 0;
                f32x16 oo = zero; float m = -3.0e38f;
                for (int kt = 0; kt < 64; ++kt) {
                    const char* tb = kvg + (size_t)kt * 3072;
                    bf16x8 k1, v0, v1; u32x4 k2v;
                    asm volatile("global_load_dwordx4 %0, %4, %7 sc1\n\tglobal_load_dwordx4 %1, %5, %7 sc1\n\tglobal_load_dwordx4 %2, %6, %7 offset:1024 sc1\n\t"
                                 "global_load_dwordx4 %3, %6, %7 offset:2048 sc1\n\ts_waitcnt vmcnt(0)"
                                 : "=&v"(k1), "=&v"(k2v), "=&v"(v0), "=&v"(v1) : "v"(lane * 16), "v"(k2o), "v"(vo), "s"(tb) : "memory");
                    f32x16 s = MFMA32(k1, qa, zero); s = MFMA32(__builtin_bit_cast(bf16x8, k2v), __builtin_bit_cast(bf16x8, qb), s);
                    float nm = fmaxf(m, xhalf_max(max16(s)));
                    float sc = __builtin_amdgcn_exp2f(m - nm); m = nm;
#pragma unroll
                    for (int i = 0; i < 16; ++i) { oo[i] *= sc; s[i] -= nm; }
                    bf16x8 p0, p1; exp_pack(s, p0, p1);
                    oo = MFMA32(v0, p0, oo); oo = MFMA32(v1, p1, oo);
                }
                if (t) o[1] = oo; else o[0] = oo;
            }
        }
#pragma unroll
        for (int t = 0; t < 2; ++t) {
            float inv = __builtin_amdgcn_rcpf(lo_bcast(o[t][10]));
            LAS unsigned* ob = (LAS unsigned*)(lds + K2_X0 + (aw * 2 + t) * 1168 + 36 * c + 8 * h);
            ob[0] = cvtpk(o[t][0] * inv, o[t][1] * inv); ob[1] = cvtpk(o[t][2] * inv, o[t][3] * inv);
            ob[4] = cvtpk(o[t][4] * inv, o[t][5] * inv); ob[5] = cvtpk(o[t][6] * inv, o[t][7] * inv);
            if (h == 0) ob[8] = cvtpk(o[t][8] * inv, o[t][9] * inv);
        }
        asm volatile("s_waitcnt lgkmcnt(0)" ::: "memory");
        if (lane == 0) __hip_atomic_fetch_add((LAS unsigned*)SY, 1u, __ATOMIC_RELAXED, __HIP_MEMORY_SCOPE_WORKGROUP);
        __builtin_amdgcn_s_setprio(APRIO);
        wait_vm<0>();
    }
    {
        const int g = lane >> 4;
        const char* ua = ws + WS_UO + (size_t)rb * 90 * 4096 + wid * 4096;
        const LAS char* la = lds + wid * K3_RW + lane * 16;
        const LAS char* lb = lds + wid * K3_RW + ((8 * g + ((lane & 15) >> 2)) * 144 + 8 * (lane & 3));
        PIN(la); PIN(lb);
#define LG0() asm volatile("s_waitcnt lgkmcnt(0)" ::: "memory"); SB()
        const unsigned wm3 = __builtin_amdgcn_readfirstlane(wm);
#define IA(i, sl) k3_a(wm3 + (sl) * K3_S, va, ua + K3_P(i) * 32768)
#define IB(i, sl) k3_b(wm3 + (sl) * K3_S, va, wb + K3_P(i) * 36864)
#define RA(d, sl) { _Pragma("unroll") for (int m = 0; m < 4; ++m) d[m] = ldsA(la + (sl) * K3_S + m * 1024); }
#define RB(d, sl) k3_rdb(d, lb + (sl) * K3_S)
        B5 rb0, rb1, rb2, rb3, rb4, rb5; bf16x8 ax[4], ay[4];
        k3_rdb(rb0, lb); k3_rdb(rb1, lb + K3_S); k3_rdb(rb2, lb + 2 * K3_S); k3_rdb(rb3, lb + 3 * K3_S); LG0();
        IB(4, 0); IB(5, 1); IB(6, 2); IB(7, 3);
        if (wid >= 4) {
            while (SY[0] < 4) __builtin_amdgcn_s_sleep(1);
            const int orow = pm * 128 + j * 8;
            char* ob = ws + WS_UO + ((size_t)((orow >> 6) * 90 + 72) * 4 + ((orow >> 4) & 3)) * 1024 + (orow & 15) * 16;
#pragma unroll
            for (int r = 0; r < 3; ++r) {
                const int i = (wid - 4) * 144 + r * 64 + lane, ck = i >> 3, row = i & 7;
                if (r < 2 || lane < 16) st16(ob + (ck >> 2) * 4096 + (ck & 3) * 256 + row * 16, *(const LAS u32x4*)(lds + K2_X0 + row * 1168 + ck * 16));
            }
        }
        wait_vm<10>();
        k3_rdb(rb4, lb); k3_rdb(rb5, lb + K3_S); LG0();
        {
            u64 f = __hip_atomic_load(FLr + (lane & 31), RLX_AGENT);
            wait_vm<0>();
            if (wid >= 4 && lane == 0) __hip_atomic_store((gu64*)(ws + WS_FL) + (pm * 2 + (j >> 3)) * 64 + 32 + (j & 7) * 4 + (wid - 4), epoch, RLX_AGENT);
            if (!__all(f == epoch)) {
                const unsigned long long t0 = __builtin_amdgcn_s_memrealtime();
                do {
                    __builtin_amdgcn_s_sleep(POLLSLEEP);
                    f = __hip_atomic_load(FLr + (lane & 31), RLX_AGENT);
                } while (!__all(f == epoch) && __builtin_amdgcn_s_memrealtime() - t0 < 5000000ull);
            }
        }
        STAMP(2, 0); STAMP(2, 1);
#define STEP(ac_, rb_, W, READS, ISSUES) k3_mm<0, 1>(ac, ac_, rb_); wait_vm<W>(); READS; SB(); k3_mm<1, 3>(ac, ac_, rb_); LG0(); ISSUES; SB(); k3_mm<3, 5>(ac, ac_, rb_)
        IA(0, 0); IA(1, 1);
        f32x4 ac[4][5] = {};
        u32x4 xv[2]; unsigned fo;
        wait_vm<4>(); RA(ax, 0); LG0(); IA(2, 0); SB();
        STEP(ax, rb0, 4, RA(ay, 1), IA(3, 1));
        STEP(ay, rb1, 4, RA(ax, 0); RB(rb0, 2), IA(4, 0); IA(5, 2));
        STEP(ax, rb2, 8, RA(ay, 1); RB(rb1, 3), IB(8, 1); IA(6, 3));
        STEP(ay, rb3, 13, RA(ax, 0),
             asm volatile("s_mov_b32 m0, %2\n\ts_nop 0\n\tglobal_load_lds_dword %0, %1 sc1" ::"v"(lane * 4), "s"(FLr + 32), "s"(lds0 + TOPL + 64 + wid * 256)); IA(7, 0));
        STEP(ax, rb4, 14, RA(ay, 2), IB(9, 2));
        STEP(ay, rb5, 10, RA(ax, 3); RB(rb2, 1), IA(8, 1); IB(10, 3));
        STEP(ax, rb0, 14, RA(ay, 0); fo = *(const volatile LAS unsigned*)(lds + TOPL + 64 + wid * 256 + lane * 4),
             if (__builtin_expect(!__all(fo == ((lane & 1) ? (unsigned)(epoch >> 32) : (unsigned)epoch)), 0)) {
                 const unsigned long long t0 = __builtin_amdgcn_s_memrealtime();
                 u64 f2; do { f2 = __hip_atomic_load(FLr + lane, RLX_AGENT); } while (!__all(f2 == epoch) && __builtin_amdgcn_s_memrealtime() - t0 < 5000000ull);
             }
             IA(9, 0));
        STEP(ay, rb1, 9, RA(ax, 1); RB(rb3, 2), IA(10, 1); if (wid < 2) IB(11, 2));
        STEP(ax, rb2, 4, RA(ay, 0); RB(rb4, 3), if (wid < 2) IA(11, 0);
             _Pragma("unroll") for (int i = 0; i < 2; ++i) { int ci = tid + 512 * i; if (ci >= 576) ci = 575; xv[i] = *(const u32x4*)(x + (size_t)(rb * 64 + ci / 9) * D_ + 72 * j3 + (ci % 9) * 8); });
        STEP(ay, rb3, 0, RA(ax, 1), );
        if (wid < 2) { RA(ay, 0); RB(rb5, 2); SB(); }
        k3_mm<0, 5>(ac, ax, rb4);
        if (wid < 2) k3_mm<0, 5>(ac, ay, rb5);
        STAMP(2, 2);
        float* et = (float*)(smem + wid * K3_RW) + (lane & 15) * 72 + 4 * g;
#pragma unroll
        for (int m = 0; m < 4; ++m)
#pragma unroll
            for (int nn = 0; nn < 5; ++nn) if (nn < 4 || g < 2) *(f32x4*)(et + m * 16 * 72 + nn * 16) = ac[m][nn];
        asm volatile("s_waitcnt lgkmcnt(0)\n\ts_barrier" ::: "memory");
#pragma unroll
        for (int i = 0; i < 2; ++i) {
            const int ci = tid + 512 * i;
            if (i == 0 || wid == 0) {
                int row = ci / 9, ch = ci % 9;
                f32x4 s0 = {bflo(xv[i][0]), bfhi(xv[i][0]), bflo(xv[i][1]), bfhi(xv[i][1])}, s1 = {bflo(xv[i][2]), bfhi(xv[i][2]), bflo(xv[i][3]), bfhi(xv[i][3])};
#pragma unroll
                for (int w = 0; w < 8; ++w) {
                    const float* e = (const float*)(smem + w * K3_RW) + ci * 8;
                    s0 += *(const f32x4*)e; s1 += *(const f32x4*)(e + 4);
                }
                u32x4 o = {cvtpk(s0[0], s0[1]), cvtpk(s0[2], s0[3]), cvtpk(s1[0], s1[1]), cvtpk(s1[2], s1[3])};
                *(u32x4*)(out + (size_t)(rb * 64 + row) * D_ + 72 * j3 + ch * 8) = o;
            }
        }
    }
    STAMP_DRAIN(); STAMP(2, 3);
}
__global__ __launch_bounds__(512, 2) void fused_kernel(const u16* __restrict__ x, const u16* __restrict__ wq, const u16* __restrict__ wk, const u16* __restrict__ wv,
                                                       const u16* __restrict__ w_in, const u16* __restrict__ w_sp, const u16* __restrict__ b_sp, const u16* __restrict__ w_out,
                                                       const u16* __restrict__ wo, char* __restrict__ ws, u16* __restrict__ out) {
    const u64 epoch = phase1(x, wq, wk, wv, w_in, w_sp, b_sp, w_out, wo, ws);
    phase2(w_sp, b_sp, x, ws, out, epoch, __builtin_amdgcn_readfirstlane(threadIdx.x >> 6));
}
__global__ void text_end_marker() { asm volatile(".Lk3_end:"); }

extern "C" void kernel_launch(void* const* d_in, const int* in_sizes, int n_in, void* d_out, int out_size, void* d_ws, size_t ws_size, hipStream_t stream) {
    const u16* x = (const u16*)d_in[0]; const u16* wq = (const u16*)d_in[1]; const u16* wk = (const u16*)d_in[2]; const u16* wv = (const u16*)d_in[3];
    const u16* wo = (const u16*)d_in[4]; const u16* w_in = (const u16*)d_in[6]; const u16* w_sp = (const u16*)d_in[7]; const u16* b_sp = (const u16*)d_in[8];
    const u16* w_out = (const u16*)d_in[9]; char* ws = (char*)d_ws;
    (void)hipFuncSetAttribute((const void*)fused_kernel, hipFuncAttributeMaxDynamicSharedMemorySize, LDS_ALL);
    fused_kernel<<<256, 512, LDS_ALL, stream>>>(x, wq, wk, wv, w_in, w_sp, b_sp, w_out, wo, ws, (u16*)d_out);
}
```

```cpp
#include <hip/hip_runtime.h>
#include <hip/hip_bf16.h>
#include <stdint.h>

#define STNOP 1
typedef unsigned short u16;
typedef unsigned long long u64;
typedef short bf16x8 __attribute__((ext_vector_type(8)));
typedef short s16x4 __attribute__((ext_vector_type(4)));
typedef float f32x2 __attribute__((ext_vector_type(2)));
typedef float f32x4 __attribute__((ext_vector_type(4)));
typedef float f32x16 __attribute__((ext_vector_type(16)));
typedef unsigned u32x4 __attribute__((ext_vector_type(4)));
typedef unsigned u32x2 __attribute__((ext_vector_type(2)));
typedef __bf16 bf16x2_t __attribute__((ext_vector_type(2)));
#define LAS __attribute__((address_space(3)))
#define DI __device__ __forceinline__

constexpr int Z1L = 122880, TOPL = 159744, LDS_ALL = 163840;
constexpr int K2_SA = 16384, K2_SB = 18432, K2_SK = 6144, K2_B0 = 3 * K2_SA, K2_K0 = K2_B0 + 3 * K2_SB, K2_X0 = 147520, K2_LDS = K2_X0 + 9344 + 64 + 2048;
constexpr int S_ = 2048, D_ = 576, FH = 2304, F_ = 4608;

constexpr size_t WS_Q = 0;
constexpr size_t WS_KV = WS_Q + (size_t)S_ * D_ * 2;
constexpr size_t WS_Z1 = WS_KV + 64 * 3072;
constexpr size_t WS_Z2T = WS_Z1 + (size_t)S_ * FH * 2;
constexpr size_t WS_UO = WS_Z2T + (size_t)S_ * FH * 2;
constexpr size_t WS_ST = WS_UO + (size_t)S_ * 2880 * 2;
constexpr size_t WS_XCH = WS_ST + (size_t)S_ * 16 * 8;
constexpr size_t WS_XR = WS_XCH + 4096 + 16384;
constexpr size_t WS_FL = WS_XR + 4096;
constexpr size_t WS_FA = WS_FL + 16384;
constexpr size_t WS_FJ = WS_FA + 2048;
constexpr size_t WS_FP = WS_FJ + 8192;
constexpr size_t WS_W3T = WS_FP + 2048;
constexpr size_t WS_DBG = 128u << 20;
#define STAMP(kid, i)
#define STAMP_DRAIN()
#define STAMPT(tid, kid, i)

DI unsigned cvtpk(float lo, float hi) { f32x2 v = {lo, hi}; return __builtin_bit_cast(unsigned, __builtin_convertvector(v, bf16x2_t)); }
DI float bf2f(u16 b) { return __uint_as_float(((unsigned)b) << 16); }
DI float bflo(unsigned w) { return __uint_as_float(w << 16); }
DI float bfhi(unsigned w) { return __uint_as_float(w & 0xffff0000u); }
#define SB() __builtin_amdgcn_sched_barrier(0)
#define PIN(x) asm volatile("" : "+v"(x))
#define EX(x) __builtin_amdgcn_exp2f(x)
#define MFMA16(a, b, c) __builtin_amdgcn_mfma_f32_16x16x32_bf16(a, b, c, 0, 0, 0)
#define MFMA32(a, b, c) __builtin_amdgcn_mfma_f32_32x32x16_bf16(a, b, c, 0, 0, 0)

DI s16x4 trread(const LAS char* p) { return __builtin_bit_cast(s16x4, __builtin_amdgcn_ds_read_tr16_b64_v4i16((LAS s16x4*)p)); }
template <int N> DI void wait_vm_bar() { asm volatile("s_waitcnt vmcnt(%0) lgkmcnt(0)\n\ts_barrier" ::"n"(N) : "memory"); }
template <int N> DI void wait_vm() { asm volatile("s_waitcnt vmcnt(%0)" ::"n"(N) : "memory"); }
template <int OFF> DI void glds_s(unsigned voff, const char* sbase) { asm volatile("global_load_lds_dwordx4 %0, %1 offset:%2" ::"v"(voff), "s"(sbase), "n"(OFF)); }
template <int OFF> DI void glds_sc1(unsigned voff, const char* sbase) { asm volatile("global_load_lds_dwordx4 %0, %1 offset:%2 sc1" ::"v"(voff), "s"(sbase), "n"(OFF)); }
DI void glds_v(const char* p, unsigned m0v) { asm volatile("s_mov_b32 m0, %1\n\ts_nop 0\n\tglobal_load_lds_dwordx4 %0, off" ::"v"(p), "s"(m0v)); }
DI unsigned ld_sc1_async(const char* sb, unsigned vo) { unsigned v; asm volatile("global_load_dword %0, %1, %2 sc1" : "=v"(v) : "v"(vo), "s"(sb) : "memory"); return v; }
#define WT
DI void st16(void* p, u32x4 v) { asm volatile("global_store_dwordx4 %0, %1, off sc1\n\ts_nop 1" :: "v"(p), "v"(v) : "memory"); }
DI void st8(void* p, u32x2 v) { asm volatile("global_store_dwordx2 %0, %1, off sc1" :: "v"(p), "v"(v) : "memory"); }
template <int OFF> DI void st16s(const char* sb, unsigned vo, u32x4 v) { asm volatile("global_store_dwordx4 %0, %1, %2 offset:%3 sc1\n\ts_nop %4" :: "v"(vo), "v"(v), "s"(sb), "n"(OFF), "n"(STNOP) : "memory"); }
template <int OFF> DI void st8s(const char* sb, unsigned vo, u32x2 v) { asm volatile("global_store_dwordx2 %0, %1, %2 offset:%3 sc1" :: "v"(vo), "v"(v), "s"(sb), "n"(OFF) : "memory"); }
DI float fadd1(float a, float b) { float r; asm("v_add_f32 %0, %1, %2" : "=v"(r) : "v"(a), "v"(b)); return r; }
DI float fmul1(float a, float b) { float r; asm("v_mul_f32 %0, %1, %2" : "=v"(r) : "v"(a), "v"(b)); return r; }
DI float ffma1(float a, float b, float c) { float r; asm("v_fma_f32 %0, %1, %2, %3" : "=v"(r) : "v"(a), "v"(b), "v"(c)); return r; }
DI void set_m0(unsigned v) { asm volatile("s_mov_b32 m0, %0\n\ts_nop 0" ::"s"(v)); }
DI bf16x8 ldsA(const LAS char* p) { return *(const LAS bf16x8*)p; }
DI bf16x8 ldsB2(const LAS char* p0, const LAS char* p1) { s16x4 lo = trread(p0), hi = trread(p1); return __builtin_shufflevector(lo, hi, 0, 1, 2, 3, 4, 5, 6, 7); }
DI bf16x8 ldsB(const LAS char* p) { return ldsB2(p, p + 256); }

DI float gelu_t(float x) {
    const float c0 = -2.0f * 1.4426950408889634f * 0.7978845608028654f;
    const float c1 = c0 * 0.044715f;
    float x2 = x * x;
    float t = __builtin_fmaf(x2, c1, c0);
    float e = __builtin_amdgcn_exp2f(x * t);
    return x * __builtin_amdgcn_rcpf(1.0f + e);
}
constexpr float QSCALE = 0.23570226039551584f * 1.4426950408889634f;

#define KO1 (3 * (((idx & 7) + (idx >> 3)) % 3))
#define KO2 (((idx & 7) + 2 * (idx >> 3) + 4 * (xcd & 1)) % 9)
#define K2_KO (8 * (xcd >> 1))
#define POLLSLEEP 2
#define APRIO 1
#define K3MAP 2
#define K3ROT 0
#define APRE 0
#define AGSLOT 1
#define ZHALF 1
#define POLLW 0
#define POLLSLEEP1 1
#define PLSLOT 0
constexpr int K1_SA = 16384, K1_SB = 24576, K1_B0 = 3 * K1_SA, K1_TAB = K1_B0 + 3 * K1_SB, K1_X0 = K1_TAB + 10240, K1_LDS = K1_X0 + 16384 + 2048;
typedef __attribute__((address_space(1))) unsigned gu32;
typedef __attribute__((address_space(1))) unsigned long long gu64;
#define RLX_AGENT __ATOMIC_RELAXED, __HIP_MEMORY_SCOPE_AGENT
struct K1F { bf16x8 a[4], b[3], x; };
struct K1S {
    unsigned va[2], vb[2]; const char* sa; const char* sb;
    const char* gp; unsigned st;
    int k1, k2;
    unsigned m0a, m0b;
    const LAS char* pa[2]; const LAS char* px[2]; const LAS char* pb[3];
};
DI void k1_issue(K1S& s, int i, int w) {
    if (i == 0) { set_m0(s.m0a + w * K1_SA); glds_s<0>(s.va[0], s.sa); }
    if (i == 1) { glds_s<1024>(s.va[1], s.sa); s.sa += s.k1 == 8 ? -8 * 128 : 128; asm volatile("" : "+s"(s.sa)); }
    if (i == 2) { glds_v(s.gp + (unsigned long long)s.st * (unsigned)s.k1, s.m0b + w * K1_SB + 2048); s.k1 = s.k1 == 8 ? 0 : s.k1 + 1; asm volatile("" : "+s"(s.k1)); }
    if (i == 3) { set_m0(s.m0b + w * K1_SB); glds_s<0>(s.vb[0], s.sb); }
    if (i == 4) { glds_s<1024>(s.vb[1], s.sb); s.sb += s.k2 == 8 ? -8 * 128 * F_ : 128 * F_; s.k2 = s.k2 == 8 ? 0 : s.k2 + 1; asm volatile("" : "+s"(s.sb), "+s"(s.k2)); }
}
DI f32x2 gelu_y2(f32x2 y, float k0, float k1, float ir) {
    f32x2 t = (y * y) * k1 + k0;
    f32x2 a = y * t;
    f32x2 e = {__builtin_amdgcn_exp2f(a[0]), __builtin_amdgcn_exp2f(a[1])};
    f32x2 d = e * ir + ir;
    f32x2 rc = {__builtin_amdgcn_rcpf(d[0]), __builtin_amdgcn_rcpf(d[1])};
    return y * rc;
}
DI float dpp_x1(float x) { return __int_as_float(__builtin_amdgcn_mov_dpp(__float_as_int(x), 0xB1, 0xf, 0xf, true)); }
DI float dpp_x2(float x) { return __int_as_float(__builtin_amdgcn_mov_dpp(__float_as_int(x), 0x4E, 0xf, 0xf, true)); }
DI void kv_store(const char* kv, int sv, int j, int c, int g, f32x4 v) {
    if (j == 4) { st8s<0>(kv + sv * 256, ((g >> 1) * 32 + c) * 16 + (g & 1) * 8, u32x2{cvtpk(v[0], v[1]), cvtpk(v[2], v[3])}); return; }
    const unsigned kk = cvtpk(v[2], v[3]);
    const bool o1 = c & 1, o2 = c & 2;
    float t0 = dpp_x1(o1 ? v[0] : v[1]), t1 = dpp_x1(o1 ? v[2] : v[3]);
    if (o1) { v[0] = t0; v[2] = t1; } else { v[1] = t0; v[3] = t1; }
    t0 = dpp_x2(o2 ? v[0] : v[2]); t1 = dpp_x2(o2 ? v[1] : v[3]);
    if (o2) { v[0] = t0; v[1] = t1; } else { v[2] = t0; v[3] = t1; }
    u32x2 w = {cvtpk(v[0], v[1]), cvtpk(v[2], v[3])};
    const int kq = c >> 2; unsigned off;
    if (j == 5) {
        off = g < 2 ? c * 16 + (g ^ 1) * 8 : 256 + ((kq & 1) * 8 + 4 * (g - 2) + (c & 3)) * 16 + (kq >> 1) * 8;
        if (g < 2) w = g ? u32x2{kk, 0x3f80u} : u32x2{0u, 0u};
    } else {
        off = 512 + ((kq & 1) * 16 + 4 * g + (c & 3)) * 16 + (kq >> 1) * 8;
        if (g == 3 && !o2) { const unsigned cv = o1 ? 0u : 0x3f803f80u; w = u32x2{cv, cv}; }
    }
    st8s<1024>(kv + sv * 1024, off, w);
}
template <int KS, int GL, int RS, int WS, bool RX> DI void k1_p1(K1S& s, const K1F& f, K1F& n, f32x4 (&acc)[4][2], f32x4& sacc, f32x4 (&cso)[2], f32x4& rs, f32x4& rq, const bf16x8& ones) {
    constexpr int AO = RS * K1_SA, BO = RS * K1_SB + KS * 12288;
#define MX(d, a_, b_) d = MFMA16(a_, b_, d); PIN(d); SB()
#define MM(m, nn) MX(acc[m][nn], f.b[nn], f.a[m])
    MM(0, 0); n.b[0] = ldsB(s.pb[0] + BO); SB();
    MM(1, 0); n.a[0] = ldsA(s.pa[KS] + AO); SB();
    MM(2, 0); n.a[1] = ldsA(s.pa[KS] + AO + 2048); SB();
    MM(3, 0); n.a[2] = ldsA(s.pa[KS] + AO + 4096); SB();
    MX(cso[0], f.b[0], ones); n.a[3] = ldsA(s.pa[KS] + AO + 6144); SB();
    MX(sacc, f.b[2], f.x); n.b[1] = ldsB(s.pb[1] + BO); SB();
    MX(cso[1], f.b[2], ones); n.b[2] = ldsB(s.pb[2] + BO); SB();
    MX(rs, ones, f.x); k1_issue(s, GL == 1 ? 0 : 3, WS); SB();
    MX(rq, f.x, f.x); if (RX) n.x = ldsA(s.px[KS] + AO); SB();
    MM(0, 1); k1_issue(s, GL == 1 ? 1 : 4, WS); SB();
    MM(1, 1); if (GL == 1) k1_issue(s, 2, WS); SB();
    MM(2, 1);
    MM(3, 1);
}
template <int KS, int GL, int RS, int WS, bool RD, class E> DI void k1_p2(K1S& s, const K1F& f, K1F& n, f32x4 (&acc)[4][3], f32x4 (&cso)[2], const bf16x8& ones, E epi) {
    constexpr int AO = RS * K1_SA, BO = RS * K1_SB + KS * 12288;
    MM(0, 0); if (RD) n.b[0] = ldsB(s.pb[0] + BO); epi(0); SB();
    MM(1, 0); if (RD) n.a[0] = ldsA(s.pa[KS] + AO); epi(1); SB();
    MM(2, 0); if (RD) n.a[1] = ldsA(s.pa[KS] + AO + 2048); epi(2); SB();
    MM(3, 0); if (RD) n.a[2] = ldsA(s.pa[KS] + AO + 4096); epi(3); SB();
    MX(cso[0], f.b[0], ones); if (RD) n.a[3] = ldsA(s.pa[KS] + AO + 6144); epi(4); SB();
    MM(0, 1); if (RD) n.b[1] = ldsB(s.pb[1] + BO); epi(5); SB();
    MM(1, 1); if (RD) n.b[2] = ldsB(s.pb[2] + BO); epi(6); SB();
    MM(2, 1); if (GL) k1_issue(s, GL == 1 ? 0 : 3, WS); epi(7); SB();
    MM(3, 1); epi(8); SB();
    MX(cso[1], f.b[1], ones); if (GL) k1_issue(s, GL == 1 ? 1 : 4, WS); epi(9); SB();
    MM(0, 2); epi(10); SB();
    MM(1, 2); if (GL == 1) k1_issue(s, 2, WS); epi(11); SB();
    MM(2, 2); epi(12); SB();
    MM(3, 2); epi(13); SB();
#undef MM
#undef MX
}

constexpr unsigned XSALT = 0x9e3779b9u;
DI void epoch_reply(char* ws, int lane, const unsigned (&nv)[4], u64 epoch) {
    const unsigned lo = (unsigned)epoch, hi = (unsigned)(epoch >> 32);
    st16s<0>(ws + WS_XR, lane * 16, u32x4{nv[0], lo, hi, nv[0] ^ lo ^ hi ^ XSALT}); st16s<1024>(ws + WS_XR, lane * 16, u32x4{nv[1], lo, hi, nv[1] ^ lo ^ hi ^ XSALT});
    st16s<2048>(ws + WS_XR, lane * 16, u32x4{nv[2], lo, hi, nv[2] ^ lo ^ hi ^ XSALT}); st16s<3072>(ws + WS_XR, lane * 16, u32x4{nv[3], lo, hi, nv[3] ^ lo ^ hi ^ XSALT});
}
#define XN_LOADS "global_load_dword %0, %4, %5 sc1\n\tglobal_load_dword %1, %4, %5 offset:256 sc1\n\tglobal_load_dword %2, %4, %5 offset:512 sc1\n\tglobal_load_dword %3, %4, %5 offset:768 sc1\n"
DI void epoch_service(char* ws, int lane, u64 epoch) {
    unsigned nv[4];
    asm volatile(XN_LOADS "\ts_waitcnt vmcnt(0)" : "=&v"(nv[0]), "=&v"(nv[1]), "=&v"(nv[2]), "=&v"(nv[3]) : "v"(lane * 4), "s"(ws + WS_XCH) : "memory");
    epoch_reply(ws, lane, nv, epoch);
}
DI unsigned g2_va(int wid, int lane, int i) { int row = 8 * (wid * 4 + i) + (lane >> 3), c = (lane & 7) ^ ((row >> 1) & 7); return row * (S_ * 2) + c * 16 + 4096 - i * 1024; }
DI u64 phase1(const u16* __restrict__ x, const u16* __restrict__ wq, const u16* __restrict__ wk, const u16* __restrict__ wv,
                                                    const u16* __restrict__ w_in, const u16* __restrict__ w_sp, const u16* __restrict__ b_sp, const u16* __restrict__ w_out, const u16* __restrict__ wo, char* __restrict__ ws) {
    extern __shared__ __attribute__((aligned(16))) char smem[];
    const int bid = blockIdx.x, xcd = bid & 7, idx = bid >> 3;
    const int pm = (xcd & 1) * 8 + (idx & 7), j = (xcd >> 1) * 4 + (idx >> 3);
    const int lane = threadIdx.x & 63, wid = __builtin_amdgcn_readfirstlane(threadIdx.x >> 6), wr = wid >> 2, wc = wid & 3;
    const unsigned lds0 = (unsigned)(uintptr_t)smem;
    const LAS char* lds = (const LAS char*)smem;
    {
        const char* pc; unsigned len;
        asm volatile("s_getpc_b64 %0\n.Lk1_here:\n\ts_mov_b32 %1, .Lk3_end-.Lk1_here" : "=s"(pc), "=s"(len));
        unsigned off = ((idx * 8 + wid) * 2 + (lane & 1)) * 128; off = off < len ? off : len;
        asm volatile("s_mov_b32 m0, %2\n\ts_nop 0\n\tglobal_load_lds_dword %0, %1" ::"v"(off), "s"(pc), "s"(lds0 + K1_TAB + 9216 + 512));
    }
    K1S s; s.m0a = lds0 + wid * 2048; s.m0b = lds0 + K1_B0 + wid * 3072;
    const int brow = (lane >> 2) & 7, bc = (lane & 3) ^ (2 * (wid & 1)), bk = 8 * wid + brow;
    const int ko2 = KO2, ko1 = KO1;
    unsigned oa = pm * 128 * D_ * 2, ob = 288 * j, oa1 = oa + ko1 * 128, ob1 = ob + FH * 2 + ko1 * 128 * F_, vg = bk * (F_ * 2) + 256 + 16 * (bc & 1);
    {
        int r8 = lane >> 3;
        s.va[0] = (16 * wid + r8) * (D_ * 2) + (((lane & 7) ^ (r8 >> 1)) * 16);
        s.va[1] = (16 * wid + 8 + r8) * (D_ * 2) + (((lane & 7) ^ (4 + (r8 >> 1))) * 16) - 1024;
        s.vb[0] = bk * (F_ * 2) + (lane >> 5) * 64 + bc * 16 + 1024; s.vb[1] = s.vb[0] + 128 - 1024;
        s.k1 = s.k2 = ko1; s.st = 128 * F_;
        PIN(s.va[0]); PIN(s.va[1]); PIN(s.vb[0]); PIN(s.vb[1]); PIN(vg);
        asm volatile("" : "+s"(oa1), "+s"(ob1), "+s"(s.m0a), "+s"(s.m0b));
    }
    SB();
    s.sa = (const char*)x + oa1; s.sb = (const char*)w_in - 1024 + ob1;
    k1_issue(s, 0, 0); k1_issue(s, 1, 0); k1_issue(s, 3, 0); k1_issue(s, 4, 0);
    SB();
    STAMP(0, 0);
    s.gp = (const char*)w_in + (ob + FH * 2) + vg;
    k1_issue(s, 2, 0);
    const char* sa0 = (const char*)x + oa;
    const char* sb0 = (const char*)w_in - 1024 + ob;
    gu32* XN = (gu32*)(ws + WS_XCH) + pm * 16;
    gu32* XE = (gu32*)(ws + WS_XCH + 4096) + pm * 256;
    gu64* XP = (gu64*)(ws + WS_ST) + (size_t)pm * 16 * 128;
    const char* xe_s = ws + WS_XCH + 4096 + (pm * 256 + j * 16) * 4;
    const unsigned le = (lane & 16) ? 16384 - (pm * 16 + j) * 48 + (lane & 3) * 4 : (lane & 15) * 4;
    unsigned eo = ld_sc1_async(xe_s, le);
#pragma unroll
    for (int t = 1; t < 3; ++t)
#pragma unroll
        for (int i = 0; i < 5; ++i) if (t < 2 || i < 3) k1_issue(s, i, t);
    unsigned nonce = 0;
    const char* gp2; unsigned st2;
    {
        const u16* p; int rs;
        const u16* sp0 = j < 4 ? wq + 512 + 16 * j : (j == 5 ? wk + 10 : (j == 6 ? wv + 8 : wk));
        const u16* sp1 = j < 4 ? wq + 520 + 16 * j : (j == 5 ? wv : (j == 6 ? wv + 10 : wk + 8));
        const int srs = j < 4 ? D_ : 18;
        if (lane >= 32) { p = wq + 32 * j + 8 * bc; rs = D_; }
        else if (bc < 2) { p = w_in + 144 * j + 128 + 8 * bc; rs = F_; }
        else { p = bc == 2 ? sp0 : sp1; rs = srs; }
        gp2 = (const char*)(p + (size_t)bk * rs); st2 = 128 * rs; PIN(gp2); PIN(st2);
    }
    const int r16 = lane & 15, g = lane >> 4, cg = wc ^ (2 * wr);
    const int nf3 = cg == 3 ? 8 : (cg == 2 ? 9 : 10 + cg);
#define NF1(n) ((n) == 2 ? 8 : 2 * wc + ((n) ^ wr))
#define NF2(n) ((n) == 0 ? nf3 : 2 * cg + (((n) - 1) ^ wr))
    const LAS char* pb2[3];
    {
        int q = r16 >> 2, p = lane & 3;
#pragma unroll
        for (int ks = 0; ks < 2; ++ks) { s.pa[ks] = lds + (r16 * 128 + (((4 * ks + g) ^ (r16 >> 1)) * 16) + wr * 8192); s.px[ks] = s.pa[ks] + wc * 2048; PIN(s.pa[ks]); PIN(s.px[ks]); }
#pragma unroll
        for (int n = 0; n < 3; ++n) {
            int nf = NF1(n); s.pb[n] = lds + (K1_B0 + g * 3072 + q * 64 + 8 * p + (nf >> 1) * 512 + (((nf ^ g) & 1) * 32)); PIN(s.pb[n]);
            nf = NF2(n); pb2[n] = lds + (K1_B0 + g * 3072 + q * 64 + 8 * p + (nf >> 1) * 512 + (((nf ^ g) & 1) * 32)); PIN(pb2[n]);
        }
    }
    f32x4 acc1[4][2] = {}; f32x4 sacc = {}; f32x4 cso[2] = {};
    f32x4 rs = {}, rq = {};
#pragma unroll
    for (int m = 0; m < 4; ++m) { PIN(acc1[m][0]); PIN(acc1[m][1]); }
    PIN(sacc); PIN(cso[0]); PIN(cso[1]); PIN(rs); PIN(rq);
    const u32x4 onesv = {0x3f803f80u, 0x3f803f80u, 0x3f803f80u, 0x3f803f80u}; bf16x8 ones = __builtin_bit_cast(bf16x8, onesv); PIN(ones);
    K1F f0, f1;
    SB();
    wait_vm_bar<8>();
    SB();
    f0.b[0] = ldsB(s.pb[0]);
    f0.a[0] = ldsA(s.pa[0]); f0.a[1] = ldsA(s.pa[0] + 2048); f0.a[2] = ldsA(s.pa[0] + 4096); f0.a[3] = ldsA(s.pa[0] + 6144);
    f0.b[2] = ldsB(s.pb[2]); f0.x = ldsA(s.px[0]); f0.b[1] = ldsB(s.pb[1]);
    SB();
    PIN(eo);
    if (wid == 7) {
        nonce = 1;
        while (__ballot(eo == nonce) != 0ull) ++nonce;
        if (lane == 0) __hip_atomic_store(XN + j, nonce, RLX_AGENT);
        __hip_atomic_store(XP + j * 128 + lane, ~0ull, RLX_AGENT); __hip_atomic_store(XP + j * 128 + 64 + lane, ~0ull, RLX_AGENT);
    }
    STAMP(0, 1);
#define P1_A(c) k1_p1<1, 2, (c), ((c) + 2) % 3, true>(s, f0, f1, acc1, sacc, cso, rs, rq, ones); wait_vm_bar<5>(); SB()
#define P1_B(c, RX) k1_p1<0, 1, ((c) + 1) % 3, (c), RX>(s, f1, f0, acc1, sacc, cso, rs, rq, ones)
#pragma unroll 1
    for (int it = 0; it < 2; ++it) { P1_A(0); P1_B(0, true); P1_A(1); P1_B(1, true); P1_A(2); P1_B(2, true); }
    const char* xn_s = ws + WS_XCH + pm * 64; const unsigned l4 = (lane & 15) * 4;
    P1_A(0);
    unsigned echoed = ld_sc1_async(xn_s, l4);
    unsigned alln[4];
    asm volatile("s_cmp_lg_u32 %6, 0\n\ts_cbranch_scc1 .Lld%=\n\t" XN_LOADS ".Lld%=:" : "=&v"(alln[0]), "=&v"(alln[1]), "=&v"(alln[2]), "=&v"(alln[3])
                 : "v"(lane * 4), "s"(ws + WS_XCH), "s"(bid) : "memory", "scc");
    s.k1 = s.k2 = ko2; s.sa = sa0 + ko2 * 128; s.sb = sb0 + (size_t)ko2 * 128 * F_; s.gp = gp2; s.st = st2; SB();
    P1_B(0, true);
    P1_A(1);
    PIN(echoed);
    if (wid == 7 && lane < 16) __hip_atomic_store(XE + lane * 16 + j, echoed, RLX_AGENT);
    PIN(alln[0]); PIN(alln[1]); PIN(alln[2]); PIN(alln[3]);
    if (bid == 0 && wid == 6) epoch_reply(ws, lane, alln, __builtin_amdgcn_s_memrealtime() | (1ull << 63));
    SB();
    P1_B(1, true);
    P1_A(2);
    s.pb[0] = pb2[0]; s.pb[1] = pb2[1]; s.pb[2] = pb2[2];
    P1_B(2, false);
    STAMP(3, 0);
    float* CS = (float*)(smem + K1_TAB);
    f32x4* RS = (f32x4*)(smem + K1_TAB + 2048);
    float* red = (float*)(smem + K1_TAB + 4096);
    f32x2* ZS = (f32x2*)(smem + K1_TAB + 9216);
    volatile LAS unsigned* HS = (volatile LAS unsigned*)(lds + K1_TAB + 1792);
    const float c0 = -2.0f * 1.4426950408889634f * 0.7978845608028654f, c1 = c0 * 0.044715f;
    f32x4 acc2[4][3] = {}; f32x4 cs2[2] = {};
    auto none = [](int) {};
    auto tabs = [&](int st) {
        if (st == 2) { if (r16 == 0) { *(f32x4*)(CS + NF1(0) * 16 + 4 * g) = cso[0]; *(f32x4*)(CS + 128 + 4 * g) = cso[1]; } }
        if (st == 3) {
            float sq = (r16 & 2) ? ((r16 & 1) ? rq[3] : rq[2]) : ((r16 & 1) ? rq[1] : rq[0]);
            float r0_ = rs[0]; PIN(r0_); PIN(sq);
            float mean = fmul1(r0_, 1.0f / D_), var = fmaxf(ffma1(-mean, mean, fmul1(sq, 1.0f / D_)), 0.f);
            rs[0] = mean; rs[1] = var + 1e-5f; PIN(rs);
        }
        if (st == 5) { rs[2] = __builtin_amdgcn_rsqf(rs[1]); PIN(rs); }
        if (st == 7) { float rstd = rs[2]; if (g == (r16 >> 2)) RS[wr * 64 + wc * 16 + r16] = f32x4{rs[0], fmul1(rstd, c0), fmul1(fmul1(rstd, rstd), fmul1(rstd, c1)), fmul1(rs[1], rstd)}; }
    };
    f32x4 er[5], cs1[3], e[3];
    f32x2 su[5], sq[5];
#define ZU(u) ((u) == 8 ? sacc : acc1[((u) >> 1) & 3][(u) & 1])
#define UM(u) ((u) == 8 ? 4 : (u) >> 1)
#define UN(u) ((u) == 8 ? 2 : (u) & 1)
#define PRE(i, u) { f32x4 y = ZU(u) - cs1[UN(u)] * er[UM(u)][0]; e[i] = ((y * y) * er[UM(u)][2] + er[UM(u)][1]) * y; ZU(u) = y; PIN(ZU(u)); PIN(e[i]); }
#define FIN(i, u, first) { ZU(u) *= e[i]; PIN(ZU(u)); f32x4 v = ZU(u); f32x2 lo = {v[0], v[1]}, hi = {v[2], v[3]}; \
        if (first) { su[UM(u)] = lo + hi; sq[UM(u)] = lo * lo + hi * hi; } else { su[UM(u)] += lo; su[UM(u)] += hi; sq[UM(u)] += lo * lo; sq[UM(u)] += hi * hi; } PIN(su[UM(u)]); PIN(sq[UM(u)]); }
#define RED(m) { float a_ = su[m][0] + su[m][1], b_ = sq[m][0] + sq[m][1]; \
            auto x1 = __builtin_amdgcn_permlane16_swap(__float_as_uint(a_), __float_as_uint(b_), false, false);       \
            float t_ = __uint_as_float(x1[0]) + __uint_as_float(x1[1]);                                               \
            auto x2 = __builtin_amdgcn_permlane32_swap(__float_as_uint(t_), __float_as_uint(t_), false, false); \
            t_ = __uint_as_float(x2[0]) + __uint_as_float(x2[1]);                                                     \
            if ((m) < 4) redp[(m) * 160] = t_; else reds[0] = t_; }
    float* redp = red + ((wr * 64 + r16) * 5 + wc) * 2 + (g & 1);
    float* reds = red + ((wr * 64 + wc * 16 + r16) * 5 + 4) * 2 + (g & 1);
#define T1(op, v) asm volatile(op " %0, %0" : "+v"(v))
#define DE1(v, sd) asm volatile("v_fma_f32 %0, %0, %1, %1" : "+v"(v) : "v"(sd))
#define EPI_E(u0, u1, u2) [&](int st) { \
        if (st < 12) { float t_ = e[st >> 2][st & 3]; T1("v_exp_f32", t_); e[st >> 2][st & 3] = t_; } \
        if (st >= 2) { const int v_ = st - 2; float t_ = e[v_ >> 2][v_ & 3]; DE1(t_, er[v_ < 4 ? UM(u0) : (v_ < 8 ? UM(u1) : UM(u2))][3]); e[v_ >> 2][v_ & 3] = t_; } }
#define EPI_R(m0, m1) [&](int st) { if (st < 12) { float t_ = e[st >> 2][st & 3]; T1("v_rcp_f32", t_); e[st >> 2][st & 3] = t_; } \
        if (st == 12 && (m0) >= 0) er[(m0) & 3] = RS[wr * 64 + (m0) * 16 + r16]; if (st == 13 && (m1) >= 0) er[(m1) & 3] = RS[wr * 64 + (m1) * 16 + r16]; }
#define VSYNC() SB(); asm volatile("s_waitcnt lgkmcnt(0)\n\ts_barrier" ::: "memory"); SB()
#define P2_A(c, GL, E) k1_p2<1, GL ? 2 : 0, (c), ((c) + 2) % 3, true>(s, f0, f1, acc2, cs2, ones, E)
#define P2_B(c, GL, RD, E) k1_p2<0, GL ? 1 : 0, ((c) + 1) % 3, (c), RD>(s, f1, f0, acc2, cs2, ones, E)
    P2_A(0, true, tabs); wait_vm_bar<5>(); SB();
    er[0] = RS[wr * 64 + r16]; er[4] = RS[wr * 64 + wc * 16 + r16];
#pragma unroll
    for (int n = 0; n < 3; ++n) cs1[n] = *(const f32x4*)(CS + NF1(n) * 16 + 4 * g);
    PRE(0, 0) PRE(1, 1) PRE(2, 8) VSYNC();
    P2_B(0, true, true, EPI_E(0, 1, 8));
    P2_A(1, true, EPI_R(1, 2)); wait_vm_bar<5>(); SB();
    unsigned ev = ld_sc1_async(xe_s, le);
    FIN(0, 0, true) FIN(1, 1, false) FIN(2, 8, true) PRE(0, 2) PRE(1, 3) PRE(2, 4) RED(0) RED(4) VSYNC();
    P2_B(1, true, true, EPI_E(2, 3, 4));
    const int w3p = bid < 240 ? bid : 239;
    auto epi_r11 = EPI_R(3, -1);
    auto cold = [&](int st) {
        epi_r11(st);
        if (st == 12) {
            const char* sp = (const char*)w_sp + (size_t)(pm * 128 + (idx >> 3) * 32 + wid * 4) * (S_ * 2);
            const unsigned vo = (lane >> 4) * (S_ * 2) + (lane & 15) * 16;
            set_m0(lds0 + K1_X0 + 16384); glds_s<0>(vo, sp); glds_s<1024>(vo, sp - 768);
            asm volatile("s_mov_b32 m0, %2\n\ts_nop 0\n\tglobal_load_lds_dword %0, %1" ::"v"(lane * 4), "s"(b_sp + pm * 128), "s"(lds0 + TOPL + 2304));
        }
        if (st == 13) {
            const int jj = w3p / 30, pc = w3p - jj * 30;
            const char* sp = (pc < 24 ? (const char*)w_out + pc * 96 * 1152 : (const char*)wo + (pc - 24) * 96 * 1152) + jj * 144;
            set_m0(lds0 + K1_X0 + wid * 2048);
#pragma unroll
            for (int i = 0; i < 2; ++i) {
                unsigned c_ = threadIdx.x + 512 * i; c_ = c_ < 864 ? c_ : 863;
                const unsigned row = (c_ * 7282) >> 16, vo = row * (1152 - 144) + c_ * 16;
                if (i == 0) glds_s<0>(vo, sp); else glds_s<1024>(vo, sp - 1024);
            }
        }
    };
    P2_A(2, true, cold); wait_vm_bar<10>(); SB();
    PIN(ev);
    bool hs = false; u64 epoch = 0;
    auto xcheck = [&]() {
        hs = hs || __all((lane & 16) || ev == nonce);
        const unsigned w0 = __builtin_amdgcn_readlane(ev, 16), w1 = __builtin_amdgcn_readlane(ev, 17), w2 = __builtin_amdgcn_readlane(ev, 18), w3 = __builtin_amdgcn_readlane(ev, 19);
        if (w0 == nonce && w3 == (w0 ^ w1 ^ w2 ^ XSALT)) epoch = ((u64)w2 << 32) | w1;
    };
    if (wid == 7) xcheck();
    unsigned nn = ld_sc1_async(xn_s, l4);
    FIN(0, 2, true) FIN(1, 3, false) FIN(2, 4, true) PRE(0, 5) PRE(1, 6) PRE(2, 7) RED(1) VSYNC();
    P2_B(2, true, true, EPI_E(5, 6, 7));
    P2_A(0, true, EPI_R(-1, -1)); wait_vm_bar<11>(); SB();
    FIN(0, 5, false) FIN(1, 6, true) FIN(2, 7, false) RED(2) RED(3) VSYNC();
    f32x2 pu[5];
    auto pub = [&](int st) {
        if (st == 0) {
#pragma unroll
            for (int i = 0; i < 5; ++i) pu[i] = *(const f32x2*)(red + (wid * 16 + r16) * 10 + 2 * i);
        }
        if (st == 4) {
            float a = fadd1(fadd1(fadd1(pu[0][0], pu[1][0]), fadd1(pu[2][0], pu[3][0])), pu[4][0]), b = fadd1(fadd1(fadd1(pu[0][1], pu[1][1]), fadd1(pu[2][1], pu[3][1])), pu[4][1]);
            unsigned bb = __float_as_uint(b); if (bb == 0xffffffffu) bb = 0x7fc00000u;
            st8s<0>((const char*)(XP + j * 128 + wid * 16), r16 * 8, u32x2{__float_as_uint(a), bb});
        }
    };
    P2_B(0, true, true, pub);
    P2_A(1, true, none);
    STAMP(3, 1);
    wait_vm_bar<5>(); SB();
    ev = ld_sc1_async(xe_s, le);
    auto reecho = [&](int st) { if (st == 2) { PIN(nn); if (wid == 7 && nn != echoed) { echoed = nn; if (lane < 16) __hip_atomic_store(XE + lane * 16 + j, nn, RLX_AGENT); } } };
    P2_B(1, true, true, reecho);
    const char* xp_s = ws + WS_ST + (size_t)pm * 16384 + wid * 128; const unsigned xp_v = g * 4096 + r16 * 8;
    u32x2 pv[4];
#define PLOAD() asm volatile("global_load_dwordx2 %0, %4, %5 sc1\n\tglobal_load_dwordx2 %1, %4, %5 offset:1024 sc1\n\tglobal_load_dwordx2 %2, %4, %5 offset:2048 sc1\n\tglobal_load_dwordx2 %3, %4, %5 offset:3072 sc1" \
        : "=&v"(pv[0]), "=&v"(pv[1]), "=&v"(pv[2]), "=&v"(pv[3]) : "v"(xp_v), "s"(xp_s) : "memory")
    u32x4 w3v[2];
    auto w3st = [&](int st) {
        if (st == 8) { w3v[0] = *(const LAS u32x4*)(lds + K1_X0 + wid * 2048 + lane * 16); w3v[1] = *(const LAS u32x4*)(lds + K1_X0 + wid * 2048 + 1024 + lane * 16); }
        if (st == 11) st16s<0>(ws + WS_W3T + w3p * 13824, threadIdx.x * 16, w3v[0]);
        if (st == 12) { if (threadIdx.x < 352) st16s<0>(ws + WS_W3T + w3p * 13824 + 8192, threadIdx.x * 16, w3v[1]); }
        if (st == 13) PLOAD();
    };
    P2_A(2, true, w3st); wait_vm_bar<10>(); SB();
    PIN(ev);
    if (wid == 7) { const bool early = hs; xcheck(); *HS = early ? 1 : (hs ? 2 : 0); }
    P2_B(2, true, true, none);
    P2_A(0, true, none); wait_vm_bar<10>(); SB();
#define PEMPTY() __any(pv[0][1] == 0xffffffffu || pv[1][1] == 0xffffffffu || pv[2][1] == 0xffffffffu || pv[3][1] == 0xffffffffu)
    const unsigned hsf = *HS;
    auto again = [&](int st) {
        if (st == AGSLOT) {
            wait_vm<5>();
            PIN(pv[0]); PIN(pv[1]); PIN(pv[2]); PIN(pv[3]);
            const unsigned long long em = __builtin_amdgcn_ballot_w64(hsf != 1 || pv[0][1] == 0xffffffffu || pv[1][1] == 0xffffffffu || pv[2][1] == 0xffffffffu || pv[3][1] == 0xffffffffu);
            asm volatile("s_cmp_eq_u64 %6, 0\n\ts_cbranch_scc1 .Lpl%=\n\tglobal_load_dwordx2 %0, %4, %5 sc1\n\tglobal_load_dwordx2 %1, %4, %5 offset:1024 sc1\n\t"
                         "global_load_dwordx2 %2, %4, %5 offset:2048 sc1\n\tglobal_load_dwordx2 %3, %4, %5 offset:3072 sc1\n.Lpl%=:"
                         : "+v"(pv[0]), "+v"(pv[1]), "+v"(pv[2]), "+v"(pv[3]) : "v"(xp_v), "s"(xp_s), "s"(em) : "memory", "scc");
        }
    };
    P2_B(0, false, true, again);
    float za, zb;
    auto zstat = [&](int st) {
        if (st == 1) {
            wait_vm<0>();
            PIN(pv[0]); PIN(pv[1]); PIN(pv[2]); PIN(pv[3]);
            if (__builtin_expect(!hsf || PEMPTY(), 0)) {
                const unsigned long long t0 = __builtin_amdgcn_s_memrealtime();
                if (wid == 7) {
                    while (!hs) {
                        ev = ld_sc1_async(xe_s, le); wait_vm<0>(); PIN(ev);
                        xcheck();
                        nn = __hip_atomic_load(XN + (lane & 15), RLX_AGENT);
                        if (nn != echoed) { echoed = nn; if (lane < 16) __hip_atomic_store(XE + lane * 16 + j, nn, RLX_AGENT); }
                        if (__builtin_amdgcn_s_memrealtime() - t0 > 5000000ull) break;
                    }
                    *HS = 1;
                }
                while (*HS == 0 && __builtin_amdgcn_s_memrealtime() - t0 < 6000000ull) __builtin_amdgcn_s_sleep(2);
                do {
                    PLOAD(); wait_vm<0>(); PIN(pv[0]); PIN(pv[1]); PIN(pv[2]); PIN(pv[3]);
                } while (PEMPTY() && __builtin_amdgcn_s_memrealtime() - t0 < 6000000ull);
            }
        }
#define UF(x) __uint_as_float(x)
        if (st == 3) {
            za = fadd1(fadd1(UF(pv[0][0]), UF(pv[1][0])), fadd1(UF(pv[2][0]), UF(pv[3][0]))); PIN(za);
            zb = fadd1(fadd1(UF(pv[0][1]), UF(pv[1][1])), fadd1(UF(pv[2][1]), UF(pv[3][1]))); PIN(zb);
        }
        if (st == 5) {
            auto a1 = __builtin_amdgcn_permlane16_swap(__float_as_uint(za), __float_as_uint(za), false, false); za = fadd1(UF(a1[0]), UF(a1[1])); PIN(za);
            auto b1 = __builtin_amdgcn_permlane16_swap(__float_as_uint(zb), __float_as_uint(zb), false, false); zb = fadd1(UF(b1[0]), UF(b1[1])); PIN(zb);
        }
        if (st == 7) {
            auto a2 = __builtin_amdgcn_permlane32_swap(__float_as_uint(za), __float_as_uint(za), false, false); za = fadd1(UF(a2[0]), UF(a2[1])); PIN(za);
            auto b2 = __builtin_amdgcn_permlane32_swap(__float_as_uint(zb), __float_as_uint(zb), false, false); zb = fadd1(UF(b2[0]), UF(b2[1])); PIN(zb);
        }
        if (st == 9) {
            float m = fmul1(za, 1.0f / FH), var = fmaxf(ffma1(-m, m, fmul1(zb, 1.0f / FH)), 0.f), r = __builtin_amdgcn_rsqf(var + 1e-5f); PIN(r);
            ZS[wid * 16 + r16] = f32x2{r, fmul1(-m, r)};
        }
    };
    P2_A(1, false, zstat); wait_vm_bar<0>(); SB();
    auto merge = [&](u32x2 A, u32x2 B) {
        auto r0 = __builtin_amdgcn_permlane16_swap(A[0], B[0], false, false); auto r1 = __builtin_amdgcn_permlane16_swap(A[1], B[1], false, false);
        return u32x4{r0[0], r1[0], r0[1], r1[1]};
    };
    auto pk4 = [&](f32x4 v) { return u32x2{cvtpk(v[0], v[1]), cvtpk(v[2], v[3])}; };
    {
        const char* z2b = ws + WS_Z2T + (size_t)(j * 32 + 2 * pm + wr) * 18432;
        f32x2 zs[5];
#pragma unroll
        for (int m = 0; m < 5; ++m) zs[m] = ZS[wr * 64 + (m < 4 ? m : wc) * 16 + r16];
        const unsigned vs = (r16 >> 3) * 2304 + (((r16 & 7) ^ (4 * (r16 >> 3))) * 32), v2 = vs + (g & 1) * 4608 + (g >> 1) * 16;
#pragma unroll
        for (int n = 0; n < 2; ++n)
#pragma unroll
            for (int m = 0; m < 4; m += 2)
                st16s<0>(z2b + NF1(n) * 256 + m * 4608, v2, merge(pk4(acc1[m][n] * zs[m][0] + zs[m][1]), pk4(acc1[m + 1][n] * zs[m + 1][0] + zs[m + 1][1])));
        st8s<2048>(z2b + wc * 4608, vs + g * 8, pk4(sacc * zs[4][0] + zs[4][1]));
    }
    VSYNC();
    P2_B(1, false, true, none);
    P2_A(2, false, none);
    P2_B(2, false, false, none);
    if (r16 == 0) { *(f32x4*)(CS + 192 + NF2(0) * 16 + 4 * g) = cs2[0]; *(f32x4*)(CS + 192 + NF2(1) * 16 + 4 * g) = cs2[1]; }
    f32x4* HX = (f32x4*)(smem + K1_B0) + wr * 256 + lane;
    if (cg == 3) { HX[0] = acc2[0][0]; HX[64] = acc2[1][0]; HX[128] = acc2[2][0]; HX[192] = acc2[3][0]; }
    asm volatile("s_waitcnt lgkmcnt(0)\n\ts_barrier" ::: "memory");
    STAMP(3, 2);
    auto gel = [&](f32x4 a, f32x4 c, f32x4 rr) {
        f32x4 y = a - c * rr[0];
        f32x2 y0 = gelu_y2(f32x2{y[0], y[1]}, rr[1], rr[2], rr[3]), y1 = gelu_y2(f32x2{y[2], y[3]}, rr[1], rr[2], rr[3]);
        return u32x2{cvtpk(y0[0], y0[1]), cvtpk(y1[0], y1[1])};
    };
#pragma unroll
    for (int n = 0; n < 3; ++n) cs1[n] = *(const f32x4*)(CS + 192 + NF2(n) * 16 + 4 * g);
#pragma unroll
    for (int m = 0; m < 4; ++m) er[m] = RS[wr * 64 + m * 16 + r16];
    if (cg == 3) {
    } else if (nf3 >= 10 || j < 4) {
        const char* qb = ws + WS_Q + (pm * 16 + wr * 8) * 9216 + (nf3 >= 10 ? 4 * j + (nf3 - 10) * 2 : 64 + 2 * j) * 128;
        const unsigned vq = ((g & 1) * 2 + (r16 >> 3)) * 9216 + (g >> 1) * 128 + (r16 & 7) * 16;
#define QV(m) pk4((acc2[m][0] - cs1[0] * er[m][0]) * (er[m][1] * (QSCALE / c0)))
        st16s<0>(qb, vq, merge(QV(0), QV(1))); st16s<0>(qb + 4 * 9216, vq, merge(QV(2), QV(3)));
    } else if (j < 7) {
#pragma unroll
        for (int m = 0; m < 4; ++m)
            kv_store(ws + WS_KV + (size_t)(pm * 4 + wr * 2 + (m >> 1)) * 3072, m & 1, j, r16, g, (acc2[m][0] - cs1[0] * er[m][0]) * (er[m][1] * (1.0f / c0)));
    }
    SB();
    f32x4 h8 = HX[wc * 64], c8 = *(const f32x4*)(CS + 192 + 128 + 4 * g); er[4] = RS[wr * 64 + wc * 16 + r16];
    u32x4 zw[4];
#pragma unroll
    for (int m = 0; m < ZHALF; ++m) { zw[m] = merge(gel(acc2[m][1], cs1[1], er[m]), gel(acc2[m][2], cs1[2], er[m])); PIN(zw[m]); }
    volatile LAS u64* EW = (volatile LAS u64*)(lds + TOPL + 8);
    if (wid == 7) {
        if (__builtin_expect(epoch == 0, 0)) {
            const u64 t0 = __builtin_amdgcn_s_memrealtime();
            do { ev = ld_sc1_async(xe_s, le); wait_vm<0>(); PIN(ev); xcheck(); } while (epoch == 0 && __builtin_amdgcn_s_memrealtime() - t0 < 5000000ull);
        }
        *EW = epoch;
    }
    SB();
    wait_vm_bar<0>();
    STAMP(0, 3);
    if (wid == 7) {
        const bool kvp = j >= 4 && j < 7;
        const unsigned fo = lane == 0 ? (unsigned)(WS_FJ - WS_FA) + (j * 64 + pm) * 8 : (lane == 1 ? (unsigned)(WS_FP - WS_FA) + (pm * 16 + j) * 8 : (lane == 2 ? bid * 8
                            : (unsigned)(WS_FJ - WS_FA) + ((lane & 15) * 64 + 16 + pm * 3 + (j - 4)) * 8));
        if (lane < 3 || (kvp && g == 1)) __hip_atomic_store((gu64*)(ws + WS_FA + fo), epoch, RLX_AGENT);
    } else if (wid < 4) {
        const char* sa = (const char*)w_sp - 4096 + pm * 128 * S_ * 2;
#pragma unroll
        for (int t = 0; t < 2; ++t) {
            set_m0(lds0 + wid * 4096 + t * K2_SA);
            glds_s<0>(g2_va(wid, lane, 0), sa + t * 128); glds_s<1024>(g2_va(wid, lane, 1), sa + t * 128); glds_s<2048>(g2_va(wid, lane, 2), sa + t * 128); glds_s<3072>(g2_va(wid, lane, 3), sa + t * 128);
        }
    }
    SB();
#pragma unroll
    for (int m = ZHALF; m < 4; ++m) zw[m] = merge(gel(acc2[m][1], cs1[1], er[m]), gel(acc2[m][2], cs1[2], er[m]));
    u32x2 zs8 = gel(h8, c8, er[4]);
    {
        LAS char* zl = (LAS char*)lds + Z1L + wr * 1024 + r16 * 16;
#pragma unroll
        for (int m = 0; m < 4; ++m) *(LAS u32x4*)(zl + cg * 8192 + (2 * ((g & 1) ^ wr) + (g >> 1)) * 2048 + m * 256) = zw[m];
        *(LAS u32x2*)(zl + 32768 + wc * 256 + (g >> 1) * 2048 + (g & 1) * 8) = zs8;
    }
    const u64 e_ = *EW;
    const unsigned eh = __builtin_amdgcn_readfirstlane((unsigned)(e_ >> 32)), el = __builtin_amdgcn_readfirstlane((unsigned)e_);
    return ((u64)eh << 32) | el;
}

DI void wait_in(char* ws, u64 epoch, int lane, int wid, int pm, int j) {
    if (wid == POLLW) {
        const u64 t0 = __builtin_amdgcn_s_memrealtime();
        for (;;) {
            u64 f0_, f1_;
            asm volatile("global_load_dwordx2 %0, %2, %3 sc1\n\tglobal_load_dwordx2 %1, %4, %5 sc1\n\ts_waitcnt vmcnt(0)" : "=&v"(f0_), "=&v"(f1_)
                         : "v"(lane * 8), "s"(ws + WS_FJ + j * 512), "v"((lane & 15) * 8), "s"(ws + WS_FP + pm * 128) : "memory");
            if (__all(f0_ == epoch && f1_ == epoch)) break;
            const u64 dt = __builtin_amdgcn_s_memrealtime() - t0;
            if (dt > 5000000ull) break;
            if (dt > 1000 && blockIdx.x == 0) epoch_service(ws, lane, epoch);
            __builtin_amdgcn_s_sleep(POLLSLEEP1);
        }
    }
    asm volatile("s_barrier" ::: "memory");
}
DI void wait_fa(char* ws, u64 epoch, int lane) {
    const u64 t0 = __builtin_amdgcn_s_memrealtime();
    for (;;) {
        u32x4 a, b;
        asm volatile("global_load_dwordx4 %0, %2, %3 sc1\n\tglobal_load_dwordx4 %1, %2, %3 offset:1024 sc1\n\ts_waitcnt vmcnt(0)" : "=&v"(a), "=&v"(b) : "v"(lane * 16), "s"(ws + WS_FA) : "memory");
        const unsigned lo = (unsigned)epoch, hi = (unsigned)(epoch >> 32);
        if (__all(a[0] == lo && a[1] == hi && a[2] == lo && a[3] == hi && b[0] == lo && b[1] == hi && b[2] == lo && b[3] == hi)) break;
        if (__builtin_amdgcn_s_memrealtime() - t0 > 5000000ull) break;
        if (blockIdx.x == 0) epoch_service(ws, lane, epoch);
        __builtin_amdgcn_s_sleep(8);
    }
}

constexpr float ATT_BIAS = 72.0f;
DI float xhalf_max(float m) { auto rr = __builtin_amdgcn_permlane32_swap(__float_as_uint(m), __float_as_uint(m), false, false); return fmaxf(__uint_as_float(rr[0]), __uint_as_float(rr[1])); }
DI float lo_bcast(float m) { auto rr = __builtin_amdgcn_permlane32_swap(__float_as_uint(m), __float_as_uint(m), false, false); return __uint_as_float(rr[0]); }
DI float bf_round(float x) { return __uint_as_float(cvtpk(x, 0.f) << 16); }
DI float max16(const f32x16& s) {
    float a = fmaxf(fmaxf(s[0], s[1]), s[2]), b = fmaxf(fmaxf(s[3], s[4]), s[5]);
    a = fmaxf(fmaxf(a, s[6]), s[7]); b = fmaxf(fmaxf(b, s[8]), s[9]); a = fmaxf(fmaxf(a, s[10]), s[11]); b = fmaxf(fmaxf(b, s[12]), s[13]);
    a = fmaxf(fmaxf(a, s[14]), s[15]); return fmaxf(a, b);
}
DI void exp_pack(const f32x16& s, bf16x8& p0, bf16x8& p1) {
    float e[16];
#pragma unroll
    for (int i = 0; i < 16; ++i) e[i] = __builtin_amdgcn_exp2f(s[i]);
    u32x4 a = {cvtpk(e[0], e[1]), cvtpk(e[2], e[3]), cvtpk(e[4], e[5]), cvtpk(e[6], e[7])};
    u32x4 b = {cvtpk(e[8], e[9]), cvtpk(e[10], e[11]), cvtpk(e[12], e[13]), cvtpk(e[14], e[15])};
    p0 = __builtin_bit_cast(bf16x8, a); p1 = __builtin_bit_cast(bf16x8, b);
}

struct KVF { bf16x8 k1, k2, v0, v1; };
DI void kv_read(KVF& f, const LAS char* tb, int lane, int k2o, int vo) {
    f.k1 = *(const LAS bf16x8*)(tb + lane * 16); f.k2 = *(const LAS bf16x8*)(tb + k2o);
    f.v0 = *(const LAS bf16x8*)(tb + 1024 + vo); f.v1 = *(const LAS bf16x8*)(tb + 2048 + vo);
}
#define k2frag(x) (x)
template <bool RD>
DI void half_step(f32x16& sx, bf16x8& px0, bf16x8& px1, f32x16& sy, const KVF kq, const bf16x8& qy1, const bf16x8& qy2,
                  f32x16& oy, const KVF kv, const bf16x8& py0, const bf16x8& py1, KVF& ld, const LAS char* tb, int lane, int k2o, int vo) {
    const f32x16 zero = {};
    unsigned a0, a1, a2, a3, b0, b1, b2, b3; float e0, e1, e2, e3;
    f32x16 t = MFMA32(kq.k1, qy1, zero); SB();
    if (RD) ld.k1 = *(const LAS bf16x8*)(tb + lane * 16);
    e0 = EX(sx[0]); e1 = EX(sx[1]); e2 = EX(sx[2]); e3 = EX(sx[3]); PIN(e0); PIN(e1); PIN(e2); PIN(e3); SB();
    sy = MFMA32(kq.k2, qy2, t); SB();
    if (RD) ld.k2 = *(const LAS bf16x8*)(tb + k2o);
    a0 = cvtpk(e0, e1); a1 = cvtpk(e2, e3); PIN(a0); PIN(a1);
    e0 = EX(sx[4]); e1 = EX(sx[5]); e2 = EX(sx[6]); e3 = EX(sx[7]); PIN(e0); PIN(e1); PIN(e2); PIN(e3); SB();
    oy = MFMA32(kv.v0, py0, oy); SB();
    if (RD) ld.v0 = *(const LAS bf16x8*)(tb + 1024 + vo);
    a2 = cvtpk(e0, e1); a3 = cvtpk(e2, e3); PIN(a2); PIN(a3);
    e0 = EX(sx[8]); e1 = EX(sx[9]); e2 = EX(sx[10]); e3 = EX(sx[11]); PIN(e0); PIN(e1); PIN(e2); PIN(e3); SB();
    oy = MFMA32(kv.v1, py1, oy); SB();
    if (RD) ld.v1 = *(const LAS bf16x8*)(tb + 2048 + vo);
    b0 = cvtpk(e0, e1); b1 = cvtpk(e2, e3); PIN(b0); PIN(b1);
    e0 = EX(sx[12]); e1 = EX(sx[13]); e2 = EX(sx[14]); e3 = EX(sx[15]);
    b2 = cvtpk(e0, e1); b3 = cvtpk(e2, e3); PIN(b2); PIN(b3); SB();
    u32x4 a = {a0, a1, a2, a3}, b = {b0, b1, b2, b3};
    px0 = __builtin_bit_cast(bf16x8, a); px1 = __builtin_bit_cast(bf16x8, b);
}
DI bf16x8 p_scale(bf16x8 p, int sh) {
    u32x4 w = __builtin_bit_cast(u32x4, p);
#pragma unroll
    for (int i = 0; i < 4; ++i) w[i] = cvtpk(__builtin_amdgcn_ldexpf(bflo(w[i]), sh), __builtin_amdgcn_ldexpf(bfhi(w[i]), sh));
    return __builtin_bit_cast(bf16x8, w);
}
template <bool PP> DI void att_rescale(f32x16& o, f32x16& s, bf16x8& p0, bf16x8& p1, float& mref, bf16x8& q2, bool& bad, int c, int h) {
    bool nf = false;
#pragma unroll
    for (int i = 0; i < 11; ++i) nf = nf || (!(fabsf(o[i]) <= 3.0e38f) && (i < 8 || h == 0));
    if (__any(nf)) { bad = true; return; }
    float lb = lo_bcast(o[10]);
    int e = __builtin_amdgcn_frexp_expf(lb);
    float d = (float)(e + (int)ATT_BIAS); d = d < 0 ? 0 : d;
    float nm = bf_round(mref + d); float dd = nm - mref; mref = nm;
    int sh = -(int)dd;
#pragma unroll
    for (int i = 0; i < 16; ++i) { o[i] = __builtin_amdgcn_ldexpf(o[i], sh); if (!PP) { float b_ = s[i] - dd; PIN(b_); s[i] = b_; } }
    if (PP) { p0 = p_scale(p0, sh); p1 = p_scale(p1, sh); }
    u32x4 b = __builtin_bit_cast(u32x4, q2); b[1] = h ? 0u : (__float_as_uint(-nm) >> 16); q2 = __builtin_bit_cast(bf16x8, b);
}

#define W3LD 1
#define W3ST 3
struct G2F { bf16x8 a[4], b[4], sb; };
struct G2S {
    unsigned va[4], vl, vl2;
    const char* sa; const char* sl; unsigned lstep;
    unsigned m0a, m0l[3];
    const LAS char* pa[2][2]; const LAS char* pb[2]; const LAS char* ps[2];
};
struct NoHook { DI void operator()(int) const {} };
template <int KS, int GL, bool RD, int RS, int WS, class H = NoHook> DI void g2_phase(G2S& s, const G2F& f, G2F& n, f32x4 (&acc)[4][4], f32x4 (&sacc)[2], H hook = H()) {
    const LAS char* a0 = s.pa[0][KS] + RS * K2_SA; const LAS char* a1 = s.pa[1][KS] + RS * K2_SA;
    constexpr int BO = RS * K2_SB + KS * 9216;
    const LAS char* b0 = s.pb[0] + BO; const LAS char* b1 = s.pb[1] + BO;
#define MM(m, nn) acc[m][nn] = MFMA16(f.b[nn], f.a[m], acc[m][nn]); PIN(acc[m][nn]); SB(); hook((nn) * 4 + (m)); SB()
    MM(0, 0); if (RD) n.b[0] = ldsB2(b0, b1); SB();
    MM(1, 0); if (RD) n.a[0] = ldsA(a0); SB();
    MM(2, 0); if (RD) n.a[1] = ldsA(a0 + 2048); SB();
    MM(3, 0); if (RD) n.a[2] = ldsA(a1); SB();
    MM(0, 1); if (RD) n.a[3] = ldsA(a1 + 2048); SB();
    MM(1, 1); if (RD) n.b[1] = ldsB2(b0 + 256, b1 + 256); SB();
    MM(2, 1); if (RD) n.b[2] = ldsB2(b0 + 512, b1 + 512); SB();
    MM(3, 1); if (RD) n.b[3] = ldsB2(b0 + 768, b1 + 768); SB();
    MM(0, 2); if (RD) n.sb = ldsB2(s.ps[0] + BO, s.ps[1] + BO); SB();
    MM(1, 2); if (GL == 1) { set_m0(s.m0a + WS * K2_SA); glds_s<0>(s.va[0], s.sa); } if (GL == 2) { glds_sc1<1024>(s.vl, s.sl); } SB();
    MM(2, 2);
    MM(3, 2); if (GL == 1) glds_s<1024>(s.va[1], s.sa); if (GL == 2) glds_sc1<2048>(s.vl, s.sl); SB();
    MM(0, 3);
    MM(1, 3); if (GL == 1) glds_s<2048>(s.va[2], s.sa); if (GL == 2) glds_sc1<3072>(s.vl, s.sl); SB();
    MM(2, 3);
    MM(3, 3); if (GL == 1) { glds_s<3072>(s.va[3], s.sa); s.sa += 128; asm volatile("" : "+s"(s.sa)); } if (GL == 2) { set_m0(s.m0l[WS] + 4096); glds_sc1<0>(s.vl2, s.sl); } SB();
    sacc[0] = MFMA16(f.sb, f.a[0], sacc[0]); PIN(sacc[0]); SB();
    if (GL == 1) { set_m0(s.m0l[WS]); glds_sc1<0>(s.vl, s.sl); } if (GL == 2) { glds_sc1<1024>(s.vl2, s.sl); s.sl += s.lstep; asm volatile("" : "+s"(s.sl)); } SB();
    sacc[1] = MFMA16(f.sb, f.a[1], sacc[1]); PIN(sacc[1]); SB();
#undef MM
}
template <int C, bool G2nd, bool G1st, int VM, bool RDN> DI void g2_iter(G2S& s, G2F& f0, G2F& f1, f32x4 (&acc)[4][4], f32x4 (&sacc)[2]) {
    g2_phase<1, G2nd ? 2 : 0, true, C, (C + 2) % 3>(s, f0, f1, acc, sacc);
    wait_vm_bar<VM>(); SB();
    g2_phase<0, G1st ? 1 : 0, RDN, (C + 1) % 3, C>(s, f1, f0, acc, sacc);
}

constexpr int K3_S = 4608, K3_RW = 4 * K3_S;
struct B5 { bf16x8 b[5]; };
DI unsigned k3_so(int w, int sl) {
    const int r = sl == 1 ? 2 : sl - 2;
    if (sl == 0) return Z1L + w * K3_S;
    return w < 3 ? r * K2_SA + w * K3_S : (w < 7 ? K2_B0 + r * K2_SB + (w - 3) * K3_S : K2_K0 + r * K2_SK);
}
DI unsigned k3_orow(int r) { return r < 6 ? (r >> 1) * K2_SA + 3 * K3_S + 64 + (r & 1) * 1168 : K2_K0 + (r - 6) * K2_SK + K3_S + 64; }
DI void k3_b1(int p, unsigned m0v, unsigned va, const char* sb) {
    if (p == 0) { set_m0(m0v); glds_sc1<0>(va, sb); }
    if (p == 1) glds_sc1<1024>(va, sb);
    if (p == 2) glds_sc1<2048>(va, sb);
    if (p == 3) glds_sc1<3072>(va, sb);
    if (p == 4) asm volatile("s_mov_b32 m0, %2\n\ts_mov_b32 exec_hi, 0\n\tglobal_load_lds_dwordx4 %0, %1 sc1\n\ts_mov_b32 exec_hi, -1" ::"v"(va), "s"(sb + 4096), "s"(m0v + 4096));
}
DI void k3_a(unsigned m0v, unsigned va, const char* sa) {
    set_m0(m0v); glds_sc1<0>(va, sa); glds_sc1<1024>(va, sa); glds_sc1<2048>(va, sa); glds_sc1<3072>(va, sa);
}
DI void k3_b(unsigned m0v, unsigned va, const char* sb) {
#pragma unroll
    for (int p = 0; p < 5; ++p) k3_b1(p, m0v, va, sb);
}
DI void k3_rdb(B5& r, const LAS char* p) {
#pragma unroll
    for (int n = 0; n < 5; ++n) r.b[n] = ldsB2(p + n * 32, p + n * 32 + 576);
}
template <int N0, int N1> DI void k3_mm(f32x4 (&acc)[4][5], const bf16x8 (&a)[4], const B5& r) {
#pragma unroll
    for (int n = N0; n < N1; ++n)
#pragma unroll
        for (int m = 0; m < 4; ++m) { acc[m][n] = MFMA16(r.b[n], a[m], acc[m][n]); PIN(acc[m][n]); }
    SB();
}

DI void phase2(const u16* __restrict__ w_sp, const u16* __restrict__ b_sp, const u16* __restrict__ x, char* __restrict__ ws, u16* __restrict__ out, const u64 epoch, const int wid) {
    extern __shared__ __attribute__((aligned(16))) char smem[];
    int bid = blockIdx.x, xcd = bid & 7, idx = bid >> 3;
    int pm = (xcd & 1) * 8 + (idx & 7), j = (xcd >> 1) * 4 + (idx >> 3);
    unsigned zz = 0; asm volatile("" : "+s"(zz));
    const int lane = __builtin_amdgcn_mbcnt_hi(~0u, __builtin_amdgcn_mbcnt_lo(~0u, zz)) & 63;
    const int tid = wid * 64 + lane;
    const unsigned lds0 = (unsigned)(uintptr_t)smem; const LAS char* lds = (const LAS char*)smem;
    STAMP(1, 0);
    const int rb = ((xcd & 1) ^ 1) * 16 + (xcd >> 1) * 4 + (idx & 3), j3 = idx >> 2;
    volatile LAS unsigned* SY = (volatile LAS unsigned*)(lds + TOPL);
    gu64* FLr = (gu64*)(ws + WS_FL) + rb * 64;
    const unsigned va = lane * 16;
    const char* wb = ws + WS_W3T + (size_t)j3 * 90 * 4608 + wid * 4608;
    if (__builtin_expect(wid < 4, 1)) {
        const int wr = wid >> 1, wc = wid & 1;
        G2S s;
#pragma unroll
        for (int i = 0; i < 4; ++i) { s.va[i] = g2_va(wid, lane, i); PIN(s.va[i]); }
        s.vl = lane * 16; s.vl2 = lane * 16 + 4096;
        s.lstep = wid < 3 ? 18432 : 6144;
        unsigned ol = (wid < 3 ? (unsigned)WS_Z2T + j * 32 * 18432 + wid * 6144 : (unsigned)WS_KV), oa = pm * 128 * S_ * 2;
        s.m0a = lds0 + wid * 4096; s.m0l[0] = lds0 + (wid < 3 ? K2_B0 + wid * 6144 : K2_K0); s.m0l[1] = s.m0l[0] + s.lstep; s.m0l[2] = s.m0l[1] + s.lstep;
        PIN(s.vl); PIN(s.vl2);
        asm volatile("" : "+s"(ol), "+s"(oa), "+s"(s.m0a), "+s"(s.m0l[0]));
        SB();
        s.sa = (const char*)w_sp - 4096 + oa; s.sl = ws + ol;
        s.sa += 256;
        {
            int r = lane & 15, g = lane >> 4, q = r >> 2, p = lane & 3;
#pragma unroll
            for (int ks = 0; ks < 2; ++ks) {
                int o = r * 128 + (((4 * ks + g) ^ (r >> 1)) * 16) + wr * 8192;
                s.pa[0][ks] = lds + o + wc * 4096; s.pa[1][ks] = lds + o + (1 - wc) * 4096; PIN(s.pa[0][ks]); PIN(s.pa[1][ks]);
            }
            int ob = K2_B0 + g * 2304 + q * 32 + 8 * p;
            s.pb[0] = lds + ob + wc * 1024 + ((g & 1) * 128); s.pb[1] = lds + ob + wc * 1024 + (((g & 1) ^ 1) * 128);
            s.ps[0] = lds + ob + 2048 + ((g & 1) * 128); s.ps[1] = lds + ob + 2048 + (((g & 1) ^ 1) * 128);
            PIN(s.pb[0]); PIN(s.pb[1]); PIN(s.ps[0]); PIN(s.ps[1]);
        }
        G2F f0, f1;
        SB();
        f32x4 acc[4][4], sacc[2];
#pragma unroll
        for (int m = 0; m < 4; ++m) {
            const float b = bf2f(*(const LAS u16*)(lds + TOPL + 2304 + (wr * 64 + (m ^ (2 * wc)) * 16 + (lane & 15)) * 2));
#pragma unroll
            for (int n = 0; n < 4; ++n) { acc[m][n] = f32x4{b, b, b, b}; PIN(acc[m][n]); }
            if (m < 2) { sacc[m] = f32x4{b, b, b, b}; PIN(sacc[m]); }
        }
        SB();
        wait_in(ws, epoch, lane, wid, pm, j);
        STAMPT(0, 3, 3);
#pragma unroll
        for (int t = 0; t < 2; ++t) {
            set_m0(s.m0l[t]); glds_sc1<0>(s.vl, s.sl); glds_sc1<1024>(s.vl, s.sl); glds_sc1<2048>(s.vl, s.sl); glds_sc1<3072>(s.vl, s.sl);
            set_m0(s.m0l[t] + 4096); glds_sc1<0>(s.vl2, s.sl); glds_sc1<1024>(s.vl2, s.sl); s.sl += s.lstep;
        }
        set_m0(s.m0a + 2 * K2_SA); glds_s<0>(s.va[0], s.sa); glds_s<1024>(s.va[1], s.sa); glds_s<2048>(s.va[2], s.sa); glds_s<3072>(s.va[3], s.sa); s.sa += 128;
        set_m0(s.m0l[2]); glds_sc1<0>(s.vl, s.sl);
        wait_vm_bar<11>();
        STAMP(1, 1);
        f0.b[0] = ldsB2(s.pb[0], s.pb[1]);
        f0.a[0] = ldsA(s.pa[0][0]); f0.a[1] = ldsA(s.pa[0][0] + 2048); f0.a[2] = ldsA(s.pa[1][0]); f0.a[3] = ldsA(s.pa[1][0] + 2048);
        f0.b[1] = ldsB2(s.pb[0] + 256, s.pb[1] + 256); f0.b[2] = ldsB2(s.pb[0] + 512, s.pb[1] + 512); f0.b[3] = ldsB2(s.pb[0] + 768, s.pb[1] + 768); f0.sb = ldsB2(s.ps[0], s.ps[1]);
#pragma unroll 1
        for (int t = 0; t < 9; ++t) {
            g2_iter<0, true, true, 10, true>(s, f0, f1, acc, sacc); g2_iter<1, true, true, 10, true>(s, f0, f1, acc, sacc); g2_iter<2, true, true, 10, true>(s, f0, f1, acc, sacc);
        }
        u64 fa;
        asm volatile("global_load_dwordx2 %0, %1, %2 sc1" : "=v"(fa) : "v"(lane * 8), "s"(ws + WS_FA + wid * 512) : "memory");
        g2_iter<0, true, true, 10, true>(s, f0, f1, acc, sacc); g2_iter<1, true, true, 10, true>(s, f0, f1, acc, sacc);
        PIN(fa);
        if (__builtin_expect(!__all(fa == epoch), 0)) wait_fa(ws, epoch, lane);
        const int c = lane & 15, g = lane >> 4;
        const LAS char* z1b = lds + Z1L + (g >> 1) * 2048 + (wr * 64 + c) * 16 + (g & 1) * 8;
        u32x2 z1r[4][4], z1s[2];
#pragma unroll
        for (int m = 0; m < 4; ++m) {
            const int mo = (m ^ (2 * wc)) * 16;
#pragma unroll
            for (int n = 0; n < 4; ++n) z1r[m][n] = *(const LAS u32x2*)(z1b + (wc * 4 + n) * 4096 + mo * 16);
            if (m < 2) z1s[m] = *(const LAS u32x2*)(z1b + 8 * 4096 + mo * 16);
        }
        SB();
#define PREB(i) [&](int st) { if (st < 5) k3_b1(st, lds0 + k3_so(wid, i), va, wb + (i) * 36864); else if (st < 10) k3_b1(st - 5, lds0 + k3_so(wid + 4, i), va, wb + (i) * 36864 + 4 * 4608); }
        g2_phase<1, 2, true, 2, 1>(s, f0, f1, acc, sacc);
        wait_vm_bar<10>(); SB();
        g2_phase<0, 0, true, 0, 2>(s, f1, f0, acc, sacc, PREB(0));
        g2_phase<1, 0, true, 0, 2>(s, f0, f1, acc, sacc, PREB(1));
        wait_vm_bar<20>(); SB();
        g2_phase<0, 0, true, 1, 0>(s, f1, f0, acc, sacc, PREB(2));
        unsigned vu[3];
#pragma unroll
        for (int i = 0; i < 3; ++i) {
            const int cgl = 18 * j + (i < 2 ? 2 * (wc * 4 + 2 * i + (g & 1)) : 16) + (g >> 1);
            vu[i] = (cgl >> 2) * 4096 + (cgl & 3) * 256 + c * 16 + (i == 2 ? (g & 1) * 1024 : 0); PIN(vu[i]);
        }
        const char* ub0 = ws + WS_UO + (size_t)(pm * 2 + wr) * 90 * 4096 + wc * 2048; const char* ub1 = ub0 + 2048 - wc * 4096;
        SB();
        g2_phase<1, 0, true, 1, 0>(s, f0, f1, acc, sacc);
        wait_vm_bar<0>(); SB();
        g2_phase<0, 0, false, 2, 1>(s, f1, f0, acc, sacc);
        __builtin_amdgcn_s_setprio(2);
        STAMP(1, 2);
        auto fin = [&](f32x4 v, u32x2 z) { return u32x2{cvtpk(fmul1(bflo(z[0]), v[0]), fmul1(bfhi(z[0]), v[1])), cvtpk(fmul1(bflo(z[1]), v[2]), fmul1(bfhi(z[1]), v[3]))}; };
        auto mrg = [&](u32x2 A, u32x2 B) {
            auto r0 = __builtin_amdgcn_permlane16_swap(A[0], B[0], false, false); auto r1 = __builtin_amdgcn_permlane16_swap(A[1], B[1], false, false);
            return u32x4{r0[0], r1[0], r0[1], r1[1]};
        };
#define PUT(m, OFF, ub_) st16s<OFF>(ub_, vu[0], mrg(fin(acc[m][0], z1r[m][0]), fin(acc[m][1], z1r[m][1]))); st16s<OFF>(ub_, vu[1], mrg(fin(acc[m][2], z1r[m][2]), fin(acc[m][3], z1r[m][3])))
        PUT(0, 0, ub0); PUT(1, 1024, ub0); PUT(2, 0, ub1); PUT(3, 1024, ub1);
        st16s<0>(ub0, vu[2], mrg(fin(sacc[0], z1s[0]), fin(sacc[1], z1s[1])));
        SB(); k3_b(lds0 + k3_so(wid, 3), va, wb + 3 * 36864);
        wait_vm<5>();
        if (lane == 0) __hip_atomic_store((gu64*)(ws + WS_FL) + (pm * 2 + wr) * 64 + j * 2 + wc, epoch, RLX_AGENT);
        STAMP(1, 3);
        __builtin_amdgcn_s_setprio(0);
    } else {
        __builtin_amdgcn_s_setprio(1);
        const int c = lane & 31, h = lane >> 5, aw = wid - 4;
        const char* kvg = ws + WS_KV;
        bf16x8 q1[2], q2[2]; float mref[2];
        const f32x16 zero = {};
        {
            const int qbase = (aw < 3 ? K2_B0 + 2 * K2_SB + aw * 6144 : K2_K0 + 2 * K2_SK) + 1024;
            const LAS char* ql = lds + qbase;
            unsigned qo[5];
#pragma unroll
            for (int i = 0; i < 5; ++i) { const int k = 18 * c + (i < 4 ? 8 * h + 2 * i : 16); qo[i] = (k >> 3) * 32 + (k & 7) * 2; PIN(qo[i]); }
            const char* qs = ws + WS_Q + (pm * 16 + j) * 9216 + aw * 32;
            unsigned vq = (lane >> 1) * 128 + (lane & 1) * 16; PIN(vq);
            wait_in(ws, epoch, lane, wid, pm, j);
            set_m0(lds0 + qbase); glds_sc1<0>(vq, qs); glds_sc1<1024>(vq, qs + 3072);
            if (lane < 16) glds_sc1<2048>(vq, qs + 6144);
            wait_vm<0>();
#pragma unroll
            for (int t = 0; t < 2; ++t) {
                u32x4 a;
#pragma unroll
                for (int i = 0; i < 4; ++i) a[i] = *(const LAS unsigned*)(ql + qo[i] + t * 16);
                q1[t] = __builtin_bit_cast(bf16x8, a);
                const unsigned e = *(const LAS unsigned*)(ql + qo[4] + t * 16);
                u32x4 b = {h ? 0u : e, 0u, 0u, 0u}; q2[t] = __builtin_bit_cast(bf16x8, b);
                PIN(q1[t]); PIN(q2[t]);
            }
        }
        f32x16 oA = zero, oB = zero;
        bool bad = false;
        const int k2o = 1024 + (c >> 4) * 1024 + (c & 15) * 16;
        const int vo = c < 8 ? 256 + (h * 8 + c) * 16 : 512 + (h * 16 + (c < 16 ? c - 8 : (c < 18 ? c - 2 : (c == 18 ? 12 : 13)))) * 16;
        const LAS char* kvl = lds + K2_K0;
        if (tid == 256) SY[0] = 0;
        asm volatile("s_waitcnt vmcnt(0) lgkmcnt(0)\n\ts_barrier" ::: "memory");
        KVF X, Y;
        kv_read(X, kvl, lane, k2o, vo); kv_read(Y, kvl + 3072, lane, k2o, vo);
#pragma unroll
        for (int t = 0; t < 2; ++t) {
            f32x16 s0 = MFMA32(X.k1, q1[t], zero); s0 = MFMA32(X.k2, q2[t], s0);
            f32x16 s1 = MFMA32(Y.k1, q1[t], zero); s1 = MFMA32(Y.k2, q2[t], s1);
            float m = xhalf_max(fmaxf(max16(s0), max16(s1)));
            mref[t] = bf_round(rintf(m) + ATT_BIAS);
            u32x4 b = __builtin_bit_cast(u32x4, q2[t]); b[1] = h ? 0u : (__float_as_uint(-mref[t]) >> 16); q2[t] = __builtin_bit_cast(bf16x8, b);
        }
        f32x16 sA = MFMA32(X.k1, q1[0], zero); sA = MFMA32(X.k2, q2[0], sA);
        f32x16 sB = zero;
        bf16x8 pA0, pA1, pB0 = {}, pB1 = {};
        half_step<false>(sA, pA0, pA1, sB, X, q1[1], q2[1], oB, X, pB0, pB1, Y, kvl, lane, k2o, vo);
        half_step<false>(sB, pB0, pB1, sA, Y, q1[0], q2[0], oA, X, pA0, pA1, Y, kvl, lane, k2o, vo);
        asm volatile("s_waitcnt lgkmcnt(0)\n\ts_barrier" ::: "memory");
#define A_IV(sl, CHK) { const LAS char* tb = kvl + (sl) * K2_SK; \
            half_step<true>(sA, pA0, pA1, sB, Y, q1[1], q2[1], oB, X, pB0, pB1, X, tb, lane, k2o, vo); \
            half_step<false>(sB, pB0, pB1, sA, X, q1[0], q2[0], oA, Y, pA0, pA1, X, tb, lane, k2o, vo); \
            if (CHK) { mB = __builtin_amdgcn_ballot_w64(!(oB[10] <= 0x1p20f)); }                  \
            half_step<true>(sA, pA0, pA1, sB, X, q1[1], q2[1], oB, Y, pB0, pB1, Y, tb + 3072, lane, k2o, vo); \
            if (CHK) { mA = __builtin_amdgcn_ballot_w64(!(oA[10] <= 0x1p20f)); any = mA | mB; asm volatile("" : "+s"(any)); } \
            half_step<false>(sB, pB0, pB1, sA, Y, q1[0], q2[0], oA, X, pA0, pA1, Y, tb, lane, k2o, vo); }
#define A_BAR() asm volatile("s_waitcnt lgkmcnt(0)\n\ts_barrier" ::: "memory")
        unsigned long long mA, mB, any;
#pragma unroll 1
        for (int t = 0; t < 10; ++t) {
            A_IV(1, false); A_BAR();
            A_IV(2, false); A_BAR();
            A_IV(0, true);
            if (__builtin_expect(any != 0, 0)) {
                if (mA) att_rescale<false>(oA, sA, pA0, pA1, mref[0], q2[0], bad, c, h);
                if (mB) att_rescale<true>(oB, sB, pB0, pB1, mref[1], q2[1], bad, c, h);
            }
            A_BAR();
        }
        A_IV(1, false); A_BAR();
        k3_b(lds0 + k3_so(wid, 3), va, wb + 3 * 36864); SB();
        half_step<false>(sA, pA0, pA1, sB, Y, q1[1], q2[1], oB, X, pB0, pB1, X, kvl, lane, k2o, vo);
        exp_pack(sB, pB0, pB1);
        oA = MFMA32(Y.v0, pA0, oA); oA = MFMA32(Y.v1, pA1, oA);
        oB = MFMA32(Y.v0, pB0, oB); oB = MFMA32(Y.v1, pB1, oB);
        {
            float sa_ = fabsf(oA[10]) * 0x1p27f, sb_ = fabsf(oB[10]) * 0x1p27f;
#pragma unroll
            for (int i = 0; i < 10; ++i) { sa_ += fabsf(oA[i]); PIN(sa_); sb_ += fabsf(oB[i]); PIN(sb_); }
            if (__builtin_amdgcn_ballot_w64(!(sa_ + sb_ <= 3.0e38f))) bad = true;
        }
        f32x16 o[2] = {oA, oB};
        if (__builtin_expect(bad, 0)) {
#pragma unroll 1
            for (int t = 0; t < 2; ++t) {
                bf16x8 qa = t ? q1[1] : q1[0]; u32x4 qb = __builtin_bit_cast(u32x4, t ? q2[1] : q2[0]); qb[1] = 0;
                f32x16 oo = zero; float m = -3.0e38f;
                for (int kt = 0; kt < 64; ++kt) {
                    const char* tb = kvg + (size_t)kt * 3072;
                    bf16x8 k1, v0, v1; u32x4 k2v;
                    asm volatile("global_load_dwordx4 %0, %4, %7 sc1\n\tglobal_load_dwordx4 %1, %5, %7 sc1\n\tglobal_load_dwordx4 %2, %6, %7 offset:1024 sc1\n\t"
                                 "global_load_dwordx4 %3, %6, %7 offset:2048 sc1\n\ts_waitcnt vmcnt(0)"
                                 : "=&v"(k1), "=&v"(k2v), "=&v"(v0), "=&v"(v1) : "v"(lane * 16), "v"(k2o), "v"(vo), "s"(tb) : "memory");
                    f32x16 s = MFMA32(k1, qa, zero); s = MFMA32(__builtin_bit_cast(bf16x8, k2v), __builtin_bit_cast(bf16x8, qb), s);
                    float nm = fmaxf(m, xhalf_max(max16(s)));
                    float sc = __builtin_amdgcn_exp2f(m - nm); m = nm;
#pragma unroll
                    for (int i = 0; i < 16; ++i) { float a_ = oo[i] * sc, b_ = s[i] - nm; PIN(a_); PIN(b_); oo[i] = a_; s[i] = b_; }
                    bf16x8 p0, p1; exp_pack(s, p0, p1);
                    oo = MFMA32(v0, p0, oo); oo = MFMA32(v1, p1, oo);
                }
                if (t) o[1] = oo; else o[0] = oo;
            }
        }
#pragma unroll
        for (int t = 0; t < 2; ++t) {
            float inv = __builtin_amdgcn_rcpf(lo_bcast(o[t][10])); PIN(inv);
            LAS unsigned* ob = (LAS unsigned*)(lds + k3_orow(aw * 2 + t) + 36 * c + 8 * h);
#define ON(i) cvtpk(fmul1(o[t][i], inv), fmul1(o[t][(i) + 1], inv))
            ob[0] = ON(0); ob[1] = ON(2); ob[4] = ON(4); ob[5] = ON(6);
            if (h == 0) ob[8] = ON(8);
        }
        asm volatile("s_waitcnt lgkmcnt(0)" ::: "memory");
        if (lane == 0) __hip_atomic_fetch_add((LAS unsigned*)SY, 1u, __ATOMIC_RELAXED, __HIP_MEMORY_SCOPE_WORKGROUP);
        __builtin_amdgcn_s_setprio(APRIO);
    }
    {
        const int g = lane >> 4;
        const char* ua = ws + WS_UO + (size_t)rb * 90 * 4096 + wid * 4096;
        const LAS char* la = lds + lane * 16;
        const LAS char* lb = lds + ((8 * g + ((lane & 15) >> 2)) * 144 + 8 * (lane & 3));
        PIN(la); PIN(lb);
        unsigned so[4];
#pragma unroll
        for (int i = 0; i < 4; ++i) so[i] = __builtin_amdgcn_readfirstlane(k3_so(wid, i));
#define LG0() asm volatile("s_waitcnt lgkmcnt(0)" ::: "memory"); SB()
#define IA(i, sl) k3_a(lds0 + so[sl], va, ua + (i) * 32768)
#define IB(i, sl) k3_b(lds0 + so[sl], va, wb + (i) * 36864)
#define RA(d, sl) { asm volatile("" : "+s"(so[sl])); const LAS char* p_ = la + so[sl]; _Pragma("unroll") for (int m = 0; m < 4; ++m) d[m] = ldsA(p_ + m * 1024); }
#define RB(d, sl) { asm volatile("" : "+s"(so[sl])); k3_rdb(d, lb + so[sl]); }
        B5 rb0, rb1, rb2, rb3, rb4, rb5; bf16x8 ax[4], ay[4];
        RB(rb0, 0); RB(rb1, 1); RB(rb2, 2); LG0();
        IB(4, 0); IB(5, 1); IB(6, 2);
        if (wid < 4) wait_vm<16>(); else wait_vm<15>();
        RB(rb3, 3); LG0();
        IB(7, 3);
        if (wid >= 4) {
            while (SY[0] < 4) __builtin_amdgcn_s_sleep(1);
            const int orow = pm * 128 + j * 8;
            char* ob = ws + WS_UO + ((size_t)((orow >> 6) * 90 + 72) * 4 + ((orow >> 4) & 3)) * 1024 + (orow & 15) * 16;
#pragma unroll
            for (int r = 0; r < 3; ++r) {
                const int i = (wid - 4) * 144 + r * 64 + lane, ck = i >> 3, row = i & 7;
                if (r < 2 || lane < 16) st16(ob + (ck >> 2) * 4096 + (ck & 3) * 256 + row * 16, *(const LAS u32x4*)(lds + k3_orow(row) + ck * 16));
            }
        }
        wait_vm<10>();
        RB(rb4, 0); RB(rb5, 1); LG0();
        {
            u64 f = __hip_atomic_load(FLr + (lane & 31), RLX_AGENT);
            wait_vm<0>();
            if (wid >= 4 && lane == 0) __hip_atomic_store((gu64*)(ws + WS_FL) + (pm * 2 + (j >> 3)) * 64 + 32 + (j & 7) * 4 + (wid - 4), epoch, RLX_AGENT);
            if (!__all(f == epoch)) {
                const unsigned long long t0 = __builtin_amdgcn_s_memrealtime();
                do {
                    __builtin_amdgcn_s_sleep(POLLSLEEP);
                    f = __hip_atomic_load(FLr + (lane & 31), RLX_AGENT);
                } while (!__all(f == epoch) && __builtin_amdgcn_s_memrealtime() - t0 < 5000000ull);
            }
        }
        STAMP(2, 0); STAMP(2, 1);
#define STEP(ac_, rb_, W, READS, ISSUES) k3_mm<0, 1>(ac, ac_, rb_); wait_vm<W>(); READS; SB(); k3_mm<1, 3>(ac, ac_, rb_); LG0(); ISSUES; SB(); k3_mm<3, 5>(ac, ac_, rb_)
        IA(0, 0); IA(1, 1);
        f32x4 ac[4][5] = {};
        u32x4 xv[2]; unsigned fo;
        wait_vm<4>(); RA(ax, 0); LG0(); IA(2, 0); SB();
        STEP(ax, rb0, 4, RA(ay, 1), IA(3, 1));
        STEP(ay, rb1, 4, RA(ax, 0); RB(rb0, 2), IA(4, 0); IA(5, 2));
        STEP(ax, rb2, 8, RA(ay, 1); RB(rb1, 3), IB(8, 1); IA(6, 3));
        STEP(ay, rb3, 13, RA(ax, 0),
             asm volatile("s_mov_b32 m0, %2\n\ts_nop 0\n\tglobal_load_lds_dword %0, %1 sc1" ::"v"(lane * 4), "s"(FLr + 32), "s"(lds0 + TOPL + 64 + wid * 256)); IA(7, 0));
        STEP(ax, rb4, 14, RA(ay, 2), IB(9, 2));
        STEP(ay, rb5, 10, RA(ax, 3); RB(rb2, 1), IA(8, 1); IB(10, 3));
        STEP(ax, rb0, 14, RA(ay, 0); fo = *(const volatile LAS unsigned*)(lds + TOPL + 64 + wid * 256 + lane * 4),
             if (__builtin_expect(!__all(fo == ((lane & 1) ? (unsigned)(epoch >> 32) : (unsigned)epoch)), 0)) {
                 const unsigned long long t0 = __builtin_amdgcn_s_memrealtime();
                 u64 f2; do { f2 = __hip_atomic_load(FLr + lane, RLX_AGENT); } while (!__all(f2 == epoch) && __builtin_amdgcn_s_memrealtime() - t0 < 5000000ull);
             }
             IA(9, 0));
        STEP(ay, rb1, 9, RA(ax, 1); RB(rb3, 2), IA(10, 1); if (wid < 2) IB(11, 2));
        STEP(ax, rb2, 4, RA(ay, 0); RB(rb4, 3), if (wid < 2) IA(11, 0);
             _Pragma("unroll") for (int i = 0; i < 2; ++i) { int ci = tid + 512 * i; if (ci >= 576) ci = 575; xv[i] = *(const u32x4*)(x + (size_t)(rb * 64 + ci / 9) * D_ + 72 * j3 + (ci % 9) * 8); });
        STEP(ay, rb3, 0, RA(ax, 1), );
        if (wid < 2) { RA(ay, 0); RB(rb5, 2); SB(); }
        k3_mm<0, 5>(ac, ax, rb4);
        if (wid < 2) k3_mm<0, 5>(ac, ay, rb5);
        STAMP(2, 2);
#pragma unroll
        for (int m = 0; m < 4; ++m) {
            float* et = (float*)(smem + so[m]) + (lane & 15) * 72 + 4 * g;
#pragma unroll
            for (int nn = 0; nn < 5; ++nn) if (nn < 4 || g < 2) *(f32x4*)(et + nn * 16) = ac[m][nn];
        }
        asm volatile("s_waitcnt lgkmcnt(0)\n\ts_barrier" ::: "memory");
#pragma unroll
        for (int i = 0; i < 2; ++i) {
            const int ci = tid + 512 * i;
            if (i == 0 || wid == 0) {
                int row = ci / 9, ch = ci % 9;
                f32x4 s0 = {bflo(xv[i][0]), bfhi(xv[i][0]), bflo(xv[i][1]), bfhi(xv[i][1])}, s1 = {bflo(xv[i][2]), bfhi(xv[i][2]), bflo(xv[i][3]), bfhi(xv[i][3])};
#pragma unroll
                for (int w = 0; w < 8; ++w) {
                    const float* e = (const float*)(smem + k3_so(w, row >> 4)) + (ci - (row >> 4) * 144) * 8;
                    s0 += *(const f32x4*)e; s1 += *(const f32x4*)(e + 4);
                }
                u32x4 o = {cvtpk(s0[0], s0[1]), cvtpk(s0[2], s0[3]), cvtpk(s1[0], s1[1]), cvtpk(s1[2], s1[3])};
                *(u32x4*)(out + (size_t)(rb * 64 + row) * D_ + 72 * j3 + ch * 8) = o;
            }
        }
    }
    STAMP_DRAIN(); STAMP(2, 3);
}
__global__ __launch_bounds__(512, 2) void fused_kernel(const u16* __restrict__ x, const u16* __restrict__ wq, const u16* __restrict__ wk, const u16* __restrict__ wv,
                                                       const u16* __restrict__ w_in, const u16* __restrict__ w_sp, const u16* __restrict__ b_sp, const u16* __restrict__ w_out,
                                                       const u16* __restrict__ wo, char* __restrict__ ws, u16* __restrict__ out) {
    const u64 epoch = phase1(x, wq, wk, wv, w_in, w_sp, b_sp, w_out, wo, ws);
    phase2(w_sp, b_sp, x, ws, out, epoch, __builtin_amdgcn_readfirstlane(threadIdx.x >> 6));
}
__global__ void text_end_marker() { asm volatile(".Lk3_end:"); }

extern "C" void kernel_launch(void* const* d_in, const int* in_sizes, int n_in, void* d_out, int out_size, void* d_ws, size_t ws_size, hipStream_t stream) {
    const u16* x = (const u16*)d_in[0]; const u16* wq = (const u16*)d_in[1]; const u16* wk = (const u16*)d_in[2]; const u16* wv = (const u16*)d_in[3];
    const u16* wo = (const u16*)d_in[4]; const u16* w_in = (const u16*)d_in[6]; const u16* w_sp = (const u16*)d_in[7]; const u16* b_sp = (const u16*)d_in[8];
    const u16* w_out = (const u16*)d_in[9]; char* ws = (char*)d_ws;
    (void)hipFuncSetAttribute((const void*)fused_kernel, hipFuncAttributeMaxDynamicSharedMemorySize, LDS_ALL);
    fused_kernel<<<256, 512, LDS_ALL, stream>>>(x, wq, wk, wv, w_in, w_sp, b_sp, w_out, wo, ws, (u16*)d_out);
}
```

```cpp
#include <hip/hip_runtime.h>
#include <hip/hip_bf16.h>
#include <stdint.h>

#define STNOP 1
typedef unsigned short u16;
typedef unsigned long long u64;
typedef short bf16x8 __attribute__((ext_vector_type(8)));
typedef short s16x4 __attribute__((ext_vector_type(4)));
typedef float f32x2 __attribute__((ext_vector_type(2)));
typedef float f32x4 __attribute__((ext_vector_type(4)));
typedef float f32x16 __attribute__((ext_vector_type(16)));
typedef unsigned u32x4 __attribute__((ext_vector_type(4)));
typedef unsigned u32x2 __attribute__((ext_vector_type(2)));
typedef __bf16 bf16x2_t __attribute__((ext_vector_type(2)));
#define LAS __attribute__((address_space(3)))
#define DI __device__ __forceinline__

constexpr int Z1L = 122880, TOPL = 159744, LDS_ALL = 163840;
constexpr int K2_SA = 16384, K2_SB = 18432, K2_SK = 6144, K2_B0 = 3 * K2_SA, K2_K0 = K2_B0 + 3 * K2_SB, K2_X0 = 147520, K2_LDS = K2_X0 + 9344 + 64 + 2048;
constexpr int S_ = 2048, D_ = 576, FH = 2304, F_ = 4608;

constexpr size_t WS_Q = 0;
constexpr size_t WS_KV = WS_Q + (size_t)S_ * D_ * 2;
constexpr size_t WS_Z1 = WS_KV + 64 * 3072;
constexpr size_t WS_Z2T = WS_Z1 + (size_t)S_ * FH * 2;
constexpr size_t WS_UO = WS_Z2T + (size_t)S_ * FH * 2;
constexpr size_t WS_ST = WS_UO + (size_t)S_ * 2880 * 2;
constexpr size_t WS_XCH = WS_ST + (size_t)S_ * 16 * 8;
constexpr size_t WS_XR = WS_XCH + 4096 + 16384;
constexpr size_t WS_FL = WS_XR + 4096;
constexpr size_t WS_FA = WS_FL + 16384;
constexpr size_t WS_FJ = WS_FA + 2048;
constexpr size_t WS_FP = WS_FJ + 8192;
constexpr size_t WS_W3T = WS_FP + 2048;
constexpr size_t WS_DBG = 128u << 20;
#define STAMP(kid, i)
#define STAMP_DRAIN()
#define STAMPT(tid, kid, i)

DI unsigned cvtpk(float lo, float hi) { f32x2 v = {lo, hi}; return __builtin_bit_cast(unsigned, __builtin_convertvector(v, bf16x2_t)); }
DI float bf2f(u16 b) { return __uint_as_float(((unsigned)b) << 16); }
DI float bflo(unsigned w) { return __uint_as_float(w << 16); }
DI float bfhi(unsigned w) { return __uint_as_float(w & 0xffff0000u); }
#define SB() __builtin_amdgcn_sched_barrier(0)
#define PIN(x) asm volatile("" : "+v"(x))
#define EX(x) __builtin_amdgcn_exp2f(x)
#define MFMA16(a, b, c) __builtin_amdgcn_mfma_f32_16x16x32_bf16(a, b, c, 0, 0, 0)
#define MFMA32(a, b, c) __builtin_amdgcn_mfma_f32_32x32x16_bf16(a, b, c, 0, 0, 0)

DI s16x4 trread(const LAS char* p) { return __builtin_bit_cast(s16x4, __builtin_amdgcn_ds_read_tr16_b64_v4i16((LAS s16x4*)p)); }
template <int N> DI void wait_vm_bar() { asm volatile("s_waitcnt vmcnt(%0) lgkmcnt(0)\n\ts_barrier" ::"n"(N) : "memory"); }
template <int N> DI void wait_vm() { asm volatile("s_waitcnt vmcnt(%0)" ::"n"(N) : "memory"); }
template <int OFF> DI void glds_s(unsigned voff, const char* sbase) { asm volatile("global_load_lds_dwordx4 %0, %1 offset:%2" ::"v"(voff), "s"(sbase), "n"(OFF)); }
template <int OFF> DI void glds_sc1(unsigned voff, const char* sbase) { asm volatile("global_load_lds_dwordx4 %0, %1 offset:%2 sc1" ::"v"(voff), "s"(sbase), "n"(OFF)); }
DI void glds_v(const char* p, unsigned m0v, u64 em) { asm volatile("s_mov_b32 m0, %1\n\ts_mov_b64 exec, %2\n\tglobal_load_lds_dwordx4 %0, off\n\ts_mov_b64 exec, -1" ::"v"(p), "s"(m0v), "s"(em)); }
DI unsigned ld_sc1_async(const char* sb, unsigned vo) { unsigned v; asm volatile("global_load_dword %0, %1, %2 sc1" : "=v"(v) : "v"(vo), "s"(sb) : "memory"); return v; }
#define WT
DI void st16(void* p, u32x4 v) { asm volatile("global_store_dwordx4 %0, %1, off sc1\n\ts_nop 1" :: "v"(p), "v"(v) : "memory"); }
DI void st8(void* p, u32x2 v) { asm volatile("global_store_dwordx2 %0, %1, off sc1" :: "v"(p), "v"(v) : "memory"); }
template <int OFF> DI void st16s(const char* sb, unsigned vo, u32x4 v) { asm volatile("global_store_dwordx4 %0, %1, %2 offset:%3 sc1\n\ts_nop %4" :: "v"(vo), "v"(v), "s"(sb), "n"(OFF), "n"(STNOP) : "memory"); }
template <int OFF> DI void st8s(const char* sb, unsigned vo, u32x2 v) { asm volatile("global_store_dwordx2 %0, %1, %2 offset:%3 sc1" :: "v"(vo), "v"(v), "s"(sb), "n"(OFF) : "memory"); }
DI float fadd1(float a, float b) { float r; asm("v_add_f32 %0, %1, %2" : "=v"(r) : "v"(a), "v"(b)); return r; }
DI float fmul1(float a, float b) { float r; asm("v_mul_f32 %0, %1, %2" : "=v"(r) : "v"(a), "v"(b)); return r; }
DI float ffma1(float a, float b, float c) { float r; asm("v_fma_f32 %0, %1, %2, %3" : "=v"(r) : "v"(a), "v"(b), "v"(c)); return r; }
DI void set_m0(unsigned v) { asm volatile("s_mov_b32 m0, %0\n\ts_nop 0" ::"s"(v)); }
DI bf16x8 ldsA(const LAS char* p) { return *(const LAS bf16x8*)p; }
DI bf16x8 ldsB2(const LAS char* p0, const LAS char* p1) { s16x4 lo = trread(p0), hi = trread(p1); return __builtin_shufflevector(lo, hi, 0, 1, 2, 3, 4, 5, 6, 7); }
DI bf16x8 ldsB(const LAS char* p) { return ldsB2(p, p + 256); }

DI float gelu_t(float x) {
    const float c0 = -2.0f * 1.4426950408889634f * 0.7978845608028654f;
    const float c1 = c0 * 0.044715f;
    float x2 = x * x;
    float t = __builtin_fmaf(x2, c1, c0);
    float e = __builtin_amdgcn_exp2f(x * t);
    return x * __builtin_amdgcn_rcpf(1.0f + e);
}
constexpr float QSCALE = 0.23570226039551584f * 1.4426950408889634f;

#define KO1 (3 * (((idx & 7) + (idx >> 3)) % 3))
#define KO2 (((idx & 7) + 2 * (idx >> 3) + 4 * (xcd & 1)) % 9)
#define K2_KO (8 * (xcd >> 1))
#define EPRIO 1
#define PFW 0
#define TFA 8
#define PW2 7
#define PEARLY 0
#define POLLSLEEP 0
#define APRIO 1
#define K3MAP 2
#define K3ROT 0
#define APRE 0
#define AGSLOT 1
#define ZHALF 1
#define POLLW 0
#define POLLSLEEP1 1
#define PLSLOT 0
constexpr int K1_SA = 16384, K1_SB = 24576, K1_B0 = 3 * K1_SA, K1_TAB = K1_B0 + 3 * K1_SB, K1_X0 = K1_TAB + 10240, K1_LDS = K1_X0 + 16384 + 2048;
typedef __attribute__((address_space(1))) unsigned gu32;
typedef __attribute__((address_space(1))) unsigned long long gu64;
#define RLX_AGENT __ATOMIC_RELAXED, __HIP_MEMORY_SCOPE_AGENT
struct K1F { bf16x8 a[4], b[3], x; };
struct K1S {
    unsigned va[2], vb[2]; const char* sa; const char* sb;
    const char* gp; unsigned st;
    int k1, k2;
    unsigned m0a, m0b; u64 em;
    const LAS char* pa[2]; const LAS char* px[2]; const LAS char* pb[3];
};
DI void k1_issue(K1S& s, int i, int w) {
    if (i == 0) { set_m0(s.m0a + w * K1_SA); glds_s<0>(s.va[0], s.sa); }
    if (i == 1) { glds_s<1024>(s.va[1], s.sa); s.sa += s.k1 == 8 ? -8 * 128 : 128; asm volatile("" : "+s"(s.sa)); }
    if (i == 2) { glds_v(s.gp + (unsigned long long)s.st * (unsigned)s.k1, s.m0b + w * K1_SB + 2048, s.em); s.k1 = s.k1 == 8 ? 0 : s.k1 + 1; asm volatile("" : "+s"(s.k1)); }
    if (i == 3) { set_m0(s.m0b + w * K1_SB); glds_s<0>(s.vb[0], s.sb); }
    if (i == 4) { glds_s<1024>(s.vb[1], s.sb); s.sb += s.k2 == 8 ? -8 * 128 * F_ : 128 * F_; s.k2 = s.k2 == 8 ? 0 : s.k2 + 1; asm volatile("" : "+s"(s.sb), "+s"(s.k2)); }
}
DI f32x2 gelu_y2(f32x2 y, float k0, float k1, float ir) {
    f32x2 t = (y * y) * k1 + k0;
    f32x2 a = y * t;
    f32x2 e = {__builtin_amdgcn_exp2f(a[0]), __builtin_amdgcn_exp2f(a[1])};
    f32x2 d = e * ir + ir;
    f32x2 rc = {__builtin_amdgcn_rcpf(d[0]), __builtin_amdgcn_rcpf(d[1])};
    return y * rc;
}
DI float dpp_x1(float x) { return __int_as_float(__builtin_amdgcn_mov_dpp(__float_as_int(x), 0xB1, 0xf, 0xf, true)); }
DI float dpp_x2(float x) { return __int_as_float(__builtin_amdgcn_mov_dpp(__float_as_int(x), 0x4E, 0xf, 0xf, true)); }
DI void kv_store(const char* kv, int sv, int j, int c, int g, f32x4 v) {
    if (j == 4) { st8s<0>(kv + sv * 256, ((g >> 1) * 32 + c) * 16 + (g & 1) * 8, u32x2{cvtpk(v[0], v[1]), cvtpk(v[2], v[3])}); return; }
    const unsigned kk = cvtpk(v[2], v[3]);
    const bool o1 = c & 1, o2 = c & 2;
    float t0 = dpp_x1(o1 ? v[0] : v[1]), t1 = dpp_x1(o1 ? v[2] : v[3]);
    if (o1) { v[0] = t0; v[2] = t1; } else { v[1] = t0; v[3] = t1; }
    t0 = dpp_x2(o2 ? v[0] : v[2]); t1 = dpp_x2(o2 ? v[1] : v[3]);
    if (o2) { v[0] = t0; v[1] = t1; } else { v[2] = t0; v[3] = t1; }
    u32x2 w = {cvtpk(v[0], v[1]), cvtpk(v[2], v[3])};
    const int kq = c >> 2; unsigned off;
    if (j == 5) {
        off = g < 2 ? c * 16 + (g ^ 1) * 8 : 256 + ((kq & 1) * 8 + 4 * (g - 2) + (c & 3)) * 16 + (kq >> 1) * 8;
        if (g < 2) w = g ? u32x2{kk, 0x3f80u} : u32x2{0u, 0u};
    } else {
        off = 512 + ((kq & 1) * 16 + 4 * g + (c & 3)) * 16 + (kq >> 1) * 8;
        if (g == 3 && !o2) { const unsigned cv = o1 ? 0u : 0x3f803f80u; w = u32x2{cv, cv}; }
    }
    st8s<1024>(kv + sv * 1024, off, w);
}
template <int KS, int GL, int RS, int WS, bool RX> DI void k1_p1(K1S& s, const K1F& f, K1F& n, f32x4 (&acc)[4][2], f32x4& sacc, f32x4 (&cso)[2], f32x4& rs, f32x4& rq, const bf16x8& ones) {
    constexpr int AO = RS * K1_SA, BO = RS * K1_SB + KS * 12288;
#define MX(d, a_, b_) d = MFMA16(a_, b_, d); PIN(d); SB()
#define MM(m, nn) MX(acc[m][nn], f.b[nn], f.a[m])
    MM(0, 0); n.b[0] = ldsB(s.pb[0] + BO); SB();
    MM(1, 0); n.a[0] = ldsA(s.pa[KS] + AO); SB();
    MM(2, 0); n.a[1] = ldsA(s.pa[KS] + AO + 2048); SB();
    MM(3, 0); n.a[2] = ldsA(s.pa[KS] + AO + 4096); SB();
    MX(cso[0], f.b[0], ones); n.a[3] = ldsA(s.pa[KS] + AO + 6144); SB();
    MX(sacc, f.b[2], f.x); n.b[1] = ldsB(s.pb[1] + BO); SB();
    MX(cso[1], f.b[2], ones); n.b[2] = ldsB(s.pb[2] + BO); SB();
    MX(rs, ones, f.x); k1_issue(s, GL == 1 ? 0 : 3, WS); SB();
    MX(rq, f.x, f.x); if (RX) n.x = ldsA(s.px[KS] + AO); SB();
    MM(0, 1); k1_issue(s, GL == 1 ? 1 : 4, WS); SB();
    MM(1, 1); if (GL == 1) k1_issue(s, 2, WS); SB();
    MM(2, 1);
    MM(3, 1);
}
template <int KS, int GL, int RS, int WS, bool RD, class E> DI void k1_p2(K1S& s, const K1F& f, K1F& n, f32x4 (&acc)[4][3], f32x4 (&cso)[2], const bf16x8& ones, E epi) {
    constexpr int AO = RS * K1_SA, BO = RS * K1_SB + KS * 12288;
    MM(0, 0); if (RD) n.b[0] = ldsB(s.pb[0] + BO); epi(0); SB();
    MM(1, 0); if (RD) n.a[0] = ldsA(s.pa[KS] + AO); epi(1); SB();
    MM(2, 0); if (RD) n.a[1] = ldsA(s.pa[KS] + AO + 2048); epi(2); SB();
    MM(3, 0); if (RD) n.a[2] = ldsA(s.pa[KS] + AO + 4096); epi(3); SB();
    MX(cso[0], f.b[0], ones); if (RD) n.a[3] = ldsA(s.pa[KS] + AO + 6144); epi(4); SB();
    MM(0, 1); if (RD) n.b[1] = ldsB(s.pb[1] + BO); epi(5); SB();
    MM(1, 1); if (RD) n.b[2] = ldsB(s.pb[2] + BO); epi(6); SB();
    MM(2, 1); if (GL) k1_issue(s, GL == 1 ? 0 : 3, WS); epi(7); SB();
    MM(3, 1); epi(8); SB();
    MX(cso[1], f.b[1], ones); if (GL) k1_issue(s, GL == 1 ? 1 : 4, WS); epi(9); SB();
    MM(0, 2); epi(10); SB();
    MM(1, 2); if (GL == 1) k1_issue(s, 2, WS); epi(11); SB();
    MM(2, 2); epi(12); SB();
    MM(3, 2); epi(13); SB();
#undef MM
#undef MX
}

constexpr unsigned XSALT = 0x9e3779b9u;
DI void epoch_reply(char* ws, int lane, const unsigned (&nv)[4], u64 epoch) {
    const unsigned lo = (unsigned)epoch, hi = (unsigned)(epoch >> 32);
    st16s<0>(ws + WS_XR, lane * 16, u32x4{nv[0], lo, hi, nv[0] ^ lo ^ hi ^ XSALT}); st16s<1024>(ws + WS_XR, lane * 16, u32x4{nv[1], lo, hi, nv[1] ^ lo ^ hi ^ XSALT});
    st16s<2048>(ws + WS_XR, lane * 16, u32x4{nv[2], lo, hi, nv[2] ^ lo ^ hi ^ XSALT}); st16s<3072>(ws + WS_XR, lane * 16, u32x4{nv[3], lo, hi, nv[3] ^ lo ^ hi ^ XSALT});
}
#define XN_LOADS "global_load_dword %0, %4, %5 sc1\n\tglobal_load_dword %1, %4, %5 offset:256 sc1\n\tglobal_load_dword %2, %4, %5 offset:512 sc1\n\tglobal_load_dword %3, %4, %5 offset:768 sc1\n"
DI void epoch_service(char* ws, int lane, u64 epoch) {
    unsigned nv[4];
    asm volatile(XN_LOADS "\ts_waitcnt vmcnt(0)" : "=&v"(nv[0]), "=&v"(nv[1]), "=&v"(nv[2]), "=&v"(nv[3]) : "v"(lane * 4), "s"(ws + WS_XCH) : "memory");
    epoch_reply(ws, lane, nv, epoch);
}
DI unsigned g2_va(int wid, int lane, int i) { int row = 8 * (wid * 4 + i) + (lane >> 3), c = (lane & 7) ^ ((row >> 1) & 7); return row * (S_ * 2) + c * 16 + 4096 - i * 1024; }
DI u64 phase1(const u16* __restrict__ x, const u16* __restrict__ wq, const u16* __restrict__ wk, const u16* __restrict__ wv,
                                                    const u16* __restrict__ w_in, const u16* __restrict__ w_sp, const u16* __restrict__ b_sp, const u16* __restrict__ w_out, const u16* __restrict__ wo, char* __restrict__ ws) {
    extern __shared__ __attribute__((aligned(16))) char smem[];
    const int bid = blockIdx.x, xcd = bid & 7, idx = bid >> 3;
    const int pm = (xcd & 1) * 8 + (idx & 7), j = (xcd >> 1) * 4 + (idx >> 3);
    const int lane = threadIdx.x & 63, wid = __builtin_amdgcn_readfirstlane(threadIdx.x >> 6), wr = wid >> 2, wc = wid & 3;
    const unsigned lds0 = (unsigned)(uintptr_t)smem;
    const LAS char* lds = (const LAS char*)smem;
    {
        const char* pc; unsigned len;
        asm volatile("s_getpc_b64 %0\n.Lk1_here:\n\ts_mov_b32 %1, .Lk3_end-.Lk1_here" : "=s"(pc), "=s"(len));
        unsigned off = ((idx * 8 + wid) * 2 + (lane & 1)) * 128; off = off < len ? off : len;
        asm volatile("s_mov_b32 m0, %2\n\ts_nop 0\n\tglobal_load_lds_dword %0, %1" ::"v"(off), "s"(pc), "s"(lds0 + K1_TAB + 9216 + 512));
    }
    K1S s; s.em = (wid & 1) ? 0xccccccccull : 0x33333333ull;
    s.m0a = lds0 + wid * 2048; s.m0b = lds0 + K1_B0 + wid * 3072;
    const int brow = (lane >> 2) & 7, bc = (lane & 3) ^ (2 * (wid & 1)), bk = 8 * wid + brow;
    const int ko2 = KO2, ko1 = KO1;
    unsigned oa = pm * 128 * D_ * 2, ob = 256 * j, oa1 = oa + ko1 * 128, ob1 = ob + FH * 2 + ko1 * 128 * F_, vg = bk * (F_ * 2) + 16 * (bc & 1);
    {
        int r8 = lane >> 3;
        s.va[0] = (16 * wid + r8) * (D_ * 2) + (((lane & 7) ^ (r8 >> 1)) * 16);
        s.va[1] = (16 * wid + 8 + r8) * (D_ * 2) + (((lane & 7) ^ (4 + (r8 >> 1))) * 16) - 1024;
        s.vb[0] = bk * (F_ * 2) + (lane >> 5) * 64 + bc * 16 + 1024; s.vb[1] = s.vb[0] + 128 - 1024;
        s.k1 = s.k2 = ko1; s.st = 128 * F_;
        PIN(s.va[0]); PIN(s.va[1]); PIN(s.vb[0]); PIN(s.vb[1]); PIN(vg);
        asm volatile("" : "+s"(oa1), "+s"(ob1), "+s"(s.m0a), "+s"(s.m0b));
    }
    SB();
    s.sa = (const char*)x + oa1; s.sb = (const char*)w_in - 1024 + ob1;
    k1_issue(s, 0, 0); k1_issue(s, 1, 0); k1_issue(s, 3, 0); k1_issue(s, 4, 0);
    SB();
    STAMP(0, 0);
    s.gp = (const char*)w_in + (4096 + 32 * j + FH * 2) + vg;
    k1_issue(s, 2, 0);
    const char* sa0 = (const char*)x + oa;
    const char* sb0 = (const char*)w_in - 1024 + ob;
    gu32* XN = (gu32*)(ws + WS_XCH) + pm * 16;
    gu32* XE = (gu32*)(ws + WS_XCH + 4096) + pm * 256;
    gu64* XP = (gu64*)(ws + WS_ST) + (size_t)pm * 16 * 128;
    const char* xe_s = ws + WS_XCH + 4096 + (pm * 256 + j * 16) * 4;
    const unsigned le = (lane & 16) ? 16384 - (pm * 16 + j) * 48 + (lane & 3) * 4 : (lane & 15) * 4;
    unsigned eo = ld_sc1_async(xe_s, le);
#pragma unroll
    for (int t = 1; t < 3; ++t)
#pragma unroll
        for (int i = 0; i < 5; ++i) if (t < 2 || i < 3) k1_issue(s, i, t);
    unsigned nonce = 0;
    const char* gp2; unsigned st2;
    {
        const u16* p; int rs;
        const u16* sp0 = j < 4 ? wq + 512 + 16 * j : (j == 5 ? wk + 10 : (j == 6 ? wv + 8 : wk));
        const u16* sp1 = j < 4 ? wq + 520 + 16 * j : (j == 5 ? wv : (j == 6 ? wv + 10 : wk + 8));
        const int srs = j < 4 ? D_ : 18;
        if (lane >= 32) { p = wq + 32 * j + 8 * bc; rs = D_; }
        else if (bc < 2) { p = w_in + 2048 + 16 * j + 8 * bc; rs = F_; }
        else { p = bc == 2 ? sp0 : sp1; rs = srs; }
        gp2 = (const char*)(p + (size_t)bk * rs); st2 = 128 * rs; PIN(gp2); PIN(st2);
    }
    const int r16 = lane & 15, g = lane >> 4, cg = wc ^ (2 * wr);
    const int nf3 = cg == 3 ? 8 : (cg == 2 ? 9 : 10 + cg);
#define NF1(n) ((n) == 2 ? 8 : 2 * wc + ((n) ^ wr))
#define NF2(n) ((n) == 0 ? nf3 : 2 * cg + (((n) - 1) ^ wr))
    const LAS char* pb2[3];
    {
        int q = r16 >> 2, p = lane & 3;
#pragma unroll
        for (int ks = 0; ks < 2; ++ks) { s.pa[ks] = lds + (r16 * 128 + (((4 * ks + g) ^ (r16 >> 1)) * 16) + wr * 8192); s.px[ks] = s.pa[ks] + wc * 2048; PIN(s.pa[ks]); PIN(s.px[ks]); }
#pragma unroll
        for (int n = 0; n < 3; ++n) {
            int nf = NF1(n); s.pb[n] = lds + (K1_B0 + g * 3072 + q * 64 + 8 * p + (nf >> 1) * 512 + (((nf ^ g) & 1) * 32)); PIN(s.pb[n]);
            nf = NF2(n); pb2[n] = lds + (K1_B0 + g * 3072 + q * 64 + 8 * p + (nf >> 1) * 512 + (((nf ^ g) & 1) * 32)); PIN(pb2[n]);
        }
    }
    f32x4 acc1[4][2] = {}; f32x4 sacc = {}; f32x4 cso[2] = {};
    f32x4 rs = {}, rq = {};
#pragma unroll
    for (int m = 0; m < 4; ++m) { PIN(acc1[m][0]); PIN(acc1[m][1]); }
    PIN(sacc); PIN(cso[0]); PIN(cso[1]); PIN(rs); PIN(rq);
    const u32x4 onesv = {0x3f803f80u, 0x3f803f80u, 0x3f803f80u, 0x3f803f80u}; bf16x8 ones = __builtin_bit_cast(bf16x8, onesv); PIN(ones);
    K1F f0, f1;
    SB();
    wait_vm_bar<8>();
    SB();
    f0.b[0] = ldsB(s.pb[0]);
    f0.a[0] = ldsA(s.pa[0]); f0.a[1] = ldsA(s.pa[0] + 2048); f0.a[2] = ldsA(s.pa[0] + 4096); f0.a[3] = ldsA(s.pa[0] + 6144);
    f0.b[2] = ldsB(s.pb[2]); f0.x = ldsA(s.px[0]); f0.b[1] = ldsB(s.pb[1]);
    SB();
    PIN(eo);
    if (wid == 7) {
        nonce = 1;
        while (__ballot(eo == nonce) != 0ull) ++nonce;
        if (lane == 0) __hip_atomic_store(XN + j, nonce, RLX_AGENT);
        __hip_atomic_store(XP + j * 128 + lane, ~0ull, RLX_AGENT); __hip_atomic_store(XP + j * 128 + 64 + lane, ~0ull, RLX_AGENT);
    }
    STAMP(0, 1);
#define P1_A(c) k1_p1<1, 2, (c), ((c) + 2) % 3, true>(s, f0, f1, acc1, sacc, cso, rs, rq, ones); wait_vm_bar<5>(); SB()
#define P1_B(c, RX) k1_p1<0, 1, ((c) + 1) % 3, (c), RX>(s, f1, f0, acc1, sacc, cso, rs, rq, ones)
#pragma unroll 1
    for (int it = 0; it < 2; ++it) { P1_A(0); P1_B(0, true); P1_A(1); P1_B(1, true); P1_A(2); P1_B(2, true); }
    const char* xn_s = ws + WS_XCH + pm * 64; const unsigned l4 = (lane & 15) * 4;
    P1_A(0);
    unsigned echoed = ld_sc1_async(xn_s, l4);
    unsigned alln[4];
    asm volatile("s_cmp_lg_u32 %6, 0\n\ts_cbranch_scc1 .Lld%=\n\t" XN_LOADS ".Lld%=:" : "=&v"(alln[0]), "=&v"(alln[1]), "=&v"(alln[2]), "=&v"(alln[3])
                 : "v"(lane * 4), "s"(ws + WS_XCH), "s"(bid) : "memory", "scc");
    s.k1 = s.k2 = ko2; s.sa = sa0 + ko2 * 128; s.sb = sb0 + (size_t)ko2 * 128 * F_; s.gp = gp2; s.st = st2; s.em = ~0ull; SB();
    P1_B(0, true);
    P1_A(1);
    PIN(echoed);
    if (wid == 7 && lane < 16) __hip_atomic_store(XE + lane * 16 + j, echoed, RLX_AGENT);
    PIN(alln[0]); PIN(alln[1]); PIN(alln[2]); PIN(alln[3]);
    if (bid == 0 && wid == 6) epoch_reply(ws, lane, alln, __builtin_amdgcn_s_memrealtime() | (1ull << 63));
    SB();
    P1_B(1, true);
    P1_A(2);
    s.pb[0] = pb2[0]; s.pb[1] = pb2[1]; s.pb[2] = pb2[2];
    P1_B(2, false);
    STAMP(3, 0);
    float* CS = (float*)(smem + K1_TAB);
    f32x4* RS = (f32x4*)(smem + K1_TAB + 2048);
    float* red = (float*)(smem + K1_TAB + 4096);
    f32x2* ZS = (f32x2*)(smem + K1_TAB + 9216);
    volatile LAS unsigned* HS = (volatile LAS unsigned*)(lds + K1_TAB + 1792);
    const float c0 = -2.0f * 1.4426950408889634f * 0.7978845608028654f, c1 = c0 * 0.044715f;
    f32x4 acc2[4][3] = {}; f32x4 cs2[2] = {};
    auto none = [](int) {};
    auto tabs = [&](int st) {
        if (st == 2) { if (r16 == 0) { *(f32x4*)(CS + NF1(0) * 16 + 4 * g) = cso[0]; *(f32x4*)(CS + 128 + 4 * g) = cso[1]; } }
        if (st == 3) {
            float sq = (r16 & 2) ? ((r16 & 1) ? rq[3] : rq[2]) : ((r16 & 1) ? rq[1] : rq[0]);
            float r0_ = rs[0]; PIN(r0_); PIN(sq);
            float mean = fmul1(r0_, 1.0f / D_), var = fmaxf(ffma1(-mean, mean, fmul1(sq, 1.0f / D_)), 0.f);
            rs[0] = mean; rs[1] = var + 1e-5f; PIN(rs);
        }
        if (st == 5) { rs[2] = __builtin_amdgcn_rsqf(rs[1]); PIN(rs); }
        if (st == 7) { float rstd = rs[2]; if (g == (r16 >> 2)) RS[wr * 64 + wc * 16 + r16] = f32x4{rs[0], fmul1(rstd, c0), fmul1(fmul1(rstd, rstd), fmul1(rstd, c1)), fmul1(rs[1], rstd)}; }
    };
    f32x4 er[5], cs1[3], e[3];
    f32x2 su[5], sq[5];
#define ZU(u) ((u) == 8 ? sacc : acc1[((u) >> 1) & 3][(u) & 1])
#define UM(u) ((u) == 8 ? 4 : (u) >> 1)
#define UN(u) ((u) == 8 ? 2 : (u) & 1)
#define PRE(i, u) { f32x4 y = ZU(u) - cs1[UN(u)] * er[UM(u)][0]; e[i] = ((y * y) * er[UM(u)][2] + er[UM(u)][1]) * y; ZU(u) = y; PIN(ZU(u)); PIN(e[i]); }
#define FIN(i, u, first) { ZU(u) *= e[i]; PIN(ZU(u)); f32x4 v = ZU(u); f32x2 lo = {v[0], v[1]}, hi = {v[2], v[3]}; \
        if (first) { su[UM(u)] = lo + hi; sq[UM(u)] = lo * lo + hi * hi; } else { su[UM(u)] += lo; su[UM(u)] += hi; sq[UM(u)] += lo * lo; sq[UM(u)] += hi * hi; } PIN(su[UM(u)]); PIN(sq[UM(u)]); }
#define RED(m) { float a_ = su[m][0] + su[m][1], b_ = sq[m][0] + sq[m][1]; \
            auto x1 = __builtin_amdgcn_permlane16_swap(__float_as_uint(a_), __float_as_uint(b_), false, false);       \
            float t_ = __uint_as_float(x1[0]) + __uint_as_float(x1[1]);                                               \
            auto x2 = __builtin_amdgcn_permlane32_swap(__float_as_uint(t_), __float_as_uint(t_), false, false); \
            t_ = __uint_as_float(x2[0]) + __uint_as_float(x2[1]);                                                     \
            if ((m) < 4) redp[(m) * 160] = t_; else reds[0] = t_; }
    float* redp = red + ((wr * 64 + r16) * 5 + wc) * 2 + (g & 1);
    float* reds = red + ((wr * 64 + wc * 16 + r16) * 5 + 4) * 2 + (g & 1);
#define T1(op, v) asm volatile(op " %0, %0" : "+v"(v))
#define DE1(v, sd) asm volatile("v_fma_f32 %0, %0, %1, %1" : "+v"(v) : "v"(sd))
#define EPI_E(u0, u1, u2) [&](int st) { \
        if (st < 12) { float t_ = e[st >> 2][st & 3]; T1("v_exp_f32", t_); e[st >> 2][st & 3] = t_; } \
        if (st >= 2) { const int v_ = st - 2; float t_ = e[v_ >> 2][v_ & 3]; DE1(t_, er[v_ < 4 ? UM(u0) : (v_ < 8 ? UM(u1) : UM(u2))][3]); e[v_ >> 2][v_ & 3] = t_; } }
#define EPI_R(m0, m1) [&](int st) { if (st < 12) { float t_ = e[st >> 2][st & 3]; T1("v_rcp_f32", t_); e[st >> 2][st & 3] = t_; } \
        if (st == 12 && (m0) >= 0) er[(m0) & 3] = RS[wr * 64 + (m0) * 16 + r16]; if (st == 13 && (m1) >= 0) er[(m1) & 3] = RS[wr * 64 + (m1) * 16 + r16]; }
#define VSYNC() SB(); asm volatile("s_waitcnt lgkmcnt(0)\n\ts_barrier" ::: "memory"); SB()
#define P2_A(c, GL, E) k1_p2<1, GL ? 2 : 0, (c), ((c) + 2) % 3, true>(s, f0, f1, acc2, cs2, ones, E)
#define P2_B(c, GL, RD, E) k1_p2<0, GL ? 1 : 0, ((c) + 1) % 3, (c), RD>(s, f1, f0, acc2, cs2, ones, E)
    P2_A(0, true, tabs); wait_vm_bar<5>(); SB();
    er[0] = RS[wr * 64 + r16]; er[4] = RS[wr * 64 + wc * 16 + r16];
#pragma unroll
    for (int n = 0; n < 3; ++n) cs1[n] = *(const f32x4*)(CS + NF1(n) * 16 + 4 * g);
    PRE(0, 0) PRE(1, 1) PRE(2, 8) VSYNC();
    P2_B(0, true, true, EPI_E(0, 1, 8));
    P2_A(1, true, EPI_R(1, 2)); wait_vm_bar<5>(); SB();
    unsigned ev = ld_sc1_async(xe_s, le);
    FIN(0, 0, true) FIN(1, 1, false) FIN(2, 8, true) PRE(0, 2) PRE(1, 3) PRE(2, 4) RED(0) RED(4) VSYNC();
    P2_B(1, true, true, EPI_E(2, 3, 4));
    const int w3p = bid < 240 ? bid : 239;
    auto epi_r11 = EPI_R(3, -1);
    auto cold = [&](int st) {
        epi_r11(st);
        if (st == 12) {
            const char* sp = (const char*)w_sp + (size_t)(pm * 128 + (idx >> 3) * 32 + wid * 4) * (S_ * 2);
            const unsigned vo = (lane >> 4) * (S_ * 2) + (lane & 15) * 16;
            set_m0(lds0 + K1_X0 + 16384); glds_s<0>(vo, sp); glds_s<1024>(vo, sp - 768);
            asm volatile("s_mov_b32 m0, %2\n\ts_nop 0\n\tglobal_load_lds_dword %0, %1" ::"v"(lane * 4), "s"(b_sp + pm * 128), "s"(lds0 + TOPL + 2304));
        }
        if (st == 13) {
            const int jj = w3p / 30, pc = w3p - jj * 30;
            const char* sp = (pc < 24 ? (const char*)w_out : (const char*)wo - 2304 * 1152) + jj * 144;
#pragma unroll
            for (int i = 0; i < 2; ++i) {
                unsigned c_ = threadIdx.x + 512 * i; c_ = c_ < 864 ? c_ : 863;
                const unsigned row = (c_ * 7282) >> 16, k_ = pc * 96 + row, jg = (k_ * 29128) >> 22, l_ = k_ - jg * 144;
                const unsigned src = pc < 24 ? (l_ < 128 ? 128 * jg + l_ : 1920 + 16 * jg + l_) : k_, vo = src * 1152 + (c_ - row * 9) * 16;
                set_m0(lds0 + K1_X0 + i * 8192 + wid * 1024); glds_s<0>(vo, sp);
            }
        }
    };
    P2_A(2, true, cold); wait_vm_bar<10>(); SB();
    PIN(ev);
    bool hs = false; u64 epoch = 0;
    auto xcheck = [&]() {
        hs = hs || __all((lane & 16) || ev == nonce);
        const unsigned w0 = __builtin_amdgcn_readlane(ev, 16), w1 = __builtin_amdgcn_readlane(ev, 17), w2 = __builtin_amdgcn_readlane(ev, 18), w3 = __builtin_amdgcn_readlane(ev, 19);
        if (w0 == nonce && w3 == (w0 ^ w1 ^ w2 ^ XSALT)) epoch = ((u64)w2 << 32) | w1;
    };
    if (wid == 7) xcheck();
    unsigned nn = ld_sc1_async(xn_s, l4);
    FIN(0, 2, true) FIN(1, 3, false) FIN(2, 4, true) PRE(0, 5) PRE(1, 6) PRE(2, 7) RED(1) VSYNC();
    P2_B(2, true, true, EPI_E(5, 6, 7));
    P2_A(0, true, EPI_R(-1, -1)); wait_vm_bar<11>(); SB();
    FIN(0, 5, false) FIN(1, 6, true) FIN(2, 7, false) RED(2) RED(3) VSYNC();
    f32x2 pu[5];
    auto pub = [&](int st) {
        if (st == 0) {
#pragma unroll
            for (int i = 0; i < 5; ++i) pu[i] = *(const f32x2*)(red + (wid * 16 + r16) * 10 + 2 * i);
        }
        if (st == 4) {
            float a = fadd1(fadd1(fadd1(pu[0][0], pu[1][0]), fadd1(pu[2][0], pu[3][0])), pu[4][0]), b = fadd1(fadd1(fadd1(pu[0][1], pu[1][1]), fadd1(pu[2][1], pu[3][1])), pu[4][1]);
            unsigned bb = __float_as_uint(b); if (bb == 0xffffffffu) bb = 0x7fc00000u;
            st8s<0>((const char*)(XP + j * 128 + wid * 16), r16 * 8, u32x2{__float_as_uint(a), bb});
        }
    };
    P2_B(0, true, true, pub);
    P2_A(1, true, none);
    STAMP(3, 1);
    wait_vm_bar<5>(); SB();
    ev = ld_sc1_async(xe_s, le);
    auto reecho = [&](int st) { if (st == 2) { PIN(nn); if (wid == 7 && nn != echoed) { echoed = nn; if (lane < 16) __hip_atomic_store(XE + lane * 16 + j, nn, RLX_AGENT); } } };
    P2_B(1, true, true, reecho);
    const char* xp_s = ws + WS_ST + (size_t)pm * 16384 + wid * 128; const unsigned xp_v = g * 4096 + r16 * 8;
    u32x2 pv[4];
#define PLOAD() asm volatile("global_load_dwordx2 %0, %4, %5 sc1\n\tglobal_load_dwordx2 %1, %4, %5 offset:1024 sc1\n\tglobal_load_dwordx2 %2, %4, %5 offset:2048 sc1\n\tglobal_load_dwordx2 %3, %4, %5 offset:3072 sc1" \
        : "=&v"(pv[0]), "=&v"(pv[1]), "=&v"(pv[2]), "=&v"(pv[3]) : "v"(xp_v), "s"(xp_s) : "memory")
    bf16x8 w3v[2];
    auto w3st = [&](int st) {
        const int u1 = wid < 7 ? wid + 8 : 14, k0 = wid >= 5, n0 = wid - 5 * k0, k1 = u1 >= 10 ? 2 : 1, n1 = u1 - 5 * k1;
        if (st == 8) {
            const LAS char* p = lds + (K1_X0 + (8 * g + (r16 >> 2)) * 144 + 8 * (lane & 3));
            w3v[0] = ldsB2(p + k0 * 4608 + n0 * 32, p + k0 * 4608 + n0 * 32 + 576); w3v[1] = ldsB2(p + k1 * 4608 + n1 * 32, p + k1 * 4608 + n1 * 32 + 576);
        }
        const unsigned vh = (g * 8 + r16) * 16;
        if (st == 11) { if (n0 < 4 || r16 < 8) st16s<0>(ws + WS_W3T + w3p * 13824 + k0 * 4608 + n0 * 1024, n0 < 4 ? lane * 16 : vh, __builtin_bit_cast(u32x4, w3v[0])); }
        if (st == 12) { if (n1 < 4 || r16 < 8) st16s<0>(ws + WS_W3T + w3p * 13824 + k1 * 4608 + n1 * 1024, n1 < 4 ? lane * 16 : vh, __builtin_bit_cast(u32x4, w3v[1])); }
        if (st == 13) PLOAD();
    };
    P2_A(2, true, w3st); wait_vm_bar<10>(); SB();
    PIN(ev);
    if (wid == 7) { const bool early = hs; xcheck(); *HS = early ? 1 : (hs ? 2 : 0); }
    P2_B(2, true, true, none);
    P2_A(0, true, none); wait_vm_bar<10>(); SB();
#define PEMPTY() __any(pv[0][1] == 0xffffffffu || pv[1][1] == 0xffffffffu || pv[2][1] == 0xffffffffu || pv[3][1] == 0xffffffffu)
    const unsigned hsf = *HS;
    auto again = [&](int st) {
        if (st == AGSLOT) {
            wait_vm<5>();
            PIN(pv[0]); PIN(pv[1]); PIN(pv[2]); PIN(pv[3]);
            const unsigned long long em = __builtin_amdgcn_ballot_w64(hsf != 1 || pv[0][1] == 0xffffffffu || pv[1][1] == 0xffffffffu || pv[2][1] == 0xffffffffu || pv[3][1] == 0xffffffffu);
            asm volatile("s_cmp_eq_u64 %6, 0\n\ts_cbranch_scc1 .Lpl%=\n\tglobal_load_dwordx2 %0, %4, %5 sc1\n\tglobal_load_dwordx2 %1, %4, %5 offset:1024 sc1\n\t"
                         "global_load_dwordx2 %2, %4, %5 offset:2048 sc1\n\tglobal_load_dwordx2 %3, %4, %5 offset:3072 sc1\n.Lpl%=:"
                         : "+v"(pv[0]), "+v"(pv[1]), "+v"(pv[2]), "+v"(pv[3]) : "v"(xp_v), "s"(xp_s), "s"(em) : "memory", "scc");
        }
    };
    P2_B(0, false, true, again);
    float za, zb;
    auto zstat = [&](int st) {
        if (st == 1) {
            wait_vm<0>();
            PIN(pv[0]); PIN(pv[1]); PIN(pv[2]); PIN(pv[3]);
            if (__builtin_expect(!hsf || PEMPTY(), 0)) {
                const unsigned long long t0 = __builtin_amdgcn_s_memrealtime();
                if (wid == 7) {
                    while (!hs) {
                        ev = ld_sc1_async(xe_s, le); wait_vm<0>(); PIN(ev);
                        xcheck();
                        nn = __hip_atomic_load(XN + (lane & 15), RLX_AGENT);
                        if (nn != echoed) { echoed = nn; if (lane < 16) __hip_atomic_store(XE + lane * 16 + j, nn, RLX_AGENT); }
                        if (__builtin_amdgcn_s_memrealtime() - t0 > 5000000ull) break;
                    }
                    *HS = 1;
                }
                while (*HS == 0 && __builtin_amdgcn_s_memrealtime() - t0 < 6000000ull) __builtin_amdgcn_s_sleep(2);
                do {
                    PLOAD(); wait_vm<0>(); PIN(pv[0]); PIN(pv[1]); PIN(pv[2]); PIN(pv[3]);
                } while (PEMPTY() && __builtin_amdgcn_s_memrealtime() - t0 < 6000000ull);
            }
        }
#define UF(x) __uint_as_float(x)
        if (st == 3) {
            za = fadd1(fadd1(UF(pv[0][0]), UF(pv[1][0])), fadd1(UF(pv[2][0]), UF(pv[3][0]))); PIN(za);
            zb = fadd1(fadd1(UF(pv[0][1]), UF(pv[1][1])), fadd1(UF(pv[2][1]), UF(pv[3][1]))); PIN(zb);
        }
        if (st == 5) {
            auto a1 = __builtin_amdgcn_permlane16_swap(__float_as_uint(za), __float_as_uint(za), false, false); za = fadd1(UF(a1[0]), UF(a1[1])); PIN(za);
            auto b1 = __builtin_amdgcn_permlane16_swap(__float_as_uint(zb), __float_as_uint(zb), false, false); zb = fadd1(UF(b1[0]), UF(b1[1])); PIN(zb);
        }
        if (st == 7) {
            auto a2 = __builtin_amdgcn_permlane32_swap(__float_as_uint(za), __float_as_uint(za), false, false); za = fadd1(UF(a2[0]), UF(a2[1])); PIN(za);
            auto b2 = __builtin_amdgcn_permlane32_swap(__float_as_uint(zb), __float_as_uint(zb), false, false); zb = fadd1(UF(b2[0]), UF(b2[1])); PIN(zb);
        }
        if (st == 9) {
            float m = fmul1(za, 1.0f / FH), var = fmaxf(ffma1(-m, m, fmul1(zb, 1.0f / FH)), 0.f), r = __builtin_amdgcn_rsqf(var + 1e-5f); PIN(r);
            ZS[wid * 16 + r16] = f32x2{r, fmul1(-m, r)};
        }
    };
    P2_A(1, false, zstat); wait_vm_bar<0>(); SB();
    auto merge = [&](u32x2 A, u32x2 B) {
        auto r0 = __builtin_amdgcn_permlane16_swap(A[0], B[0], false, false); auto r1 = __builtin_amdgcn_permlane16_swap(A[1], B[1], false, false);
        return u32x4{r0[0], r1[0], r0[1], r1[1]};
    };
    auto pk4 = [&](f32x4 v) { return u32x2{cvtpk(v[0], v[1]), cvtpk(v[2], v[3])}; };
    {
        const char* z2b = ws + WS_Z2T + (size_t)(j * 32 + 2 * pm + wr) * 18432;
        f32x2 zs[5];
#pragma unroll
        for (int m = 0; m < 5; ++m) zs[m] = ZS[wr * 64 + (m < 4 ? m : wc) * 16 + r16];
        const unsigned vs = (r16 >> 3) * 2304 + (((r16 & 7) ^ (4 * (r16 >> 3))) * 32), v2 = vs + (g & 1) * 4608 + (g >> 1) * 16;
#pragma unroll
        for (int n = 0; n < 2; ++n)
#pragma unroll
            for (int m = 0; m < 4; m += 2)
                st16s<0>(z2b + NF1(n) * 256 + m * 4608, v2, merge(pk4(acc1[m][n] * zs[m][0] + zs[m][1]), pk4(acc1[m + 1][n] * zs[m + 1][0] + zs[m + 1][1])));
        st8s<2048>(z2b + wc * 4608, vs + g * 8, pk4(sacc * zs[4][0] + zs[4][1]));
    }
    VSYNC();
    P2_B(1, false, true, none);
    P2_A(2, false, none);
    P2_B(2, false, false, none);
    if (r16 == 0) { *(f32x4*)(CS + 192 + NF2(0) * 16 + 4 * g) = cs2[0]; *(f32x4*)(CS + 192 + NF2(1) * 16 + 4 * g) = cs2[1]; }
    f32x4* HX = (f32x4*)(smem + K1_B0) + wr * 256 + lane;
    if (cg == 3) { HX[0] = acc2[0][0]; HX[64] = acc2[1][0]; HX[128] = acc2[2][0]; HX[192] = acc2[3][0]; }
    asm volatile("s_waitcnt lgkmcnt(0)\n\ts_barrier" ::: "memory");
    STAMP(3, 2);
    auto gel = [&](f32x4 a, f32x4 c, f32x4 rr) {
        f32x4 y = a - c * rr[0];
        f32x2 y0 = gelu_y2(f32x2{y[0], y[1]}, rr[1], rr[2], rr[3]), y1 = gelu_y2(f32x2{y[2], y[3]}, rr[1], rr[2], rr[3]);
        return u32x2{cvtpk(y0[0], y0[1]), cvtpk(y1[0], y1[1])};
    };
#pragma unroll
    for (int n = 0; n < 3; ++n) cs1[n] = *(const f32x4*)(CS + 192 + NF2(n) * 16 + 4 * g);
#pragma unroll
    for (int m = 0; m < 4; ++m) er[m] = RS[wr * 64 + m * 16 + r16];
    if (cg == 3) {
    } else if (nf3 >= 10 || j < 4) {
        const char* qb = ws + WS_Q + (pm * 16 + wr * 8) * 9216 + (nf3 >= 10 ? 4 * j + (nf3 - 10) * 2 : 64 + 2 * j) * 128;
        const unsigned vq = ((g & 1) * 2 + (r16 >> 3)) * 9216 + (g >> 1) * 128 + (r16 & 7) * 16;
#define QV(m) pk4((acc2[m][0] - cs1[0] * er[m][0]) * (er[m][1] * (QSCALE / c0)))
        st16s<0>(qb, vq, merge(QV(0), QV(1))); st16s<0>(qb + 4 * 9216, vq, merge(QV(2), QV(3)));
    } else if (j < 7) {
#pragma unroll
        for (int m = 0; m < 4; ++m)
            kv_store(ws + WS_KV + (size_t)(pm * 4 + wr * 2 + (m >> 1)) * 3072, m & 1, j, r16, g, (acc2[m][0] - cs1[0] * er[m][0]) * (er[m][1] * (1.0f / c0)));
    }
    SB();
    f32x4 h8 = HX[wc * 64], c8 = *(const f32x4*)(CS + 192 + 128 + 4 * g); er[4] = RS[wr * 64 + wc * 16 + r16];
    u32x4 zw[4];
#pragma unroll
    for (int m = 0; m < ZHALF; ++m) { zw[m] = merge(gel(acc2[m][1], cs1[1], er[m]), gel(acc2[m][2], cs1[2], er[m])); PIN(zw[m]); }
    volatile LAS u64* EW = (volatile LAS u64*)(lds + TOPL + 8);
    if (wid == 7) {
        if (__builtin_expect(epoch == 0, 0)) {
            const u64 t0 = __builtin_amdgcn_s_memrealtime();
            do { ev = ld_sc1_async(xe_s, le); wait_vm<0>(); PIN(ev); xcheck(); } while (epoch == 0 && __builtin_amdgcn_s_memrealtime() - t0 < 5000000ull);
        }
        *EW = epoch;
    }
    SB();
    wait_vm_bar<0>();
    STAMP(0, 3);
    if (wid == 7) {
        const bool kvp = j >= 4 && j < 7;
        const unsigned fo = lane == 0 ? (unsigned)(WS_FJ - WS_FA) + (j * 64 + pm) * 8 : (lane == 1 ? (unsigned)(WS_FP - WS_FA) + (pm * 16 + j) * 8 : (lane == 2 ? bid * 8
                            : (unsigned)(WS_FJ - WS_FA) + ((lane & 15) * 64 + 16 + pm * 3 + (j - 4)) * 8));
        if (lane < 3 || (kvp && g == 1)) __hip_atomic_store((gu64*)(ws + WS_FA + fo), epoch, RLX_AGENT);
    } else if (wid < 4) {
        const char* sa = (const char*)w_sp - 4096 + pm * 128 * S_ * 2;
#pragma unroll
        for (int t = 0; t < 2; ++t) {
            set_m0(lds0 + wid * 4096 + t * K2_SA);
            glds_s<0>(g2_va(wid, lane, 0), sa + t * 128); glds_s<1024>(g2_va(wid, lane, 1), sa + t * 128); glds_s<2048>(g2_va(wid, lane, 2), sa + t * 128); glds_s<3072>(g2_va(wid, lane, 3), sa + t * 128);
        }
    }
    SB();
#pragma unroll
    for (int m = ZHALF; m < 4; ++m) zw[m] = merge(gel(acc2[m][1], cs1[1], er[m]), gel(acc2[m][2], cs1[2], er[m]));
    u32x2 zs8 = gel(h8, c8, er[4]);
    {
        LAS char* zl = (LAS char*)lds + Z1L + wr * 1024 + r16 * 16;
#pragma unroll
        for (int m = 0; m < 4; ++m) *(LAS u32x4*)(zl + cg * 8192 + (2 * ((g & 1) ^ wr) + (g >> 1)) * 2048 + m * 256) = zw[m];
        *(LAS u32x2*)(zl + 32768 + wc * 256 + (g >> 1) * 2048 + (g & 1) * 8) = zs8;
    }
    const u64 e_ = *EW;
    const unsigned eh = __builtin_amdgcn_readfirstlane((unsigned)(e_ >> 32)), el = __builtin_amdgcn_readfirstlane((unsigned)e_);
    return ((u64)eh << 32) | el;
}

DI void wait_in(char* ws, u64 epoch, int lane, int wid, int pm, int j) {
    if (wid == POLLW) {
        const u64 t0 = __builtin_amdgcn_s_memrealtime();
        for (;;) {
            u64 f0_, f1_;
            asm volatile("global_load_dwordx2 %0, %2, %3 sc1\n\tglobal_load_dwordx2 %1, %4, %5 sc1\n\ts_waitcnt vmcnt(0)" : "=&v"(f0_), "=&v"(f1_)
                         : "v"(lane * 8), "s"(ws + WS_FJ + j * 512), "v"((lane & 15) * 8), "s"(ws + WS_FP + pm * 128) : "memory");
            if (__all(f0_ == epoch && f1_ == epoch)) break;
            const u64 dt = __builtin_amdgcn_s_memrealtime() - t0;
            if (dt > 5000000ull) break;
            if (dt > 1000 && blockIdx.x == 0) epoch_service(ws, lane, epoch);
            __builtin_amdgcn_s_sleep(POLLSLEEP1);
        }
    }
    asm volatile("s_barrier" ::: "memory");
}
DI void wait_fa(char* ws, u64 epoch, int lane) {
    const u64 t0 = __builtin_amdgcn_s_memrealtime();
    for (;;) {
        u32x4 a, b;
        asm volatile("global_load_dwordx4 %0, %2, %3 sc1\n\tglobal_load_dwordx4 %1, %2, %3 offset:1024 sc1\n\ts_waitcnt vmcnt(0)" : "=&v"(a), "=&v"(b) : "v"(lane * 16), "s"(ws + WS_FA) : "memory");
        const unsigned lo = (unsigned)epoch, hi = (unsigned)(epoch >> 32);
        if (__all(a[0] == lo && a[1] == hi && a[2] == lo && a[3] == hi && b[0] == lo && b[1] == hi && b[2] == lo && b[3] == hi)) break;
        if (__builtin_amdgcn_s_memrealtime() - t0 > 5000000ull) break;
        if (blockIdx.x == 0) epoch_service(ws, lane, epoch);
        __builtin_amdgcn_s_sleep(8);
    }
}

constexpr float ATT_BIAS = 72.0f;
DI float xhalf_max(float m) { auto rr = __builtin_amdgcn_permlane32_swap(__float_as_uint(m), __float_as_uint(m), false, false); return fmaxf(__uint_as_float(rr[0]), __uint_as_float(rr[1])); }
DI float lo_bcast(float m) { auto rr = __builtin_amdgcn_permlane32_swap(__float_as_uint(m), __float_as_uint(m), false, false); return __uint_as_float(rr[0]); }
DI float bf_round(float x) { return __uint_as_float(cvtpk(x, 0.f) << 16); }
DI float max16(const f32x16& s) {
    float a = fmaxf(fmaxf(s[0], s[1]), s[2]), b = fmaxf(fmaxf(s[3], s[4]), s[5]);
    a = fmaxf(fmaxf(a, s[6]), s[7]); b = fmaxf(fmaxf(b, s[8]), s[9]); a = fmaxf(fmaxf(a, s[10]), s[11]); b = fmaxf(fmaxf(b, s[12]), s[13]);
    a = fmaxf(fmaxf(a, s[14]), s[15]); return fmaxf(a, b);
}
DI void exp_pack(const f32x16& s, bf16x8& p0, bf16x8& p1) {
    float e[16];
#pragma unroll
    for (int i = 0; i < 16; ++i) e[i] = __builtin_amdgcn_exp2f(s[i]);
    u32x4 a = {cvtpk(e[0], e[1]), cvtpk(e[2], e[3]), cvtpk(e[4], e[5]), cvtpk(e[6], e[7])};
    u32x4 b = {cvtpk(e[8], e[9]), cvtpk(e[10], e[11]), cvtpk(e[12], e[13]), cvtpk(e[14], e[15])};
    p0 = __builtin_bit_cast(bf16x8, a); p1 = __builtin_bit_cast(bf16x8, b);
}

struct KVF { bf16x8 k1, k2, v0, v1; };
DI void kv_read(KVF& f, const LAS char* tb, int lane, int k2o, int vo) {
    f.k1 = *(const LAS bf16x8*)(tb + lane * 16); f.k2 = *(const LAS bf16x8*)(tb + k2o);
    f.v0 = *(const LAS bf16x8*)(tb + 1024 + vo); f.v1 = *(const LAS bf16x8*)(tb + 2048 + vo);
}
#define k2frag(x) (x)
struct NoHook { DI void operator()(int) const {} };
template <bool RD, class H = NoHook>
DI void half_step(f32x16& sx, bf16x8& px0, bf16x8& px1, f32x16& sy, const KVF kq, const bf16x8& qy1, const bf16x8& qy2,
                  f32x16& oy, const KVF kv, const bf16x8& py0, const bf16x8& py1, KVF& ld, const LAS char* tb, int lane, int k2o, int vo, H hook = H(), int hs = 0) {
    const f32x16 zero = {};
    unsigned a0, a1, a2, a3, b0, b1, b2, b3; float e0, e1, e2, e3;
    f32x16 t = MFMA32(kq.k1, qy1, zero); SB();
    hook(hs); SB();
    if (RD) ld.k1 = *(const LAS bf16x8*)(tb + lane * 16);
    e0 = EX(sx[0]); e1 = EX(sx[1]); e2 = EX(sx[2]); e3 = EX(sx[3]); PIN(e0); PIN(e1); PIN(e2); PIN(e3); SB();
    sy = MFMA32(kq.k2, qy2, t); SB();
    if (RD) ld.k2 = *(const LAS bf16x8*)(tb + k2o);
    a0 = cvtpk(e0, e1); a1 = cvtpk(e2, e3); PIN(a0); PIN(a1);
    e0 = EX(sx[4]); e1 = EX(sx[5]); e2 = EX(sx[6]); e3 = EX(sx[7]); PIN(e0); PIN(e1); PIN(e2); PIN(e3); SB();
    oy = MFMA32(kv.v0, py0, oy); SB();
    hook(hs + 1); SB();
    if (RD) ld.v0 = *(const LAS bf16x8*)(tb + 1024 + vo);
    a2 = cvtpk(e0, e1); a3 = cvtpk(e2, e3); PIN(a2); PIN(a3);
    e0 = EX(sx[8]); e1 = EX(sx[9]); e2 = EX(sx[10]); e3 = EX(sx[11]); PIN(e0); PIN(e1); PIN(e2); PIN(e3); SB();
    oy = MFMA32(kv.v1, py1, oy); SB();
    if (RD) ld.v1 = *(const LAS bf16x8*)(tb + 2048 + vo);
    b0 = cvtpk(e0, e1); b1 = cvtpk(e2, e3); PIN(b0); PIN(b1);
    e0 = EX(sx[12]); e1 = EX(sx[13]); e2 = EX(sx[14]); e3 = EX(sx[15]);
    b2 = cvtpk(e0, e1); b3 = cvtpk(e2, e3); PIN(b2); PIN(b3); SB();
    u32x4 a = {a0, a1, a2, a3}, b = {b0, b1, b2, b3};
    px0 = __builtin_bit_cast(bf16x8, a); px1 = __builtin_bit_cast(bf16x8, b);
}
DI bf16x8 p_scale(bf16x8 p, int sh) {
    u32x4 w = __builtin_bit_cast(u32x4, p);
#pragma unroll
    for (int i = 0; i < 4; ++i) w[i] = cvtpk(__builtin_amdgcn_ldexpf(bflo(w[i]), sh), __builtin_amdgcn_ldexpf(bfhi(w[i]), sh));
    return __builtin_bit_cast(bf16x8, w);
}
template <bool PP> DI void att_rescale(f32x16& o, f32x16& s, bf16x8& p0, bf16x8& p1, float& mref, bf16x8& q2, bool& bad, int c, int h) {
    bool nf = false;
#pragma unroll
    for (int i = 0; i < 11; ++i) nf = nf || (!(fabsf(o[i]) <= 3.0e38f) && (i < 8 || h == 0));
    if (__any(nf)) { bad = true; return; }
    float lb = lo_bcast(o[10]);
    int e = __builtin_amdgcn_frexp_expf(lb);
    float d = (float)(e + (int)ATT_BIAS); d = d < 0 ? 0 : d;
    float nm = bf_round(mref + d); float dd = nm - mref; mref = nm;
    int sh = -(int)dd;
#pragma unroll
    for (int i = 0; i < 16; ++i) { o[i] = __builtin_amdgcn_ldexpf(o[i], sh); if (!PP) { float b_ = s[i] - dd; PIN(b_); s[i] = b_; } }
    if (PP) { p0 = p_scale(p0, sh); p1 = p_scale(p1, sh); }
    u32x4 b = __builtin_bit_cast(u32x4, q2); b[1] = h ? 0u : (__float_as_uint(-nm) >> 16); q2 = __builtin_bit_cast(bf16x8, b);
}

#define W3LD 1
#define W3ST 3
struct G2F { bf16x8 a[4], b[4], sb; };
struct G2S {
    unsigned va[4], vl, vl2;
    const char* sa; const char* sl; unsigned lstep;
    unsigned m0a, m0l[3];
    const LAS char* pa[2][2]; const LAS char* pb[2]; const LAS char* ps[2];
};
template <int KS, int GL, bool RD, int RS, int WS, class H = NoHook> DI void g2_phase(G2S& s, const G2F& f, G2F& n, f32x4 (&acc)[4][4], f32x4 (&sacc)[2], H hook = H()) {
    const LAS char* a0 = s.pa[0][KS] + RS * K2_SA; const LAS char* a1 = s.pa[1][KS] + RS * K2_SA;
    constexpr int BO = RS * K2_SB + KS * 9216;
    const LAS char* b0 = s.pb[0] + BO; const LAS char* b1 = s.pb[1] + BO;
#define MM(m, nn) acc[m][nn] = MFMA16(f.b[nn], f.a[m], acc[m][nn]); PIN(acc[m][nn]); SB(); hook((nn) * 4 + (m)); SB()
    MM(0, 0); if (RD) n.b[0] = ldsB2(b0, b1); SB();
    MM(1, 0); if (RD) n.a[0] = ldsA(a0); SB();
    MM(2, 0); if (RD) n.a[1] = ldsA(a0 + 2048); SB();
    MM(3, 0); if (RD) n.a[2] = ldsA(a1); SB();
    MM(0, 1); if (RD) n.a[3] = ldsA(a1 + 2048); SB();
    MM(1, 1); if (RD) n.b[1] = ldsB2(b0 + 256, b1 + 256); SB();
    MM(2, 1); if (RD) n.b[2] = ldsB2(b0 + 512, b1 + 512); SB();
    MM(3, 1); if (RD) n.b[3] = ldsB2(b0 + 768, b1 + 768); SB();
    MM(0, 2); if (RD) n.sb = ldsB2(s.ps[0] + BO, s.ps[1] + BO); SB();
    MM(1, 2); if (GL == 1) { set_m0(s.m0a + WS * K2_SA); glds_s<0>(s.va[0], s.sa); } if (GL == 2) { glds_sc1<1024>(s.vl, s.sl); } SB();
    MM(2, 2);
    MM(3, 2); if (GL == 1) glds_s<1024>(s.va[1], s.sa); if (GL == 2) glds_sc1<2048>(s.vl, s.sl); SB();
    MM(0, 3);
    MM(1, 3); if (GL == 1) glds_s<2048>(s.va[2], s.sa); if (GL == 2) glds_sc1<3072>(s.vl, s.sl); SB();
    MM(2, 3);
    MM(3, 3); if (GL == 1) { glds_s<3072>(s.va[3], s.sa); s.sa += 128; asm volatile("" : "+s"(s.sa)); } if (GL == 2) { set_m0(s.m0l[WS] + 4096); glds_sc1<0>(s.vl2, s.sl); } SB();
    sacc[0] = MFMA16(f.sb, f.a[0], sacc[0]); PIN(sacc[0]); SB();
    if (GL == 1) { set_m0(s.m0l[WS]); glds_sc1<0>(s.vl, s.sl); } if (GL == 2) { glds_sc1<1024>(s.vl2, s.sl); s.sl += s.lstep; asm volatile("" : "+s"(s.sl)); } SB();
    sacc[1] = MFMA16(f.sb, f.a[1], sacc[1]); PIN(sacc[1]); SB();
#undef MM
}
template <int C, bool G2nd, bool G1st, int VM, bool RDN> DI void g2_iter(G2S& s, G2F& f0, G2F& f1, f32x4 (&acc)[4][4], f32x4 (&sacc)[2]) {
    g2_phase<1, G2nd ? 2 : 0, true, C, (C + 2) % 3>(s, f0, f1, acc, sacc);
    wait_vm_bar<VM>(); SB();
    g2_phase<0, G1st ? 1 : 0, RDN, (C + 1) % 3, C>(s, f1, f0, acc, sacc);
}

constexpr int K3_S = 4608, K3_RW = 4 * K3_S;
struct B5 { bf16x8 b[5]; };
DI unsigned k3_so(int w, int sl) {
    const int r = sl == 1 ? 2 : sl - 2;
    if (sl == 0) return Z1L + w * K3_S;
    return w < 3 ? r * K2_SA + w * K3_S : (w < 7 ? K2_B0 + r * K2_SB + (w - 3) * K3_S : K2_K0 + r * K2_SK);
}
DI unsigned k3_orow(int r) { return r < 6 ? (r >> 1) * K2_SA + 3 * K3_S + 64 + (r & 1) * 1168 : K2_K0 + (r - 6) * K2_SK + K3_S + 64; }
DI void k3_b1(int p, unsigned m0v, unsigned va, const char* sb) {
    if (p == 0) { set_m0(m0v); glds_sc1<0>(va, sb); }
    if (p == 1) glds_sc1<1024>(va, sb);
    if (p == 2) glds_sc1<2048>(va, sb);
    if (p == 3) glds_sc1<3072>(va, sb);
    if (p == 4) asm volatile("s_mov_b32 m0, %2\n\ts_mov_b32 exec_hi, 0\n\tglobal_load_lds_dwordx4 %0, %1 sc1\n\ts_mov_b32 exec_hi, -1" ::"v"(va), "s"(sb + 4096), "s"(m0v + 4096));
}
DI void k3_a(unsigned m0v, unsigned va, const char* sa) {
    set_m0(m0v); glds_sc1<0>(va, sa); glds_sc1<1024>(va, sa); glds_sc1<2048>(va, sa); glds_sc1<3072>(va, sa);
}
DI void k3_b(unsigned m0v, unsigned va, const char* sb) {
#pragma unroll
    for (int p = 0; p < 5; ++p) k3_b1(p, m0v, va, sb);
}
DI void k3_rdb(B5& r, const LAS char* p, const LAS char* p5) {
#pragma unroll
    for (int n = 0; n < 4; ++n) r.b[n] = ldsA(p + n * 1024);
    r.b[4] = ldsA(p5);
}
DI void k3_lb1(int p, B5& r, unsigned va, unsigned va5, const char* sb) {
    if (p < 4) asm volatile("global_load_dwordx4 %0, %1, %2 offset:%3 sc1" : "+v"(r.b[p & 3]) : "v"(va), "s"(sb), "n"((p & 3) * 1024) : "memory");
    else asm volatile("global_load_dwordx4 %0, %1, %2 sc1" : "+v"(r.b[4]) : "v"(va5), "s"(sb) : "memory");
}
#define LB5 "global_load_dwordx4 %0, %5, %7 sc1\n\tglobal_load_dwordx4 %1, %5, %7 offset:1024 sc1\n\tglobal_load_dwordx4 %2, %5, %7 offset:2048 sc1\n\t" \
            "global_load_dwordx4 %3, %5, %7 offset:3072 sc1\n\tglobal_load_dwordx4 %4, %6, %7 sc1\n"
DI void k3_lb(B5& r, unsigned va, unsigned va5, const char* sb) {
    asm volatile(LB5 : "=&v"(r.b[0]), "=&v"(r.b[1]), "=&v"(r.b[2]), "=&v"(r.b[3]), "=&v"(r.b[4]) : "v"(va), "v"(va5), "s"(sb) : "memory");
}
DI void k3_lb_if(B5& r, unsigned va, unsigned va5, const char* sb, int a, int b) {
    asm volatile("s_cmp_lg_u32 %8, %9\n\ts_cbranch_scc1 .Llb%=\n\t" LB5 ".Llb%=:" : "+v"(r.b[0]), "+v"(r.b[1]), "+v"(r.b[2]), "+v"(r.b[3]), "+v"(r.b[4])
                 : "v"(va), "v"(va5), "s"(sb), "s"(a), "n"(b) : "memory", "scc");
}
DI void pin5(B5& r) { PIN(r.b[0]); PIN(r.b[1]); PIN(r.b[2]); PIN(r.b[3]); PIN(r.b[4]); }
template <int N0, int N1, bool FIRST = false> DI void k3_mm(f32x4 (&acc)[4][5], const bf16x8 (&a)[4], const B5& r) {
#pragma unroll
    for (int n = N0; n < N1; ++n)
#pragma unroll
        for (int m = 0; m < 4; ++m) {
            if (FIRST) asm volatile("v_mfma_f32_16x16x32_bf16 %0, %1, %2, 0" : "=&v"(acc[m][n]) : "v"(r.b[n]), "v"(a[m]));
            else asm volatile("v_mfma_f32_16x16x32_bf16 %0, %1, %2, %0" : "+v"(acc[m][n]) : "v"(r.b[n]), "v"(a[m]));
        }
    SB();
}

#define K3MM(d, b_, a_) asm volatile("v_mfma_f32_16x16x32_bf16 %0, %1, %2, %0" : "+v"(d) : "v"(b_), "v"(a_))
#define ILV 1
struct UA { const char* p[4]; };
template <int NX, bool FIRST = false> DI void k3_step(f32x4 (&acc)[4][5], bf16x8 (&a)[4], const B5& r, unsigned va, const UA& ua) {
#pragma unroll
    for (int m = 0; m < 4; ++m) {
#pragma unroll
        for (int n = 0; n < 5; ++n) {
            if (FIRST && m < 3) asm volatile("v_mfma_f32_16x16x32_bf16 %0, %1, %2, 0" : "=&v"(acc[m][n]) : "v"(r.b[n]), "v"(a[m]));
            else K3MM(acc[m][n], r.b[n], a[m]);
        }
        if (NX >= 0 && ILV) asm volatile("global_load_dwordx4 %0, %1, %2 sc1" : "+v"(a[m]) : "v"(va), "s"(ua.p[m] + (NX < 0 ? 0 : NX) * 32768) : "memory");
    }
    if (NX >= 0 && !ILV) {
#pragma unroll
        for (int m = 0; m < 4; ++m) asm volatile("global_load_dwordx4 %0, %1, %2 sc1" : "+v"(a[m]) : "v"(va), "s"(ua.p[m] + (NX < 0 ? 0 : NX) * 32768) : "memory");
    }
    SB();
}

DI void phase2(const u16* __restrict__ w_sp, const u16* __restrict__ b_sp, const u16* __restrict__ x, char* __restrict__ ws, u16* __restrict__ out, const u64 epoch, const int wid) {
    extern __shared__ __attribute__((aligned(16))) char smem[];
    int bid = blockIdx.x, xcd = bid & 7, idx = bid >> 3;
    int pm = (xcd & 1) * 8 + (idx & 7), j = (xcd >> 1) * 4 + (idx >> 3);
    unsigned zz = 0; asm volatile("" : "+s"(zz));
    const int lane = __builtin_amdgcn_mbcnt_hi(~0u, __builtin_amdgcn_mbcnt_lo(~0u, zz)) & 63;
    const int tid = wid * 64 + lane;
    const unsigned lds0 = (unsigned)(uintptr_t)smem; const LAS char* lds = (const LAS char*)smem;
    STAMP(1, 0);
    const int rb = ((xcd & 1) ^ 1) * 16 + (xcd >> 1) * 4 + (idx & 3), j3 = idx >> 2;
    volatile LAS unsigned* SY = (volatile LAS unsigned*)(lds + TOPL);
    gu64* FLr = (gu64*)(ws + WS_FL) + rb * 64;
    const unsigned va = lane * 16;
    unsigned va5 = 4096 + ((lane >> 4) * 8 + (lane & 7)) * 16; PIN(va5);
    B5 rb0, rb1, rb2, rb3;
    const char* wb = ws + WS_W3T + (size_t)j3 * 90 * 4608 + wid * 4608;
    if (__builtin_expect(wid < 4, 1)) {
        const int wr = wid >> 1, wc = wid & 1;
        G2S s;
#pragma unroll
        for (int i = 0; i < 4; ++i) { s.va[i] = g2_va(wid, lane, i); PIN(s.va[i]); }
        s.vl = lane * 16; s.vl2 = lane * 16 + 4096;
        s.lstep = wid < 3 ? 18432 : 6144;
        unsigned ol = (wid < 3 ? (unsigned)WS_Z2T + j * 32 * 18432 + wid * 6144 : (unsigned)WS_KV), oa = pm * 128 * S_ * 2;
        s.m0a = lds0 + wid * 4096; s.m0l[0] = lds0 + (wid < 3 ? K2_B0 + wid * 6144 : K2_K0); s.m0l[1] = s.m0l[0] + s.lstep; s.m0l[2] = s.m0l[1] + s.lstep;
        PIN(s.vl); PIN(s.vl2);
        asm volatile("" : "+s"(ol), "+s"(oa), "+s"(s.m0a), "+s"(s.m0l[0]));
        SB();
        s.sa = (const char*)w_sp - 4096 + oa; s.sl = ws + ol;
        s.sa += 256;
        {
            int r = lane & 15, g = lane >> 4, q = r >> 2, p = lane & 3;
#pragma unroll
            for (int ks = 0; ks < 2; ++ks) {
                int o = r * 128 + (((4 * ks + g) ^ (r >> 1)) * 16) + wr * 8192;
                s.pa[0][ks] = lds + o + wc * 4096; s.pa[1][ks] = lds + o + (1 - wc) * 4096; PIN(s.pa[0][ks]); PIN(s.pa[1][ks]);
            }
            int ob = K2_B0 + g * 2304 + q * 32 + 8 * p;
            s.pb[0] = lds + ob + wc * 1024 + ((g & 1) * 128); s.pb[1] = lds + ob + wc * 1024 + (((g & 1) ^ 1) * 128);
            s.ps[0] = lds + ob + 2048 + ((g & 1) * 128); s.ps[1] = lds + ob + 2048 + (((g & 1) ^ 1) * 128);
            PIN(s.pb[0]); PIN(s.pb[1]); PIN(s.ps[0]); PIN(s.ps[1]);
        }
        G2F f0, f1;
        SB();
        f32x4 acc[4][4], sacc[2];
#pragma unroll
        for (int m = 0; m < 4; ++m) {
            const float b = bf2f(*(const LAS u16*)(lds + TOPL + 2304 + (wr * 64 + (m ^ (2 * wc)) * 16 + (lane & 15)) * 2));
#pragma unroll
            for (int n = 0; n < 4; ++n) { acc[m][n] = f32x4{b, b, b, b}; PIN(acc[m][n]); }
            if (m < 2) { sacc[m] = f32x4{b, b, b, b}; PIN(sacc[m]); }
        }
        SB();
        wait_in(ws, epoch, lane, wid, pm, j);
        STAMPT(0, 3, 3);
#pragma unroll
        for (int t = 0; t < 2; ++t) {
            set_m0(s.m0l[t]); glds_sc1<0>(s.vl, s.sl); glds_sc1<1024>(s.vl, s.sl); glds_sc1<2048>(s.vl, s.sl); glds_sc1<3072>(s.vl, s.sl);
            set_m0(s.m0l[t] + 4096); glds_sc1<0>(s.vl2, s.sl); glds_sc1<1024>(s.vl2, s.sl); s.sl += s.lstep;
        }
        set_m0(s.m0a + 2 * K2_SA); glds_s<0>(s.va[0], s.sa); glds_s<1024>(s.va[1], s.sa); glds_s<2048>(s.va[2], s.sa); glds_s<3072>(s.va[3], s.sa); s.sa += 128;
        set_m0(s.m0l[2]); glds_sc1<0>(s.vl, s.sl);
        wait_vm_bar<11>();
        STAMP(1, 1);
        f0.b[0] = ldsB2(s.pb[0], s.pb[1]);
        f0.a[0] = ldsA(s.pa[0][0]); f0.a[1] = ldsA(s.pa[0][0] + 2048); f0.a[2] = ldsA(s.pa[1][0]); f0.a[3] = ldsA(s.pa[1][0] + 2048);
        f0.b[1] = ldsB2(s.pb[0] + 256, s.pb[1] + 256); f0.b[2] = ldsB2(s.pb[0] + 512, s.pb[1] + 512); f0.b[3] = ldsB2(s.pb[0] + 768, s.pb[1] + 768); f0.sb = ldsB2(s.ps[0], s.ps[1]);
#pragma unroll 1
        for (int t = 0; t < 9; ++t) {
            g2_iter<0, true, true, 10, true>(s, f0, f1, acc, sacc); g2_iter<1, true, true, 10, true>(s, f0, f1, acc, sacc); g2_iter<2, true, true, 10, true>(s, f0, f1, acc, sacc);
        }
        asm volatile("" : "=v"(rb0.b[0]), "=v"(rb0.b[1]), "=v"(rb0.b[2]), "=v"(rb0.b[3]), "=v"(rb0.b[4]), "=v"(rb1.b[0]), "=v"(rb1.b[1]), "=v"(rb1.b[2]), "=v"(rb1.b[3]), "=v"(rb1.b[4]));
        g2_phase<1, 2, true, 0, 2>(s, f0, f1, acc, sacc);
        wait_vm_bar<10>(); SB();
        g2_phase<0, 1, true, 1, 0>(s, f1, f0, acc, sacc, [&](int st) { if (st < 10 && !(st & 1)) k3_lb1(st >> 1, rb0, va, va5, wb); });
        g2_phase<1, 2, true, 1, 0>(s, f0, f1, acc, sacc);
        wait_vm_bar<15>(); SB();
        g2_phase<0, 1, true, 2, 1>(s, f1, f0, acc, sacc, [&](int st) { if (st < 10 && !(st & 1)) k3_lb1(st >> 1, rb1, va, va5, wb + 36864); });
        const int c = lane & 15, g = lane >> 4;
        const LAS char* z1b = lds + Z1L + (g >> 1) * 2048 + (wr * 64 + c) * 16 + (g & 1) * 8;
        u32x2 z1r[4][4], z1s[2];
#pragma unroll
        for (int m = 0; m < 4; ++m) {
            const int mo = (m ^ (2 * wc)) * 16;
#pragma unroll
            for (int n = 0; n < 4; ++n) z1r[m][n] = *(const LAS u32x2*)(z1b + (wc * 4 + n) * 4096 + mo * 16);
            if (m < 2) z1s[m] = *(const LAS u32x2*)(z1b + 8 * 4096 + mo * 16);
        }
        SB();
#define PREB(i) [&](int st) { if (st < 5) k3_b1(st, lds0 + k3_so(wid, i), va, wb + ((i) + 6) * 36864); else if (st < 10) k3_b1(st - 5, lds0 + k3_so(wid + 4, i), va, wb + ((i) + 6) * 36864 + 4 * 4608); }
        g2_phase<1, 2, true, 2, 1>(s, f0, f1, acc, sacc);
        wait_vm_bar<15>(); SB();
        g2_phase<0, 0, true, 0, 2>(s, f1, f0, acc, sacc, PREB(0));
        g2_phase<1, 0, true, 0, 2>(s, f0, f1, acc, sacc, PREB(1));
        wait_vm_bar<20>(); SB();
        g2_phase<0, 0, true, 1, 0>(s, f1, f0, acc, sacc, PREB(2));
        unsigned vu[3];
#pragma unroll
        for (int i = 0; i < 3; ++i) {
            const int cgl = 18 * j + (i < 2 ? 2 * (wc * 4 + 2 * i + (g & 1)) : 16) + (g >> 1);
            vu[i] = (cgl >> 2) * 4096 + (cgl & 3) * 256 + c * 16 + (i == 2 ? (g & 1) * 1024 : 0); PIN(vu[i]);
        }
        const char* ub0 = ws + WS_UO + (size_t)(pm * 2 + wr) * 90 * 4096 + wc * 2048; const char* ub1 = ub0 + 2048 - wc * 4096;
        SB();
        g2_phase<1, 0, true, 1, 0>(s, f0, f1, acc, sacc);
        wait_vm_bar<0>(); SB();
        __builtin_amdgcn_s_setprio(2);
        auto fin = [&](f32x4 v, u32x2 z) { return u32x2{cvtpk(fmul1(bflo(z[0]), v[0]), fmul1(bfhi(z[0]), v[1])), cvtpk(fmul1(bflo(z[1]), v[2]), fmul1(bfhi(z[1]), v[3]))}; };
        auto mrg = [&](u32x2 A, u32x2 B) {
            auto r0 = __builtin_amdgcn_permlane16_swap(A[0], B[0], false, false); auto r1 = __builtin_amdgcn_permlane16_swap(A[1], B[1], false, false);
            return u32x4{r0[0], r1[0], r0[1], r1[1]};
        };
#define M2(m, n) acc[m][n] = MFMA16(f1.b[n], f1.a[m], acc[m][n]); acc[m][(n) + 1] = MFMA16(f1.b[(n) + 1], f1.a[m], acc[m][(n) + 1]); PIN(acc[m][n]); PIN(acc[m][(n) + 1]); SB()
#define PUT(m, n) st16s<((m) & 1) * 1024>((m) < 2 ? ub0 : ub1, vu[(n) >> 1], mrg(fin(acc[m][n], z1r[m][n]), fin(acc[m][(n) + 1], z1r[m][(n) + 1]))); SB()
        M2(0, 0); M2(1, 0); M2(2, 0); PUT(0, 0);
        M2(3, 0); PUT(1, 0);
        sacc[0] = MFMA16(f1.sb, f1.a[0], sacc[0]); sacc[1] = MFMA16(f1.sb, f1.a[1], sacc[1]); PIN(sacc[0]); PIN(sacc[1]); SB(); PUT(2, 0);
        M2(0, 2); PUT(3, 0);
        M2(1, 2); st16s<0>(ub0, vu[2], mrg(fin(sacc[0], z1s[0]), fin(sacc[1], z1s[1]))); SB();
        M2(2, 2); PUT(0, 2);
        M2(3, 2); PUT(1, 2);
        STAMP(1, 2);
        PUT(2, 2); PUT(3, 2);
        SB(); k3_b(lds0 + k3_so(wid, 3), va, wb + 9 * 36864);
        wait_vm<5>();
        if (lane == 0) __hip_atomic_store((gu64*)(ws + WS_FL) + (pm * 2 + wr) * 64 + j * 2 + wc, epoch, RLX_AGENT);
        pin5(rb0); pin5(rb1);
        asm volatile("" : "=v"(rb2.b[0]), "=v"(rb2.b[1]), "=v"(rb2.b[2]), "=v"(rb2.b[3]), "=v"(rb2.b[4]), "=v"(rb3.b[0]), "=v"(rb3.b[1]), "=v"(rb3.b[2]), "=v"(rb3.b[3]), "=v"(rb3.b[4]));
        STAMP(1, 3);
        __builtin_amdgcn_s_setprio(0);
    } else {
        __builtin_amdgcn_s_setprio(1);
        const int c = lane & 31, h = lane >> 5, aw = wid - 4;
        const char* kvg = ws + WS_KV;
        bf16x8 q1[2], q2[2]; float mref[2];
        const f32x16 zero = {};
        {
            const int qbase = (aw < 3 ? K2_B0 + 2 * K2_SB + aw * 6144 : K2_K0 + 2 * K2_SK) + 1024;
            const LAS char* ql = lds + qbase;
            unsigned qo[5];
#pragma unroll
            for (int i = 0; i < 5; ++i) { const int k = 18 * c + (i < 4 ? 8 * h + 2 * i : 16); qo[i] = (k >> 3) * 32 + (k & 7) * 2; PIN(qo[i]); }
            const char* qs = ws + WS_Q + (pm * 16 + j) * 9216 + aw * 32;
            unsigned vq = (lane >> 1) * 128 + (lane & 1) * 16; PIN(vq);
            wait_in(ws, epoch, lane, wid, pm, j);
            set_m0(lds0 + qbase); glds_sc1<0>(vq, qs); glds_sc1<1024>(vq, qs + 3072);
            if (lane < 16) glds_sc1<2048>(vq, qs + 6144);
            wait_vm<0>();
#pragma unroll
            for (int t = 0; t < 2; ++t) {
                u32x4 a;
#pragma unroll
                for (int i = 0; i < 4; ++i) a[i] = *(const LAS unsigned*)(ql + qo[i] + t * 16);
                q1[t] = __builtin_bit_cast(bf16x8, a);
                const unsigned e = *(const LAS unsigned*)(ql + qo[4] + t * 16);
                u32x4 b = {h ? 0u : e, 0u, 0u, 0u}; q2[t] = __builtin_bit_cast(bf16x8, b);
                PIN(q1[t]); PIN(q2[t]);
            }
        }
        f32x16 oA = zero, oB = zero;
        bool bad = false;
        const int k2o = 1024 + (c >> 4) * 1024 + (c & 15) * 16;
        const int vo = c < 8 ? 256 + (h * 8 + c) * 16 : 512 + (h * 16 + (c < 16 ? c - 8 : (c < 18 ? c - 2 : (c == 18 ? 12 : 13)))) * 16;
        const LAS char* kvl = lds + K2_K0;
        if (tid == 256) { SY[0] = 0; SY[1] = 0; SY[4] = 0; }
        asm volatile("s_waitcnt vmcnt(0) lgkmcnt(0)\n\ts_barrier" ::: "memory");
        KVF X, Y;
        kv_read(X, kvl, lane, k2o, vo); kv_read(Y, kvl + 3072, lane, k2o, vo);
#pragma unroll
        for (int t = 0; t < 2; ++t) {
            f32x16 s0 = MFMA32(X.k1, q1[t], zero); s0 = MFMA32(X.k2, q2[t], s0);
            f32x16 s1 = MFMA32(Y.k1, q1[t], zero); s1 = MFMA32(Y.k2, q2[t], s1);
            float m = xhalf_max(fmaxf(max16(s0), max16(s1)));
            mref[t] = bf_round(rintf(m) + ATT_BIAS);
            u32x4 b = __builtin_bit_cast(u32x4, q2[t]); b[1] = h ? 0u : (__float_as_uint(-mref[t]) >> 16); q2[t] = __builtin_bit_cast(bf16x8, b);
        }
        f32x16 sA = MFMA32(X.k1, q1[0], zero); sA = MFMA32(X.k2, q2[0], sA);
        f32x16 sB = zero;
        bf16x8 pA0, pA1, pB0 = {}, pB1 = {};
        half_step<false>(sA, pA0, pA1, sB, X, q1[1], q2[1], oB, X, pB0, pB1, Y, kvl, lane, k2o, vo);
        half_step<false>(sB, pB0, pB1, sA, Y, q1[0], q2[0], oA, X, pA0, pA1, Y, kvl, lane, k2o, vo);
        asm volatile("s_waitcnt lgkmcnt(0)\n\ts_barrier" ::: "memory");
#define A_IV(sl, CHK, H, hs) { const LAS char* tb = kvl + (sl) * K2_SK; \
            half_step<true>(sA, pA0, pA1, sB, Y, q1[1], q2[1], oB, X, pB0, pB1, X, tb, lane, k2o, vo, H, hs); \
            half_step<false>(sB, pB0, pB1, sA, X, q1[0], q2[0], oA, Y, pA0, pA1, X, tb, lane, k2o, vo, H, hs + 2); \
            if (CHK) { mB = __builtin_amdgcn_ballot_w64(!(oB[10] <= 0x1p20f)); }                  \
            half_step<true>(sA, pA0, pA1, sB, X, q1[1], q2[1], oB, Y, pB0, pB1, Y, tb + 3072, lane, k2o, vo, H, hs + 4); \
            if (CHK) { mA = __builtin_amdgcn_ballot_w64(!(oA[10] <= 0x1p20f)); any = mA | mB; asm volatile("" : "+s"(any)); } \
            half_step<false>(sB, pB0, pB1, sA, Y, q1[0], q2[0], oA, X, pA0, pA1, Y, tb, lane, k2o, vo, H, hs + 6); }
#define A_BAR() asm volatile("s_waitcnt lgkmcnt(0)\n\ts_barrier" ::: "memory")
        unsigned long long mA, mB, any;
#define UNDEF5(r) asm volatile("" : "=v"(r.b[0]), "=v"(r.b[1]), "=v"(r.b[2]), "=v"(r.b[3]), "=v"(r.b[4]))
        u32x4 fa0, fa1;
        asm volatile("global_load_dwordx4 %0, %2, %3 sc1\n\tglobal_load_dwordx4 %1, %2, %3 offset:1024 sc1" : "=&v"(fa0), "=&v"(fa1) : "v"(va), "s"(ws + WS_FA) : "memory");
        const NoHook nh;
        const char* pf = ws + WS_W3T + (size_t)j3 * 90 * 4608 + 16 * 4608 + min(((idx & 3) * 4 + aw) * 192, 2664 - 192) * 128;
        auto wl = [&](int i) {
            if (i < 20) k3_lb1(i % 5, i < 5 ? rb0 : (i < 10 ? rb1 : (i < 15 ? rb2 : rb3)), va, va5, wb + (i / 5) * 36864);
            else if (i < 23 && PFW)
                asm volatile("s_mov_b32 m0, %2\n\ts_nop 0\n\tglobal_load_lds_dword %0, %1 sc1" ::"v"(va * 8), "s"(pf + (i - 20) * 8192), "s"(lds0 + TOPL + 64 + wid * 256));
        };
#define A_IT(H) A_IV(1, false, H, 0); A_BAR(); A_IV(2, false, H, 8); A_BAR(); A_IV(0, true, H, 16); \
            if (__builtin_expect(any != 0, 0)) { \
                if (mA) att_rescale<false>(oA, sA, pA0, pA1, mref[0], q2[0], bad, c, h); \
                if (mB) att_rescale<true>(oB, sB, pB0, pB1, mref[1], q2[1], bad, c, h); \
            } \
            A_BAR()
#pragma unroll 1
        for (int t = 0; t < TFA; ++t) { A_IT(nh); }
        {
            wait_vm<0>(); PIN(fa0); PIN(fa1);
            const unsigned lo = (unsigned)epoch, hi = (unsigned)(epoch >> 32);
            if (__builtin_expect(!__all(fa0[0] == lo && fa0[1] == hi && fa0[2] == lo && fa0[3] == hi && fa1[0] == lo && fa1[1] == hi && fa1[2] == lo && fa1[3] == hi), 0)) wait_fa(ws, epoch, lane);
            UNDEF5(rb0); UNDEF5(rb1); UNDEF5(rb2); UNDEF5(rb3);
            SB();
            A_IT(wl);
        }
#pragma unroll 1
        for (int t = TFA + 1; t < 10; ++t) { A_IT(nh); }
        A_IV(1, false, nh, 0); A_BAR();
        wait_vm<0>(); pin5(rb0); pin5(rb1); pin5(rb2); pin5(rb3);
        k3_b(lds0 + k3_so(wid, 3), va, wb + 9 * 36864); SB();
        half_step<false>(sA, pA0, pA1, sB, Y, q1[1], q2[1], oB, X, pB0, pB1, X, kvl, lane, k2o, vo);
        exp_pack(sB, pB0, pB1);
        oA = MFMA32(Y.v0, pA0, oA); oA = MFMA32(Y.v1, pA1, oA);
        oB = MFMA32(Y.v0, pB0, oB); oB = MFMA32(Y.v1, pB1, oB);
        {
            float sa_ = fabsf(oA[10]) * 0x1p27f, sb_ = fabsf(oB[10]) * 0x1p27f;
#pragma unroll
            for (int i = 0; i < 10; ++i) { sa_ += fabsf(oA[i]); PIN(sa_); sb_ += fabsf(oB[i]); PIN(sb_); }
            if (__builtin_amdgcn_ballot_w64(!(sa_ + sb_ <= 3.0e38f))) bad = true;
        }
        f32x16 o[2] = {oA, oB};
        if (__builtin_expect(bad, 0)) {
#pragma unroll 1
            for (int t = 0; t < 2; ++t) {
                bf16x8 qa = t ? q1[1] : q1[0]; u32x4 qb = __builtin_bit_cast(u32x4, t ? q2[1] : q2[0]); qb[1] = 0;
                f32x16 oo = zero; float m = -3.0e38f;
                for (int kt = 0; kt < 64; ++kt) {
                    const char* tb = kvg + (size_t)kt * 3072;
                    bf16x8 k1, v0, v1; u32x4 k2v;
                    asm volatile("global_load_dwordx4 %0, %4, %7 sc1\n\tglobal_load_dwordx4 %1, %5, %7 sc1\n\tglobal_load_dwordx4 %2, %6, %7 offset:1024 sc1\n\t"
                                 "global_load_dwordx4 %3, %6, %7 offset:2048 sc1\n\ts_waitcnt vmcnt(0)"
                                 : "=&v"(k1), "=&v"(k2v), "=&v"(v0), "=&v"(v1) : "v"(lane * 16), "v"(k2o), "v"(vo), "s"(tb) : "memory");
                    f32x16 s = MFMA32(k1, qa, zero); s = MFMA32(__builtin_bit_cast(bf16x8, k2v), __builtin_bit_cast(bf16x8, qb), s);
                    float nm = fmaxf(m, xhalf_max(max16(s)));
                    float sc = __builtin_amdgcn_exp2f(m - nm); m = nm;
#pragma unroll
                    for (int i = 0; i < 16; ++i) { float a_ = oo[i] * sc, b_ = s[i] - nm; PIN(a_); PIN(b_); oo[i] = a_; s[i] = b_; }
                    bf16x8 p0, p1; exp_pack(s, p0, p1);
                    oo = MFMA32(v0, p0, oo); oo = MFMA32(v1, p1, oo);
                }
                if (t) o[1] = oo; else o[0] = oo;
            }
        }
#pragma unroll
        for (int t = 0; t < 2; ++t) {
            float inv = __builtin_amdgcn_rcpf(lo_bcast(o[t][10])); PIN(inv);
            LAS unsigned* ob = (LAS unsigned*)(lds + k3_orow(aw * 2 + t) + 36 * c + 8 * h);
#define ON(i) cvtpk(fmul1(o[t][i], inv), fmul1(o[t][(i) + 1], inv))
            ob[0] = ON(0); ob[1] = ON(2); ob[4] = ON(4); ob[5] = ON(6);
            if (h == 0) ob[8] = ON(8);
        }
        asm volatile("s_waitcnt lgkmcnt(0)" ::: "memory");
        if (lane == 0) __hip_atomic_fetch_add((LAS unsigned*)SY, 1u, __ATOMIC_RELAXED, __HIP_MEMORY_SCOPE_WORKGROUP);
        __builtin_amdgcn_s_setprio(APRIO);
    }
    {
#define TK()
#define TP()
        TP();
        UA ua;
#pragma unroll
        for (int i = 0; i < 4; ++i) ua.p[i] = ws + WS_UO + (size_t)rb * 90 * 4096 + wid * 4096 + ((wid + 1 + i) & 3) * 1024;
        auto relane = [&]() { unsigned t_ = va; PIN(t_); return (int)(t_ >> 4); };
        unsigned so[4];
#pragma unroll
        for (int i = 0; i < 4; ++i) so[i] = __builtin_amdgcn_readfirstlane(k3_so(wid, i));
#define LG0() asm volatile("s_waitcnt lgkmcnt(0)" ::: "memory"); SB()
#define LB(d, i) k3_lb(d, va, va5, wb + (i) * 36864)
#define RB(d, sl) { asm volatile("" : "+s"(so[sl])); k3_rdb(d, lds + va + so[sl], lds + va5 + so[sl]); }
        B5 rb4, rb5; bf16x8 a0[4], a1[4], a2[4], a3[4], a4[4];
        k3_lb_if(rb2, va, va5, wb + 2 * 36864, wid >> 2, 0); k3_lb_if(rb3, va, va5, wb + 3 * 36864, wid >> 2, 0);
        TP();
        f32x4 ac[4][5];
        const size_t xo = (size_t)(rb * 64 + (wid & 3) * 16) * D_ + 72 * j3;
        if (wid >= 4) {
            u32x2 xv[5];
            const unsigned vx = (lane & 15) * (D_ * 2) + (lane >> 4) * 8;
            asm volatile("global_load_dwordx2 %0, %5, %7\n\tglobal_load_dwordx2 %1, %5, %7 offset:32\n\tglobal_load_dwordx2 %2, %5, %7 offset:64\n\tglobal_load_dwordx2 %3, %5, %7 offset:96\n\t"
                         "global_load_dwordx2 %4, %6, %7 offset:128" : "=&v"(xv[0]), "=&v"(xv[1]), "=&v"(xv[2]), "=&v"(xv[3]), "=&v"(xv[4]) : "v"(vx), "v"(vx & ~16u), "s"(x + xo) : "memory");
            while (SY[0] < 4) __builtin_amdgcn_s_sleep(1);
            const int orow = pm * 128 + j * 8;
            char* ob = ws + WS_UO + ((size_t)((orow >> 6) * 90 + 72) * 4 + ((orow >> 4) & 3)) * 1024 + (orow & 15) * 16;
#pragma unroll
            for (int r = 0; r < 3; ++r) {
                const int i = (wid - 4) * 144 + r * 64 + lane, ck = i >> 3, row = i & 7;
                if (r < 2 || lane < 16) st16(ob + (ck >> 2) * 4096 + (ck & 3) * 256 + row * 16, *(const LAS u32x4*)(lds + k3_orow(row) + ck * 16));
            }
            TP(); LB(rb4, 4); LB(rb5, 5); TP();
            wait_vm<10>();
            if (lane == 0) __hip_atomic_store((gu64*)(ws + WS_FL) + (pm * 2 + (j >> 3)) * 64 + 32 + (j & 7) * 4 + (wid - 4), epoch, RLX_AGENT);
#pragma unroll
            for (int n = 0; n < 5; ++n) { PIN(xv[n]); ac[3][n] = f32x4{bflo(xv[n][0]), bfhi(xv[n][0]), bflo(xv[n][1]), bfhi(xv[n][1])}; PIN(ac[3][n]); }
        } else {
            TP(); LB(rb4, 4); LB(rb5, 5); TP();
#pragma unroll
            for (int n = 0; n < 5; ++n) { ac[3][n] = f32x4{}; PIN(ac[3][n]); }
        }
        SB();
        TP();
        if (wid == PW2) {
            const unsigned long long t0 = __builtin_amdgcn_s_memrealtime();
            u64 f = __hip_atomic_load(FLr + (lane & 31), RLX_AGENT);
            while (!__all(f == epoch) && __builtin_amdgcn_s_memrealtime() - t0 < 5000000ull) {
                __builtin_amdgcn_s_sleep(POLLSLEEP);
                f = __hip_atomic_load(FLr + (lane & 31), RLX_AGENT);
            }
            SY[1] = 1;
        }
        while (SY[1] == 0) __builtin_amdgcn_s_sleep(1);
        TP();
        STAMP(2, 0); STAMP(2, 1);
#define LA4 "global_load_dwordx4 %0, %4, %5 sc1\n\tglobal_load_dwordx4 %1, %4, %6 sc1\n\tglobal_load_dwordx4 %2, %4, %7 sc1\n\tglobal_load_dwordx4 %3, %4, %8 sc1\n"
#define LAO(d, o_) : "=&v"(d[0]), "=&v"(d[1]), "=&v"(d[2]), "=&v"(d[3]) : "v"(va), "s"(ua.p[0] + (o_)), "s"(ua.p[1] + (o_)), "s"(ua.p[2] + (o_)), "s"(ua.p[3] + (o_))
#define LA(d, i) asm volatile(LA4 LAO(d, (i) * 32768) : "memory")
#define PIN4(d) PIN(d[0]); PIN(d[1]); PIN(d[2]); PIN(d[3])
#define GO(d, rb_, W, NX, ...) wait_vm<W>(); TK(); PIN4(d); SB(); k3_step<NX, ##__VA_ARGS__>(ac, d, rb_, va, ua)
        TK();
        LA(a0, 0); LA(a1, 1);
        const bool ext = (wid & 6) == 4;
        GO(a0, rb0, 4, 2, true);
        pin5(rb2); pin5(rb3); pin5(rb4); pin5(rb5);
        GO(a1, rb1, 4, 3);
        asm volatile("s_mov_b32 m0, %2\n\ts_nop 0\n\tglobal_load_lds_dword %0, %1 sc1" ::"v"(relane() * 4), "s"(FLr + 32), "s"(lds0 + TOPL + 64 + wid * 256));
        GO(a0, rb2, 5, 4);
        GO(a1, rb3, 5, 5); LB(rb0, 10);
        GO(a0, rb4, 9, 6); k3_lb_if(rb1, va, va5, wb + 11 * 36864 - 4 * 4608, wid >> 1, 2); RB(rb2, 0);
        const int l2 = relane();
        const unsigned fo = *(const volatile LAS unsigned*)(lds + TOPL + 64 + wid * 256 + l2 * 4);
        SB();
        GO(a1, rb5, 9, 7); LA(a2, 8); RB(rb3, 1); SB();
        if (__builtin_expect(!__all(fo == ((l2 & 1) ? (unsigned)(epoch >> 32) : (unsigned)epoch)), 0)) {
            const unsigned long long t0 = __builtin_amdgcn_s_memrealtime();
            u64 f2; do { f2 = __hip_atomic_load(FLr + l2, RLX_AGENT); } while (!__all(f2 == epoch) && __builtin_amdgcn_s_memrealtime() - t0 < 5000000ull);
        }
        GO(a0, rb2, 8, 9); LA(a3, 10); RB(rb4, 2); SB();
        asm volatile("s_cmp_lg_u32 %9, 2\n\ts_cbranch_scc1 .Lla%=\n\t" LA4 ".Lla%=:" LAO(a4, 11 * 32768 - 4 * 4096), "s"(wid >> 1) : "memory", "scc");
        GO(a1, rb3, 12, -1); RB(rb5, 3); SB();
        wait_vm<0>(); TK(); PIN4(a0); PIN4(a2); PIN4(a3); PIN4(a4); pin5(rb0); pin5(rb1);
        if (ext) k3_step<-1>(ac, a4, rb1, va, ua);
        const int l3 = relane(), r16 = l3 & 15, g = l3 >> 4;
        LAS char* pw = (LAS char*)lds + va;
        SB();
#pragma unroll
        for (int m = 0; m < 4; ++m)
#pragma unroll
            for (int q = 0; q < 15; ++q) {
                if (q < 5) K3MM(ac[m][q], rb4.b[q], a2[m]); else if (q < 10) K3MM(ac[m][q - 5], rb0.b[q - 5], a3[m]); else K3MM(ac[m][q - 10], rb5.b[q - 10], a0[m]);
                SB();
                if (m > 0 && q % 3 == 0) { const int n = q / 3; if (n < 4 || g < 2) *(LAS f32x4*)(pw + so[m - 1] + n * 1024) = ac[m - 1][n]; SB(); }
            }
        asm volatile("s_nop 7\n\ts_nop 7\n\ts_waitcnt lgkmcnt(0)" ::: "memory");
        TK();
        STAMP(2, 2);
        const LAS char* pr = lds + va;
#define PART(k, n) *(const LAS f32x4*)(pr + k3_so((wid & 4) + ((wid + 3 - (k)) & 3), k) + (n) * 1024)
        if (wid >= 4) {
            __builtin_amdgcn_s_setprio(EPRIO);
            if (l3 == 0) __hip_atomic_fetch_add((LAS unsigned*)SY + 4, 1u, __ATOMIC_RELAXED, __HIP_MEMORY_SCOPE_WORKGROUP);
            while (SY[4] < 4) __builtin_amdgcn_s_sleep(1);
            SB();
#pragma unroll
            for (int n = 0; n < 5; ++n) {
                const f32x4 p0 = PART(0, n), p1 = PART(1, n), p2 = PART(2, n);
#pragma unroll
                for (int e = 0; e < 4; ++e) ac[3][n][e] = fadd1(fadd1(ac[3][n][e], p0[e]), fadd1(p1[e], p2[e]));
            }
#pragma unroll
            for (int n = 0; n < 5; ++n) if (n < 4 || g < 2) *(LAS f32x4*)(pw + so[3] + n * 1024) = ac[3][n];
        }
        asm volatile("s_waitcnt lgkmcnt(0)" ::: "memory"); TK();
        asm volatile("s_barrier" ::: "memory"); TK();
        if (wid < 4) {
            u32x2 o[5];
#pragma unroll
            for (int n = 0; n < 5; ++n) {
                const f32x4 r = ((ac[3][n] + PART(0, n)) + (PART(1, n) + PART(2, n))) + *(const LAS f32x4*)(pr + k3_so(wid + 4, 3) + n * 1024);
                o[n] = u32x2{cvtpk(r[0], r[1]), cvtpk(r[2], r[3])};
            }
            u16* op = out + xo + r16 * D_;
#pragma unroll
            for (int n = 0; n < 4; n += 2) {
                auto r0 = __builtin_amdgcn_permlane16_swap(o[n][0], o[n + 1][0], false, false); auto r1 = __builtin_amdgcn_permlane16_swap(o[n][1], o[n + 1][1], false, false);
                *(u32x4*)(op + (n + (g & 1)) * 16 + (g >> 1) * 8) = u32x4{r0[0], r1[0], r0[1], r1[1]};
            }
            if (g < 2) *(u32x2*)(op + 64 + g * 4) = o[4];
        }
    }
    STAMP_DRAIN(); STAMP(2, 3);
}
__global__ __launch_bounds__(512, 2) void fused_kernel(const u16* __restrict__ x, const u16* __restrict__ wq, const u16* __restrict__ wk, const u16* __restrict__ wv,
                                                       const u16* __restrict__ w_in, const u16* __restrict__ w_sp, const u16* __restrict__ b_sp, const u16* __restrict__ w_out,
                                                       const u16* __restrict__ wo, char* __restrict__ ws, u16* __restrict__ out) {
    const u64 epoch = phase1(x, wq, wk, wv, w_in, w_sp, b_sp, w_out, wo, ws);
    phase2(w_sp, b_sp, x, ws, out, epoch, __builtin_amdgcn_readfirstlane(threadIdx.x >> 6));
}
__global__ void text_end_marker() { asm volatile(".Lk3_end:"); }

extern "C" void kernel_launch(void* const* d_in, const int* in_sizes, int n_in, void* d_out, int out_size, void* d_ws, size_t ws_size, hipStream_t stream) {
    const u16* x = (const u16*)d_in[0]; const u16* wq = (const u16*)d_in[1]; const u16* wk = (const u16*)d_in[2]; const u16* wv = (const u16*)d_in[3];
    const u16* wo = (const u16*)d_in[4]; const u16* w_in = (const u16*)d_in[6]; const u16* w_sp = (const u16*)d_in[7]; const u16* b_sp = (const u16*)d_in[8];
    const u16* w_out = (const u16*)d_in[9]; char* ws = (char*)d_ws;
    (void)hipFuncSetAttribute((const void*)fused_kernel, hipFuncAttributeMaxDynamicSharedMemorySize, LDS_ALL);
    fused_kernel<<<256, 512, LDS_ALL, stream>>>(x, wq, wk, wv, w_in, w_sp, b_sp, w_out, wo, ws, (u16*)d_out);
}
```

```cpp
#include <hip/hip_runtime.h>
#include <hip/hip_bf16.h>
#include <stdint.h>

#define STNOP 1
typedef unsigned short u16;
typedef unsigned long long u64;
typedef short bf16x8 __attribute__((ext_vector_type(8)));
typedef short s16x4 __attribute__((ext_vector_type(4)));
typedef float f32x2 __attribute__((ext_vector_type(2)));
typedef float f32x4 __attribute__((ext_vector_type(4)));
typedef float f32x16 __attribute__((ext_vector_type(16)));
typedef unsigned u32x4 __attribute__((ext_vector_type(4)));
typedef unsigned u32x2 __attribute__((ext_vector_type(2)));
typedef __bf16 bf16x2_t __attribute__((ext_vector_type(2)));
#define LAS __attribute__((address_space(3)))
#define DI __device__ __forceinline__

constexpr int Z1L = 122880, TOPL = 159744, LDS_ALL = 163840;
constexpr int K2_SA = 16384, K2_SB = 18432, K2_SK = 6144, K2_B0 = 3 * K2_SA, K2_K0 = K2_B0 + 3 * K2_SB, K2_X0 = 147520, K2_LDS = K2_X0 + 9344 + 64 + 2048;
constexpr int S_ = 2048, D_ = 576, FH = 2304, F_ = 4608;

constexpr size_t WS_Q = 0;
constexpr size_t WS_KV = WS_Q + (size_t)S_ * D_ * 2;
constexpr size_t WS_Z1 = WS_KV + 64 * 3072;
constexpr size_t WS_Z2T = WS_Z1 + (size_t)S_ * FH * 2;
constexpr size_t WS_UO = WS_Z2T + (size_t)S_ * FH * 2;
constexpr size_t WS_ST = WS_UO + (size_t)S_ * 2880 * 2;
constexpr size_t WS_XCH = WS_ST + (size_t)S_ * 16 * 8;
constexpr size_t WS_XR = WS_XCH + 4096 + 16384;
constexpr size_t WS_FL = WS_XR + 4096;
constexpr size_t WS_FA = WS_FL + 16384;
constexpr size_t WS_FJ = WS_FA + 2048;
constexpr size_t WS_FP = WS_FJ + 8192;
constexpr size_t WS_W3T = WS_FP + 2048;
constexpr size_t WS_DBG = 128u << 20;
#define STAMP(kid, i)
#define STAMP_DRAIN()
#define STAMPT(tid, kid, i)

DI unsigned cvtpk(float lo, float hi) { f32x2 v = {lo, hi}; return __builtin_bit_cast(unsigned, __builtin_convertvector(v, bf16x2_t)); }
DI float bf2f(u16 b) { return __uint_as_float(((unsigned)b) << 16); }
DI float bflo(unsigned w) { return __uint_as_float(w << 16); }
DI float bfhi(unsigned w) { return __uint_as_float(w & 0xffff0000u); }
#define SB() __builtin_amdgcn_sched_barrier(0)
#define PIN(x) asm volatile("" : "+v"(x))
#define EX(x) __builtin_amdgcn_exp2f(x)
#define MFMA16(a, b, c) __builtin_amdgcn_mfma_f32_16x16x32_bf16(a, b, c, 0, 0, 0)
#define MFMA32(a, b, c) __builtin_amdgcn_mfma_f32_32x32x16_bf16(a, b, c, 0, 0, 0)

DI s16x4 trread(const LAS char* p) { return __builtin_bit_cast(s16x4, __builtin_amdgcn_ds_read_tr16_b64_v4i16((LAS s16x4*)p)); }
template <int N> DI void wait_vm_bar() { asm volatile("s_waitcnt vmcnt(%0) lgkmcnt(0)\n\ts_barrier" ::"n"(N) : "memory"); }
template <int N> DI void wait_vm() { asm volatile("s_waitcnt vmcnt(%0)" ::"n"(N) : "memory"); }
template <int OFF> DI void glds_s(unsigned voff, const char* sbase) { asm volatile("global_load_lds_dwordx4 %0, %1 offset:%2" ::"v"(voff), "s"(sbase), "n"(OFF)); }
template <int OFF> DI void glds_sc1(unsigned voff, const char* sbase) { asm volatile("global_load_lds_dwordx4 %0, %1 offset:%2 sc1" ::"v"(voff), "s"(sbase), "n"(OFF)); }
DI void glds_v(const char* p, unsigned m0v, u64 em) { asm volatile("s_mov_b32 m0, %1\n\ts_mov_b64 exec, %2\n\tglobal_load_lds_dwordx4 %0, off\n\ts_mov_b64 exec, -1" ::"v"(p), "s"(m0v), "s"(em)); }
DI unsigned ld_sc1_async(const char* sb, unsigned vo) { unsigned v; asm volatile("global_load_dword %0, %1, %2 sc1" : "=v"(v) : "v"(vo), "s"(sb) : "memory"); return v; }
#define WT
DI void st16(void* p, u32x4 v) { asm volatile("global_store_dwordx4 %0, %1, off sc1\n\ts_nop 1" :: "v"(p), "v"(v) : "memory"); }
DI void st8(void* p, u32x2 v) { asm volatile("global_store_dwordx2 %0, %1, off sc1" :: "v"(p), "v"(v) : "memory"); }
template <int OFF> DI void st16s(const char* sb, unsigned vo, u32x4 v) { asm volatile("global_store_dwordx4 %0, %1, %2 offset:%3 sc1\n\ts_nop %4" :: "v"(vo), "v"(v), "s"(sb), "n"(OFF), "n"(STNOP) : "memory"); }
template <int OFF> DI void st8s(const char* sb, unsigned vo, u32x2 v) { asm volatile("global_store_dwordx2 %0, %1, %2 offset:%3 sc1" :: "v"(vo), "v"(v), "s"(sb), "n"(OFF) : "memory"); }
DI float fadd1(float a, float b) { float r = a + b; asm("" : "+v"(r)); return r; }
DI float fmul1(float a, float b) { float r = a * b; asm("" : "+v"(r)); return r; }
DI float ffma1(float a, float b, float c) { float r = __builtin_fmaf(a, b, c); asm("" : "+v"(r)); return r; }
DI float lone(float a) { asm("" : "+v"(a)); return a; }
DI void set_m0(unsigned v) { asm volatile("s_mov_b32 m0, %0\n\ts_nop 0" ::"s"(v)); }
DI bf16x8 ldsA(const LAS char* p) { return *(const LAS bf16x8*)p; }
DI bf16x8 ldsB2(const LAS char* p0, const LAS char* p1) { s16x4 lo = trread(p0), hi = trread(p1); return __builtin_shufflevector(lo, hi, 0, 1, 2, 3, 4, 5, 6, 7); }
DI bf16x8 ldsB(const LAS char* p) { return ldsB2(p, p + 256); }

DI float gelu_t(float x) {
    const float c0 = -2.0f * 1.4426950408889634f * 0.7978845608028654f;
    const float c1 = c0 * 0.044715f;
    float x2 = x * x;
    float t = __builtin_fmaf(x2, c1, c0);
    float e = __builtin_amdgcn_exp2f(x * t);
    return x * __builtin_amdgcn_rcpf(1.0f + e);
}
constexpr float QSCALE = 0.23570226039551584f * 1.4426950408889634f;

#define KO1 (3 * (((idx & 7) + (idx >> 3)) % 3))
#define KO2 (((idx & 7) + 2 * (idx >> 3) + 4 * (xcd & 1)) % 9)
#define K2_KO (8 * (xcd >> 1))
#define EPRIO 1
#define PFW 0
#define SPRIO 1
#define HW 3
#define ZSCH 5
#define ZS0 1
#define TFA 8
#define PW2 7
#define PEARLY 0
#define POLLSLEEP 0
#define APRIO 1
#define K3MAP 2
#define K3ROT 0
#define APRE 0
#define AGSLOT 1
#define ZHALF 1
#define POLLW 0
#define POLLSLEEP1 1
#define PLSLOT 0
constexpr int K1_SA = 16384, K1_SB = 24576, K1_B0 = 3 * K1_SA, K1_TAB = K1_B0 + 3 * K1_SB, K1_X0 = K1_TAB + 10240, K1_LDS = K1_X0 + 16384 + 2048;
typedef __attribute__((address_space(1))) unsigned gu32;
typedef __attribute__((address_space(1))) unsigned long long gu64;
#define RLX_AGENT __ATOMIC_RELAXED, __HIP_MEMORY_SCOPE_AGENT
struct K1F { bf16x8 a[4], b[3], x; };
struct K1S {
    unsigned va[2], vb[2]; const char* sa; const char* sb;
    const char* gp; unsigned st;
    int k1, k2;
    unsigned m0a, m0b; u64 em;
    const LAS char* pa[2]; const LAS char* px[2]; const LAS char* pb[3];
};
DI void k1_issue(K1S& s, int i, int w) {
    if (i == 0) { set_m0(s.m0a + w * K1_SA); glds_s<0>(s.va[0], s.sa); }
    if (i == 1) { glds_s<1024>(s.va[1], s.sa); s.sa += s.k1 == 8 ? -8 * 128 : 128; asm volatile("" : "+s"(s.sa)); }
    if (i == 2) { glds_v(s.gp + (unsigned long long)s.st * (unsigned)s.k1, s.m0b + w * K1_SB + 2048, s.em); s.k1 = s.k1 == 8 ? 0 : s.k1 + 1; asm volatile("" : "+s"(s.k1)); }
    if (i == 3) { set_m0(s.m0b + w * K1_SB); glds_s<0>(s.vb[0], s.sb); }
    if (i == 4) { glds_s<1024>(s.vb[1], s.sb); s.sb += s.k2 == 8 ? -8 * 128 * F_ : 128 * F_; s.k2 = s.k2 == 8 ? 0 : s.k2 + 1; asm volatile("" : "+s"(s.sb), "+s"(s.k2)); }
}
DI f32x2 gelu_y2(f32x2 y, float k0, float k1, float ir) {
    f32x2 t = (y * y) * k1 + k0;
    f32x2 a = y * t;
    f32x2 e = {__builtin_amdgcn_exp2f(a[0]), __builtin_amdgcn_exp2f(a[1])};
    f32x2 d = e * ir + ir;
    f32x2 rc = {__builtin_amdgcn_rcpf(d[0]), __builtin_amdgcn_rcpf(d[1])};
    return y * rc;
}
DI float dpp_x1(float x) { return __int_as_float(__builtin_amdgcn_mov_dpp(__float_as_int(x), 0xB1, 0xf, 0xf, true)); }
DI float dpp_x2(float x) { return __int_as_float(__builtin_amdgcn_mov_dpp(__float_as_int(x), 0x4E, 0xf, 0xf, true)); }
DI void kv_store(const char* kv, int sv, int j, int c, int g, f32x4 v) {
    if (j == 4) { st8s<0>(kv + sv * 256, ((g >> 1) * 32 + c) * 16 + (g & 1) * 8, u32x2{cvtpk(v[0], v[1]), cvtpk(v[2], v[3])}); return; }
    const unsigned kk = cvtpk(v[2], v[3]);
    const bool o1 = c & 1, o2 = c & 2;
    float t0 = dpp_x1(o1 ? v[0] : v[1]), t1 = dpp_x1(o1 ? v[2] : v[3]);
    if (o1) { v[0] = t0; v[2] = t1; } else { v[1] = t0; v[3] = t1; }
    t0 = dpp_x2(o2 ? v[0] : v[2]); t1 = dpp_x2(o2 ? v[1] : v[3]);
    if (o2) { v[0] = t0; v[1] = t1; } else { v[2] = t0; v[3] = t1; }
    u32x2 w = {cvtpk(v[0], v[1]), cvtpk(v[2], v[3])};
    const int kq = c >> 2; unsigned off;
    if (j == 5) {
        off = g < 2 ? c * 16 + (g ^ 1) * 8 : 256 + ((kq & 1) * 8 + 4 * (g - 2) + (c & 3)) * 16 + (kq >> 1) * 8;
        if (g < 2) w = g ? u32x2{kk, 0x3f80u} : u32x2{0u, 0u};
    } else {
        off = 512 + ((kq & 1) * 16 + 4 * g + (c & 3)) * 16 + (kq >> 1) * 8;
        if (g == 3 && !o2) { const unsigned cv = o1 ? 0u : 0x3f803f80u; w = u32x2{cv, cv}; }
    }
    st8s<1024>(kv + sv * 1024, off, w);
}
template <int KS, int GL, int RS, int WS, bool RX> DI void k1_p1(K1S& s, const K1F& f, K1F& n, f32x4 (&acc)[4][2], f32x4& sacc, f32x4 (&cso)[2], f32x4& rs, f32x4& rq, const bf16x8& ones) {
    constexpr int AO = RS * K1_SA, BO = RS * K1_SB + KS * 12288;
#define MX(d, a_, b_) d = MFMA16(a_, b_, d); PIN(d); SB()
#define MM(m, nn) MX(acc[m][nn], f.b[nn], f.a[m])
    MM(0, 0); n.b[0] = ldsB(s.pb[0] + BO); SB();
    MM(1, 0); n.a[0] = ldsA(s.pa[KS] + AO); SB();
    MM(2, 0); n.a[1] = ldsA(s.pa[KS] + AO + 2048); SB();
    MM(3, 0); n.a[2] = ldsA(s.pa[KS] + AO + 4096); SB();
    MX(cso[0], f.b[0], ones); n.a[3] = ldsA(s.pa[KS] + AO + 6144); SB();
    MX(sacc, f.b[2], f.x); n.b[1] = ldsB(s.pb[1] + BO); SB();
    MX(cso[1], f.b[2], ones); n.b[2] = ldsB(s.pb[2] + BO); SB();
    MX(rs, ones, f.x); k1_issue(s, GL == 1 ? 0 : 3, WS); SB();
    MX(rq, f.x, f.x); if (RX) n.x = ldsA(s.px[KS] + AO); SB();
    MM(0, 1); k1_issue(s, GL == 1 ? 1 : 4, WS); SB();
    MM(1, 1); if (GL == 1) k1_issue(s, 2, WS); SB();
    MM(2, 1);
    MM(3, 1);
}
template <int KS, int GL, int RS, int WS, bool RD, class E> DI void k1_p2(K1S& s, const K1F& f, K1F& n, f32x4 (&acc)[4][3], f32x4 (&cso)[2], const bf16x8& ones, E epi) {
    constexpr int AO = RS * K1_SA, BO = RS * K1_SB + KS * 12288;
    MM(0, 0); if (RD) n.b[0] = ldsB(s.pb[0] + BO); epi(0); SB();
    MM(1, 0); if (RD) n.a[0] = ldsA(s.pa[KS] + AO); epi(1); SB();
    MM(2, 0); if (RD) n.a[1] = ldsA(s.pa[KS] + AO + 2048); epi(2); SB();
    MM(3, 0); if (RD) n.a[2] = ldsA(s.pa[KS] + AO + 4096); epi(3); SB();
    MX(cso[0], f.b[0], ones); if (RD) n.a[3] = ldsA(s.pa[KS] + AO + 6144); epi(4); SB();
    MM(0, 1); if (RD) n.b[1] = ldsB(s.pb[1] + BO); epi(5); SB();
    MM(1, 1); if (RD) n.b[2] = ldsB(s.pb[2] + BO); epi(6); SB();
    MM(2, 1); if (GL) k1_issue(s, GL == 1 ? 0 : 3, WS); epi(7); SB();
    MM(3, 1); epi(8); SB();
    MX(cso[1], f.b[1], ones); if (GL) k1_issue(s, GL == 1 ? 1 : 4, WS); epi(9); SB();
    MM(0, 2); epi(10); SB();
    MM(1, 2); if (GL == 1) k1_issue(s, 2, WS); epi(11); SB();
    MM(2, 2); epi(12); SB();
    MM(3, 2); epi(13); SB();
#undef MM
#undef MX
}

constexpr unsigned XSALT = 0x9e3779b9u;
DI void epoch_reply(char* ws, int lane, const unsigned (&nv)[4], u64 epoch) {
    const unsigned lo = (unsigned)epoch, hi = (unsigned)(epoch >> 32);
    st16s<0>(ws + WS_XR, lane * 16, u32x4{nv[0], lo, hi, nv[0] ^ lo ^ hi ^ XSALT}); st16s<1024>(ws + WS_XR, lane * 16, u32x4{nv[1], lo, hi, nv[1] ^ lo ^ hi ^ XSALT});
    st16s<2048>(ws + WS_XR, lane * 16, u32x4{nv[2], lo, hi, nv[2] ^ lo ^ hi ^ XSALT}); st16s<3072>(ws + WS_XR, lane * 16, u32x4{nv[3], lo, hi, nv[3] ^ lo ^ hi ^ XSALT});
}
#define XN_LOADS "global_load_dword %0, %4, %5 sc1\n\tglobal_load_dword %1, %4, %5 offset:256 sc1\n\tglobal_load_dword %2, %4, %5 offset:512 sc1\n\tglobal_load_dword %3, %4, %5 offset:768 sc1\n"
DI void epoch_service(char* ws, int lane, u64 epoch) {
    unsigned nv[4];
    asm volatile(XN_LOADS "\ts_waitcnt vmcnt(0)" : "=&v"(nv[0]), "=&v"(nv[1]), "=&v"(nv[2]), "=&v"(nv[3]) : "v"(lane * 4), "s"(ws + WS_XCH) : "memory");
    epoch_reply(ws, lane, nv, epoch);
}
DI unsigned g2_va(int wid, int lane, int i) { int row = 8 * (wid * 4 + i) + (lane >> 3), c = (lane & 7) ^ ((row >> 1) & 7); return row * (S_ * 2) + c * 16 + 4096 - i * 1024; }
DI u64 phase1(const u16* __restrict__ x, const u16* __restrict__ wq, const u16* __restrict__ wk, const u16* __restrict__ wv,
                                                    const u16* __restrict__ w_in, const u16* __restrict__ w_sp, const u16* __restrict__ b_sp, const u16* __restrict__ w_out, const u16* __restrict__ wo, char* __restrict__ ws) {
    extern __shared__ __attribute__((aligned(16))) char smem[];
    const int bid = blockIdx.x, xcd = bid & 7, idx = bid >> 3;
    const int pm = (xcd & 1) * 8 + (idx & 7), j = (xcd >> 1) * 4 + (idx >> 3);
    const int lane = threadIdx.x & 63, wid = __builtin_amdgcn_readfirstlane(threadIdx.x >> 6), wr = wid >> 2, wc = wid & 3;
    const unsigned lds0 = (unsigned)(uintptr_t)smem;
    const LAS char* lds = (const LAS char*)smem;
    {
        const char* pc; unsigned len;
        asm volatile("s_getpc_b64 %0\n.Lk1_here:\n\ts_mov_b32 %1, .Lk3_end-.Lk1_here" : "=s"(pc), "=s"(len));
        unsigned off = ((idx * 8 + wid) * 2 + (lane & 1)) * 128; off = off < len ? off : len;
        asm volatile("s_mov_b32 m0, %2\n\ts_nop 0\n\tglobal_load_lds_dword %0, %1" ::"v"(off), "s"(pc), "s"(lds0 + K1_TAB + 9216 + 512));
    }
    K1S s; s.em = (wid & 1) ? 0xccccccccull : 0x33333333ull;
    s.m0a = lds0 + wid * 2048; s.m0b = lds0 + K1_B0 + wid * 3072;
    const int brow = (lane >> 2) & 7, bc = (lane & 3) ^ (2 * (wid & 1)), bk = 8 * wid + brow;
    const int ko2 = KO2, ko1 = KO1;
    unsigned oa = pm * 128 * D_ * 2, ob = 256 * j, oa1 = oa + ko1 * 128, ob1 = ob + FH * 2 + ko1 * 128 * F_, vg = bk * (F_ * 2) + 16 * (bc & 1);
    {
        int r8 = lane >> 3;
        s.va[0] = (16 * wid + r8) * (D_ * 2) + (((lane & 7) ^ (r8 >> 1)) * 16);
        s.va[1] = (16 * wid + 8 + r8) * (D_ * 2) + (((lane & 7) ^ (4 + (r8 >> 1))) * 16) - 1024;
        s.vb[0] = bk * (F_ * 2) + (lane >> 5) * 64 + bc * 16 + 1024; s.vb[1] = s.vb[0] + 128 - 1024;
        s.k1 = s.k2 = ko1; s.st = 128 * F_;
        PIN(s.va[0]); PIN(s.va[1]); PIN(s.vb[0]); PIN(s.vb[1]); PIN(vg);
        asm volatile("" : "+s"(oa1), "+s"(ob1), "+s"(s.m0a), "+s"(s.m0b));
    }
    SB();
    s.sa = (const char*)x + oa1; s.sb = (const char*)w_in - 1024 + ob1;
    k1_issue(s, 0, 0); k1_issue(s, 1, 0); k1_issue(s, 3, 0); k1_issue(s, 4, 0);
    SB();
    STAMP(0, 0);
    s.gp = (const char*)w_in + (4096 + 32 * j + FH * 2) + vg;
    k1_issue(s, 2, 0);
    const char* sa0 = (const char*)x + oa;
    const char* sb0 = (const char*)w_in - 1024 + ob;
    gu32* XN = (gu32*)(ws + WS_XCH) + pm * 16;
    gu32* XE = (gu32*)(ws + WS_XCH + 4096) + pm * 256;
    gu64* XP = (gu64*)(ws + WS_ST) + (size_t)pm * 16 * 128;
    const char* xe_s = ws + WS_XCH + 4096 + (pm * 256 + j * 16) * 4;
    const unsigned le = (lane & 16) ? 16384 - (pm * 16 + j) * 48 + (lane & 3) * 4 : (lane & 15) * 4;
    unsigned eo = ld_sc1_async(xe_s, le);
    if (SPRIO == 1 && wid >= 4) __builtin_amdgcn_s_setprio(1);
    SB();
    unsigned nonce = 0;
    const char* gp2; unsigned st2;
    {
        const u16* p; int rs;
        const u16* sp0 = j < 4 ? wq + 512 + 16 * j : (j == 5 ? wk + 10 : (j == 6 ? wv + 8 : wk));
        const u16* sp1 = j < 4 ? wq + 520 + 16 * j : (j == 5 ? wv : (j == 6 ? wv + 10 : wk + 8));
        const int srs = j < 4 ? D_ : 18;
        if (lane >= 32) { p = wq + 32 * j + 8 * bc; rs = D_; }
        else if (bc < 2) { p = w_in + 2048 + 16 * j + 8 * bc; rs = F_; }
        else { p = bc == 2 ? sp0 : sp1; rs = srs; }
        gp2 = (const char*)(p + (size_t)bk * rs); st2 = 128 * rs; PIN(gp2); PIN(st2);
    }
    const int r16 = lane & 15, g = lane >> 4, cg = wc ^ (2 * wr);
    const int nf3 = cg == 3 ? 8 : (cg == 2 ? 9 : 10 + cg);
#define NF1(n) ((n) == 2 ? 8 : 2 * wc + ((n) ^ wr))
#define NF2(n) ((n) == 0 ? nf3 : 2 * cg + (((n) - 1) ^ wr))
    const LAS char* pb2[3];
    {
        int q = r16 >> 2, p = lane & 3;
#pragma unroll
        for (int ks = 0; ks < 2; ++ks) { s.pa[ks] = lds + (r16 * 128 + (((4 * ks + g) ^ (r16 >> 1)) * 16) + wr * 8192); s.px[ks] = s.pa[ks] + wc * 2048; PIN(s.pa[ks]); PIN(s.px[ks]); }
#pragma unroll
        for (int n = 0; n < 3; ++n) {
            int nf = NF1(n); s.pb[n] = lds + (K1_B0 + g * 3072 + q * 64 + 8 * p + (nf >> 1) * 512 + (((nf ^ g) & 1) * 32)); PIN(s.pb[n]);
            nf = NF2(n); pb2[n] = lds + (K1_B0 + g * 3072 + q * 64 + 8 * p + (nf >> 1) * 512 + (((nf ^ g) & 1) * 32)); PIN(pb2[n]);
        }
    }
    f32x4 acc1[4][2] = {}; f32x4 sacc = {}; f32x4 cso[2] = {};
    f32x4 rs = {}, rq = {};
#pragma unroll
    for (int m = 0; m < 4; ++m) { PIN(acc1[m][0]); PIN(acc1[m][1]); }
    PIN(sacc); PIN(cso[0]); PIN(cso[1]); PIN(rs); PIN(rq);
    const u32x4 onesv = {0x3f803f80u, 0x3f803f80u, 0x3f803f80u, 0x3f803f80u}; bf16x8 ones = __builtin_bit_cast(bf16x8, onesv); PIN(ones);
    K1F f0, f1;
    SB();
#pragma unroll
    for (int i = 0; i < 5; ++i) k1_issue(s, i, 1);
    SB();
    if (SPRIO == 2 && wid >= 4) __builtin_amdgcn_s_setprio(1);
    const int w3p = bid < 240 ? bid : 239;
    unsigned w3vo[2], w3so[2]; const char* w3sp; const LAS char* w3rd[2];
    {
        const int jj = w3p / 30, pc = w3p - jj * 30;
        w3sp = (pc < 24 ? (const char*)w_out : (const char*)wo - 2304 * 1152) + jj * 144;
#pragma unroll
        for (int i = 0; i < 2; ++i) {
            unsigned c_ = threadIdx.x + 512 * i; c_ = c_ < 864 ? c_ : 863;
            const unsigned row = (c_ * 7282) >> 16, k_ = pc * 96 + row, jg = (k_ * 29128) >> 22, l_ = k_ - jg * 144;
            const unsigned src = pc < 24 ? (l_ < 128 ? 128 * jg + l_ : 1920 + 16 * jg + l_) : k_;
            w3vo[i] = src * 1152 + (c_ - row * 9) * 16; PIN(w3vo[i]);
            const int u = i ? (wid < 7 ? wid + 8 : 14) : wid, k = u / 5, n = u - 5 * k;
            w3rd[i] = lds + (K1_X0 + (8 * g + (r16 >> 2)) * 144 + 8 * (lane & 3) + k * 4608 + n * 32); PIN(w3rd[i]);
            w3so[i] = w3p * 13824 + k * 4608 + n * 1024 + (n < 4 ? lane : g * 8 + r16) * 16;
            if (n == 4 && r16 >= 8) w3so[i] = 240 * 13824 + bid * 512 + (g * 8 + r16 - 8) * 16;
            PIN(w3so[i]);
        }
        asm volatile("" : "+s"(w3sp));
    }
    unsigned wspo = (pm * 128 + (idx >> 3) * 32 + wid * 4 + g) * (S_ * 2) + r16 * 32; PIN(wspo);
    LAS float* redp = (LAS float*)(lds + K1_TAB + 4096) + ((wr * 64 + r16) * 5 + wc) * 2 + (g & 1); PIN(redp);
    LAS float* reds = (LAS float*)(lds + K1_TAB + 4096) + ((wr * 64 + wc * 16 + r16) * 5 + 4) * 2 + (g & 1); PIN(reds);
    SB();
#pragma unroll
    for (int i = 0; i < 3; ++i) k1_issue(s, i, 2);
    __builtin_amdgcn_s_setprio(0);
    SB();
    wait_vm_bar<8>();
    SB();
    f0.b[0] = ldsB(s.pb[0]);
    f0.a[0] = ldsA(s.pa[0]); f0.a[1] = ldsA(s.pa[0] + 2048); f0.a[2] = ldsA(s.pa[0] + 4096); f0.a[3] = ldsA(s.pa[0] + 6144);
    f0.b[2] = ldsB(s.pb[2]); f0.x = ldsA(s.px[0]); f0.b[1] = ldsB(s.pb[1]);
    SB();
    PIN(eo);
    if (wid == HW) {
        nonce = 1;
        while (__ballot(eo == nonce) != 0ull) ++nonce;
        if (lane == 0) __hip_atomic_store(XN + j, nonce, RLX_AGENT);
        __hip_atomic_store(XP + j * 128 + lane, ~0ull, RLX_AGENT); __hip_atomic_store(XP + j * 128 + 64 + lane, ~0ull, RLX_AGENT);
    }
    STAMP(0, 1);
#define P1_A(c) k1_p1<1, 2, (c), ((c) + 2) % 3, true>(s, f0, f1, acc1, sacc, cso, rs, rq, ones); wait_vm_bar<5>(); SB()
#define P1_B(c, RX) k1_p1<0, 1, ((c) + 1) % 3, (c), RX>(s, f1, f0, acc1, sacc, cso, rs, rq, ones)
#pragma unroll 1
    for (int it = 0; it < 2; ++it) { P1_A(0); P1_B(0, true); P1_A(1); P1_B(1, true); P1_A(2); P1_B(2, true); }
    const char* xn_s = ws + WS_XCH + pm * 64; const unsigned l4 = (lane & 15) * 4;
    P1_A(0);
    unsigned echoed = ld_sc1_async(xn_s, l4);
    unsigned alln[4];
    asm volatile("s_cmp_lg_u32 %6, 0\n\ts_cbranch_scc1 .Lld%=\n\t" XN_LOADS ".Lld%=:" : "=&v"(alln[0]), "=&v"(alln[1]), "=&v"(alln[2]), "=&v"(alln[3])
                 : "v"(lane * 4), "s"(ws + WS_XCH), "s"(bid) : "memory", "scc");
    s.k1 = s.k2 = ko2; s.sa = sa0 + ko2 * 128; s.sb = sb0 + (size_t)ko2 * 128 * F_; s.gp = gp2; s.st = st2; s.em = ~0ull; SB();
    P1_B(0, true);
    P1_A(1);
    PIN(echoed);
    if (wid == HW && lane < 16) __hip_atomic_store(XE + lane * 16 + j, echoed, RLX_AGENT);
    PIN(alln[0]); PIN(alln[1]); PIN(alln[2]); PIN(alln[3]);
    if (bid == 0 && wid == HW - 1) epoch_reply(ws, lane, alln, __builtin_amdgcn_s_memrealtime() | (1ull << 63));
    SB();
    P1_B(1, true);
    P1_A(2);
    s.pb[0] = pb2[0]; s.pb[1] = pb2[1]; s.pb[2] = pb2[2];
    P1_B(2, false);
    STAMP(3, 0);
    float* CS = (float*)(smem + K1_TAB);
    f32x4* RS = (f32x4*)(smem + K1_TAB + 2048);
    float* red = (float*)(smem + K1_TAB + 4096);
    f32x2* ZS = (f32x2*)(smem + K1_TAB + 9216);
    volatile LAS unsigned* HS = (volatile LAS unsigned*)(lds + K1_TAB + 1792);
    const float c0 = -2.0f * 1.4426950408889634f * 0.7978845608028654f, c1 = c0 * 0.044715f;
    f32x4 acc2[4][3] = {}; f32x4 cs2[2] = {};
    auto none = [](int) {};
    auto tabs = [&](int st) {
        if (st == 2) { if (r16 == 0) { *(f32x4*)(CS + NF1(0) * 16 + 4 * g) = cso[0]; *(f32x4*)(CS + 128 + 4 * g) = cso[1]; } }
        if (st == 3) {
            float sq = (r16 & 2) ? ((r16 & 1) ? rq[3] : rq[2]) : ((r16 & 1) ? rq[1] : rq[0]);
            float r0_ = rs[0]; PIN(r0_); PIN(sq);
            float mean = fmul1(r0_, 1.0f / D_), var = fmaxf(ffma1(-mean, mean, fmul1(sq, 1.0f / D_)), 0.f);
            rs[0] = mean; rs[1] = var + 1e-5f; PIN(rs);
        }
        if (st == 5) { rs[2] = __builtin_amdgcn_rsqf(rs[1]); PIN(rs); }
        if (st == 7) { float rstd = rs[2]; if (g == (r16 >> 2)) RS[wr * 64 + wc * 16 + r16] = f32x4{rs[0], fmul1(rstd, c0), fmul1(fmul1(rstd, rstd), fmul1(rstd, c1)), fmul1(rs[1], rstd)}; }
    };
    f32x4 er[5], cs1[3], e[3];
    f32x2 su[5], sq[5];
#define ZU(u) ((u) == 8 ? sacc : acc1[((u) >> 1) & 3][(u) & 1])
#define UM(u) ((u) == 8 ? 4 : (u) >> 1)
#define UN(u) ((u) == 8 ? 2 : (u) & 1)
#define PRE(i, u) { f32x4 y = ZU(u) - cs1[UN(u)] * er[UM(u)][0]; e[i] = ((y * y) * er[UM(u)][2] + er[UM(u)][1]) * y; ZU(u) = y; PIN(ZU(u)); PIN(e[i]); }
#define FIN(i, u, first) { ZU(u) *= e[i]; PIN(ZU(u)); f32x4 v = ZU(u); f32x2 lo = {v[0], v[1]}, hi = {v[2], v[3]}; \
        if (first) { su[UM(u)] = lo + hi; sq[UM(u)] = lo * lo + hi * hi; } else { su[UM(u)] += lo; su[UM(u)] += hi; sq[UM(u)] += lo * lo; sq[UM(u)] += hi * hi; } PIN(su[UM(u)]); PIN(sq[UM(u)]); }
#define RED(m) { float a_ = su[m][0] + su[m][1], b_ = sq[m][0] + sq[m][1]; \
            auto x1 = __builtin_amdgcn_permlane16_swap(__float_as_uint(a_), __float_as_uint(b_), false, false);       \
            float t_ = __uint_as_float(x1[0]) + __uint_as_float(x1[1]);                                               \
            auto x2 = __builtin_amdgcn_permlane32_swap(__float_as_uint(t_), __float_as_uint(t_), false, false); \
            t_ = __uint_as_float(x2[0]) + __uint_as_float(x2[1]);                                                     \
            if ((m) < 4) redp[(m) * 160] = t_; else reds[0] = t_; }
#define T1(op, v) asm volatile(op " %0, %0" : "+v"(v))
#define DE1(v, sd) asm volatile("v_fma_f32 %0, %0, %1, %1" : "+v"(v) : "v"(sd))
#define EPI_E(u0, u1, u2) [&](int st) { \
        if (st < 12) { float t_ = e[st >> 2][st & 3]; T1("v_exp_f32", t_); e[st >> 2][st & 3] = t_; } \
        if (st >= 2) { const int v_ = st - 2; float t_ = e[v_ >> 2][v_ & 3]; DE1(t_, er[v_ < 4 ? UM(u0) : (v_ < 8 ? UM(u1) : UM(u2))][3]); e[v_ >> 2][v_ & 3] = t_; } }
#define EPI_R(m0, m1) [&](int st) { if (st < 12) { float t_ = e[st >> 2][st & 3]; T1("v_rcp_f32", t_); e[st >> 2][st & 3] = t_; } \
        if (st == 12 && (m0) >= 0) er[(m0) & 3] = RS[wr * 64 + (m0) * 16 + r16]; if (st == 13 && (m1) >= 0) er[(m1) & 3] = RS[wr * 64 + (m1) * 16 + r16]; }
#define VSYNC() SB(); asm volatile("s_waitcnt lgkmcnt(0)\n\ts_barrier" ::: "memory"); SB()
#define P2_A(c, GL, E) k1_p2<1, GL ? 2 : 0, (c), ((c) + 2) % 3, true>(s, f0, f1, acc2, cs2, ones, E)
#define P2_B(c, GL, RD, E) k1_p2<0, GL ? 1 : 0, ((c) + 1) % 3, (c), RD>(s, f1, f0, acc2, cs2, ones, E)
    P2_A(0, true, tabs); wait_vm_bar<5>(); SB();
    er[0] = RS[wr * 64 + r16]; er[4] = RS[wr * 64 + wc * 16 + r16];
#pragma unroll
    for (int n = 0; n < 3; ++n) cs1[n] = *(const f32x4*)(CS + NF1(n) * 16 + 4 * g);
    PRE(0, 0) PRE(1, 1) PRE(2, 8) VSYNC();
    P2_B(0, true, true, EPI_E(0, 1, 8));
    P2_A(1, true, EPI_R(1, 2)); wait_vm_bar<5>(); SB();
    unsigned ev = ld_sc1_async(xe_s, le);
    auto w3ld = [&](int i) { SB(); set_m0(lds0 + K1_X0 + i * 8192 + wid * 1024); glds_s<0>(w3vo[i], w3sp); SB(); };
    w3ld(0); FIN(0, 0, true) FIN(1, 1, false) FIN(2, 8, true) PRE(0, 2) PRE(1, 3) PRE(2, 4) w3ld(1); RED(0) RED(4) VSYNC();
    P2_B(1, true, true, EPI_E(2, 3, 4));
    P2_A(2, true, EPI_R(3, -1)); wait_vm_bar<7>(); SB();
    PIN(ev);
    bool hs = false; u64 epoch = 0;
    auto xcheck = [&]() {
        hs = hs || __all((lane & 16) || ev == nonce);
        const unsigned w0 = __builtin_amdgcn_readlane(ev, 16), w1 = __builtin_amdgcn_readlane(ev, 17), w2 = __builtin_amdgcn_readlane(ev, 18), w3 = __builtin_amdgcn_readlane(ev, 19);
        if (w0 == nonce && w3 == (w0 ^ w1 ^ w2 ^ XSALT)) epoch = ((u64)w2 << 32) | w1;
    };
    unsigned nn = ld_sc1_async(xn_s, l4);
    asm volatile("s_mov_b32 m0, %2\n\ts_nop 0\n\tglobal_load_lds_dword %0, %1" ::"v"(wspo), "s"(w_sp), "s"(lds0 + K1_X0 + 16384));
    SB(); FIN(0, 2, true) FIN(1, 3, false) FIN(2, 4, true) PRE(0, 5) PRE(1, 6) PRE(2, 7) SB();
    asm volatile("s_mov_b32 m0, %2\n\ts_nop 0\n\tglobal_load_lds_dword %0, %1" ::"v"(lane * 4), "s"(b_sp + pm * 128), "s"(lds0 + TOPL + 2304));
    SB(); RED(1) VSYNC();
    P2_B(2, true, true, EPI_E(5, 6, 7));
    P2_A(0, true, EPI_R(-1, -1)); wait_vm_bar<8>(); SB();
    {
        const bf16x8 w0 = ldsB2(w3rd[0], w3rd[0] + 576), w1 = ldsB2(w3rd[1], w3rd[1] + 576);
        SB(); FIN(0, 5, false) FIN(1, 6, true) FIN(2, 7, false) SB();
        st16s<0>(ws + WS_W3T, w3so[0], __builtin_bit_cast(u32x4, w0));
        SB(); RED(2) RED(3) SB();
        st16s<0>(ws + WS_W3T, w3so[1], __builtin_bit_cast(u32x4, w1));
        VSYNC();
    }
    f32x2 pu[5];
    auto pub = [&](int st) {
        if (st == 0) {
#pragma unroll
            for (int i = 0; i < 5; ++i) pu[i] = *(const f32x2*)(red + (wid * 16 + r16) * 10 + 2 * i);
        }
        if (st == 4) {
            float a = fadd1(fadd1(fadd1(lone(pu[0][0]), pu[1][0]), fadd1(lone(pu[2][0]), pu[3][0])), pu[4][0]), b = fadd1(fadd1(fadd1(pu[0][1], pu[1][1]), fadd1(pu[2][1], pu[3][1])), pu[4][1]);
            unsigned bb = __float_as_uint(b); if (bb == 0xffffffffu) bb = 0x7fc00000u;
            st8s<0>((const char*)(XP + j * 128 + wid * 16), r16 * 8, u32x2{__float_as_uint(a), bb});
        }
        if (st == 13 && wid == HW) { xcheck(); *HS = hs; }
    };
    P2_B(0, true, true, pub);
    P2_A(1, true, none);
    STAMP(3, 1);
    wait_vm_bar<8>(); SB();
    ev = ld_sc1_async(xe_s, le);
    auto reecho = [&](int st) { if (st == 2) { PIN(nn); if (wid == HW && nn != echoed) { echoed = nn; if (lane < 16) __hip_atomic_store(XE + lane * 16 + j, nn, RLX_AGENT); } } };
    P2_B(1, true, true, reecho);
    const char* xp_s = ws + WS_ST + (size_t)pm * 16384 + wid * 128; const unsigned xp_v = g * 4096 + r16 * 8;
    u32x2 pv[4];
#define PLOAD() asm volatile("global_load_dwordx2 %0, %4, %5 sc1\n\tglobal_load_dwordx2 %1, %4, %5 offset:1024 sc1\n\tglobal_load_dwordx2 %2, %4, %5 offset:2048 sc1\n\tglobal_load_dwordx2 %3, %4, %5 offset:3072 sc1" \
        : "=&v"(pv[0]), "=&v"(pv[1]), "=&v"(pv[2]), "=&v"(pv[3]) : "v"(xp_v), "s"(xp_s) : "memory")
    auto pload = [&](int st) { if (st >= 10) asm volatile("global_load_dwordx2 %0, %1, %2 offset:%3 sc1" : "=&v"(pv[(st - 10) & 3]) : "v"(xp_v), "s"(xp_s), "n"(((st - 10) & 3) * 1024) : "memory"); };
    P2_A(2, true, pload); wait_vm_bar<9>(); SB();
#define PEMPTY() __any(pv[0][1] == 0xffffffffu || pv[1][1] == 0xffffffffu || pv[2][1] == 0xffffffffu || pv[3][1] == 0xffffffffu)
    const unsigned hsf = *HS;
    P2_B(2, true, true, [&](int st) { if (st == 13) { PIN(ev); if (wid == HW) xcheck(); } });
    float za, zb;
    auto zstat = [&](int st) {
        if (st == ZS0) {
            wait_vm<3>();
            PIN(pv[0]); PIN(pv[1]); PIN(pv[2]); PIN(pv[3]);
            if (__builtin_expect(hsf != 1 || PEMPTY(), 0)) {
                const unsigned long long t0 = __builtin_amdgcn_s_memrealtime();
                if (wid == HW && hsf != 1) {
                    do {
                        ev = ld_sc1_async(xe_s, le); wait_vm<0>(); PIN(ev);
                        xcheck();
                        nn = __hip_atomic_load(XN + (lane & 15), RLX_AGENT);
                        if (nn != echoed) { echoed = nn; if (lane < 16) __hip_atomic_store(XE + lane * 16 + j, nn, RLX_AGENT); }
                    } while (!hs && __builtin_amdgcn_s_memrealtime() - t0 < 5000000ull);
                    *HS = 2;
                }
                while (*HS == 0 && __builtin_amdgcn_s_memrealtime() - t0 < 6000000ull) __builtin_amdgcn_s_sleep(2);
                do {
                    PLOAD(); wait_vm<0>(); PIN(pv[0]); PIN(pv[1]); PIN(pv[2]); PIN(pv[3]);
                } while (PEMPTY() && __builtin_amdgcn_s_memrealtime() - t0 < 6000000ull);
            }
        }
#define UF(x) __uint_as_float(x)
        if (st == ZS0 + 2) {
            za = fadd1(fadd1(lone(UF(pv[0][0])), UF(pv[1][0])), fadd1(lone(UF(pv[2][0])), UF(pv[3][0]))); PIN(za);
            zb = fadd1(fadd1(UF(pv[0][1]), UF(pv[1][1])), fadd1(UF(pv[2][1]), UF(pv[3][1]))); PIN(zb);
        }
        if (st == ZS0 + 4) {
            auto a1 = __builtin_amdgcn_permlane16_swap(__float_as_uint(za), __float_as_uint(za), false, false); za = fadd1(UF(a1[0]), UF(a1[1])); PIN(za);
            auto b1 = __builtin_amdgcn_permlane16_swap(__float_as_uint(zb), __float_as_uint(zb), false, false); zb = fadd1(UF(b1[0]), UF(b1[1])); PIN(zb);
        }
        if (st == ZS0 + 6) {
            auto a2 = __builtin_amdgcn_permlane32_swap(__float_as_uint(za), __float_as_uint(za), false, false); za = fadd1(UF(a2[0]), UF(a2[1])); PIN(za);
            auto b2 = __builtin_amdgcn_permlane32_swap(__float_as_uint(zb), __float_as_uint(zb), false, false); zb = fadd1(UF(b2[0]), UF(b2[1])); PIN(zb);
        }
        if (st == ZS0 + 8) {
            float m = fmul1(za, 1.0f / FH), var = fmaxf(ffma1(-m, m, fmul1(zb, 1.0f / FH)), 0.f), r = __builtin_amdgcn_rsqf(var + 1e-5f); PIN(r);
            ZS[wid * 16 + r16] = f32x2{r, fmul1(-m, r)};
        }
    };
    P2_A(0, true, zstat); wait_vm_bar<5>(); SB();
    auto merge = [&](u32x2 A, u32x2 B) {
        auto r0 = __builtin_amdgcn_permlane16_swap(A[0], B[0], false, false); auto r1 = __builtin_amdgcn_permlane16_swap(A[1], B[1], false, false);
        return u32x4{r0[0], r1[0], r0[1], r1[1]};
    };
    auto pk4 = [&](f32x4 v) { return u32x2{cvtpk(v[0], v[1]), cvtpk(v[2], v[3])}; };
    const char* z2b = ws + WS_Z2T + (size_t)(j * 32 + 2 * pm + wr) * 18432;
    f32x2 zs[5]; u32x2 za2, zb2; unsigned vs, v2;
    auto zn = [&](int q) {
        if (q == 0) {
#pragma unroll
            for (int m = 0; m < 5; ++m) zs[m] = ZS[wr * 64 + (m < 4 ? m : wc) * 16 + r16];
        }
        if (q == 1) { vs = (r16 >> 3) * 2304 + (((r16 & 7) ^ (4 * (r16 >> 3))) * 32); PIN(vs); }
        if (q == 2) { v2 = vs + (g & 1) * 4608 + (g >> 1) * 16; vs += g * 8; PIN(v2); PIN(vs); }
#define NRM(v, m, e) { v[e] = ffma1(v[e], zs[m][0], zs[m][1]); v[(e) + 1] = ffma1(v[(e) + 1], zs[m][0], zs[m][1]); PIN(v); }
        if (q >= 3 && q < 3 + 4 * ZSCH) {
            const int p = (q - 3) / ZSCH, t = (q - 3) % ZSCH, n = p >> 1, m = (p & 1) * 2;
            f32x4& A = acc1[m][n]; f32x4& B = acc1[m + 1][n];
            if (ZSCH == 5) {
                if (t == 0) NRM(A, m, 0)
                if (t == 1) { NRM(A, m, 2) za2[0] = cvtpk(A[0], A[1]); PIN(za2); }
                if (t == 2) { NRM(B, m + 1, 0) za2[1] = cvtpk(A[2], A[3]); PIN(za2); }
                if (t == 3) { NRM(B, m + 1, 2) zb2[0] = cvtpk(B[0], B[1]); PIN(zb2); }
                if (t == 4) { zb2[1] = cvtpk(B[2], B[3]); st16s<0>(z2b + NF1(n) * 256 + m * 4608, v2, merge(za2, zb2)); }
            } else {
                if (t == 0) { NRM(A, m, 0) NRM(A, m, 2) }
                if (t == 1) { NRM(B, m + 1, 0) NRM(B, m + 1, 2) }
                if (t == 2) st16s<0>(z2b + NF1(n) * 256 + m * 4608, v2, merge(pk4(A), pk4(B)));
            }
        }
        if (q == 3 + 4 * ZSCH) { NRM(sacc, 4, 0) NRM(sacc, 4, 2) }
        if (q == 4 + 4 * ZSCH) st8s<2048>(z2b + wc * 4608, vs, pk4(sacc));
    };
    P2_B(0, false, true, zn);
    P2_A(1, false, [&](int st) { zn(14 + st); }); wait_vm_bar<5>(); SB();
    P2_B(1, false, true, none);
    P2_A(2, false, none);
    P2_B(2, false, false, none);
    if (r16 == 0) { *(f32x4*)(CS + 192 + NF2(0) * 16 + 4 * g) = cs2[0]; *(f32x4*)(CS + 192 + NF2(1) * 16 + 4 * g) = cs2[1]; }
    f32x4* HX = (f32x4*)(smem + K1_B0) + wr * 256 + lane;
    if (cg == 3) { HX[0] = acc2[0][0]; HX[64] = acc2[1][0]; HX[128] = acc2[2][0]; HX[192] = acc2[3][0]; }
    asm volatile("s_waitcnt lgkmcnt(0)\n\ts_barrier" ::: "memory");
    STAMP(3, 2);
    auto gel = [&](f32x4 a, f32x4 c, f32x4 rr) {
        f32x4 y = a - c * rr[0];
        f32x2 y0 = gelu_y2(f32x2{y[0], y[1]}, rr[1], rr[2], rr[3]), y1 = gelu_y2(f32x2{y[2], y[3]}, rr[1], rr[2], rr[3]);
        return u32x2{cvtpk(y0[0], y0[1]), cvtpk(y1[0], y1[1])};
    };
#pragma unroll
    for (int n = 0; n < 3; ++n) cs1[n] = *(const f32x4*)(CS + 192 + NF2(n) * 16 + 4 * g);
#pragma unroll
    for (int m = 0; m < 4; ++m) er[m] = RS[wr * 64 + m * 16 + r16];
    if (cg == 3) {
    } else if (nf3 >= 10 || j < 4) {
        const char* qb = ws + WS_Q + (pm * 16 + wr * 8) * 9216 + (nf3 >= 10 ? 4 * j + (nf3 - 10) * 2 : 64 + 2 * j) * 128;
        const unsigned vq = ((g & 1) * 2 + (r16 >> 3)) * 9216 + (g >> 1) * 128 + (r16 & 7) * 16;
#define QV(m) pk4((acc2[m][0] - cs1[0] * er[m][0]) * (er[m][1] * (QSCALE / c0)))
        st16s<0>(qb, vq, merge(QV(0), QV(1))); st16s<0>(qb + 4 * 9216, vq, merge(QV(2), QV(3)));
    } else if (j < 7) {
#pragma unroll
        for (int m = 0; m < 4; ++m)
            kv_store(ws + WS_KV + (size_t)(pm * 4 + wr * 2 + (m >> 1)) * 3072, m & 1, j, r16, g, (acc2[m][0] - cs1[0] * er[m][0]) * (er[m][1] * (1.0f / c0)));
    }
    SB();
    f32x4 h8 = HX[wc * 64], c8 = *(const f32x4*)(CS + 192 + 128 + 4 * g); er[4] = RS[wr * 64 + wc * 16 + r16];
    u32x4 zw[4];
#pragma unroll
    for (int m = 0; m < ZHALF; ++m) { zw[m] = merge(gel(acc2[m][1], cs1[1], er[m]), gel(acc2[m][2], cs1[2], er[m])); PIN(zw[m]); }
    volatile LAS u64* EW = (volatile LAS u64*)(lds + TOPL + 8);
    if (wid == HW) {
        if (__builtin_expect(epoch == 0, 0)) {
            const u64 t0 = __builtin_amdgcn_s_memrealtime();
            do { ev = ld_sc1_async(xe_s, le); wait_vm<0>(); PIN(ev); xcheck(); } while (epoch == 0 && __builtin_amdgcn_s_memrealtime() - t0 < 5000000ull);
        }
        *EW = epoch; EW[-1] = 0; EW[1] = 0;
    }
    SB();
    wait_vm_bar<0>();
    STAMP(0, 3);
    if (wid == HW) {
        const bool kvp = j >= 4 && j < 7;
        const unsigned fo = lane == 0 ? (unsigned)(WS_FJ - WS_FA) + (j * 64 + pm) * 8 : (lane == 1 ? (unsigned)(WS_FP - WS_FA) + (pm * 16 + j) * 8 : (lane == 2 ? bid * 8
                            : (unsigned)(WS_FJ - WS_FA) + ((lane & 15) * 64 + 16 + pm * 3 + (j - 4)) * 8));
        if (lane < 3 || (kvp && g == 1)) __hip_atomic_store((gu64*)(ws + WS_FA + fo), epoch, RLX_AGENT);
    }
    if (wid < 4) {
        const char* sa = (const char*)w_sp - 4096 + pm * 128 * S_ * 2;
#pragma unroll
        for (int t = 0; t < 2; ++t) {
            set_m0(lds0 + wid * 4096 + t * K2_SA);
            glds_s<0>(g2_va(wid, lane, 0), sa + t * 128); glds_s<1024>(g2_va(wid, lane, 1), sa + t * 128); glds_s<2048>(g2_va(wid, lane, 2), sa + t * 128); glds_s<3072>(g2_va(wid, lane, 3), sa + t * 128);
        }
    }
    SB();
#pragma unroll
    for (int m = ZHALF; m < 4; ++m) zw[m] = merge(gel(acc2[m][1], cs1[1], er[m]), gel(acc2[m][2], cs1[2], er[m]));
    u32x2 zs8 = gel(h8, c8, er[4]);
    {
        LAS char* zl = (LAS char*)lds + Z1L + wr * 1024 + r16 * 16;
#pragma unroll
        for (int m = 0; m < 4; ++m) *(LAS u32x4*)(zl + cg * 8192 + (2 * ((g & 1) ^ wr) + (g >> 1)) * 2048 + m * 256) = zw[m];
        *(LAS u32x2*)(zl + 32768 + wc * 256 + (g >> 1) * 2048 + (g & 1) * 8) = zs8;
    }
    const u64 e_ = *EW;
    const unsigned eh = __builtin_amdgcn_readfirstlane((unsigned)(e_ >> 32)), el = __builtin_amdgcn_readfirstlane((unsigned)e_);
    return ((u64)eh << 32) | el;
}

DI void wait_in(char* ws, u64 epoch, int lane, int wid, int pm, int j) {
    if (wid == POLLW) {
        const u64 t0 = __builtin_amdgcn_s_memrealtime();
        for (;;) {
            u64 f0_, f1_;
            asm volatile("global_load_dwordx2 %0, %2, %3 sc1\n\tglobal_load_dwordx2 %1, %4, %5 sc1\n\ts_waitcnt vmcnt(0)" : "=&v"(f0_), "=&v"(f1_)
                         : "v"(lane * 8), "s"(ws + WS_FJ + j * 512), "v"((lane & 15) * 8), "s"(ws + WS_FP + pm * 128) : "memory");
            if (__all(f0_ == epoch && f1_ == epoch)) break;
            const u64 dt = __builtin_amdgcn_s_memrealtime() - t0;
            if (dt > 5000000ull) break;
            if (dt > 1000 && blockIdx.x == 0) epoch_service(ws, lane, epoch);
            __builtin_amdgcn_s_sleep(POLLSLEEP1);
        }
    }
    extern __shared__ __attribute__((aligned(16))) char smem[];
    volatile LAS unsigned* go = (volatile LAS unsigned*)((LAS char*)smem + TOPL + 20);
    if (wid == POLLW) *go = 1;
    else while (*go == 0) __builtin_amdgcn_s_sleep(1);
    asm volatile("" ::: "memory");
}
DI void wait_fa(char* ws, u64 epoch, int lane) {
    const u64 t0 = __builtin_amdgcn_s_memrealtime();
    for (;;) {
        u32x4 a, b;
        asm volatile("global_load_dwordx4 %0, %2, %3 sc1\n\tglobal_load_dwordx4 %1, %2, %3 offset:1024 sc1\n\ts_waitcnt vmcnt(0)" : "=&v"(a), "=&v"(b) : "v"(lane * 16), "s"(ws + WS_FA) : "memory");
        const unsigned lo = (unsigned)epoch, hi = (unsigned)(epoch >> 32);
        if (__all(a[0] == lo && a[1] == hi && a[2] == lo && a[3] == hi && b[0] == lo && b[1] == hi && b[2] == lo && b[3] == hi)) break;
        if (__builtin_amdgcn_s_memrealtime() - t0 > 5000000ull) break;
        if (blockIdx.x == 0) epoch_service(ws, lane, epoch);
        __builtin_amdgcn_s_sleep(8);
    }
}

constexpr float ATT_BIAS = 72.0f;
DI float xhalf_max(float m) { auto rr = __builtin_amdgcn_permlane32_swap(__float_as_uint(m), __float_as_uint(m), false, false); return fmaxf(__uint_as_float(rr[0]), __uint_as_float(rr[1])); }
DI float lo_bcast(float m) { auto rr = __builtin_amdgcn_permlane32_swap(__float_as_uint(m), __float_as_uint(m), false, false); return __uint_as_float(rr[0]); }
DI float bf_round(float x) { return __uint_as_float(cvtpk(x, 0.f) << 16); }
DI float max16(const f32x16& s) {
    float a = fmaxf(fmaxf(s[0], s[1]), s[2]), b = fmaxf(fmaxf(s[3], s[4]), s[5]);
    a = fmaxf(fmaxf(a, s[6]), s[7]); b = fmaxf(fmaxf(b, s[8]), s[9]); a = fmaxf(fmaxf(a, s[10]), s[11]); b = fmaxf(fmaxf(b, s[12]), s[13]);
    a = fmaxf(fmaxf(a, s[14]), s[15]); return fmaxf(a, b);
}
DI void exp_pack(const f32x16& s, bf16x8& p0, bf16x8& p1) {
    float e[16];
#pragma unroll
    for (int i = 0; i < 16; ++i) e[i] = __builtin_amdgcn_exp2f(s[i]);
    u32x4 a = {cvtpk(e[0], e[1]), cvtpk(e[2], e[3]), cvtpk(e[4], e[5]), cvtpk(e[6], e[7])};
    u32x4 b = {cvtpk(e[8], e[9]), cvtpk(e[10], e[11]), cvtpk(e[12], e[13]), cvtpk(e[14], e[15])};
    p0 = __builtin_bit_cast(bf16x8, a); p1 = __builtin_bit_cast(bf16x8, b);
}

struct KVF { bf16x8 k1, k2, v0, v1; };
DI void kv_read(KVF& f, const LAS char* tb, int lane, int k2o, int vo) {
    f.k1 = *(const LAS bf16x8*)(tb + lane * 16); f.k2 = *(const LAS bf16x8*)(tb + k2o);
    f.v0 = *(const LAS bf16x8*)(tb + 1024 + vo); f.v1 = *(const LAS bf16x8*)(tb + 2048 + vo);
}
#define k2frag(x) (x)
struct NoHook { DI void operator()(int) const {} };
template <bool RD, class H = NoHook>
DI void half_step(f32x16& sx, bf16x8& px0, bf16x8& px1, f32x16& sy, const KVF kq, const bf16x8& qy1, const bf16x8& qy2,
                  f32x16& oy, const KVF kv, const bf16x8& py0, const bf16x8& py1, KVF& ld, const LAS char* tb, int lane, int k2o, int vo, H hook = H(), int hs = 0) {
    const f32x16 zero = {};
    unsigned a0, a1, a2, a3, b0, b1, b2, b3; float e0, e1, e2, e3;
    f32x16 t = MFMA32(kq.k1, qy1, zero); SB();
    hook(hs); SB();
    if (RD) ld.k1 = *(const LAS bf16x8*)(tb + lane * 16);
    e0 = EX(sx[0]); e1 = EX(sx[1]); e2 = EX(sx[2]); e3 = EX(sx[3]); PIN(e0); PIN(e1); PIN(e2); PIN(e3); SB();
    sy = MFMA32(kq.k2, qy2, t); SB();
    if (RD) ld.k2 = *(const LAS bf16x8*)(tb + k2o);
    a0 = cvtpk(e0, e1); a1 = cvtpk(e2, e3); PIN(a0); PIN(a1);
    e0 = EX(sx[4]); e1 = EX(sx[5]); e2 = EX(sx[6]); e3 = EX(sx[7]); PIN(e0); PIN(e1); PIN(e2); PIN(e3); SB();
    oy = MFMA32(kv.v0, py0, oy); SB();
    hook(hs + 1); SB();
    if (RD) ld.v0 = *(const LAS bf16x8*)(tb + 1024 + vo);
    a2 = cvtpk(e0, e1); a3 = cvtpk(e2, e3); PIN(a2); PIN(a3);
    e0 = EX(sx[8]); e1 = EX(sx[9]); e2 = EX(sx[10]); e3 = EX(sx[11]); PIN(e0); PIN(e1); PIN(e2); PIN(e3); SB();
    oy = MFMA32(kv.v1, py1, oy); SB();
    if (RD) ld.v1 = *(const LAS bf16x8*)(tb + 2048 + vo);
    b0 = cvtpk(e0, e1); b1 = cvtpk(e2, e3); PIN(b0); PIN(b1);
    e0 = EX(sx[12]); e1 = EX(sx[13]); e2 = EX(sx[14]); e3 = EX(sx[15]);
    b2 = cvtpk(e0, e1); b3 = cvtpk(e2, e3); PIN(b2); PIN(b3); SB();
    u32x4 a = {a0, a1, a2, a3}, b = {b0, b1, b2, b3};
    px0 = __builtin_bit_cast(bf16x8, a); px1 = __builtin_bit_cast(bf16x8, b);
}
DI bf16x8 p_scale(bf16x8 p, int sh) {
    u32x4 w = __builtin_bit_cast(u32x4, p);
#pragma unroll
    for (int i = 0; i < 4; ++i) w[i] = cvtpk(__builtin_amdgcn_ldexpf(bflo(w[i]), sh), __builtin_amdgcn_ldexpf(bfhi(w[i]), sh));
    return __builtin_bit_cast(bf16x8, w);
}
template <bool PP> DI void att_rescale(f32x16& o, f32x16& s, bf16x8& p0, bf16x8& p1, float& mref, bf16x8& q2, bool& bad, int c, int h) {
    bool nf = false;
#pragma unroll
    for (int i = 0; i < 11; ++i) nf = nf || (!(fabsf(o[i]) <= 3.0e38f) && (i < 8 || h == 0));
    if (__any(nf)) { bad = true; return; }
    float lb = lo_bcast(o[10]);
    int e = __builtin_amdgcn_frexp_expf(lb);
    float d = (float)(e + (int)ATT_BIAS); d = d < 0 ? 0 : d;
    float nm = bf_round(mref + d); float dd = nm - mref; mref = nm;
    int sh = -(int)dd;
#pragma unroll
    for (int i = 0; i < 16; ++i) { o[i] = __builtin_amdgcn_ldexpf(o[i], sh); if (!PP) { float b_ = s[i] - dd; PIN(b_); s[i] = b_; } }
    if (PP) { p0 = p_scale(p0, sh); p1 = p_scale(p1, sh); }
    u32x4 b = __builtin_bit_cast(u32x4, q2); b[1] = h ? 0u : (__float_as_uint(-nm) >> 16); q2 = __builtin_bit_cast(bf16x8, b);
}

#define W3LD 1
#define W3ST 3
struct G2F { bf16x8 a[4], b[4], sb; };
struct G2S {
    unsigned va[4], vl, vl2;
    const char* sa; const char* sl; unsigned lstep;
    unsigned m0a, m0l[3];
    const LAS char* pa[2][2]; const LAS char* pb[2]; const LAS char* ps[2];
};
template <int KS, int GL, bool RD, int RS, int WS, class H = NoHook> DI void g2_phase(G2S& s, const G2F& f, G2F& n, f32x4 (&acc)[4][4], f32x4 (&sacc)[2], H hook = H()) {
    const LAS char* a0 = s.pa[0][KS] + RS * K2_SA; const LAS char* a1 = s.pa[1][KS] + RS * K2_SA;
    constexpr int BO = RS * K2_SB + KS * 9216;
    const LAS char* b0 = s.pb[0] + BO; const LAS char* b1 = s.pb[1] + BO;
#define MM(m, nn) acc[m][nn] = MFMA16(f.b[nn], f.a[m], acc[m][nn]); PIN(acc[m][nn]); SB(); hook((nn) * 4 + (m)); SB()
    MM(0, 0); if (RD) n.b[0] = ldsB2(b0, b1); SB();
    MM(1, 0); if (RD) n.a[0] = ldsA(a0); SB();
    MM(2, 0); if (RD) n.a[1] = ldsA(a0 + 2048); SB();
    MM(3, 0); if (RD) n.a[2] = ldsA(a1); SB();
    MM(0, 1); if (RD) n.a[3] = ldsA(a1 + 2048); SB();
    MM(1, 1); if (RD) n.b[1] = ldsB2(b0 + 256, b1 + 256); SB();
    MM(2, 1); if (RD) n.b[2] = ldsB2(b0 + 512, b1 + 512); SB();
    MM(3, 1); if (RD) n.b[3] = ldsB2(b0 + 768, b1 + 768); SB();
    MM(0, 2); if (RD) n.sb = ldsB2(s.ps[0] + BO, s.ps[1] + BO); SB();
    MM(1, 2); if (GL == 1) { set_m0(s.m0a + WS * K2_SA); glds_s<0>(s.va[0], s.sa); } if (GL == 2) { glds_sc1<1024>(s.vl, s.sl); } SB();
    MM(2, 2);
    MM(3, 2); if (GL == 1) glds_s<1024>(s.va[1], s.sa); if (GL == 2) glds_sc1<2048>(s.vl, s.sl); SB();
    MM(0, 3);
    MM(1, 3); if (GL == 1) glds_s<2048>(s.va[2], s.sa); if (GL == 2) glds_sc1<3072>(s.vl, s.sl); SB();
    MM(2, 3);
    MM(3, 3); if (GL == 1) { glds_s<3072>(s.va[3], s.sa); s.sa += 128; asm volatile("" : "+s"(s.sa)); } if (GL == 2) { set_m0(s.m0l[WS] + 4096); glds_sc1<0>(s.vl2, s.sl); } SB();
    sacc[0] = MFMA16(f.sb, f.a[0], sacc[0]); PIN(sacc[0]); SB();
    if (GL == 1) { set_m0(s.m0l[WS]); glds_sc1<0>(s.vl, s.sl); } if (GL == 2) { glds_sc1<1024>(s.vl2, s.sl); s.sl += s.lstep; asm volatile("" : "+s"(s.sl)); } SB();
    sacc[1] = MFMA16(f.sb, f.a[1], sacc[1]); PIN(sacc[1]); SB();
#undef MM
}
template <int C, bool G2nd, bool G1st, int VM, bool RDN> DI void g2_iter(G2S& s, G2F& f0, G2F& f1, f32x4 (&acc)[4][4], f32x4 (&sacc)[2]) {
    g2_phase<1, G2nd ? 2 : 0, true, C, (C + 2) % 3>(s, f0, f1, acc, sacc);
    wait_vm_bar<VM>(); SB();
    g2_phase<0, G1st ? 1 : 0, RDN, (C + 1) % 3, C>(s, f1, f0, acc, sacc);
}

constexpr int K3_S = 4608, K3_RW = 4 * K3_S;
struct B5 { bf16x8 b[5]; };
DI unsigned k3_so(int w, int sl) {
    const int r = sl == 1 ? 2 : sl - 2;
    if (sl == 0) return Z1L + w * K3_S;
    return w < 3 ? r * K2_SA + w * K3_S : (w < 7 ? K2_B0 + r * K2_SB + (w - 3) * K3_S : K2_K0 + r * K2_SK);
}
DI unsigned k3_orow(int r) { return r < 6 ? (r >> 1) * K2_SA + 3 * K3_S + 64 + (r & 1) * 1168 : K2_K0 + (r - 6) * K2_SK + K3_S + 64; }
DI void k3_b1(int p, unsigned m0v, unsigned va, const char* sb) {
    if (p == 0) { set_m0(m0v); glds_sc1<0>(va, sb); }
    if (p == 1) glds_sc1<1024>(va, sb);
    if (p == 2) glds_sc1<2048>(va, sb);
    if (p == 3) glds_sc1<3072>(va, sb);
    if (p == 4) asm volatile("s_mov_b32 m0, %2\n\ts_mov_b32 exec_hi, 0\n\tglobal_load_lds_dwordx4 %0, %1 sc1\n\ts_mov_b32 exec_hi, -1" ::"v"(va), "s"(sb + 4096), "s"(m0v + 4096));
}
DI void k3_a(unsigned m0v, unsigned va, const char* sa) {
    set_m0(m0v); glds_sc1<0>(va, sa); glds_sc1<1024>(va, sa); glds_sc1<2048>(va, sa); glds_sc1<3072>(va, sa);
}
DI void k3_b(unsigned m0v, unsigned va, const char* sb) {
#pragma unroll
    for (int p = 0; p < 5; ++p) k3_b1(p, m0v, va, sb);
}
DI void k3_rdb(B5& r, const LAS char* p, const LAS char* p5) {
#pragma unroll
    for (int n = 0; n < 4; ++n) r.b[n] = ldsA(p + n * 1024);
    r.b[4] = ldsA(p5);
}
DI void k3_lb1(int p, B5& r, unsigned va, unsigned va5, const char* sb) {
    if (p < 4) asm volatile("global_load_dwordx4 %0, %1, %2 offset:%3 sc1" : "+v"(r.b[p & 3]) : "v"(va), "s"(sb), "n"((p & 3) * 1024) : "memory");
    else asm volatile("global_load_dwordx4 %0, %1, %2 sc1" : "+v"(r.b[4]) : "v"(va5), "s"(sb) : "memory");
}
#define LB5 "global_load_dwordx4 %0, %5, %7 sc1\n\tglobal_load_dwordx4 %1, %5, %7 offset:1024 sc1\n\tglobal_load_dwordx4 %2, %5, %7 offset:2048 sc1\n\t" \
            "global_load_dwordx4 %3, %5, %7 offset:3072 sc1\n\tglobal_load_dwordx4 %4, %6, %7 sc1\n"
DI void k3_lb(B5& r, unsigned va, unsigned va5, const char* sb) {
    asm volatile(LB5 : "=&v"(r.b[0]), "=&v"(r.b[1]), "=&v"(r.b[2]), "=&v"(r.b[3]), "=&v"(r.b[4]) : "v"(va), "v"(va5), "s"(sb) : "memory");
}
DI void k3_lb_if(B5& r, unsigned va, unsigned va5, const char* sb, int a, int b) {
    asm volatile("s_cmp_lg_u32 %8, %9\n\ts_cbranch_scc1 .Llb%=\n\t" LB5 ".Llb%=:" : "+v"(r.b[0]), "+v"(r.b[1]), "+v"(r.b[2]), "+v"(r.b[3]), "+v"(r.b[4])
                 : "v"(va), "v"(va5), "s"(sb), "s"(a), "n"(b) : "memory", "scc");
}
DI void pin5(B5& r) { PIN(r.b[0]); PIN(r.b[1]); PIN(r.b[2]); PIN(r.b[3]); PIN(r.b[4]); }
template <int N0, int N1, bool FIRST = false> DI void k3_mm(f32x4 (&acc)[4][5], const bf16x8 (&a)[4], const B5& r) {
#pragma unroll
    for (int n = N0; n < N1; ++n)
#pragma unroll
        for (int m = 0; m < 4; ++m) {
            if (FIRST) asm volatile("v_mfma_f32_16x16x32_bf16 %0, %1, %2, 0" : "=&v"(acc[m][n]) : "v"(r.b[n]), "v"(a[m]));
            else asm volatile("v_mfma_f32_16x16x32_bf16 %0, %1, %2, %0" : "+v"(acc[m][n]) : "v"(r.b[n]), "v"(a[m]));
        }
    SB();
}

#define K3MM(d, b_, a_) asm volatile("v_mfma_f32_16x16x32_bf16 %0, %1, %2, %0" : "+v"(d) : "v"(b_), "v"(a_))
#define ILV 1
struct UA { const char* p[4]; };
template <int NX, bool FIRST = false> DI void k3_step(f32x4 (&acc)[4][5], bf16x8 (&a)[4], const B5& r, unsigned va, const UA& ua) {
#pragma unroll
    for (int m = 0; m < 4; ++m) {
#pragma unroll
        for (int n = 0; n < 5; ++n) {
            if (FIRST && m < 3) asm volatile("v_mfma_f32_16x16x32_bf16 %0, %1, %2, 0" : "=&v"(acc[m][n]) : "v"(r.b[n]), "v"(a[m]));
            else K3MM(acc[m][n], r.b[n], a[m]);
        }
        if (NX >= 0 && ILV) asm volatile("global_load_dwordx4 %0, %1, %2 sc1" : "+v"(a[m]) : "v"(va), "s"(ua.p[m] + (NX < 0 ? 0 : NX) * 32768) : "memory");
    }
    if (NX >= 0 && !ILV) {
#pragma unroll
        for (int m = 0; m < 4; ++m) asm volatile("global_load_dwordx4 %0, %1, %2 sc1" : "+v"(a[m]) : "v"(va), "s"(ua.p[m] + (NX < 0 ? 0 : NX) * 32768) : "memory");
    }
    SB();
}

DI void phase2(const u16* __restrict__ w_sp, const u16* __restrict__ b_sp, const u16* __restrict__ x, char* __restrict__ ws, u16* __restrict__ out, const u64 epoch, const int wid) {
    extern __shared__ __attribute__((aligned(16))) char smem[];
    int bid = blockIdx.x, xcd = bid & 7, idx = bid >> 3;
    int pm = (xcd & 1) * 8 + (idx & 7), j = (xcd >> 1) * 4 + (idx >> 3);
    unsigned zz = 0; asm volatile("" : "+s"(zz));
    const int lane = __builtin_amdgcn_mbcnt_hi(~0u, __builtin_amdgcn_mbcnt_lo(~0u, zz)) & 63;
    const int tid = wid * 64 + lane;
    const unsigned lds0 = (unsigned)(uintptr_t)smem; const LAS char* lds = (const LAS char*)smem;
    STAMP(1, 0);
    const int rb = ((xcd & 1) ^ 1) * 16 + (xcd >> 1) * 4 + (idx & 3), j3 = idx >> 2;
    volatile LAS unsigned* SY = (volatile LAS unsigned*)(lds + TOPL);
    gu64* FLr = (gu64*)(ws + WS_FL) + rb * 64;
    const unsigned va = lane * 16;
    unsigned va5 = 4096 + ((lane >> 4) * 8 + (lane & 7)) * 16; PIN(va5);
    B5 rb0, rb1, rb2, rb3;
    const char* wb = ws + WS_W3T + (size_t)j3 * 90 * 4608 + wid * 4608;
    if (__builtin_expect(wid < 4, 1)) {
        const int wr = wid >> 1, wc = wid & 1;
        G2S s;
        s.vl = lane * 16; s.vl2 = lane * 16 + 4096;
        s.lstep = wid < 3 ? 18432 : 6144;
        unsigned ol = (wid < 3 ? (unsigned)WS_Z2T + j * 32 * 18432 + wid * 6144 : (unsigned)WS_KV), oa = pm * 128 * S_ * 2;
        s.m0a = lds0 + wid * 4096; s.m0l[0] = lds0 + (wid < 3 ? K2_B0 + wid * 6144 : K2_K0); s.m0l[1] = s.m0l[0] + s.lstep; s.m0l[2] = s.m0l[1] + s.lstep;
        PIN(s.vl); PIN(s.vl2);
        s.sl = ws + ol;
        asm volatile("" : "+s"(s.sl), "+s"(s.m0l[0]), "+s"(s.m0l[1]));
        SB();
        wait_in(ws, epoch, lane, wid, pm, j);
        STAMPT(0, 3, 3);
#pragma unroll
        for (int t = 0; t < 2; ++t) {
            set_m0(s.m0l[t]); glds_sc1<0>(s.vl, s.sl); glds_sc1<1024>(s.vl, s.sl); glds_sc1<2048>(s.vl, s.sl); glds_sc1<3072>(s.vl, s.sl);
            set_m0(s.m0l[t] + 4096); glds_sc1<0>(s.vl2, s.sl); glds_sc1<1024>(s.vl2, s.sl); s.sl += s.lstep;
            SB();
            if (t == 0) {
#pragma unroll
                for (int i = 0; i < 4; ++i) { s.va[i] = g2_va(wid, lane, i); PIN(s.va[i]); }
                int r = lane & 15, g = lane >> 4, q = r >> 2, p = lane & 3;
#pragma unroll
                for (int ks = 0; ks < 2; ++ks) {
                    int o = r * 128 + (((4 * ks + g) ^ (r >> 1)) * 16) + wr * 8192;
                    s.pa[0][ks] = lds + o + wc * 4096; s.pa[1][ks] = lds + o + (1 - wc) * 4096; PIN(s.pa[0][ks]); PIN(s.pa[1][ks]);
                }
                int ob = K2_B0 + g * 2304 + q * 32 + 8 * p;
                s.pb[0] = lds + ob + wc * 1024 + ((g & 1) * 128); s.pb[1] = lds + ob + wc * 1024 + (((g & 1) ^ 1) * 128);
                s.ps[0] = lds + ob + 2048 + ((g & 1) * 128); s.ps[1] = lds + ob + 2048 + (((g & 1) ^ 1) * 128);
                PIN(s.pb[0]); PIN(s.pb[1]); PIN(s.ps[0]); PIN(s.ps[1]);
                SB();
            }
        }
        s.sa = (const char*)w_sp - 4096 + oa + 256;
        G2F f0, f1;
        f32x4 acc[4][4], sacc[2];
#pragma unroll
        for (int m = 0; m < 4; ++m) {
            const float b = bf2f(*(const LAS u16*)(lds + TOPL + 2304 + (wr * 64 + (m ^ (2 * wc)) * 16 + (lane & 15)) * 2));
#pragma unroll
            for (int n = 0; n < 4; ++n) { acc[m][n] = f32x4{b, b, b, b}; PIN(acc[m][n]); }
            if (m < 2) { sacc[m] = f32x4{b, b, b, b}; PIN(sacc[m]); }
        }
        SB();
        wait_vm_bar<6>();
        STAMP(1, 1);
        f0.b[0] = ldsB2(s.pb[0], s.pb[1]);
        f0.a[0] = ldsA(s.pa[0][0]); f0.a[1] = ldsA(s.pa[0][0] + 2048); f0.a[2] = ldsA(s.pa[1][0]); f0.a[3] = ldsA(s.pa[1][0] + 2048);
        f0.b[1] = ldsB2(s.pb[0] + 256, s.pb[1] + 256); f0.b[2] = ldsB2(s.pb[0] + 512, s.pb[1] + 512); f0.b[3] = ldsB2(s.pb[0] + 768, s.pb[1] + 768); f0.sb = ldsB2(s.ps[0], s.ps[1]);
        SB();
        set_m0(s.m0a + 2 * K2_SA); glds_s<0>(s.va[0], s.sa); glds_s<1024>(s.va[1], s.sa); glds_s<2048>(s.va[2], s.sa); glds_s<3072>(s.va[3], s.sa); s.sa += 128;
        set_m0(s.m0l[2]); glds_sc1<0>(s.vl, s.sl);
        SB();
#pragma unroll 1
        for (int t = 0; t < 9; ++t) {
            g2_iter<0, true, true, 10, true>(s, f0, f1, acc, sacc); g2_iter<1, true, true, 10, true>(s, f0, f1, acc, sacc); g2_iter<2, true, true, 10, true>(s, f0, f1, acc, sacc);
        }
        asm volatile("" : "=v"(rb0.b[0]), "=v"(rb0.b[1]), "=v"(rb0.b[2]), "=v"(rb0.b[3]), "=v"(rb0.b[4]), "=v"(rb1.b[0]), "=v"(rb1.b[1]), "=v"(rb1.b[2]), "=v"(rb1.b[3]), "=v"(rb1.b[4]));
        g2_phase<1, 2, true, 0, 2>(s, f0, f1, acc, sacc);
        wait_vm_bar<10>(); SB();
        g2_phase<0, 1, true, 1, 0>(s, f1, f0, acc, sacc, [&](int st) { if (st < 10 && !(st & 1)) k3_lb1(st >> 1, rb0, va, va5, wb); });
        g2_phase<1, 2, true, 1, 0>(s, f0, f1, acc, sacc);
        wait_vm_bar<15>(); SB();
        g2_phase<0, 1, true, 2, 1>(s, f1, f0, acc, sacc, [&](int st) { if (st < 10 && !(st & 1)) k3_lb1(st >> 1, rb1, va, va5, wb + 36864); });
        const int c = lane & 15, g = lane >> 4;
        const LAS char* z1b = lds + Z1L + (g >> 1) * 2048 + (wr * 64 + c) * 16 + (g & 1) * 8;
        u32x2 z1r[4][4], z1s[2];
#pragma unroll
        for (int m = 0; m < 4; ++m) {
            const int mo = (m ^ (2 * wc)) * 16;
#pragma unroll
            for (int n = 0; n < 4; ++n) z1r[m][n] = *(const LAS u32x2*)(z1b + (wc * 4 + n) * 4096 + mo * 16);
            if (m < 2) z1s[m] = *(const LAS u32x2*)(z1b + 8 * 4096 + mo * 16);
        }
        SB();
#define PREB(i) [&](int st) { if (st < 5) k3_b1(st, lds0 + k3_so(wid, i), va, wb + ((i) + 6) * 36864); else if (st < 10) k3_b1(st - 5, lds0 + k3_so(wid + 4, i), va, wb + ((i) + 6) * 36864 + 4 * 4608); }
        g2_phase<1, 2, true, 2, 1>(s, f0, f1, acc, sacc);
        wait_vm_bar<15>(); SB();
        g2_phase<0, 0, true, 0, 2>(s, f1, f0, acc, sacc, PREB(0));
        g2_phase<1, 0, true, 0, 2>(s, f0, f1, acc, sacc, PREB(1));
        wait_vm_bar<20>(); SB();
        g2_phase<0, 0, true, 1, 0>(s, f1, f0, acc, sacc, PREB(2));
        unsigned vu[3];
#pragma unroll
        for (int i = 0; i < 3; ++i) {
            const int cgl = 18 * j + (i < 2 ? 2 * (wc * 4 + 2 * i + (g & 1)) : 16) + (g >> 1);
            vu[i] = (cgl >> 2) * 4096 + (cgl & 3) * 256 + c * 16 + (i == 2 ? (g & 1) * 1024 : 0); PIN(vu[i]);
        }
        const char* ub0 = ws + WS_UO + (size_t)(pm * 2 + wr) * 90 * 4096 + wc * 2048; const char* ub1 = ub0 + 2048 - wc * 4096;
        SB();
        g2_phase<1, 0, true, 1, 0>(s, f0, f1, acc, sacc);
        wait_vm_bar<0>(); SB();
        __builtin_amdgcn_s_setprio(2);
        auto fin = [&](f32x4 v, u32x2 z) { return u32x2{cvtpk(fmul1(bflo(z[0]), v[0]), fmul1(bfhi(z[0]), v[1])), cvtpk(fmul1(bflo(z[1]), v[2]), fmul1(bfhi(z[1]), v[3]))}; };
        auto mrg = [&](u32x2 A, u32x2 B) {
            auto r0 = __builtin_amdgcn_permlane16_swap(A[0], B[0], false, false); auto r1 = __builtin_amdgcn_permlane16_swap(A[1], B[1], false, false);
            return u32x4{r0[0], r1[0], r0[1], r1[1]};
        };
#define M2(m, n) acc[m][n] = MFMA16(f1.b[n], f1.a[m], acc[m][n]); acc[m][(n) + 1] = MFMA16(f1.b[(n) + 1], f1.a[m], acc[m][(n) + 1]); PIN(acc[m][n]); PIN(acc[m][(n) + 1]); SB()
#define PUT(m, n) st16s<((m) & 1) * 1024>((m) < 2 ? ub0 : ub1, vu[(n) >> 1], mrg(fin(acc[m][n], z1r[m][n]), fin(acc[m][(n) + 1], z1r[m][(n) + 1]))); SB()
        M2(0, 0); M2(1, 0); M2(2, 0); PUT(0, 0);
        M2(3, 0); PUT(1, 0);
        sacc[0] = MFMA16(f1.sb, f1.a[0], sacc[0]); sacc[1] = MFMA16(f1.sb, f1.a[1], sacc[1]); PIN(sacc[0]); PIN(sacc[1]); SB(); PUT(2, 0);
        M2(0, 2); PUT(3, 0);
        M2(1, 2); st16s<0>(ub0, vu[2], mrg(fin(sacc[0], z1s[0]), fin(sacc[1], z1s[1]))); SB();
        M2(2, 2); PUT(0, 2);
        M2(3, 2); PUT(1, 2);
        STAMP(1, 2);
        PUT(2, 2); PUT(3, 2);
        SB(); k3_b(lds0 + k3_so(wid, 3), va, wb + 9 * 36864);
        wait_vm<5>();
        if (lane == 0) __hip_atomic_store((gu64*)(ws + WS_FL) + (pm * 2 + wr) * 64 + j * 2 + wc, epoch, RLX_AGENT);
        pin5(rb0); pin5(rb1);
        asm volatile("" : "=v"(rb2.b[0]), "=v"(rb2.b[1]), "=v"(rb2.b[2]), "=v"(rb2.b[3]), "=v"(rb2.b[4]), "=v"(rb3.b[0]), "=v"(rb3.b[1]), "=v"(rb3.b[2]), "=v"(rb3.b[3]), "=v"(rb3.b[4]));
        STAMP(1, 3);
        __builtin_amdgcn_s_setprio(0);
    } else {
        __builtin_amdgcn_s_setprio(1);
        const int c = lane & 31, h = lane >> 5, aw = wid - 4;
        const char* kvg = ws + WS_KV;
        bf16x8 q1[2], q2[2]; float mref[2];
        const f32x16 zero = {};
        f32x16 oA = zero, oB = zero;
        int k2o = 1024 + (c >> 4) * 1024 + (c & 15) * 16;
        int vo = c < 8 ? 256 + (h * 8 + c) * 16 : 512 + (h * 16 + (c < 16 ? c - 8 : (c < 18 ? c - 2 : (c == 18 ? 12 : 13)))) * 16;
        {
            const int qbase = (aw < 3 ? K2_B0 + 2 * K2_SB + aw * 6144 : K2_K0 + 2 * K2_SK) + 1024;
            const LAS char* ql = lds + qbase;
            const char* qs = ws + WS_Q + (pm * 16 + j) * 9216 + aw * 32;
            unsigned vq = (lane >> 1) * 128 + (lane & 1) * 16; PIN(vq);
            SB();
            wait_in(ws, epoch, lane, wid, pm, j);
            set_m0(lds0 + qbase); glds_sc1<0>(vq, qs); glds_sc1<1024>(vq, qs + 3072);
            if (lane < 16) glds_sc1<2048>(vq, qs + 6144);
            SB();
            const LAS char* qo[5];
#pragma unroll
            for (int i = 0; i < 5; ++i) { const int k = 18 * c + (i < 4 ? 8 * h + 2 * i : 16); qo[i] = ql + ((k >> 3) * 32 + (k & 7) * 2); PIN(qo[i]); }
            PIN(oA); PIN(oB); PIN(k2o); PIN(vo);
            SB();
            wait_vm<0>();
#pragma unroll
            for (int t = 0; t < 2; ++t) {
                u32x4 a;
#pragma unroll
                for (int i = 0; i < 4; ++i) a[i] = *(const LAS unsigned*)(qo[i] + t * 16);
                q1[t] = __builtin_bit_cast(bf16x8, a);
                const unsigned e = *(const LAS unsigned*)(qo[4] + t * 16);
                u32x4 b = {h ? 0u : e, 0u, 0u, 0u}; q2[t] = __builtin_bit_cast(bf16x8, b);
                PIN(q1[t]); PIN(q2[t]);
            }
        }
        bool bad = false;
        const LAS char* kvl = lds + K2_K0;
        asm volatile("s_waitcnt vmcnt(0) lgkmcnt(0)\n\ts_barrier" ::: "memory");
        KVF X, Y;
        kv_read(X, kvl, lane, k2o, vo); kv_read(Y, kvl + 3072, lane, k2o, vo);
#pragma unroll
        for (int t = 0; t < 2; ++t) {
            f32x16 s0 = MFMA32(X.k1, q1[t], zero); s0 = MFMA32(X.k2, q2[t], s0);
            f32x16 s1 = MFMA32(Y.k1, q1[t], zero); s1 = MFMA32(Y.k2, q2[t], s1);
            float m = xhalf_max(fmaxf(max16(s0), max16(s1)));
            mref[t] = bf_round(rintf(m) + ATT_BIAS);
            u32x4 b = __builtin_bit_cast(u32x4, q2[t]); b[1] = h ? 0u : (__float_as_uint(-mref[t]) >> 16); q2[t] = __builtin_bit_cast(bf16x8, b);
        }
        f32x16 sA = MFMA32(X.k1, q1[0], zero); sA = MFMA32(X.k2, q2[0], sA);
        f32x16 sB = zero;
        bf16x8 pA0, pA1, pB0 = {}, pB1 = {};
        half_step<false>(sA, pA0, pA1, sB, X, q1[1], q2[1], oB, X, pB0, pB1, Y, kvl, lane, k2o, vo);
        half_step<false>(sB, pB0, pB1, sA, Y, q1[0], q2[0], oA, X, pA0, pA1, Y, kvl, lane, k2o, vo);
        asm volatile("s_waitcnt lgkmcnt(0)\n\ts_barrier" ::: "memory");
#define A_IV(sl, CHK, H, hs) { const LAS char* tb = kvl + (sl) * K2_SK; \
            half_step<true>(sA, pA0, pA1, sB, Y, q1[1], q2[1], oB, X, pB0, pB1, X, tb, lane, k2o, vo, H, hs); \
            half_step<false>(sB, pB0, pB1, sA, X, q1[0], q2[0], oA, Y, pA0, pA1, X, tb, lane, k2o, vo, H, hs + 2); \
            if (CHK) { mB = __builtin_amdgcn_ballot_w64(!(oB[10] <= 0x1p20f)); }                  \
            half_step<true>(sA, pA0, pA1, sB, X, q1[1], q2[1], oB, Y, pB0, pB1, Y, tb + 3072, lane, k2o, vo, H, hs + 4); \
            if (CHK) { mA = __builtin_amdgcn_ballot_w64(!(oA[10] <= 0x1p20f)); any = mA | mB; asm volatile("" : "+s"(any)); } \
            half_step<false>(sB, pB0, pB1, sA, Y, q1[0], q2[0], oA, X, pA0, pA1, Y, tb, lane, k2o, vo, H, hs + 6); }
#define A_BAR() asm volatile("s_waitcnt lgkmcnt(0)\n\ts_barrier" ::: "memory")
        unsigned long long mA, mB, any;
#define UNDEF5(r) asm volatile("" : "=v"(r.b[0]), "=v"(r.b[1]), "=v"(r.b[2]), "=v"(r.b[3]), "=v"(r.b[4]))
        u32x4 fa0, fa1;
        asm volatile("global_load_dwordx4 %0, %2, %3 sc1\n\tglobal_load_dwordx4 %1, %2, %3 offset:1024 sc1" : "=&v"(fa0), "=&v"(fa1) : "v"(va), "s"(ws + WS_FA) : "memory");
        const NoHook nh;
        const char* pf = ws + WS_W3T + (size_t)j3 * 90 * 4608 + 16 * 4608 + min(((idx & 3) * 4 + aw) * 192, 2664 - 192) * 128;
        auto wl = [&](int i) {
            if (i < 20) k3_lb1(i % 5, i < 5 ? rb0 : (i < 10 ? rb1 : (i < 15 ? rb2 : rb3)), va, va5, wb + (i / 5) * 36864);
            else if (i < 23 && PFW)
                asm volatile("s_mov_b32 m0, %2\n\ts_nop 0\n\tglobal_load_lds_dword %0, %1 sc1" ::"v"(va * 8), "s"(pf + (i - 20) * 8192), "s"(lds0 + TOPL + 64 + wid * 256));
        };
#define A_IT(H) A_IV(1, false, H, 0); A_BAR(); A_IV(2, false, H, 8); A_BAR(); A_IV(0, true, H, 16); \
            if (__builtin_expect(any != 0, 0)) { \
                if (mA) att_rescale<false>(oA, sA, pA0, pA1, mref[0], q2[0], bad, c, h); \
                if (mB) att_rescale<true>(oB, sB, pB0, pB1, mref[1], q2[1], bad, c, h); \
            } \
            A_BAR()
#pragma unroll 1
        for (int t = 0; t < TFA; ++t) { A_IT(nh); }
        {
            wait_vm<0>(); PIN(fa0); PIN(fa1);
            const unsigned lo = (unsigned)epoch, hi = (unsigned)(epoch >> 32);
            if (__builtin_expect(!__all(fa0[0] == lo && fa0[1] == hi && fa0[2] == lo && fa0[3] == hi && fa1[0] == lo && fa1[1] == hi && fa1[2] == lo && fa1[3] == hi), 0)) wait_fa(ws, epoch, lane);
            UNDEF5(rb0); UNDEF5(rb1); UNDEF5(rb2); UNDEF5(rb3);
            SB();
            A_IT(wl);
        }
#pragma unroll 1
        for (int t = TFA + 1; t < 10; ++t) { A_IT(nh); }
        A_IV(1, false, nh, 0); A_BAR();
        wait_vm<0>(); pin5(rb0); pin5(rb1); pin5(rb2); pin5(rb3);
        k3_b(lds0 + k3_so(wid, 3), va, wb + 9 * 36864); SB();
        half_step<false>(sA, pA0, pA1, sB, Y, q1[1], q2[1], oB, X, pB0, pB1, X, kvl, lane, k2o, vo);
        exp_pack(sB, pB0, pB1);
        oA = MFMA32(Y.v0, pA0, oA); oA = MFMA32(Y.v1, pA1, oA);
        oB = MFMA32(Y.v0, pB0, oB); oB = MFMA32(Y.v1, pB1, oB);
        {
            float sa_ = fabsf(oA[10]) * 0x1p27f, sb_ = fabsf(oB[10]) * 0x1p27f;
#pragma unroll
            for (int i = 0; i < 10; ++i) { sa_ += fabsf(oA[i]); PIN(sa_); sb_ += fabsf(oB[i]); PIN(sb_); }
            if (__builtin_amdgcn_ballot_w64(!(sa_ + sb_ <= 3.0e38f))) bad = true;
        }
        f32x16 o[2] = {oA, oB};
        if (__builtin_expect(bad, 0)) {
#pragma unroll 1
            for (int t = 0; t < 2; ++t) {
                bf16x8 qa = t ? q1[1] : q1[0]; u32x4 qb = __builtin_bit_cast(u32x4, t ? q2[1] : q2[0]); qb[1] = 0;
                f32x16 oo = zero; float m = -3.0e38f;
                for (int kt = 0; kt < 64; ++kt) {
                    const char* tb = kvg + (size_t)kt * 3072;
                    bf16x8 k1, v0, v1; u32x4 k2v;
                    asm volatile("global_load_dwordx4 %0, %4, %7 sc1\n\tglobal_load_dwordx4 %1, %5, %7 sc1\n\tglobal_load_dwordx4 %2, %6, %7 offset:1024 sc1\n\t"
                                 "global_load_dwordx4 %3, %6, %7 offset:2048 sc1\n\ts_waitcnt vmcnt(0)"
                                 : "=&v"(k1), "=&v"(k2v), "=&v"(v0), "=&v"(v1) : "v"(lane * 16), "v"(k2o), "v"(vo), "s"(tb) : "memory");
                    f32x16 s = MFMA32(k1, qa, zero); s = MFMA32(__builtin_bit_cast(bf16x8, k2v), __builtin_bit_cast(bf16x8, qb), s);
                    float nm = fmaxf(m, xhalf_max(max16(s)));
                    float sc = __builtin_amdgcn_exp2f(m - nm); m = nm;
#pragma unroll
                    for (int i = 0; i < 16; ++i) { float a_ = oo[i] * sc, b_ = s[i] - nm; PIN(a_); PIN(b_); oo[i] = a_; s[i] = b_; }
                    bf16x8 p0, p1; exp_pack(s, p0, p1);
                    oo = MFMA32(v0, p0, oo); oo = MFMA32(v1, p1, oo);
                }
                if (t) o[1] = oo; else o[0] = oo;
            }
        }
#pragma unroll
        for (int t = 0; t < 2; ++t) {
            float inv = __builtin_amdgcn_rcpf(lo_bcast(o[t][10])); PIN(inv);
            LAS unsigned* ob = (LAS unsigned*)(lds + k3_orow(aw * 2 + t) + 36 * c + 8 * h);
#define ON(i) cvtpk(fmul1(o[t][i], inv), fmul1(o[t][(i) + 1], inv))
            ob[0] = ON(0); ob[1] = ON(2); ob[4] = ON(4); ob[5] = ON(6);
            if (h == 0) ob[8] = ON(8);
        }
        asm volatile("s_waitcnt lgkmcnt(0)" ::: "memory");
        if (lane == 0) __hip_atomic_fetch_add((LAS unsigned*)SY, 1u, __ATOMIC_RELAXED, __HIP_MEMORY_SCOPE_WORKGROUP);
        __builtin_amdgcn_s_setprio(APRIO);
    }
    {
#define TK()
#define TP()
        TP();
        UA ua;
#pragma unroll
        for (int i = 0; i < 4; ++i) ua.p[i] = ws + WS_UO + (size_t)rb * 90 * 4096 + wid * 4096 + ((wid + 1 + i) & 3) * 1024;
        auto relane = [&]() { unsigned t_ = va; PIN(t_); return (int)(t_ >> 4); };
        unsigned so[4];
#pragma unroll
        for (int i = 0; i < 4; ++i) so[i] = __builtin_amdgcn_readfirstlane(k3_so(wid, i));
#define LG0() asm volatile("s_waitcnt lgkmcnt(0)" ::: "memory"); SB()
#define LB(d, i) k3_lb(d, va, va5, wb + (i) * 36864)
#define RB(d, sl) { asm volatile("" : "+s"(so[sl])); k3_rdb(d, lds + va + so[sl], lds + va5 + so[sl]); }
        B5 rb4, rb5; bf16x8 a0[4], a1[4], a2[4], a3[4], a4[4];
        k3_lb_if(rb2, va, va5, wb + 2 * 36864, wid >> 2, 0); k3_lb_if(rb3, va, va5, wb + 3 * 36864, wid >> 2, 0);
        TP();
        f32x4 ac[4][5];
        const size_t xo = (size_t)(rb * 64 + (wid & 3) * 16) * D_ + 72 * j3;
        if (wid >= 4) {
            u32x2 xv[5];
            const unsigned vx = (lane & 15) * (D_ * 2) + (lane >> 4) * 8;
            asm volatile("global_load_dwordx2 %0, %5, %7\n\tglobal_load_dwordx2 %1, %5, %7 offset:32\n\tglobal_load_dwordx2 %2, %5, %7 offset:64\n\tglobal_load_dwordx2 %3, %5, %7 offset:96\n\t"
                         "global_load_dwordx2 %4, %6, %7 offset:128" : "=&v"(xv[0]), "=&v"(xv[1]), "=&v"(xv[2]), "=&v"(xv[3]), "=&v"(xv[4]) : "v"(vx), "v"(vx & ~16u), "s"(x + xo) : "memory");
            while (SY[0] < 4) __builtin_amdgcn_s_sleep(1);
            const int orow = pm * 128 + j * 8;
            char* ob = ws + WS_UO + ((size_t)((orow >> 6) * 90 + 72) * 4 + ((orow >> 4) & 3)) * 1024 + (orow & 15) * 16;
#pragma unroll
            for (int r = 0; r < 3; ++r) {
                const int i = (wid - 4) * 144 + r * 64 + lane, ck = i >> 3, row = i & 7;
                if (r < 2 || lane < 16) st16(ob + (ck >> 2) * 4096 + (ck & 3) * 256 + row * 16, *(const LAS u32x4*)(lds + k3_orow(row) + ck * 16));
            }
            TP(); LB(rb4, 4); LB(rb5, 5); TP();
            wait_vm<10>();
            if (lane == 0) __hip_atomic_store((gu64*)(ws + WS_FL) + (pm * 2 + (j >> 3)) * 64 + 32 + (j & 7) * 4 + (wid - 4), epoch, RLX_AGENT);
#pragma unroll
            for (int n = 0; n < 5; ++n) { PIN(xv[n]); ac[3][n] = f32x4{bflo(xv[n][0]), bfhi(xv[n][0]), bflo(xv[n][1]), bfhi(xv[n][1])}; PIN(ac[3][n]); }
        } else {
            TP(); LB(rb4, 4); LB(rb5, 5); TP();
#pragma unroll
            for (int n = 0; n < 5; ++n) { ac[3][n] = f32x4{}; PIN(ac[3][n]); }
        }
        SB();
        TP();
        if (wid == PW2) {
            const unsigned long long t0 = __builtin_amdgcn_s_memrealtime();
            u64 f = __hip_atomic_load(FLr + (lane & 31), RLX_AGENT);
            while (!__all(f == epoch) && __builtin_amdgcn_s_memrealtime() - t0 < 5000000ull) {
                __builtin_amdgcn_s_sleep(POLLSLEEP);
                f = __hip_atomic_load(FLr + (lane & 31), RLX_AGENT);
            }
            SY[1] = 1;
        }
        while (SY[1] == 0) __builtin_amdgcn_s_sleep(1);
        TP();
        STAMP(2, 0); STAMP(2, 1);
#define LA4 "global_load_dwordx4 %0, %4, %5 sc1\n\tglobal_load_dwordx4 %1, %4, %6 sc1\n\tglobal_load_dwordx4 %2, %4, %7 sc1\n\tglobal_load_dwordx4 %3, %4, %8 sc1\n"
#define LAO(d, o_) : "=&v"(d[0]), "=&v"(d[1]), "=&v"(d[2]), "=&v"(d[3]) : "v"(va), "s"(ua.p[0] + (o_)), "s"(ua.p[1] + (o_)), "s"(ua.p[2] + (o_)), "s"(ua.p[3] + (o_))
#define LA(d, i) asm volatile(LA4 LAO(d, (i) * 32768) : "memory")
#define PIN4(d) PIN(d[0]); PIN(d[1]); PIN(d[2]); PIN(d[3])
#define GO(d, rb_, W, NX, ...) wait_vm<W>(); TK(); PIN4(d); SB(); k3_step<NX, ##__VA_ARGS__>(ac, d, rb_, va, ua)
        TK();
        LA(a0, 0); LA(a1, 1);
        const bool ext = (wid & 6) == 4;
        GO(a0, rb0, 4, 2, true);
        pin5(rb2); pin5(rb3); pin5(rb4); pin5(rb5);
        GO(a1, rb1, 4, 3);
        asm volatile("s_mov_b32 m0, %2\n\ts_nop 0\n\tglobal_load_lds_dword %0, %1 sc1" ::"v"(relane() * 4), "s"(FLr + 32), "s"(lds0 + TOPL + 64 + wid * 256));
        GO(a0, rb2, 5, 4);
        GO(a1, rb3, 5, 5); LB(rb0, 10);
        GO(a0, rb4, 9, 6); k3_lb_if(rb1, va, va5, wb + 11 * 36864 - 4 * 4608, wid >> 1, 2); RB(rb2, 0);
        const int l2 = relane();
        const unsigned fo = *(const volatile LAS unsigned*)(lds + TOPL + 64 + wid * 256 + l2 * 4);
        SB();
        GO(a1, rb5, 9, 7); LA(a2, 8); RB(rb3, 1); SB();
        if (__builtin_expect(!__all(fo == ((l2 & 1) ? (unsigned)(epoch >> 32) : (unsigned)epoch)), 0)) {
            const unsigned long long t0 = __builtin_amdgcn_s_memrealtime();
            u64 f2; do { f2 = __hip_atomic_load(FLr + l2, RLX_AGENT); } while (!__all(f2 == epoch) && __builtin_amdgcn_s_memrealtime() - t0 < 5000000ull);
        }
        GO(a0, rb2, 8, 9); LA(a3, 10); RB(rb4, 2); SB();
        asm volatile("s_cmp_lg_u32 %9, 2\n\ts_cbranch_scc1 .Lla%=\n\t" LA4 ".Lla%=:" LAO(a4, 11 * 32768 - 4 * 4096), "s"(wid >> 1) : "memory", "scc");
        GO(a1, rb3, 12, -1); RB(rb5, 3); SB();
        wait_vm<0>(); TK(); PIN4(a0); PIN4(a2); PIN4(a3); PIN4(a4); pin5(rb0); pin5(rb1);
        if (ext) k3_step<-1>(ac, a4, rb1, va, ua);
        const int l3 = relane(), r16 = l3 & 15, g = l3 >> 4;
        LAS char* pw = (LAS char*)lds + va;
        SB();
#pragma unroll
        for (int m = 0; m < 4; ++m)
#pragma unroll
            for (int q = 0; q < 15; ++q) {
                if (q < 5) K3MM(ac[m][q], rb4.b[q], a2[m]); else if (q < 10) K3MM(ac[m][q - 5], rb0.b[q - 5], a3[m]); else K3MM(ac[m][q - 10], rb5.b[q - 10], a0[m]);
                SB();
                if (m > 0 && q % 3 == 0) { const int n = q / 3; if (n < 4 || g < 2) *(LAS f32x4*)(pw + so[m - 1] + n * 1024) = ac[m - 1][n]; SB(); }
            }
        asm volatile("s_nop 7\n\ts_nop 7\n\ts_waitcnt lgkmcnt(0)" ::: "memory");
        TK();
        STAMP(2, 2);
        const LAS char* pr = lds + va;
#define PART(k, n) *(const LAS f32x4*)(pr + k3_so((wid & 4) + ((wid + 3 - (k)) & 3), k) + (n) * 1024)
        if (wid >= 4) {
            __builtin_amdgcn_s_setprio(EPRIO);
            if (l3 == 0) __hip_atomic_fetch_add((LAS unsigned*)SY + 4, 1u, __ATOMIC_RELAXED, __HIP_MEMORY_SCOPE_WORKGROUP);
            while (SY[4] < 4) __builtin_amdgcn_s_sleep(1);
            SB();
#pragma unroll
            for (int n = 0; n < 5; ++n) {
                const f32x4 p0 = PART(0, n), p1 = PART(1, n), p2 = PART(2, n);
#pragma unroll
                for (int e = 0; e < 4; ++e) ac[3][n][e] = fadd1(fadd1(ac[3][n][e], p0[e]), fadd1(p1[e], p2[e]));
            }
#pragma unroll
            for (int n = 0; n < 5; ++n) if (n < 4 || g < 2) *(LAS f32x4*)(pw + so[3] + n * 1024) = ac[3][n];
        }
        asm volatile("s_waitcnt lgkmcnt(0)" ::: "memory"); TK();
        asm volatile("s_barrier" ::: "memory"); TK();
        if (wid < 4) {
            u32x2 o[5];
#pragma unroll
            for (int n = 0; n < 5; ++n) {
                const f32x4 r = ((ac[3][n] + PART(0, n)) + (PART(1, n) + PART(2, n))) + *(const LAS f32x4*)(pr + k3_so(wid + 4, 3) + n * 1024);
                o[n] = u32x2{cvtpk(r[0], r[1]), cvtpk(r[2], r[3])};
            }
            u16* op = out + xo + r16 * D_;
#pragma unroll
            for (int n = 0; n < 4; n += 2) {
                auto r0 = __builtin_amdgcn_permlane16_swap(o[n][0], o[n + 1][0], false, false); auto r1 = __builtin_amdgcn_permlane16_swap(o[n][1], o[n + 1][1], false, false);
                *(u32x4*)(op + (n + (g & 1)) * 16 + (g >> 1) * 8) = u32x4{r0[0], r1[0], r0[1], r1[1]};
            }
            if (g < 2) *(u32x2*)(op + 64 + g * 4) = o[4];
        }
    }
    STAMP_DRAIN(); STAMP(2, 3);
}
__global__ __launch_bounds__(512, 2) void fused_kernel(const u16* __restrict__ x, const u16* __restrict__ wq, const u16* __restrict__ wk, const u16* __restrict__ wv,
                                                       const u16* __restrict__ w_in, const u16* __restrict__ w_sp, const u16* __restrict__ b_sp, const u16* __restrict__ w_out,
                                                       const u16* __restrict__ wo, char* __restrict__ ws, u16* __restrict__ out) {
    const u64 epoch = phase1(x, wq, wk, wv, w_in, w_sp, b_sp, w_out, wo, ws);
    phase2(w_sp, b_sp, x, ws, out, epoch, __builtin_amdgcn_readfirstlane(threadIdx.x >> 6));
}
__global__ void text_end_marker() { asm volatile(".Lk3_end:"); }

extern "C" void kernel_launch(void* const* d_in, const int* in_sizes, int n_in, void* d_out, int out_size, void* d_ws, size_t ws_size, hipStream_t stream) {
    const u16* x = (const u16*)d_in[0]; const u16* wq = (const u16*)d_in[1]; const u16* wk = (const u16*)d_in[2]; const u16* wv = (const u16*)d_in[3];
    const u16* wo = (const u16*)d_in[4]; const u16* w_in = (const u16*)d_in[6]; const u16* w_sp = (const u16*)d_in[7]; const u16* b_sp = (const u16*)d_in[8];
    const u16* w_out = (const u16*)d_in[9]; char* ws = (char*)d_ws;
    (void)hipFuncSetAttribute((const void*)fused_kernel, hipFuncAttributeMaxDynamicSharedMemorySize, LDS_ALL);
    fused_kernel<<<256, 512, LDS_ALL, stream>>>(x, wq, wk, wv, w_in, w_sp, b_sp, w_out, wo, ws, (u16*)d_out);
}
```

```cpp
#define DIAG 99
#include <hip/hip_runtime.h>
#include <hip/hip_bf16.h>
#include <stdint.h>

#define STNOP 1
typedef unsigned short u16;
typedef unsigned long long u64;
typedef short bf16x8 __attribute__((ext_vector_type(8)));
typedef short s16x4 __attribute__((ext_vector_type(4)));
typedef float f32x2 __attribute__((ext_vector_type(2)));
typedef float f32x4 __attribute__((ext_vector_type(4)));
typedef float f32x16 __attribute__((ext_vector_type(16)));
typedef unsigned u32x4 __attribute__((ext_vector_type(4)));
typedef unsigned u32x2 __attribute__((ext_vector_type(2)));
typedef __bf16 bf16x2_t __attribute__((ext_vector_type(2)));
#define LAS __attribute__((address_space(3)))
#define DI __device__ __forceinline__

constexpr int Z1L = 122880, TOPL = 159744, LDS_ALL = 163840;
constexpr int K2_SA = 16384, K2_SB = 18432, K2_SK = 6144, K2_B0 = 3 * K2_SA, K2_K0 = K2_B0 + 3 * K2_SB, K2_X0 = 147520, K2_LDS = K2_X0 + 9344 + 64 + 2048;
constexpr int S_ = 2048, D_ = 576, FH = 2304, F_ = 4608;

constexpr size_t WS_Q = 0;
constexpr size_t WS_KV = WS_Q + (size_t)S_ * D_ * 2;
constexpr size_t WS_Z1 = WS_KV + 64 * 3072;
constexpr size_t WS_Z2T = WS_Z1 + (size_t)S_ * FH * 2;
constexpr size_t WS_UO = WS_Z2T + (size_t)S_ * FH * 2;
constexpr size_t WS_ST = WS_UO + (size_t)S_ * 2880 * 2;
constexpr size_t WS_XCH = WS_ST + (size_t)S_ * 16 * 8;
constexpr size_t WS_XR = WS_XCH + 4096 + 16384;
constexpr size_t WS_FL = WS_XR + 4096;
constexpr size_t WS_FA = WS_FL + 16384;
constexpr size_t WS_FJ = WS_FA + 2048;
constexpr size_t WS_FP = WS_FJ + 8192;
constexpr size_t WS_W3T = WS_FP + 2048;
constexpr size_t WS_DBG = 128u << 20;
#define STAMP(kid, i) do { if (DIAG == (kid) * 4 + (i) && threadIdx.x == 0) { extern __shared__ __attribute__((aligned(16))) char smem[]; *(volatile LAS u64*)((LAS char*)smem + TOPL + 2568) = __builtin_amdgcn_s_memrealtime(); } } while (0)
#define STAMP_DRAIN()
#define STAMPT(tid, kid, i)

DI unsigned cvtpk(float lo, float hi) { f32x2 v = {lo, hi}; return __builtin_bit_cast(unsigned, __builtin_convertvector(v, bf16x2_t)); }
DI float bf2f(u16 b) { return __uint_as_float(((unsigned)b) << 16); }
DI float bflo(unsigned w) { return __uint_as_float(w << 16); }
DI float bfhi(unsigned w) { return __uint_as_float(w & 0xffff0000u); }
#define SB() __builtin_amdgcn_sched_barrier(0)
#define PIN(x) asm volatile("" : "+v"(x))
#define EX(x) __builtin_amdgcn_exp2f(x)
#define MFMA16(a, b, c) __builtin_amdgcn_mfma_f32_16x16x32_bf16(a, b, c, 0, 0, 0)
#define MFMA32(a, b, c) __builtin_amdgcn_mfma_f32_32x32x16_bf16(a, b, c, 0, 0, 0)

DI s16x4 trread(const LAS char* p) { return __builtin_bit_cast(s16x4, __builtin_amdgcn_ds_read_tr16_b64_v4i16((LAS s16x4*)p)); }
template <int N> DI void wait_vm_bar() { asm volatile("s_waitcnt vmcnt(%0) lgkmcnt(0)\n\ts_barrier" ::"n"(N) : "memory"); }
template <int N> DI void wait_vm() { asm volatile("s_waitcnt vmcnt(%0)" ::"n"(N) : "memory"); }
template <int OFF> DI void glds_s(unsigned voff, const char* sbase) { asm volatile("global_load_lds_dwordx4 %0, %1 offset:%2" ::"v"(voff), "s"(sbase), "n"(OFF)); }
template <int OFF> DI void glds_sc1(unsigned voff, const char* sbase) { asm volatile("global_load_lds_dwordx4 %0, %1 offset:%2 sc1" ::"v"(voff), "s"(sbase), "n"(OFF)); }
DI void glds_v(const char* p, unsigned m0v, u64 em) { asm volatile("s_mov_b32 m0, %1\n\ts_mov_b64 exec, %2\n\tglobal_load_lds_dwordx4 %0, off\n\ts_mov_b64 exec, -1" ::"v"(p), "s"(m0v), "s"(em)); }
DI unsigned ld_sc1_async(const char* sb, unsigned vo) { unsigned v; asm volatile("global_load_dword %0, %1, %2 sc1" : "=v"(v) : "v"(vo), "s"(sb) : "memory"); return v; }
#define WT
DI void st16(void* p, u32x4 v) { asm volatile("global_store_dwordx4 %0, %1, off sc1\n\ts_nop 1" :: "v"(p), "v"(v) : "memory"); }
DI void st8(void* p, u32x2 v) { asm volatile("global_store_dwordx2 %0, %1, off sc1" :: "v"(p), "v"(v) : "memory"); }
template <int OFF> DI void st16s(const char* sb, unsigned vo, u32x4 v) { asm volatile("global_store_dwordx4 %0, %1, %2 offset:%3 sc1\n\ts_nop %4" :: "v"(vo), "v"(v), "s"(sb), "n"(OFF), "n"(STNOP) : "memory"); }
template <int OFF> DI void st8s(const char* sb, unsigned vo, u32x2 v) { asm volatile("global_store_dwordx2 %0, %1, %2 offset:%3 sc1" :: "v"(vo), "v"(v), "s"(sb), "n"(OFF) : "memory"); }
DI float fadd1(float a, float b) { float r = a + b; asm("" : "+v"(r)); return r; }
DI float fmul1(float a, float b) { float r = a * b; asm("" : "+v"(r)); return r; }
DI float ffma1(float a, float b, float c) { float r = __builtin_fmaf(a, b, c); asm("" : "+v"(r)); return r; }
DI float lone(float a) { asm("" : "+v"(a)); return a; }
DI void set_m0(unsigned v) { asm volatile("s_mov_b32 m0, %0\n\ts_nop 0" ::"s"(v)); }
DI bf16x8 ldsA(const LAS char* p) { return *(const LAS bf16x8*)p; }
DI bf16x8 ldsB2(const LAS char* p0, const LAS char* p1) { s16x4 lo = trread(p0), hi = trread(p1); return __builtin_shufflevector(lo, hi, 0, 1, 2, 3, 4, 5, 6, 7); }
DI bf16x8 ldsB(const LAS char* p) { return ldsB2(p, p + 256); }

DI float gelu_t(float x) {
    const float c0 = -2.0f * 1.4426950408889634f * 0.7978845608028654f;
    const float c1 = c0 * 0.044715f;
    float x2 = x * x;
    float t = __builtin_fmaf(x2, c1, c0);
    float e = __builtin_amdgcn_exp2f(x * t);
    return x * __builtin_amdgcn_rcpf(1.0f + e);
}
constexpr float QSCALE = 0.23570226039551584f * 1.4426950408889634f;

#define KO1 (3 * (((idx & 7) + (idx >> 3)) % 3))
#define KO2 (((idx & 7) + 2 * (idx >> 3) + 4 * (xcd & 1)) % 9)
#define K2_KO (8 * (xcd >> 1))
#define EPRIO 1
#define PFW 0
#define SPRIO 1
#define HW 3
#define ZSCH 5
#define ZS0 1
#define TFA 8
#define PW2 7
#define PEARLY 0
#define POLLSLEEP 0
#define APRIO 1
#define K3MAP 2
#define K3ROT 0
#define APRE 0
#define AGSLOT 1
#define ZHALF 1
#define POLLW 0
#define POLLSLEEP1 1
#define PLSLOT 0
constexpr int K1_SA = 16384, K1_SB = 24576, K1_B0 = 3 * K1_SA, K1_TAB = K1_B0 + 3 * K1_SB, K1_X0 = K1_TAB + 10240, K1_LDS = K1_X0 + 16384 + 2048;
typedef __attribute__((address_space(1))) unsigned gu32;
typedef __attribute__((address_space(1))) unsigned long long gu64;
#define RLX_AGENT __ATOMIC_RELAXED, __HIP_MEMORY_SCOPE_AGENT
struct K1F { bf16x8 a[4], b[3], x; };
struct K1S {
    unsigned va[2], vb[2]; const char* sa; const char* sb;
    const char* gp; unsigned st;
    int k1, k2;
    unsigned m0a, m0b; u64 em;
    const LAS char* pa[2]; const LAS char* px[2]; const LAS char* pb[3];
};
DI void k1_issue(K1S& s, int i, int w) {
    if (i == 0) { set_m0(s.m0a + w * K1_SA); glds_s<0>(s.va[0], s.sa); }
    if (i == 1) { glds_s<1024>(s.va[1], s.sa); s.sa += s.k1 == 8 ? -8 * 128 : 128; asm volatile("" : "+s"(s.sa)); }
    if (i == 2) { glds_v(s.gp + (unsigned long long)s.st * (unsigned)s.k1, s.m0b + w * K1_SB + 2048, s.em); s.k1 = s.k1 == 8 ? 0 : s.k1 + 1; asm volatile("" : "+s"(s.k1)); }
    if (i == 3) { set_m0(s.m0b + w * K1_SB); glds_s<0>(s.vb[0], s.sb); }
    if (i == 4) { glds_s<1024>(s.vb[1], s.sb); s.sb += s.k2 == 8 ? -8 * 128 * F_ : 128 * F_; s.k2 = s.k2 == 8 ? 0 : s.k2 + 1; asm volatile("" : "+s"(s.sb), "+s"(s.k2)); }
}
DI f32x2 gelu_y2(f32x2 y, float k0, float k1, float ir) {
    f32x2 t = (y * y) * k1 + k0;
    f32x2 a = y * t;
    f32x2 e = {__builtin_amdgcn_exp2f(a[0]), __builtin_amdgcn_exp2f(a[1])};
    f32x2 d = e * ir + ir;
    f32x2 rc = {__builtin_amdgcn_rcpf(d[0]), __builtin_amdgcn_rcpf(d[1])};
    return y * rc;
}
DI float dpp_x1(float x) { return __int_as_float(__builtin_amdgcn_mov_dpp(__float_as_int(x), 0xB1, 0xf, 0xf, true)); }
DI float dpp_x2(float x) { return __int_as_float(__builtin_amdgcn_mov_dpp(__float_as_int(x), 0x4E, 0xf, 0xf, true)); }
DI void kv_store(const char* kv, int sv, int j, int c, int g, f32x4 v) {
    if (j == 4) { st8s<0>(kv + sv * 256, ((g >> 1) * 32 + c) * 16 + (g & 1) * 8, u32x2{cvtpk(v[0], v[1]), cvtpk(v[2], v[3])}); return; }
    const unsigned kk = cvtpk(v[2], v[3]);
    const bool o1 = c & 1, o2 = c & 2;
    float t0 = dpp_x1(o1 ? v[0] : v[1]), t1 = dpp_x1(o1 ? v[2] : v[3]);
    if (o1) { v[0] = t0; v[2] = t1; } else { v[1] = t0; v[3] = t1; }
    t0 = dpp_x2(o2 ? v[0] : v[2]); t1 = dpp_x2(o2 ? v[1] : v[3]);
    if (o2) { v[0] = t0; v[1] = t1; } else { v[2] = t0; v[3] = t1; }
    u32x2 w = {cvtpk(v[0], v[1]), cvtpk(v[2], v[3])};
    const int kq = c >> 2; unsigned off;
    if (j == 5) {
        off = g < 2 ? c * 16 + (g ^ 1) * 8 : 256 + ((kq & 1) * 8 + 4 * (g - 2) + (c & 3)) * 16 + (kq >> 1) * 8;
        if (g < 2) w = g ? u32x2{kk, 0x3f80u} : u32x2{0u, 0u};
    } else {
        off = 512 + ((kq & 1) * 16 + 4 * g + (c & 3)) * 16 + (kq >> 1) * 8;
        if (g == 3 && !o2) { const unsigned cv = o1 ? 0u : 0x3f803f80u; w = u32x2{cv, cv}; }
    }
    st8s<1024>(kv + sv * 1024, off, w);
}
template <int KS, int GL, int RS, int WS, bool RX> DI void k1_p1(K1S& s, const K1F& f, K1F& n, f32x4 (&acc)[4][2], f32x4& sacc, f32x4 (&cso)[2], f32x4& rs, f32x4& rq, const bf16x8& ones) {
    constexpr int AO = RS * K1_SA, BO = RS * K1_SB + KS * 12288;
#define MX(d, a_, b_) d = MFMA16(a_, b_, d); PIN(d); SB()
#define MM(m, nn) MX(acc[m][nn], f.b[nn], f.a[m])
    MM(0, 0); n.b[0] = ldsB(s.pb[0] + BO); SB();
    MM(1, 0); n.a[0] = ldsA(s.pa[KS] + AO); SB();
    MM(2, 0); n.a[1] = ldsA(s.pa[KS] + AO + 2048); SB();
    MM(3, 0); n.a[2] = ldsA(s.pa[KS] + AO + 4096); SB();
    MX(cso[0], f.b[0], ones); n.a[3] = ldsA(s.pa[KS] + AO + 6144); SB();
    MX(sacc, f.b[2], f.x); n.b[1] = ldsB(s.pb[1] + BO); SB();
    MX(cso[1], f.b[2], ones); n.b[2] = ldsB(s.pb[2] + BO); SB();
    MX(rs, ones, f.x); k1_issue(s, GL == 1 ? 0 : 3, WS); SB();
    MX(rq, f.x, f.x); if (RX) n.x = ldsA(s.px[KS] + AO); SB();
    MM(0, 1); k1_issue(s, GL == 1 ? 1 : 4, WS); SB();
    MM(1, 1); if (GL == 1) k1_issue(s, 2, WS); SB();
    MM(2, 1);
    MM(3, 1);
}
template <int KS, int GL, int RS, int WS, bool RD, class E> DI void k1_p2(K1S& s, const K1F& f, K1F& n, f32x4 (&acc)[4][3], f32x4 (&cso)[2], const bf16x8& ones, E epi) {
    constexpr int AO = RS * K1_SA, BO = RS * K1_SB + KS * 12288;
    MM(0, 0); if (RD) n.b[0] = ldsB(s.pb[0] + BO); epi(0); SB();
    MM(1, 0); if (RD) n.a[0] = ldsA(s.pa[KS] + AO); epi(1); SB();
    MM(2, 0); if (RD) n.a[1] = ldsA(s.pa[KS] + AO + 2048); epi(2); SB();
    MM(3, 0); if (RD) n.a[2] = ldsA(s.pa[KS] + AO + 4096); epi(3); SB();
    MX(cso[0], f.b[0], ones); if (RD) n.a[3] = ldsA(s.pa[KS] + AO + 6144); epi(4); SB();
    MM(0, 1); if (RD) n.b[1] = ldsB(s.pb[1] + BO); epi(5); SB();
    MM(1, 1); if (RD) n.b[2] = ldsB(s.pb[2] + BO); epi(6); SB();
    MM(2, 1); if (GL) k1_issue(s, GL == 1 ? 0 : 3, WS); epi(7); SB();
    MM(3, 1); epi(8); SB();
    MX(cso[1], f.b[1], ones); if (GL) k1_issue(s, GL == 1 ? 1 : 4, WS); epi(9); SB();
    MM(0, 2); epi(10); SB();
    MM(1, 2); if (GL == 1) k1_issue(s, 2, WS); epi(11); SB();
    MM(2, 2); epi(12); SB();
    MM(3, 2); epi(13); SB();
#undef MM
#undef MX
}

constexpr unsigned XSALT = 0x9e3779b9u;
DI void epoch_reply(char* ws, int lane, const unsigned (&nv)[4], u64 epoch) {
    const unsigned lo = (unsigned)epoch, hi = (unsigned)(epoch >> 32);
    st16s<0>(ws + WS_XR, lane * 16, u32x4{nv[0], lo, hi, nv[0] ^ lo ^ hi ^ XSALT}); st16s<1024>(ws + WS_XR, lane * 16, u32x4{nv[1], lo, hi, nv[1] ^ lo ^ hi ^ XSALT});
    st16s<2048>(ws + WS_XR, lane * 16, u32x4{nv[2], lo, hi, nv[2] ^ lo ^ hi ^ XSALT}); st16s<3072>(ws + WS_XR, lane * 16, u32x4{nv[3], lo, hi, nv[3] ^ lo ^ hi ^ XSALT});
}
#define XN_LOADS "global_load_dword %0, %4, %5 sc1\n\tglobal_load_dword %1, %4, %5 offset:256 sc1\n\tglobal_load_dword %2, %4, %5 offset:512 sc1\n\tglobal_load_dword %3, %4, %5 offset:768 sc1\n"
DI void epoch_service(char* ws, int lane, u64 epoch) {
    unsigned nv[4];
    asm volatile(XN_LOADS "\ts_waitcnt vmcnt(0)" : "=&v"(nv[0]), "=&v"(nv[1]), "=&v"(nv[2]), "=&v"(nv[3]) : "v"(lane * 4), "s"(ws + WS_XCH) : "memory");
    epoch_reply(ws, lane, nv, epoch);
}
DI unsigned g2_va(int wid, int lane, int i) { int row = 8 * (wid * 4 + i) + (lane >> 3), c = (lane & 7) ^ ((row >> 1) & 7); return row * (S_ * 2) + c * 16 + 4096 - i * 1024; }
DI u64 phase1(const u16* __restrict__ x, const u16* __restrict__ wq, const u16* __restrict__ wk, const u16* __restrict__ wv,
                                                    const u16* __restrict__ w_in, const u16* __restrict__ w_sp, const u16* __restrict__ b_sp, const u16* __restrict__ w_out, const u16* __restrict__ wo, char* __restrict__ ws) {
    extern __shared__ __attribute__((aligned(16))) char smem[];
    const int bid = blockIdx.x, xcd = bid & 7, idx = bid >> 3;
    const int pm = (xcd & 1) * 8 + (idx & 7), j = (xcd >> 1) * 4 + (idx >> 3);
    const int lane = threadIdx.x & 63, wid = __builtin_amdgcn_readfirstlane(threadIdx.x >> 6), wr = wid >> 2, wc = wid & 3;
    const unsigned lds0 = (unsigned)(uintptr_t)smem;
    const LAS char* lds = (const LAS char*)smem;
    {
        const char* pc; unsigned len;
        asm volatile("s_getpc_b64 %0\n.Lk1_here:\n\ts_mov_b32 %1, .Lk3_end-.Lk1_here" : "=s"(pc), "=s"(len));
        unsigned off = ((idx * 8 + wid) * 2 + (lane & 1)) * 128; off = off < len ? off : len;
        asm volatile("s_mov_b32 m0, %2\n\ts_nop 0\n\tglobal_load_lds_dword %0, %1" ::"v"(off), "s"(pc), "s"(lds0 + K1_TAB + 9216 + 512));
    }
    K1S s; s.em = (wid & 1) ? 0xccccccccull : 0x33333333ull;
    s.m0a = lds0 + wid * 2048; s.m0b = lds0 + K1_B0 + wid * 3072;
    const int brow = (lane >> 2) & 7, bc = (lane & 3) ^ (2 * (wid & 1)), bk = 8 * wid + brow;
    const int ko2 = KO2, ko1 = KO1;
    unsigned oa = pm * 128 * D_ * 2, ob = 256 * j, oa1 = oa + ko1 * 128, ob1 = ob + FH * 2 + ko1 * 128 * F_, vg = bk * (F_ * 2) + 16 * (bc & 1);
    {
        int r8 = lane >> 3;
        s.va[0] = (16 * wid + r8) * (D_ * 2) + (((lane & 7) ^ (r8 >> 1)) * 16);
        s.va[1] = (16 * wid + 8 + r8) * (D_ * 2) + (((lane & 7) ^ (4 + (r8 >> 1))) * 16) - 1024;
        s.vb[0] = bk * (F_ * 2) + (lane >> 5) * 64 + bc * 16 + 1024; s.vb[1] = s.vb[0] + 128 - 1024;
        s.k1 = s.k2 = ko1; s.st = 128 * F_;
        PIN(s.va[0]); PIN(s.va[1]); PIN(s.vb[0]); PIN(s.vb[1]); PIN(vg);
        asm volatile("" : "+s"(oa1), "+s"(ob1), "+s"(s.m0a), "+s"(s.m0b));
    }
    SB();
    s.sa = (const char*)x + oa1; s.sb = (const char*)w_in - 1024 + ob1;
    k1_issue(s, 0, 0); k1_issue(s, 1, 0); k1_issue(s, 3, 0); k1_issue(s, 4, 0);
    SB();
    STAMP(0, 0);
    s.gp = (const char*)w_in + (4096 + 32 * j + FH * 2) + vg;
    k1_issue(s, 2, 0);
    const char* sa0 = (const char*)x + oa;
    const char* sb0 = (const char*)w_in - 1024 + ob;
    gu32* XN = (gu32*)(ws + WS_XCH) + pm * 16;
    gu32* XE = (gu32*)(ws + WS_XCH + 4096) + pm * 256;
    gu64* XP = (gu64*)(ws + WS_ST) + (size_t)pm * 16 * 128;
    const char* xe_s = ws + WS_XCH + 4096 + (pm * 256 + j * 16) * 4;
    const unsigned le = (lane & 16) ? 16384 - (pm * 16 + j) * 48 + (lane & 3) * 4 : (lane & 15) * 4;
    unsigned eo = ld_sc1_async(xe_s, le);
    if (SPRIO == 1 && wid >= 4) __builtin_amdgcn_s_setprio(1);
    SB();
    unsigned nonce = 0;
    const char* gp2; unsigned st2;
    {
        const u16* p; int rs;
        const u16* sp0 = j < 4 ? wq + 512 + 16 * j : (j == 5 ? wk + 10 : (j == 6 ? wv + 8 : wk));
        const u16* sp1 = j < 4 ? wq + 520 + 16 * j : (j == 5 ? wv : (j == 6 ? wv + 10 : wk + 8));
        const int srs = j < 4 ? D_ : 18;
        if (lane >= 32) { p = wq + 32 * j + 8 * bc; rs = D_; }
        else if (bc < 2) { p = w_in + 2048 + 16 * j + 8 * bc; rs = F_; }
        else { p = bc == 2 ? sp0 : sp1; rs = srs; }
        gp2 = (const char*)(p + (size_t)bk * rs); st2 = 128 * rs; PIN(gp2); PIN(st2);
    }
    const int r16 = lane & 15, g = lane >> 4, cg = wc ^ (2 * wr);
    const int nf3 = cg == 3 ? 8 : (cg == 2 ? 9 : 10 + cg);
#define NF1(n) ((n) == 2 ? 8 : 2 * wc + ((n) ^ wr))
#define NF2(n) ((n) == 0 ? nf3 : 2 * cg + (((n) - 1) ^ wr))
    const LAS char* pb2[3];
    {
        int q = r16 >> 2, p = lane & 3;
#pragma unroll
        for (int ks = 0; ks < 2; ++ks) { s.pa[ks] = lds + (r16 * 128 + (((4 * ks + g) ^ (r16 >> 1)) * 16) + wr * 8192); s.px[ks] = s.pa[ks] + wc * 2048; PIN(s.pa[ks]); PIN(s.px[ks]); }
#pragma unroll
        for (int n = 0; n < 3; ++n) {
            int nf = NF1(n); s.pb[n] = lds + (K1_B0 + g * 3072 + q * 64 + 8 * p + (nf >> 1) * 512 + (((nf ^ g) & 1) * 32)); PIN(s.pb[n]);
            nf = NF2(n); pb2[n] = lds + (K1_B0 + g * 3072 + q * 64 + 8 * p + (nf >> 1) * 512 + (((nf ^ g) & 1) * 32)); PIN(pb2[n]);
        }
    }
    f32x4 acc1[4][2] = {}; f32x4 sacc = {}; f32x4 cso[2] = {};
    f32x4 rs = {}, rq = {};
#pragma unroll
    for (int m = 0; m < 4; ++m) { PIN(acc1[m][0]); PIN(acc1[m][1]); }
    PIN(sacc); PIN(cso[0]); PIN(cso[1]); PIN(rs); PIN(rq);
    const u32x4 onesv = {0x3f803f80u, 0x3f803f80u, 0x3f803f80u, 0x3f803f80u}; bf16x8 ones = __builtin_bit_cast(bf16x8, onesv); PIN(ones);
    K1F f0, f1;
    SB();
#pragma unroll
    for (int i = 0; i < 5; ++i) k1_issue(s, i, 1);
    SB();
    if (SPRIO == 2 && wid >= 4) __builtin_amdgcn_s_setprio(1);
    const int w3p = bid < 240 ? bid : 239;
    unsigned w3vo[2], w3so[2]; const char* w3sp; const LAS char* w3rd[2];
    {
        const int jj = w3p / 30, pc = w3p - jj * 30;
        w3sp = (pc < 24 ? (const char*)w_out : (const char*)wo - 2304 * 1152) + jj * 144;
#pragma unroll
        for (int i = 0; i < 2; ++i) {
            unsigned c_ = threadIdx.x + 512 * i; c_ = c_ < 864 ? c_ : 863;
            const unsigned row = (c_ * 7282) >> 16, k_ = pc * 96 + row, jg = (k_ * 29128) >> 22, l_ = k_ - jg * 144;
            const unsigned src = pc < 24 ? (l_ < 128 ? 128 * jg + l_ : 1920 + 16 * jg + l_) : k_;
            w3vo[i] = src * 1152 + (c_ - row * 9) * 16; PIN(w3vo[i]);
            const int u = i ? (wid < 7 ? wid + 8 : 14) : wid, k = u / 5, n = u - 5 * k;
            w3rd[i] = lds + (K1_X0 + (8 * g + (r16 >> 2)) * 144 + 8 * (lane & 3) + k * 4608 + n * 32); PIN(w3rd[i]);
            w3so[i] = w3p * 13824 + k * 4608 + n * 1024 + (n < 4 ? lane : g * 8 + r16) * 16;
            if (n == 4 && r16 >= 8) w3so[i] = 240 * 13824 + bid * 512 + (g * 8 + r16 - 8) * 16;
            PIN(w3so[i]);
        }
        asm volatile("" : "+s"(w3sp));
    }
    unsigned wspo = (pm * 128 + (idx >> 3) * 32 + wid * 4 + g) * (S_ * 2) + r16 * 32; PIN(wspo);
    LAS float* redp = (LAS float*)(lds + K1_TAB + 4096) + ((wr * 64 + r16) * 5 + wc) * 2 + (g & 1); PIN(redp);
    LAS float* reds = (LAS float*)(lds + K1_TAB + 4096) + ((wr * 64 + wc * 16 + r16) * 5 + 4) * 2 + (g & 1); PIN(reds);
    SB();
#pragma unroll
    for (int i = 0; i < 3; ++i) k1_issue(s, i, 2);
    __builtin_amdgcn_s_setprio(0);
    SB();
    wait_vm_bar<8>();
    SB();
    f0.b[0] = ldsB(s.pb[0]);
    f0.a[0] = ldsA(s.pa[0]); f0.a[1] = ldsA(s.pa[0] + 2048); f0.a[2] = ldsA(s.pa[0] + 4096); f0.a[3] = ldsA(s.pa[0] + 6144);
    f0.b[2] = ldsB(s.pb[2]); f0.x = ldsA(s.px[0]); f0.b[1] = ldsB(s.pb[1]);
    SB();
    PIN(eo);
    if (wid == HW) {
        nonce = 1;
        while (__ballot(eo == nonce) != 0ull) ++nonce;
        if (lane == 0) __hip_atomic_store(XN + j, nonce, RLX_AGENT);
        __hip_atomic_store(XP + j * 128 + lane, ~0ull, RLX_AGENT); __hip_atomic_store(XP + j * 128 + 64 + lane, ~0ull, RLX_AGENT);
    }
    STAMP(0, 1);
#define P1_A(c) k1_p1<1, 2, (c), ((c) + 2) % 3, true>(s, f0, f1, acc1, sacc, cso, rs, rq, ones); wait_vm_bar<5>(); SB()
#define P1_B(c, RX) k1_p1<0, 1, ((c) + 1) % 3, (c), RX>(s, f1, f0, acc1, sacc, cso, rs, rq, ones)
#pragma unroll 1
    for (int it = 0; it < 2; ++it) { P1_A(0); P1_B(0, true); P1_A(1); P1_B(1, true); P1_A(2); P1_B(2, true); }
    const char* xn_s = ws + WS_XCH + pm * 64; const unsigned l4 = (lane & 15) * 4;
    P1_A(0);
    unsigned echoed = ld_sc1_async(xn_s, l4);
    unsigned alln[4];
    asm volatile("s_cmp_lg_u32 %6, 0\n\ts_cbranch_scc1 .Lld%=\n\t" XN_LOADS ".Lld%=:" : "=&v"(alln[0]), "=&v"(alln[1]), "=&v"(alln[2]), "=&v"(alln[3])
                 : "v"(lane * 4), "s"(ws + WS_XCH), "s"(bid) : "memory", "scc");
    s.k1 = s.k2 = ko2; s.sa = sa0 + ko2 * 128; s.sb = sb0 + (size_t)ko2 * 128 * F_; s.gp = gp2; s.st = st2; s.em = ~0ull; SB();
    P1_B(0, true);
    P1_A(1);
    PIN(echoed);
    if (wid == HW && lane < 16) __hip_atomic_store(XE + lane * 16 + j, echoed, RLX_AGENT);
    PIN(alln[0]); PIN(alln[1]); PIN(alln[2]); PIN(alln[3]);
    if (bid == 0 && wid == HW - 1) epoch_reply(ws, lane, alln, __builtin_amdgcn_s_memrealtime() | (1ull << 63));
    SB();
    P1_B(1, true);
    P1_A(2);
    s.pb[0] = pb2[0]; s.pb[1] = pb2[1]; s.pb[2] = pb2[2];
    P1_B(2, false);
    STAMP(3, 0);
    float* CS = (float*)(smem + K1_TAB);
    f32x4* RS = (f32x4*)(smem + K1_TAB + 2048);
    float* red = (float*)(smem + K1_TAB + 4096);
    f32x2* ZS = (f32x2*)(smem + K1_TAB + 9216);
    volatile LAS unsigned* HS = (volatile LAS unsigned*)(lds + K1_TAB + 1792);
    const float c0 = -2.0f * 1.4426950408889634f * 0.7978845608028654f, c1 = c0 * 0.044715f;
    f32x4 acc2[4][3] = {}; f32x4 cs2[2] = {};
    auto none = [](int) {};
    auto tabs = [&](int st) {
        if (st == 2) { if (r16 == 0) { *(f32x4*)(CS + NF1(0) * 16 + 4 * g) = cso[0]; *(f32x4*)(CS + 128 + 4 * g) = cso[1]; } }
        if (st == 3) {
            float sq = (r16 & 2) ? ((r16 & 1) ? rq[3] : rq[2]) : ((r16 & 1) ? rq[1] : rq[0]);
            float r0_ = rs[0]; PIN(r0_); PIN(sq);
            float mean = fmul1(r0_, 1.0f / D_), var = fmaxf(ffma1(-mean, mean, fmul1(sq, 1.0f / D_)), 0.f);
            rs[0] = mean; rs[1] = var + 1e-5f; PIN(rs);
        }
        if (st == 5) { rs[2] = __builtin_amdgcn_rsqf(rs[1]); PIN(rs); }
        if (st == 7) { float rstd = rs[2]; if (g == (r16 >> 2)) RS[wr * 64 + wc * 16 + r16] = f32x4{rs[0], fmul1(rstd, c0), fmul1(fmul1(rstd, rstd), fmul1(rstd, c1)), fmul1(rs[1], rstd)}; }
    };
    f32x4 er[5], cs1[3], e[3];
    f32x2 su[5], sq[5];
#define ZU(u) ((u) == 8 ? sacc : acc1[((u) >> 1) & 3][(u) & 1])
#define UM(u) ((u) == 8 ? 4 : (u) >> 1)
#define UN(u) ((u) == 8 ? 2 : (u) & 1)
#define PRE(i, u) { f32x4 y = ZU(u) - cs1[UN(u)] * er[UM(u)][0]; e[i] = ((y * y) * er[UM(u)][2] + er[UM(u)][1]) * y; ZU(u) = y; PIN(ZU(u)); PIN(e[i]); }
#define FIN(i, u, first) { ZU(u) *= e[i]; PIN(ZU(u)); f32x4 v = ZU(u); f32x2 lo = {v[0], v[1]}, hi = {v[2], v[3]}; \
        if (first) { su[UM(u)] = lo + hi; sq[UM(u)] = lo * lo + hi * hi; } else { su[UM(u)] += lo; su[UM(u)] += hi; sq[UM(u)] += lo * lo; sq[UM(u)] += hi * hi; } PIN(su[UM(u)]); PIN(sq[UM(u)]); }
#define RED(m) { float a_ = su[m][0] + su[m][1], b_ = sq[m][0] + sq[m][1]; \
            auto x1 = __builtin_amdgcn_permlane16_swap(__float_as_uint(a_), __float_as_uint(b_), false, false);       \
            float t_ = __uint_as_float(x1[0]) + __uint_as_float(x1[1]);                                               \
            auto x2 = __builtin_amdgcn_permlane32_swap(__float_as_uint(t_), __float_as_uint(t_), false, false); \
            t_ = __uint_as_float(x2[0]) + __uint_as_float(x2[1]);                                                     \
            if ((m) < 4) redp[(m) * 160] = t_; else reds[0] = t_; }
#define T1(op, v) asm volatile(op " %0, %0" : "+v"(v))
#define DE1(v, sd) asm volatile("v_fma_f32 %0, %0, %1, %1" : "+v"(v) : "v"(sd))
#define EPI_E(u0, u1, u2) [&](int st) { \
        if (st < 12) { float t_ = e[st >> 2][st & 3]; T1("v_exp_f32", t_); e[st >> 2][st & 3] = t_; } \
        if (st >= 2) { const int v_ = st - 2; float t_ = e[v_ >> 2][v_ & 3]; DE1(t_, er[v_ < 4 ? UM(u0) : (v_ < 8 ? UM(u1) : UM(u2))][3]); e[v_ >> 2][v_ & 3] = t_; } }
#define EPI_R(m0, m1) [&](int st) { if (st < 12) { float t_ = e[st >> 2][st & 3]; T1("v_rcp_f32", t_); e[st >> 2][st & 3] = t_; } \
        if (st == 12 && (m0) >= 0) er[(m0) & 3] = RS[wr * 64 + (m0) * 16 + r16]; if (st == 13 && (m1) >= 0) er[(m1) & 3] = RS[wr * 64 + (m1) * 16 + r16]; }
#define VSYNC() SB(); asm volatile("s_waitcnt lgkmcnt(0)\n\ts_barrier" ::: "memory"); SB()
#define P2_A(c, GL, E) k1_p2<1, GL ? 2 : 0, (c), ((c) + 2) % 3, true>(s, f0, f1, acc2, cs2, ones, E)
#define P2_B(c, GL, RD, E) k1_p2<0, GL ? 1 : 0, ((c) + 1) % 3, (c), RD>(s, f1, f0, acc2, cs2, ones, E)
    P2_A(0, true, tabs); wait_vm_bar<5>(); SB();
    er[0] = RS[wr * 64 + r16]; er[4] = RS[wr * 64 + wc * 16 + r16];
#pragma unroll
    for (int n = 0; n < 3; ++n) cs1[n] = *(const f32x4*)(CS + NF1(n) * 16 + 4 * g);
    PRE(0, 0) PRE(1, 1) PRE(2, 8) VSYNC();
    P2_B(0, true, true, EPI_E(0, 1, 8));
    P2_A(1, true, EPI_R(1, 2)); wait_vm_bar<5>(); SB();
    unsigned ev = ld_sc1_async(xe_s, le);
    auto w3ld = [&](int i) { SB(); set_m0(lds0 + K1_X0 + i * 8192 + wid * 1024); glds_s<0>(w3vo[i], w3sp); SB(); };
    w3ld(0); FIN(0, 0, true) FIN(1, 1, false) FIN(2, 8, true) PRE(0, 2) PRE(1, 3) PRE(2, 4) w3ld(1); RED(0) RED(4) VSYNC();
    P2_B(1, true, true, EPI_E(2, 3, 4));
    P2_A(2, true, EPI_R(3, -1)); wait_vm_bar<7>(); SB();
    PIN(ev);
    bool hs = false; u64 epoch = 0;
    auto xcheck = [&]() {
        hs = hs || __all((lane & 16) || ev == nonce);
        const unsigned w0 = __builtin_amdgcn_readlane(ev, 16), w1 = __builtin_amdgcn_readlane(ev, 17), w2 = __builtin_amdgcn_readlane(ev, 18), w3 = __builtin_amdgcn_readlane(ev, 19);
        if (w0 == nonce && w3 == (w0 ^ w1 ^ w2 ^ XSALT)) epoch = ((u64)w2 << 32) | w1;
    };
    unsigned nn = ld_sc1_async(xn_s, l4);
    asm volatile("s_mov_b32 m0, %2\n\ts_nop 0\n\tglobal_load_lds_dword %0, %1" ::"v"(wspo), "s"(w_sp), "s"(lds0 + K1_X0 + 16384));
    SB(); FIN(0, 2, true) FIN(1, 3, false) FIN(2, 4, true) PRE(0, 5) PRE(1, 6) PRE(2, 7) SB();
    asm volatile("s_mov_b32 m0, %2\n\ts_nop 0\n\tglobal_load_lds_dword %0, %1" ::"v"(lane * 4), "s"(b_sp + pm * 128), "s"(lds0 + TOPL + 2304));
    SB(); RED(1) VSYNC();
    P2_B(2, true, true, EPI_E(5, 6, 7));
    P2_A(0, true, EPI_R(-1, -1)); wait_vm_bar<8>(); SB();
    {
        const bf16x8 w0 = ldsB2(w3rd[0], w3rd[0] + 576), w1 = ldsB2(w3rd[1], w3rd[1] + 576);
        SB(); FIN(0, 5, false) FIN(1, 6, true) FIN(2, 7, false) SB();
        st16s<0>(ws + WS_W3T, w3so[0], __builtin_bit_cast(u32x4, w0));
        SB(); RED(2) RED(3) SB();
        st16s<0>(ws + WS_W3T, w3so[1], __builtin_bit_cast(u32x4, w1));
        VSYNC();
    }
    f32x2 pu[5];
    auto pub = [&](int st) {
        if (st == 0) {
#pragma unroll
            for (int i = 0; i < 5; ++i) pu[i] = *(const f32x2*)(red + (wid * 16 + r16) * 10 + 2 * i);
        }
        if (st == 4) {
            float a = fadd1(fadd1(fadd1(lone(pu[0][0]), pu[1][0]), fadd1(lone(pu[2][0]), pu[3][0])), pu[4][0]), b = fadd1(fadd1(fadd1(pu[0][1], pu[1][1]), fadd1(pu[2][1], pu[3][1])), pu[4][1]);
            unsigned bb = __float_as_uint(b); if (bb == 0xffffffffu) bb = 0x7fc00000u;
            st8s<0>((const char*)(XP + j * 128 + wid * 16), r16 * 8, u32x2{__float_as_uint(a), bb});
        }
        if (st == 13 && wid == HW) { xcheck(); *HS = hs; }
    };
    P2_B(0, true, true, pub);
    P2_A(1, true, none);
    STAMP(3, 1);
    wait_vm_bar<8>(); SB();
    ev = ld_sc1_async(xe_s, le);
    auto reecho = [&](int st) { if (st == 2) { PIN(nn); if (wid == HW && nn != echoed) { echoed = nn; if (lane < 16) __hip_atomic_store(XE + lane * 16 + j, nn, RLX_AGENT); } } };
    P2_B(1, true, true, reecho);
    const char* xp_s = ws + WS_ST + (size_t)pm * 16384 + wid * 128; const unsigned xp_v = g * 4096 + r16 * 8;
    u32x2 pv[4];
#define PLOAD() asm volatile("global_load_dwordx2 %0, %4, %5 sc1\n\tglobal_load_dwordx2 %1, %4, %5 offset:1024 sc1\n\tglobal_load_dwordx2 %2, %4, %5 offset:2048 sc1\n\tglobal_load_dwordx2 %3, %4, %5 offset:3072 sc1" \
        : "=&v"(pv[0]), "=&v"(pv[1]), "=&v"(pv[2]), "=&v"(pv[3]) : "v"(xp_v), "s"(xp_s) : "memory")
    auto pload = [&](int st) { if (st >= 10) asm volatile("global_load_dwordx2 %0, %1, %2 offset:%3 sc1" : "=&v"(pv[(st - 10) & 3]) : "v"(xp_v), "s"(xp_s), "n"(((st - 10) & 3) * 1024) : "memory"); };
    P2_A(2, true, pload); wait_vm_bar<9>(); SB();
#define PEMPTY() __any(pv[0][1] == 0xffffffffu || pv[1][1] == 0xffffffffu || pv[2][1] == 0xffffffffu || pv[3][1] == 0xffffffffu)
    const unsigned hsf = *HS;
    P2_B(2, true, true, [&](int st) { if (st == 13) { PIN(ev); if (wid == HW) xcheck(); } });
    float za, zb;
    auto zstat = [&](int st) {
        if (st == ZS0) {
            wait_vm<3>();
            PIN(pv[0]); PIN(pv[1]); PIN(pv[2]); PIN(pv[3]);
            if (__builtin_expect(hsf != 1 || PEMPTY(), 0)) {
                const unsigned long long t0 = __builtin_amdgcn_s_memrealtime();
                if (wid == HW && hsf != 1) {
                    do {
                        ev = ld_sc1_async(xe_s, le); wait_vm<0>(); PIN(ev);
                        xcheck();
                        nn = __hip_atomic_load(XN + (lane & 15), RLX_AGENT);
                        if (nn != echoed) { echoed = nn; if (lane < 16) __hip_atomic_store(XE + lane * 16 + j, nn, RLX_AGENT); }
                    } while (!hs && __builtin_amdgcn_s_memrealtime() - t0 < 5000000ull);
                    *HS = 2;
                }
                while (*HS == 0 && __builtin_amdgcn_s_memrealtime() - t0 < 6000000ull) __builtin_amdgcn_s_sleep(2);
                do {
                    PLOAD(); wait_vm<0>(); PIN(pv[0]); PIN(pv[1]); PIN(pv[2]); PIN(pv[3]);
                } while (PEMPTY() && __builtin_amdgcn_s_memrealtime() - t0 < 6000000ull);
            }
        }
#define UF(x) __uint_as_float(x)
        if (st == ZS0 + 2) {
            za = fadd1(fadd1(lone(UF(pv[0][0])), UF(pv[1][0])), fadd1(lone(UF(pv[2][0])), UF(pv[3][0]))); PIN(za);
            zb = fadd1(fadd1(UF(pv[0][1]), UF(pv[1][1])), fadd1(UF(pv[2][1]), UF(pv[3][1]))); PIN(zb);
        }
        if (st == ZS0 + 4) {
            auto a1 = __builtin_amdgcn_permlane16_swap(__float_as_uint(za), __float_as_uint(za), false, false); za = fadd1(UF(a1[0]), UF(a1[1])); PIN(za);
            auto b1 = __builtin_amdgcn_permlane16_swap(__float_as_uint(zb), __float_as_uint(zb), false, false); zb = fadd1(UF(b1[0]), UF(b1[1])); PIN(zb);
        }
        if (st == ZS0 + 6) {
            auto a2 = __builtin_amdgcn_permlane32_swap(__float_as_uint(za), __float_as_uint(za), false, false); za = fadd1(UF(a2[0]), UF(a2[1])); PIN(za);
            auto b2 = __builtin_amdgcn_permlane32_swap(__float_as_uint(zb), __float_as_uint(zb), false, false); zb = fadd1(UF(b2[0]), UF(b2[1])); PIN(zb);
        }
        if (st == ZS0 + 8) {
            float m = fmul1(za, 1.0f / FH), var = fmaxf(ffma1(-m, m, fmul1(zb, 1.0f / FH)), 0.f), r = __builtin_amdgcn_rsqf(var + 1e-5f); PIN(r);
            ZS[wid * 16 + r16] = f32x2{r, fmul1(-m, r)};
        }
    };
    P2_A(0, true, zstat); wait_vm_bar<5>(); SB();
    auto merge = [&](u32x2 A, u32x2 B) {
        auto r0 = __builtin_amdgcn_permlane16_swap(A[0], B[0], false, false); auto r1 = __builtin_amdgcn_permlane16_swap(A[1], B[1], false, false);
        return u32x4{r0[0], r1[0], r0[1], r1[1]};
    };
    auto pk4 = [&](f32x4 v) { return u32x2{cvtpk(v[0], v[1]), cvtpk(v[2], v[3])}; };
    const char* z2b = ws + WS_Z2T + (size_t)(j * 32 + 2 * pm + wr) * 18432;
    f32x2 zs[5]; u32x2 za2, zb2; unsigned vs, v2;
    auto zn = [&](int q) {
        if (q == 0) {
#pragma unroll
            for (int m = 0; m < 5; ++m) zs[m] = ZS[wr * 64 + (m < 4 ? m : wc) * 16 + r16];
        }
        if (q == 1) { vs = (r16 >> 3) * 2304 + (((r16 & 7) ^ (4 * (r16 >> 3))) * 32); PIN(vs); }
        if (q == 2) { v2 = vs + (g & 1) * 4608 + (g >> 1) * 16; vs += g * 8; PIN(v2); PIN(vs); }
#define NRM(v, m, e) { v[e] = ffma1(v[e], zs[m][0], zs[m][1]); v[(e) + 1] = ffma1(v[(e) + 1], zs[m][0], zs[m][1]); PIN(v); }
        if (q >= 3 && q < 3 + 4 * ZSCH) {
            const int p = (q - 3) / ZSCH, t = (q - 3) % ZSCH, n = p >> 1, m = (p & 1) * 2;
            f32x4& A = acc1[m][n]; f32x4& B = acc1[m + 1][n];
            if (ZSCH == 5) {
                if (t == 0) NRM(A, m, 0)
                if (t == 1) { NRM(A, m, 2) za2[0] = cvtpk(A[0], A[1]); PIN(za2); }
                if (t == 2) { NRM(B, m + 1, 0) za2[1] = cvtpk(A[2], A[3]); PIN(za2); }
                if (t == 3) { NRM(B, m + 1, 2) zb2[0] = cvtpk(B[0], B[1]); PIN(zb2); }
                if (t == 4) { zb2[1] = cvtpk(B[2], B[3]); st16s<0>(z2b + NF1(n) * 256 + m * 4608, v2, merge(za2, zb2)); }
            } else {
                if (t == 0) { NRM(A, m, 0) NRM(A, m, 2) }
                if (t == 1) { NRM(B, m + 1, 0) NRM(B, m + 1, 2) }
                if (t == 2) st16s<0>(z2b + NF1(n) * 256 + m * 4608, v2, merge(pk4(A), pk4(B)));
            }
        }
        if (q == 3 + 4 * ZSCH) { NRM(sacc, 4, 0) NRM(sacc, 4, 2) }
        if (q == 4 + 4 * ZSCH) st8s<2048>(z2b + wc * 4608, vs, pk4(sacc));
    };
    P2_B(0, false, true, zn);
    P2_A(1, false, [&](int st) { zn(14 + st); }); wait_vm_bar<5>(); SB();
    P2_B(1, false, true, none);
    P2_A(2, false, none);
    P2_B(2, false, false, none);
    if (r16 == 0) { *(f32x4*)(CS + 192 + NF2(0) * 16 + 4 * g) = cs2[0]; *(f32x4*)(CS + 192 + NF2(1) * 16 + 4 * g) = cs2[1]; }
    f32x4* HX = (f32x4*)(smem + K1_B0) + wr * 256 + lane;
    if (cg == 3) { HX[0] = acc2[0][0]; HX[64] = acc2[1][0]; HX[128] = acc2[2][0]; HX[192] = acc2[3][0]; }
    asm volatile("s_waitcnt lgkmcnt(0)\n\ts_barrier" ::: "memory");
    STAMP(3, 2);
    auto gel = [&](f32x4 a, f32x4 c, f32x4 rr) {
        f32x4 y = a - c * rr[0];
        f32x2 y0 = gelu_y2(f32x2{y[0], y[1]}, rr[1], rr[2], rr[3]), y1 = gelu_y2(f32x2{y[2], y[3]}, rr[1], rr[2], rr[3]);
        return u32x2{cvtpk(y0[0], y0[1]), cvtpk(y1[0], y1[1])};
    };
#pragma unroll
    for (int n = 0; n < 3; ++n) cs1[n] = *(const f32x4*)(CS + 192 + NF2(n) * 16 + 4 * g);
#pragma unroll
    for (int m = 0; m < 4; ++m) er[m] = RS[wr * 64 + m * 16 + r16];
    if (cg == 3) {
    } else if (nf3 >= 10 || j < 4) {
        const char* qb = ws + WS_Q + (pm * 16 + wr * 8) * 9216 + (nf3 >= 10 ? 4 * j + (nf3 - 10) * 2 : 64 + 2 * j) * 128;
        const unsigned vq = ((g & 1) * 2 + (r16 >> 3)) * 9216 + (g >> 1) * 128 + (r16 & 7) * 16;
#define QV(m) pk4((acc2[m][0] - cs1[0] * er[m][0]) * (er[m][1] * (QSCALE / c0)))
        st16s<0>(qb, vq, merge(QV(0), QV(1))); st16s<0>(qb + 4 * 9216, vq, merge(QV(2), QV(3)));
    } else if (j < 7) {
#pragma unroll
        for (int m = 0; m < 4; ++m)
            kv_store(ws + WS_KV + (size_t)(pm * 4 + wr * 2 + (m >> 1)) * 3072, m & 1, j, r16, g, (acc2[m][0] - cs1[0] * er[m][0]) * (er[m][1] * (1.0f / c0)));
    }
    SB();
    f32x4 h8 = HX[wc * 64], c8 = *(const f32x4*)(CS + 192 + 128 + 4 * g); er[4] = RS[wr * 64 + wc * 16 + r16];
    u32x4 zw[4];
#pragma unroll
    for (int m = 0; m < ZHALF; ++m) { zw[m] = merge(gel(acc2[m][1], cs1[1], er[m]), gel(acc2[m][2], cs1[2], er[m])); PIN(zw[m]); }
    volatile LAS u64* EW = (volatile LAS u64*)(lds + TOPL + 8);
    if (wid == HW) {
        if (__builtin_expect(epoch == 0, 0)) {
            const u64 t0 = __builtin_amdgcn_s_memrealtime();
            do { ev = ld_sc1_async(xe_s, le); wait_vm<0>(); PIN(ev); xcheck(); } while (epoch == 0 && __builtin_amdgcn_s_memrealtime() - t0 < 5000000ull);
        }
        *EW = epoch; EW[-1] = 0; EW[1] = 0;
    }
    SB();
    wait_vm_bar<0>();
    STAMP(0, 3);
    if (wid == HW) {
        const bool kvp = j >= 4 && j < 7;
        const unsigned fo = lane == 0 ? (unsigned)(WS_FJ - WS_FA) + (j * 64 + pm) * 8 : (lane == 1 ? (unsigned)(WS_FP - WS_FA) + (pm * 16 + j) * 8 : (lane == 2 ? bid * 8
                            : (unsigned)(WS_FJ - WS_FA) + ((lane & 15) * 64 + 16 + pm * 3 + (j - 4)) * 8));
        if (lane < 3 || (kvp && g == 1)) __hip_atomic_store((gu64*)(ws + WS_FA + fo), epoch, RLX_AGENT);
    }
    if (wid < 4) {
        const char* sa = (const char*)w_sp - 4096 + pm * 128 * S_ * 2;
#pragma unroll
        for (int t = 0; t < 2; ++t) {
            set_m0(lds0 + wid * 4096 + t * K2_SA);
            glds_s<0>(g2_va(wid, lane, 0), sa + t * 128); glds_s<1024>(g2_va(wid, lane, 1), sa + t * 128); glds_s<2048>(g2_va(wid, lane, 2), sa + t * 128); glds_s<3072>(g2_va(wid, lane, 3), sa + t * 128);
        }
    }
    SB();
#pragma unroll
    for (int m = ZHALF; m < 4; ++m) zw[m] = merge(gel(acc2[m][1], cs1[1], er[m]), gel(acc2[m][2], cs1[2], er[m]));
    u32x2 zs8 = gel(h8, c8, er[4]);
    {
        LAS char* zl = (LAS char*)lds + Z1L + wr * 1024 + r16 * 16;
#pragma unroll
        for (int m = 0; m < 4; ++m) *(LAS u32x4*)(zl + cg * 8192 + (2 * ((g & 1) ^ wr) + (g >> 1)) * 2048 + m * 256) = zw[m];
        *(LAS u32x2*)(zl + 32768 + wc * 256 + (g >> 1) * 2048 + (g & 1) * 8) = zs8;
    }
    const u64 e_ = *EW;
    const unsigned eh = __builtin_amdgcn_readfirstlane((unsigned)(e_ >> 32)), el = __builtin_amdgcn_readfirstlane((unsigned)e_);
    return ((u64)eh << 32) | el;
}

DI void wait_in(char* ws, u64 epoch, int lane, int wid, int pm, int j) {
    if (wid == POLLW) {
        const u64 t0 = __builtin_amdgcn_s_memrealtime();
        for (;;) {
            u64 f0_, f1_;
            asm volatile("global_load_dwordx2 %0, %2, %3 sc1\n\tglobal_load_dwordx2 %1, %4, %5 sc1\n\ts_waitcnt vmcnt(0)" : "=&v"(f0_), "=&v"(f1_)
                         : "v"(lane * 8), "s"(ws + WS_FJ + j * 512), "v"((lane & 15) * 8), "s"(ws + WS_FP + pm * 128) : "memory");
            if (__all(f0_ == epoch && f1_ == epoch)) break;
            const u64 dt = __builtin_amdgcn_s_memrealtime() - t0;
            if (dt > 5000000ull) break;
            if (dt > 1000 && blockIdx.x == 0) epoch_service(ws, lane, epoch);
            __builtin_amdgcn_s_sleep(POLLSLEEP1);
        }
    }
    extern __shared__ __attribute__((aligned(16))) char smem[];
    volatile LAS unsigned* go = (volatile LAS unsigned*)((LAS char*)smem + TOPL + 20);
    if (wid == POLLW) *go = 1;
    else while (*go == 0) __builtin_amdgcn_s_sleep(1);
    asm volatile("" ::: "memory");
}
DI void wait_fa(char* ws, u64 epoch, int lane) {
    const u64 t0 = __builtin_amdgcn_s_memrealtime();
    for (;;) {
        u32x4 a, b;
        asm volatile("global_load_dwordx4 %0, %2, %3 sc1\n\tglobal_load_dwordx4 %1, %2, %3 offset:1024 sc1\n\ts_waitcnt vmcnt(0)" : "=&v"(a), "=&v"(b) : "v"(lane * 16), "s"(ws + WS_FA) : "memory");
        const unsigned lo = (unsigned)epoch, hi = (unsigned)(epoch >> 32);
        if (__all(a[0] == lo && a[1] == hi && a[2] == lo && a[3] == hi && b[0] == lo && b[1] == hi && b[2] == lo && b[3] == hi)) break;
        if (__builtin_amdgcn_s_memrealtime() - t0 > 5000000ull) break;
        if (blockIdx.x == 0) epoch_service(ws, lane, epoch);
        __builtin_amdgcn_s_sleep(8);
    }
}

constexpr float ATT_BIAS = 72.0f;
DI float xhalf_max(float m) { auto rr = __builtin_amdgcn_permlane32_swap(__float_as_uint(m), __float_as_uint(m), false, false); return fmaxf(__uint_as_float(rr[0]), __uint_as_float(rr[1])); }
DI float lo_bcast(float m) { auto rr = __builtin_amdgcn_permlane32_swap(__float_as_uint(m), __float_as_uint(m), false, false); return __uint_as_float(rr[0]); }
DI float bf_round(float x) { return __uint_as_float(cvtpk(x, 0.f) << 16); }
DI float max16(const f32x16& s) {
    float a = fmaxf(fmaxf(s[0], s[1]), s[2]), b = fmaxf(fmaxf(s[3], s[4]), s[5]);
    a = fmaxf(fmaxf(a, s[6]), s[7]); b = fmaxf(fmaxf(b, s[8]), s[9]); a = fmaxf(fmaxf(a, s[10]), s[11]); b = fmaxf(fmaxf(b, s[12]), s[13]);
    a = fmaxf(fmaxf(a, s[14]), s[15]); return fmaxf(a, b);
}
DI void exp_pack(const f32x16& s, bf16x8& p0, bf16x8& p1) {
    float e[16];
#pragma unroll
    for (int i = 0; i < 16; ++i) e[i] = __builtin_amdgcn_exp2f(s[i]);
    u32x4 a = {cvtpk(e[0], e[1]), cvtpk(e[2], e[3]), cvtpk(e[4], e[5]), cvtpk(e[6], e[7])};
    u32x4 b = {cvtpk(e[8], e[9]), cvtpk(e[10], e[11]), cvtpk(e[12], e[13]), cvtpk(e[14], e[15])};
    p0 = __builtin_bit_cast(bf16x8, a); p1 = __builtin_bit_cast(bf16x8, b);
}

struct KVF { bf16x8 k1, k2, v0, v1; };
DI void kv_read(KVF& f, const LAS char* tb, int lane, int k2o, int vo) {
    f.k1 = *(const LAS bf16x8*)(tb + lane * 16); f.k2 = *(const LAS bf16x8*)(tb + k2o);
    f.v0 = *(const LAS bf16x8*)(tb + 1024 + vo); f.v1 = *(const LAS bf16x8*)(tb + 2048 + vo);
}
#define k2frag(x) (x)
struct NoHook { DI void operator()(int) const {} };
template <bool RD, class H = NoHook>
DI void half_step(f32x16& sx, bf16x8& px0, bf16x8& px1, f32x16& sy, const KVF kq, const bf16x8& qy1, const bf16x8& qy2,
                  f32x16& oy, const KVF kv, const bf16x8& py0, const bf16x8& py1, KVF& ld, const LAS char* tb, int lane, int k2o, int vo, H hook = H(), int hs = 0) {
    const f32x16 zero = {};
    unsigned a0, a1, a2, a3, b0, b1, b2, b3; float e0, e1, e2, e3;
    f32x16 t = MFMA32(kq.k1, qy1, zero); SB();
    hook(hs); SB();
    if (RD) ld.k1 = *(const LAS bf16x8*)(tb + lane * 16);
    e0 = EX(sx[0]); e1 = EX(sx[1]); e2 = EX(sx[2]); e3 = EX(sx[3]); PIN(e0); PIN(e1); PIN(e2); PIN(e3); SB();
    sy = MFMA32(kq.k2, qy2, t); SB();
    if (RD) ld.k2 = *(const LAS bf16x8*)(tb + k2o);
    a0 = cvtpk(e0, e1); a1 = cvtpk(e2, e3); PIN(a0); PIN(a1);
    e0 = EX(sx[4]); e1 = EX(sx[5]); e2 = EX(sx[6]); e3 = EX(sx[7]); PIN(e0); PIN(e1); PIN(e2); PIN(e3); SB();
    oy = MFMA32(kv.v0, py0, oy); SB();
    hook(hs + 1); SB();
    if (RD) ld.v0 = *(const LAS bf16x8*)(tb + 1024 + vo);
    a2 = cvtpk(e0, e1); a3 = cvtpk(e2, e3); PIN(a2); PIN(a3);
    e0 = EX(sx[8]); e1 = EX(sx[9]); e2 = EX(sx[10]); e3 = EX(sx[11]); PIN(e0); PIN(e1); PIN(e2); PIN(e3); SB();
    oy = MFMA32(kv.v1, py1, oy); SB();
    if (RD) ld.v1 = *(const LAS bf16x8*)(tb + 2048 + vo);
    b0 = cvtpk(e0, e1); b1 = cvtpk(e2, e3); PIN(b0); PIN(b1);
    e0 = EX(sx[12]); e1 = EX(sx[13]); e2 = EX(sx[14]); e3 = EX(sx[15]);
    b2 = cvtpk(e0, e1); b3 = cvtpk(e2, e3); PIN(b2); PIN(b3); SB();
    u32x4 a = {a0, a1, a2, a3}, b = {b0, b1, b2, b3};
    px0 = __builtin_bit_cast(bf16x8, a); px1 = __builtin_bit_cast(bf16x8, b);
}
DI bf16x8 p_scale(bf16x8 p, int sh) {
    u32x4 w = __builtin_bit_cast(u32x4, p);
#pragma unroll
    for (int i = 0; i < 4; ++i) w[i] = cvtpk(__builtin_amdgcn_ldexpf(bflo(w[i]), sh), __builtin_amdgcn_ldexpf(bfhi(w[i]), sh));
    return __builtin_bit_cast(bf16x8, w);
}
template <bool PP> DI void att_rescale(f32x16& o, f32x16& s, bf16x8& p0, bf16x8& p1, float& mref, bf16x8& q2, bool& bad, int c, int h) {
    bool nf = false;
#pragma unroll
    for (int i = 0; i < 11; ++i) nf = nf || (!(fabsf(o[i]) <= 3.0e38f) && (i < 8 || h == 0));
    if (__any(nf)) { bad = true; return; }
    float lb = lo_bcast(o[10]);
    int e = __builtin_amdgcn_frexp_expf(lb);
    float d = (float)(e + (int)ATT_BIAS); d = d < 0 ? 0 : d;
    float nm = bf_round(mref + d); float dd = nm - mref; mref = nm;
    int sh = -(int)dd;
#pragma unroll
    for (int i = 0; i < 16; ++i) { o[i] = __builtin_amdgcn_ldexpf(o[i], sh); if (!PP) { float b_ = s[i] - dd; PIN(b_); s[i] = b_; } }
    if (PP) { p0 = p_scale(p0, sh); p1 = p_scale(p1, sh); }
    u32x4 b = __builtin_bit_cast(u32x4, q2); b[1] = h ? 0u : (__float_as_uint(-nm) >> 16); q2 = __builtin_bit_cast(bf16x8, b);
}

#define W3LD 1
#define W3ST 3
struct G2F { bf16x8 a[4], b[4], sb; };
struct G2S {
    unsigned va[4], vl, vl2;
    const char* sa; const char* sl; unsigned lstep;
    unsigned m0a, m0l[3];
    const LAS char* pa[2][2]; const LAS char* pb[2]; const LAS char* ps[2];
};
template <int KS, int GL, bool RD, int RS, int WS, class H = NoHook> DI void g2_phase(G2S& s, const G2F& f, G2F& n, f32x4 (&acc)[4][4], f32x4 (&sacc)[2], H hook = H()) {
    const LAS char* a0 = s.pa[0][KS] + RS * K2_SA; const LAS char* a1 = s.pa[1][KS] + RS * K2_SA;
    constexpr int BO = RS * K2_SB + KS * 9216;
    const LAS char* b0 = s.pb[0] + BO; const LAS char* b1 = s.pb[1] + BO;
#define MM(m, nn) acc[m][nn] = MFMA16(f.b[nn], f.a[m], acc[m][nn]); PIN(acc[m][nn]); SB(); hook((nn) * 4 + (m)); SB()
    MM(0, 0); if (RD) n.b[0] = ldsB2(b0, b1); SB();
    MM(1, 0); if (RD) n.a[0] = ldsA(a0); SB();
    MM(2, 0); if (RD) n.a[1] = ldsA(a0 + 2048); SB();
    MM(3, 0); if (RD) n.a[2] = ldsA(a1); SB();
    MM(0, 1); if (RD) n.a[3] = ldsA(a1 + 2048); SB();
    MM(1, 1); if (RD) n.b[1] = ldsB2(b0 + 256, b1 + 256); SB();
    MM(2, 1); if (RD) n.b[2] = ldsB2(b0 + 512, b1 + 512); SB();
    MM(3, 1); if (RD) n.b[3] = ldsB2(b0 + 768, b1 + 768); SB();
    MM(0, 2); if (RD) n.sb = ldsB2(s.ps[0] + BO, s.ps[1] + BO); SB();
    MM(1, 2); if (GL == 1) { set_m0(s.m0a + WS * K2_SA); glds_s<0>(s.va[0], s.sa); } if (GL == 2) { glds_sc1<1024>(s.vl, s.sl); } SB();
    MM(2, 2);
    MM(3, 2); if (GL == 1) glds_s<1024>(s.va[1], s.sa); if (GL == 2) glds_sc1<2048>(s.vl, s.sl); SB();
    MM(0, 3);
    MM(1, 3); if (GL == 1) glds_s<2048>(s.va[2], s.sa); if (GL == 2) glds_sc1<3072>(s.vl, s.sl); SB();
    MM(2, 3);
    MM(3, 3); if (GL == 1) { glds_s<3072>(s.va[3], s.sa); s.sa += 128; asm volatile("" : "+s"(s.sa)); } if (GL == 2) { set_m0(s.m0l[WS] + 4096); glds_sc1<0>(s.vl2, s.sl); } SB();
    sacc[0] = MFMA16(f.sb, f.a[0], sacc[0]); PIN(sacc[0]); SB();
    if (GL == 1) { set_m0(s.m0l[WS]); glds_sc1<0>(s.vl, s.sl); } if (GL == 2) { glds_sc1<1024>(s.vl2, s.sl); s.sl += s.lstep; asm volatile("" : "+s"(s.sl)); } SB();
    sacc[1] = MFMA16(f.sb, f.a[1], sacc[1]); PIN(sacc[1]); SB();
#undef MM
}
template <int C, bool G2nd, bool G1st, int VM, bool RDN> DI void g2_iter(G2S& s, G2F& f0, G2F& f1, f32x4 (&acc)[4][4], f32x4 (&sacc)[2]) {
    g2_phase<1, G2nd ? 2 : 0, true, C, (C + 2) % 3>(s, f0, f1, acc, sacc);
    wait_vm_bar<VM>(); SB();
    g2_phase<0, G1st ? 1 : 0, RDN, (C + 1) % 3, C>(s, f1, f0, acc, sacc);
}

constexpr int K3_S = 4608, K3_RW = 4 * K3_S;
struct B5 { bf16x8 b[5]; };
DI unsigned k3_so(int w, int sl) {
    const int r = sl == 1 ? 2 : sl - 2;
    if (sl == 0) return Z1L + w * K3_S;
    return w < 3 ? r * K2_SA + w * K3_S : (w < 7 ? K2_B0 + r * K2_SB + (w - 3) * K3_S : K2_K0 + r * K2_SK);
}
DI unsigned k3_orow(int r) { return r < 6 ? (r >> 1) * K2_SA + 3 * K3_S + 64 + (r & 1) * 1168 : K2_K0 + (r - 6) * K2_SK + K3_S + 64; }
DI void k3_b1(int p, unsigned m0v, unsigned va, const char* sb) {
    if (p == 0) { set_m0(m0v); glds_sc1<0>(va, sb); }
    if (p == 1) glds_sc1<1024>(va, sb);
    if (p == 2) glds_sc1<2048>(va, sb);
    if (p == 3) glds_sc1<3072>(va, sb);
    if (p == 4) asm volatile("s_mov_b32 m0, %2\n\ts_mov_b32 exec_hi, 0\n\tglobal_load_lds_dwordx4 %0, %1 sc1\n\ts_mov_b32 exec_hi, -1" ::"v"(va), "s"(sb + 4096), "s"(m0v + 4096));
}
DI void k3_a(unsigned m0v, unsigned va, const char* sa) {
    set_m0(m0v); glds_sc1<0>(va, sa); glds_sc1<1024>(va, sa); glds_sc1<2048>(va, sa); glds_sc1<3072>(va, sa);
}
DI void k3_b(unsigned m0v, unsigned va, const char* sb) {
#pragma unroll
    for (int p = 0; p < 5; ++p) k3_b1(p, m0v, va, sb);
}
DI void k3_rdb(B5& r, const LAS char* p, const LAS char* p5) {
#pragma unroll
    for (int n = 0; n < 4; ++n) r.b[n] = ldsA(p + n * 1024);
    r.b[4] = ldsA(p5);
}
DI void k3_lb1(int p, B5& r, unsigned va, unsigned va5, const char* sb) {
    if (p < 4) asm volatile("global_load_dwordx4 %0, %1, %2 offset:%3 sc1" : "+v"(r.b[p & 3]) : "v"(va), "s"(sb), "n"((p & 3) * 1024) : "memory");
    else asm volatile("global_load_dwordx4 %0, %1, %2 sc1" : "+v"(r.b[4]) : "v"(va5), "s"(sb) : "memory");
}
#define LB5 "global_load_dwordx4 %0, %5, %7 sc1\n\tglobal_load_dwordx4 %1, %5, %7 offset:1024 sc1\n\tglobal_load_dwordx4 %2, %5, %7 offset:2048 sc1\n\t" \
            "global_load_dwordx4 %3, %5, %7 offset:3072 sc1\n\tglobal_load_dwordx4 %4, %6, %7 sc1\n"
DI void k3_lb(B5& r, unsigned va, unsigned va5, const char* sb) {
    asm volatile(LB5 : "=&v"(r.b[0]), "=&v"(r.b[1]), "=&v"(r.b[2]), "=&v"(r.b[3]), "=&v"(r.b[4]) : "v"(va), "v"(va5), "s"(sb) : "memory");
}
DI void k3_lb_if(B5& r, unsigned va, unsigned va5, const char* sb, int a, int b) {
    asm volatile("s_cmp_lg_u32 %8, %9\n\ts_cbranch_scc1 .Llb%=\n\t" LB5 ".Llb%=:" : "+v"(r.b[0]), "+v"(r.b[1]), "+v"(r.b[2]), "+v"(r.b[3]), "+v"(r.b[4])
                 : "v"(va), "v"(va5), "s"(sb), "s"(a), "n"(b) : "memory", "scc");
}
DI void pin5(B5& r) { PIN(r.b[0]); PIN(r.b[1]); PIN(r.b[2]); PIN(r.b[3]); PIN(r.b[4]); }
template <int N0, int N1, bool FIRST = false> DI void k3_mm(f32x4 (&acc)[4][5], const bf16x8 (&a)[4], const B5& r) {
#pragma unroll
    for (int n = N0; n < N1; ++n)
#pragma unroll
        for (int m = 0; m < 4; ++m) {
            if (FIRST) asm volatile("v_mfma_f32_16x16x32_bf16 %0, %1, %2, 0" : "=&v"(acc[m][n]) : "v"(r.b[n]), "v"(a[m]));
            else asm volatile("v_mfma_f32_16x16x32_bf16 %0, %1, %2, %0" : "+v"(acc[m][n]) : "v"(r.b[n]), "v"(a[m]));
        }
    SB();
}

#define K3MM(d, b_, a_) asm volatile("v_mfma_f32_16x16x32_bf16 %0, %1, %2, %0" : "+v"(d) : "v"(b_), "v"(a_))
#define ILV 1
struct UA { const char* p[4]; };
template <int NX, bool FIRST = false> DI void k3_step(f32x4 (&acc)[4][5], bf16x8 (&a)[4], const B5& r, unsigned va, const UA& ua) {
#pragma unroll
    for (int m = 0; m < 4; ++m) {
#pragma unroll
        for (int n = 0; n < 5; ++n) {
            if (FIRST && m < 3) asm volatile("v_mfma_f32_16x16x32_bf16 %0, %1, %2, 0" : "=&v"(acc[m][n]) : "v"(r.b[n]), "v"(a[m]));
            else K3MM(acc[m][n], r.b[n], a[m]);
        }
        if (NX >= 0 && ILV) asm volatile("global_load_dwordx4 %0, %1, %2 sc1" : "+v"(a[m]) : "v"(va), "s"(ua.p[m] + (NX < 0 ? 0 : NX) * 32768) : "memory");
    }
    if (NX >= 0 && !ILV) {
#pragma unroll
        for (int m = 0; m < 4; ++m) asm volatile("global_load_dwordx4 %0, %1, %2 sc1" : "+v"(a[m]) : "v"(va), "s"(ua.p[m] + (NX < 0 ? 0 : NX) * 32768) : "memory");
    }
    SB();
}

DI void phase2(const u16* __restrict__ w_sp, const u16* __restrict__ b_sp, const u16* __restrict__ x, char* __restrict__ ws, u16* __restrict__ out, const u64 epoch, const int wid) {
    extern __shared__ __attribute__((aligned(16))) char smem[];
    int bid = blockIdx.x, xcd = bid & 7, idx = bid >> 3;
    int pm = (xcd & 1) * 8 + (idx & 7), j = (xcd >> 1) * 4 + (idx >> 3);
    unsigned zz = 0; asm volatile("" : "+s"(zz));
    const int lane = __builtin_amdgcn_mbcnt_hi(~0u, __builtin_amdgcn_mbcnt_lo(~0u, zz)) & 63;
    const int tid = wid * 64 + lane;
    const unsigned lds0 = (unsigned)(uintptr_t)smem; const LAS char* lds = (const LAS char*)smem;
    STAMP(1, 0);
    const int rb = ((xcd & 1) ^ 1) * 16 + (xcd >> 1) * 4 + (idx & 3), j3 = idx >> 2;
    volatile LAS unsigned* SY = (volatile LAS unsigned*)(lds + TOPL);
    gu64* FLr = (gu64*)(ws + WS_FL) + rb * 64;
    const unsigned va = lane * 16;
    unsigned va5 = 4096 + ((lane >> 4) * 8 + (lane & 7)) * 16; PIN(va5);
    B5 rb0, rb1, rb2, rb3;
    const char* wb = ws + WS_W3T + (size_t)j3 * 90 * 4608 + wid * 4608;
    if (__builtin_expect(wid < 4, 1)) {
        const int wr = wid >> 1, wc = wid & 1;
        G2S s;
        s.vl = lane * 16; s.vl2 = lane * 16 + 4096;
        s.lstep = wid < 3 ? 18432 : 6144;
        unsigned ol = (wid < 3 ? (unsigned)WS_Z2T + j * 32 * 18432 + wid * 6144 : (unsigned)WS_KV), oa = pm * 128 * S_ * 2;
        s.m0a = lds0 + wid * 4096; s.m0l[0] = lds0 + (wid < 3 ? K2_B0 + wid * 6144 : K2_K0); s.m0l[1] = s.m0l[0] + s.lstep; s.m0l[2] = s.m0l[1] + s.lstep;
        PIN(s.vl); PIN(s.vl2);
        s.sl = ws + ol;
        asm volatile("" : "+s"(s.sl), "+s"(s.m0l[0]), "+s"(s.m0l[1]));
        SB();
        wait_in(ws, epoch, lane, wid, pm, j);
        STAMPT(0, 3, 3);
#pragma unroll
        for (int t = 0; t < 2; ++t) {
            set_m0(s.m0l[t]); glds_sc1<0>(s.vl, s.sl); glds_sc1<1024>(s.vl, s.sl); glds_sc1<2048>(s.vl, s.sl); glds_sc1<3072>(s.vl, s.sl);
            set_m0(s.m0l[t] + 4096); glds_sc1<0>(s.vl2, s.sl); glds_sc1<1024>(s.vl2, s.sl); s.sl += s.lstep;
            SB();
            if (t == 0) {
#pragma unroll
                for (int i = 0; i < 4; ++i) { s.va[i] = g2_va(wid, lane, i); PIN(s.va[i]); }
                int r = lane & 15, g = lane >> 4, q = r >> 2, p = lane & 3;
#pragma unroll
                for (int ks = 0; ks < 2; ++ks) {
                    int o = r * 128 + (((4 * ks + g) ^ (r >> 1)) * 16) + wr * 8192;
                    s.pa[0][ks] = lds + o + wc * 4096; s.pa[1][ks] = lds + o + (1 - wc) * 4096; PIN(s.pa[0][ks]); PIN(s.pa[1][ks]);
                }
                int ob = K2_B0 + g * 2304 + q * 32 + 8 * p;
                s.pb[0] = lds + ob + wc * 1024 + ((g & 1) * 128); s.pb[1] = lds + ob + wc * 1024 + (((g & 1) ^ 1) * 128);
                s.ps[0] = lds + ob + 2048 + ((g & 1) * 128); s.ps[1] = lds + ob + 2048 + (((g & 1) ^ 1) * 128);
                PIN(s.pb[0]); PIN(s.pb[1]); PIN(s.ps[0]); PIN(s.ps[1]);
                SB();
            }
        }
        s.sa = (const char*)w_sp - 4096 + oa + 256;
        G2F f0, f1;
        f32x4 acc[4][4], sacc[2];
#pragma unroll
        for (int m = 0; m < 4; ++m) {
            const float b = bf2f(*(const LAS u16*)(lds + TOPL + 2304 + (wr * 64 + (m ^ (2 * wc)) * 16 + (lane & 15)) * 2));
#pragma unroll
            for (int n = 0; n < 4; ++n) { acc[m][n] = f32x4{b, b, b, b}; PIN(acc[m][n]); }
            if (m < 2) { sacc[m] = f32x4{b, b, b, b}; PIN(sacc[m]); }
        }
        SB();
        wait_vm_bar<6>();
        STAMP(1, 1);
        f0.b[0] = ldsB2(s.pb[0], s.pb[1]);
        f0.a[0] = ldsA(s.pa[0][0]); f0.a[1] = ldsA(s.pa[0][0] + 2048); f0.a[2] = ldsA(s.pa[1][0]); f0.a[3] = ldsA(s.pa[1][0] + 2048);
        f0.b[1] = ldsB2(s.pb[0] + 256, s.pb[1] + 256); f0.b[2] = ldsB2(s.pb[0] + 512, s.pb[1] + 512); f0.b[3] = ldsB2(s.pb[0] + 768, s.pb[1] + 768); f0.sb = ldsB2(s.ps[0], s.ps[1]);
        SB();
        set_m0(s.m0a + 2 * K2_SA); glds_s<0>(s.va[0], s.sa); glds_s<1024>(s.va[1], s.sa); glds_s<2048>(s.va[2], s.sa); glds_s<3072>(s.va[3], s.sa); s.sa += 128;
        set_m0(s.m0l[2]); glds_sc1<0>(s.vl, s.sl);
        SB();
#pragma unroll 1
        for (int t = 0; t < 9; ++t) {
            g2_iter<0, true, true, 10, true>(s, f0, f1, acc, sacc); g2_iter<1, true, true, 10, true>(s, f0, f1, acc, sacc); g2_iter<2, true, true, 10, true>(s, f0, f1, acc, sacc);
        }
        asm volatile("" : "=v"(rb0.b[0]), "=v"(rb0.b[1]), "=v"(rb0.b[2]), "=v"(rb0.b[3]), "=v"(rb0.b[4]), "=v"(rb1.b[0]), "=v"(rb1.b[1]), "=v"(rb1.b[2]), "=v"(rb1.b[3]), "=v"(rb1.b[4]));
        g2_phase<1, 2, true, 0, 2>(s, f0, f1, acc, sacc);
        wait_vm_bar<10>(); SB();
        g2_phase<0, 1, true, 1, 0>(s, f1, f0, acc, sacc, [&](int st) { if (st < 10 && !(st & 1)) k3_lb1(st >> 1, rb0, va, va5, wb); });
        g2_phase<1, 2, true, 1, 0>(s, f0, f1, acc, sacc);
        wait_vm_bar<15>(); SB();
        g2_phase<0, 1, true, 2, 1>(s, f1, f0, acc, sacc, [&](int st) { if (st < 10 && !(st & 1)) k3_lb1(st >> 1, rb1, va, va5, wb + 36864); });
        const int c = lane & 15, g = lane >> 4;
        const LAS char* z1b = lds + Z1L + (g >> 1) * 2048 + (wr * 64 + c) * 16 + (g & 1) * 8;
        u32x2 z1r[4][4], z1s[2];
#pragma unroll
        for (int m = 0; m < 4; ++m) {
            const int mo = (m ^ (2 * wc)) * 16;
#pragma unroll
            for (int n = 0; n < 4; ++n) z1r[m][n] = *(const LAS u32x2*)(z1b + (wc * 4 + n) * 4096 + mo * 16);
            if (m < 2) z1s[m] = *(const LAS u32x2*)(z1b + 8 * 4096 + mo * 16);
        }
        SB();
#define PREB(i) [&](int st) { if (st < 5) k3_b1(st, lds0 + k3_so(wid, i), va, wb + ((i) + 6) * 36864); else if (st < 10) k3_b1(st - 5, lds0 + k3_so(wid + 4, i), va, wb + ((i) + 6) * 36864 + 4 * 4608); }
        g2_phase<1, 2, true, 2, 1>(s, f0, f1, acc, sacc);
        wait_vm_bar<15>(); SB();
        g2_phase<0, 0, true, 0, 2>(s, f1, f0, acc, sacc, PREB(0));
        g2_phase<1, 0, true, 0, 2>(s, f0, f1, acc, sacc, PREB(1));
        wait_vm_bar<20>(); SB();
        g2_phase<0, 0, true, 1, 0>(s, f1, f0, acc, sacc, PREB(2));
        unsigned vu[3];
#pragma unroll
        for (int i = 0; i < 3; ++i) {
            const int cgl = 18 * j + (i < 2 ? 2 * (wc * 4 + 2 * i + (g & 1)) : 16) + (g >> 1);
            vu[i] = (cgl >> 2) * 4096 + (cgl & 3) * 256 + c * 16 + (i == 2 ? (g & 1) * 1024 : 0); PIN(vu[i]);
        }
        const char* ub0 = ws + WS_UO + (size_t)(pm * 2 + wr) * 90 * 4096 + wc * 2048; const char* ub1 = ub0 + 2048 - wc * 4096;
        SB();
        g2_phase<1, 0, true, 1, 0>(s, f0, f1, acc, sacc);
        wait_vm_bar<0>(); SB();
        __builtin_amdgcn_s_setprio(2);
        auto fin = [&](f32x4 v, u32x2 z) { return u32x2{cvtpk(fmul1(bflo(z[0]), v[0]), fmul1(bfhi(z[0]), v[1])), cvtpk(fmul1(bflo(z[1]), v[2]), fmul1(bfhi(z[1]), v[3]))}; };
        auto mrg = [&](u32x2 A, u32x2 B) {
            auto r0 = __builtin_amdgcn_permlane16_swap(A[0], B[0], false, false); auto r1 = __builtin_amdgcn_permlane16_swap(A[1], B[1], false, false);
            return u32x4{r0[0], r1[0], r0[1], r1[1]};
        };
#define M2(m, n) acc[m][n] = MFMA16(f1.b[n], f1.a[m], acc[m][n]); acc[m][(n) + 1] = MFMA16(f1.b[(n) + 1], f1.a[m], acc[m][(n) + 1]); PIN(acc[m][n]); PIN(acc[m][(n) + 1]); SB()
#define PUT(m, n) st16s<((m) & 1) * 1024>((m) < 2 ? ub0 : ub1, vu[(n) >> 1], mrg(fin(acc[m][n], z1r[m][n]), fin(acc[m][(n) + 1], z1r[m][(n) + 1]))); SB()
        M2(0, 0); M2(1, 0); M2(2, 0); PUT(0, 0);
        M2(3, 0); PUT(1, 0);
        sacc[0] = MFMA16(f1.sb, f1.a[0], sacc[0]); sacc[1] = MFMA16(f1.sb, f1.a[1], sacc[1]); PIN(sacc[0]); PIN(sacc[1]); SB(); PUT(2, 0);
        M2(0, 2); PUT(3, 0);
        M2(1, 2); st16s<0>(ub0, vu[2], mrg(fin(sacc[0], z1s[0]), fin(sacc[1], z1s[1]))); SB();
        M2(2, 2); PUT(0, 2);
        M2(3, 2); PUT(1, 2);
        STAMP(1, 2);
        PUT(2, 2); PUT(3, 2);
        SB(); k3_b(lds0 + k3_so(wid, 3), va, wb + 9 * 36864);
        wait_vm<5>();
        if (lane == 0) __hip_atomic_store((gu64*)(ws + WS_FL) + (pm * 2 + wr) * 64 + j * 2 + wc, epoch, RLX_AGENT);
        pin5(rb0); pin5(rb1);
        asm volatile("" : "=v"(rb2.b[0]), "=v"(rb2.b[1]), "=v"(rb2.b[2]), "=v"(rb2.b[3]), "=v"(rb2.b[4]), "=v"(rb3.b[0]), "=v"(rb3.b[1]), "=v"(rb3.b[2]), "=v"(rb3.b[3]), "=v"(rb3.b[4]));
        STAMP(1, 3);
        __builtin_amdgcn_s_setprio(0);
    } else {
        __builtin_amdgcn_s_setprio(1);
        const int c = lane & 31, h = lane >> 5, aw = wid - 4;
        const char* kvg = ws + WS_KV;
        bf16x8 q1[2], q2[2]; float mref[2];
        const f32x16 zero = {};
        f32x16 oA = zero, oB = zero;
        int k2o = 1024 + (c >> 4) * 1024 + (c & 15) * 16;
        int vo = c < 8 ? 256 + (h * 8 + c) * 16 : 512 + (h * 16 + (c < 16 ? c - 8 : (c < 18 ? c - 2 : (c == 18 ? 12 : 13)))) * 16;
        {
            const int qbase = (aw < 3 ? K2_B0 + 2 * K2_SB + aw * 6144 : K2_K0 + 2 * K2_SK) + 1024;
            const LAS char* ql = lds + qbase;
            const char* qs = ws + WS_Q + (pm * 16 + j) * 9216 + aw * 32;
            unsigned vq = (lane >> 1) * 128 + (lane & 1) * 16; PIN(vq);
            SB();
            wait_in(ws, epoch, lane, wid, pm, j);
            set_m0(lds0 + qbase); glds_sc1<0>(vq, qs); glds_sc1<1024>(vq, qs + 3072);
            if (lane < 16) glds_sc1<2048>(vq, qs + 6144);
            SB();
            const LAS char* qo[5];
#pragma unroll
            for (int i = 0; i < 5; ++i) { const int k = 18 * c + (i < 4 ? 8 * h + 2 * i : 16); qo[i] = ql + ((k >> 3) * 32 + (k & 7) * 2); PIN(qo[i]); }
            PIN(oA); PIN(oB); PIN(k2o); PIN(vo);
            SB();
            wait_vm<0>();
#pragma unroll
            for (int t = 0; t < 2; ++t) {
                u32x4 a;
#pragma unroll
                for (int i = 0; i < 4; ++i) a[i] = *(const LAS unsigned*)(qo[i] + t * 16);
                q1[t] = __builtin_bit_cast(bf16x8, a);
                const unsigned e = *(const LAS unsigned*)(qo[4] + t * 16);
                u32x4 b = {h ? 0u : e, 0u, 0u, 0u}; q2[t] = __builtin_bit_cast(bf16x8, b);
                PIN(q1[t]); PIN(q2[t]);
            }
        }
        bool bad = false;
        const LAS char* kvl = lds + K2_K0;
        asm volatile("s_waitcnt vmcnt(0) lgkmcnt(0)\n\ts_barrier" ::: "memory");
        KVF X, Y;
        kv_read(X, kvl, lane, k2o, vo); kv_read(Y, kvl + 3072, lane, k2o, vo);
#pragma unroll
        for (int t = 0; t < 2; ++t) {
            f32x16 s0 = MFMA32(X.k1, q1[t], zero); s0 = MFMA32(X.k2, q2[t], s0);
            f32x16 s1 = MFMA32(Y.k1, q1[t], zero); s1 = MFMA32(Y.k2, q2[t], s1);
            float m = xhalf_max(fmaxf(max16(s0), max16(s1)));
            mref[t] = bf_round(rintf(m) + ATT_BIAS);
            u32x4 b = __builtin_bit_cast(u32x4, q2[t]); b[1] = h ? 0u : (__float_as_uint(-mref[t]) >> 16); q2[t] = __builtin_bit_cast(bf16x8, b);
        }
        f32x16 sA = MFMA32(X.k1, q1[0], zero); sA = MFMA32(X.k2, q2[0], sA);
        f32x16 sB = zero;
        bf16x8 pA0, pA1, pB0 = {}, pB1 = {};
        half_step<false>(sA, pA0, pA1, sB, X, q1[1], q2[1], oB, X, pB0, pB1, Y, kvl, lane, k2o, vo);
        half_step<false>(sB, pB0, pB1, sA, Y, q1[0], q2[0], oA, X, pA0, pA1, Y, kvl, lane, k2o, vo);
        asm volatile("s_waitcnt lgkmcnt(0)\n\ts_barrier" ::: "memory");
#define A_IV(sl, CHK, H, hs) { const LAS char* tb = kvl + (sl) * K2_SK; \
            half_step<true>(sA, pA0, pA1, sB, Y, q1[1], q2[1], oB, X, pB0, pB1, X, tb, lane, k2o, vo, H, hs); \
            half_step<false>(sB, pB0, pB1, sA, X, q1[0], q2[0], oA, Y, pA0, pA1, X, tb, lane, k2o, vo, H, hs + 2); \
            if (CHK) { mB = __builtin_amdgcn_ballot_w64(!(oB[10] <= 0x1p20f)); }                  \
            half_step<true>(sA, pA0, pA1, sB, X, q1[1], q2[1], oB, Y, pB0, pB1, Y, tb + 3072, lane, k2o, vo, H, hs + 4); \
            if (CHK) { mA = __builtin_amdgcn_ballot_w64(!(oA[10] <= 0x1p20f)); any = mA | mB; asm volatile("" : "+s"(any)); } \
            half_step<false>(sB, pB0, pB1, sA, Y, q1[0], q2[0], oA, X, pA0, pA1, Y, tb, lane, k2o, vo, H, hs + 6); }
#define A_BAR() asm volatile("s_waitcnt lgkmcnt(0)\n\ts_barrier" ::: "memory")
        unsigned long long mA, mB, any;
#define UNDEF5(r) asm volatile("" : "=v"(r.b[0]), "=v"(r.b[1]), "=v"(r.b[2]), "=v"(r.b[3]), "=v"(r.b[4]))
        u32x4 fa0, fa1;
        asm volatile("global_load_dwordx4 %0, %2, %3 sc1\n\tglobal_load_dwordx4 %1, %2, %3 offset:1024 sc1" : "=&v"(fa0), "=&v"(fa1) : "v"(va), "s"(ws + WS_FA) : "memory");
        const NoHook nh;
        const char* pf = ws + WS_W3T + (size_t)j3 * 90 * 4608 + 16 * 4608 + min(((idx & 3) * 4 + aw) * 192, 2664 - 192) * 128;
        auto wl = [&](int i) {
            if (i < 20) k3_lb1(i % 5, i < 5 ? rb0 : (i < 10 ? rb1 : (i < 15 ? rb2 : rb3)), va, va5, wb + (i / 5) * 36864);
            else if (i < 23 && PFW)
                asm volatile("s_mov_b32 m0, %2\n\ts_nop 0\n\tglobal_load_lds_dword %0, %1 sc1" ::"v"(va * 8), "s"(pf + (i - 20) * 8192), "s"(lds0 + TOPL + 64 + wid * 256));
        };
#define A_IT(H) A_IV(1, false, H, 0); A_BAR(); A_IV(2, false, H, 8); A_BAR(); A_IV(0, true, H, 16); \
            if (__builtin_expect(any != 0, 0)) { \
                if (mA) att_rescale<false>(oA, sA, pA0, pA1, mref[0], q2[0], bad, c, h); \
                if (mB) att_rescale<true>(oB, sB, pB0, pB1, mref[1], q2[1], bad, c, h); \
            } \
            A_BAR()
#pragma unroll 1
        for (int t = 0; t < TFA; ++t) { A_IT(nh); }
        {
            wait_vm<0>(); PIN(fa0); PIN(fa1);
            const unsigned lo = (unsigned)epoch, hi = (unsigned)(epoch >> 32);
            if (__builtin_expect(!__all(fa0[0] == lo && fa0[1] == hi && fa0[2] == lo && fa0[3] == hi && fa1[0] == lo && fa1[1] == hi && fa1[2] == lo && fa1[3] == hi), 0)) wait_fa(ws, epoch, lane);
            UNDEF5(rb0); UNDEF5(rb1); UNDEF5(rb2); UNDEF5(rb3);
            SB();
            A_IT(wl);
        }
#pragma unroll 1
        for (int t = TFA + 1; t < 10; ++t) { A_IT(nh); }
        A_IV(1, false, nh, 0); A_BAR();
        wait_vm<0>(); pin5(rb0); pin5(rb1); pin5(rb2); pin5(rb3);
        k3_b(lds0 + k3_so(wid, 3), va, wb + 9 * 36864); SB();
        half_step<false>(sA, pA0, pA1, sB, Y, q1[1], q2[1], oB, X, pB0, pB1, X, kvl, lane, k2o, vo);
        exp_pack(sB, pB0, pB1);
        oA = MFMA32(Y.v0, pA0, oA); oA = MFMA32(Y.v1, pA1, oA);
        oB = MFMA32(Y.v0, pB0, oB); oB = MFMA32(Y.v1, pB1, oB);
        {
            float sa_ = fabsf(oA[10]) * 0x1p27f, sb_ = fabsf(oB[10]) * 0x1p27f;
#pragma unroll
            for (int i = 0; i < 10; ++i) { sa_ += fabsf(oA[i]); PIN(sa_); sb_ += fabsf(oB[i]); PIN(sb_); }
            if (__builtin_amdgcn_ballot_w64(!(sa_ + sb_ <= 3.0e38f))) bad = true;
        }
        f32x16 o[2] = {oA, oB};
        if (__builtin_expect(bad, 0)) {
#pragma unroll 1
            for (int t = 0; t < 2; ++t) {
                bf16x8 qa = t ? q1[1] : q1[0]; u32x4 qb = __builtin_bit_cast(u32x4, t ? q2[1] : q2[0]); qb[1] = 0;
                f32x16 oo = zero; float m = -3.0e38f;
                for (int kt = 0; kt < 64; ++kt) {
                    const char* tb = kvg + (size_t)kt * 3072;
                    bf16x8 k1, v0, v1; u32x4 k2v;
                    asm volatile("global_load_dwordx4 %0, %4, %7 sc1\n\tglobal_load_dwordx4 %1, %5, %7 sc1\n\tglobal_load_dwordx4 %2, %6, %7 offset:1024 sc1\n\t"
                                 "global_load_dwordx4 %3, %6, %7 offset:2048 sc1\n\ts_waitcnt vmcnt(0)"
                                 : "=&v"(k1), "=&v"(k2v), "=&v"(v0), "=&v"(v1) : "v"(lane * 16), "v"(k2o), "v"(vo), "s"(tb) : "memory");
                    f32x16 s = MFMA32(k1, qa, zero); s = MFMA32(__builtin_bit_cast(bf16x8, k2v), __builtin_bit_cast(bf16x8, qb), s);
                    float nm = fmaxf(m, xhalf_max(max16(s)));
                    float sc = __builtin_amdgcn_exp2f(m - nm); m = nm;
#pragma unroll
                    for (int i = 0; i < 16; ++i) { float a_ = oo[i] * sc, b_ = s[i] - nm; PIN(a_); PIN(b_); oo[i] = a_; s[i] = b_; }
                    bf16x8 p0, p1; exp_pack(s, p0, p1);
                    oo = MFMA32(v0, p0, oo); oo = MFMA32(v1, p1, oo);
                }
                if (t) o[1] = oo; else o[0] = oo;
            }
        }
#pragma unroll
        for (int t = 0; t < 2; ++t) {
            float inv = __builtin_amdgcn_rcpf(lo_bcast(o[t][10])); PIN(inv);
            LAS unsigned* ob = (LAS unsigned*)(lds + k3_orow(aw * 2 + t) + 36 * c + 8 * h);
#define ON(i) cvtpk(fmul1(o[t][i], inv), fmul1(o[t][(i) + 1], inv))
            ob[0] = ON(0); ob[1] = ON(2); ob[4] = ON(4); ob[5] = ON(6);
            if (h == 0) ob[8] = ON(8);
        }
        asm volatile("s_waitcnt lgkmcnt(0)" ::: "memory");
        if (lane == 0) __hip_atomic_fetch_add((LAS unsigned*)SY, 1u, __ATOMIC_RELAXED, __HIP_MEMORY_SCOPE_WORKGROUP);
        __builtin_amdgcn_s_setprio(APRIO);
    }
    {
#define TK()
#define TP()
        TP();
        UA ua;
#pragma unroll
        for (int i = 0; i < 4; ++i) ua.p[i] = ws + WS_UO + (size_t)rb * 90 * 4096 + wid * 4096 + ((wid + 1 + i) & 3) * 1024;
        auto relane = [&]() { unsigned t_ = va; PIN(t_); return (int)(t_ >> 4); };
        unsigned so[4];
#pragma unroll
        for (int i = 0; i < 4; ++i) so[i] = __builtin_amdgcn_readfirstlane(k3_so(wid, i));
#define LG0() asm volatile("s_waitcnt lgkmcnt(0)" ::: "memory"); SB()
#define LB(d, i) k3_lb(d, va, va5, wb + (i) * 36864)
#define RB(d, sl) { asm volatile("" : "+s"(so[sl])); k3_rdb(d, lds + va + so[sl], lds + va5 + so[sl]); }
        B5 rb4, rb5; bf16x8 a0[4], a1[4], a2[4], a3[4], a4[4];
        k3_lb_if(rb2, va, va5, wb + 2 * 36864, wid >> 2, 0); k3_lb_if(rb3, va, va5, wb + 3 * 36864, wid >> 2, 0);
        TP();
        f32x4 ac[4][5];
        const size_t xo = (size_t)(rb * 64 + (wid & 3) * 16) * D_ + 72 * j3;
        if (wid >= 4) {
            u32x2 xv[5];
            const unsigned vx = (lane & 15) * (D_ * 2) + (lane >> 4) * 8;
            asm volatile("global_load_dwordx2 %0, %5, %7\n\tglobal_load_dwordx2 %1, %5, %7 offset:32\n\tglobal_load_dwordx2 %2, %5, %7 offset:64\n\tglobal_load_dwordx2 %3, %5, %7 offset:96\n\t"
                         "global_load_dwordx2 %4, %6, %7 offset:128" : "=&v"(xv[0]), "=&v"(xv[1]), "=&v"(xv[2]), "=&v"(xv[3]), "=&v"(xv[4]) : "v"(vx), "v"(vx & ~16u), "s"(x + xo) : "memory");
            while (SY[0] < 4) __builtin_amdgcn_s_sleep(1);
            const int orow = pm * 128 + j * 8;
            char* ob = ws + WS_UO + ((size_t)((orow >> 6) * 90 + 72) * 4 + ((orow >> 4) & 3)) * 1024 + (orow & 15) * 16;
#pragma unroll
            for (int r = 0; r < 3; ++r) {
                const int i = (wid - 4) * 144 + r * 64 + lane, ck = i >> 3, row = i & 7;
                if (r < 2 || lane < 16) st16(ob + (ck >> 2) * 4096 + (ck & 3) * 256 + row * 16, *(const LAS u32x4*)(lds + k3_orow(row) + ck * 16));
            }
            TP(); LB(rb4, 4); LB(rb5, 5); TP();
            wait_vm<10>();
            if (lane == 0) __hip_atomic_store((gu64*)(ws + WS_FL) + (pm * 2 + (j >> 3)) * 64 + 32 + (j & 7) * 4 + (wid - 4), epoch, RLX_AGENT);
#pragma unroll
            for (int n = 0; n < 5; ++n) { PIN(xv[n]); ac[3][n] = f32x4{bflo(xv[n][0]), bfhi(xv[n][0]), bflo(xv[n][1]), bfhi(xv[n][1])}; PIN(ac[3][n]); }
        } else {
            TP(); LB(rb4, 4); LB(rb5, 5); TP();
#pragma unroll
            for (int n = 0; n < 5; ++n) { ac[3][n] = f32x4{}; PIN(ac[3][n]); }
        }
        SB();
        TP();
        if (wid == PW2) {
            const unsigned long long t0 = __builtin_amdgcn_s_memrealtime();
            u64 f = __hip_atomic_load(FLr + (lane & 31), RLX_AGENT);
            while (!__all(f == epoch) && __builtin_amdgcn_s_memrealtime() - t0 < 5000000ull) {
                __builtin_amdgcn_s_sleep(POLLSLEEP);
                f = __hip_atomic_load(FLr + (lane & 31), RLX_AGENT);
            }
            SY[1] = 1;
        }
        while (SY[1] == 0) __builtin_amdgcn_s_sleep(1);
        TP();
        STAMP(2, 0); STAMP(2, 1);
#define LA4 "global_load_dwordx4 %0, %4, %5 sc1\n\tglobal_load_dwordx4 %1, %4, %6 sc1\n\tglobal_load_dwordx4 %2, %4, %7 sc1\n\tglobal_load_dwordx4 %3, %4, %8 sc1\n"
#define LAO(d, o_) : "=&v"(d[0]), "=&v"(d[1]), "=&v"(d[2]), "=&v"(d[3]) : "v"(va), "s"(ua.p[0] + (o_)), "s"(ua.p[1] + (o_)), "s"(ua.p[2] + (o_)), "s"(ua.p[3] + (o_))
#define LA(d, i) asm volatile(LA4 LAO(d, (i) * 32768) : "memory")
#define PIN4(d) PIN(d[0]); PIN(d[1]); PIN(d[2]); PIN(d[3])
#define GO(d, rb_, W, NX, ...) wait_vm<W>(); TK(); PIN4(d); SB(); k3_step<NX, ##__VA_ARGS__>(ac, d, rb_, va, ua)
        TK();
        LA(a0, 0); LA(a1, 1);
        const bool ext = (wid & 6) == 4;
        GO(a0, rb0, 4, 2, true);
        pin5(rb2); pin5(rb3); pin5(rb4); pin5(rb5);
        GO(a1, rb1, 4, 3);
        asm volatile("s_mov_b32 m0, %2\n\ts_nop 0\n\tglobal_load_lds_dword %0, %1 sc1" ::"v"(relane() * 4), "s"(FLr + 32), "s"(lds0 + TOPL + 64 + wid * 256));
        GO(a0, rb2, 5, 4);
        GO(a1, rb3, 5, 5); LB(rb0, 10);
        GO(a0, rb4, 9, 6); k3_lb_if(rb1, va, va5, wb + 11 * 36864 - 4 * 4608, wid >> 1, 2); RB(rb2, 0);
        const int l2 = relane();
        const unsigned fo = *(const volatile LAS unsigned*)(lds + TOPL + 64 + wid * 256 + l2 * 4);
        SB();
        GO(a1, rb5, 9, 7); LA(a2, 8); RB(rb3, 1); SB();
        if (__builtin_expect(!__all(fo == ((l2 & 1) ? (unsigned)(epoch >> 32) : (unsigned)epoch)), 0)) {
            const unsigned long long t0 = __builtin_amdgcn_s_memrealtime();
            u64 f2; do { f2 = __hip_atomic_load(FLr + l2, RLX_AGENT); } while (!__all(f2 == epoch) && __builtin_amdgcn_s_memrealtime() - t0 < 5000000ull);
        }
        GO(a0, rb2, 8, 9); LA(a3, 10); RB(rb4, 2); SB();
        asm volatile("s_cmp_lg_u32 %9, 2\n\ts_cbranch_scc1 .Lla%=\n\t" LA4 ".Lla%=:" LAO(a4, 11 * 32768 - 4 * 4096), "s"(wid >> 1) : "memory", "scc");
        GO(a1, rb3, 12, -1); RB(rb5, 3); SB();
        wait_vm<0>(); TK(); PIN4(a0); PIN4(a2); PIN4(a3); PIN4(a4); pin5(rb0); pin5(rb1);
        if (ext) k3_step<-1>(ac, a4, rb1, va, ua);
        const int l3 = relane(), r16 = l3 & 15, g = l3 >> 4;
        LAS char* pw = (LAS char*)lds + va;
        SB();
#pragma unroll
        for (int m = 0; m < 4; ++m)
#pragma unroll
            for (int q = 0; q < 15; ++q) {
                if (q < 5) K3MM(ac[m][q], rb4.b[q], a2[m]); else if (q < 10) K3MM(ac[m][q - 5], rb0.b[q - 5], a3[m]); else K3MM(ac[m][q - 10], rb5.b[q - 10], a0[m]);
                SB();
                if (m > 0 && q % 3 == 0) { const int n = q / 3; if (n < 4 || g < 2) *(LAS f32x4*)(pw + so[m - 1] + n * 1024) = ac[m - 1][n]; SB(); }
            }
        asm volatile("s_nop 7\n\ts_nop 7\n\ts_waitcnt lgkmcnt(0)" ::: "memory");
        TK();
        STAMP(2, 2);
        const LAS char* pr = lds + va;
#define PART(k, n) *(const LAS f32x4*)(pr + k3_so((wid & 4) + ((wid + 3 - (k)) & 3), k) + (n) * 1024)
        if (wid >= 4) {
            __builtin_amdgcn_s_setprio(EPRIO);
            if (l3 == 0) __hip_atomic_fetch_add((LAS unsigned*)SY + 4, 1u, __ATOMIC_RELAXED, __HIP_MEMORY_SCOPE_WORKGROUP);
            while (SY[4] < 4) __builtin_amdgcn_s_sleep(1);
            SB();
#pragma unroll
            for (int n = 0; n < 5; ++n) {
                const f32x4 p0 = PART(0, n), p1 = PART(1, n), p2 = PART(2, n);
#pragma unroll
                for (int e = 0; e < 4; ++e) ac[3][n][e] = fadd1(fadd1(ac[3][n][e], p0[e]), fadd1(p1[e], p2[e]));
            }
#pragma unroll
            for (int n = 0; n < 5; ++n) if (n < 4 || g < 2) *(LAS f32x4*)(pw + so[3] + n * 1024) = ac[3][n];
        }
        asm volatile("s_waitcnt lgkmcnt(0)" ::: "memory"); TK();
        asm volatile("s_barrier" ::: "memory"); TK();
        if (wid < 4) {
            u32x2 o[5];
#pragma unroll
            for (int n = 0; n < 5; ++n) {
                const f32x4 r = ((ac[3][n] + PART(0, n)) + (PART(1, n) + PART(2, n))) + *(const LAS f32x4*)(pr + k3_so(wid + 4, 3) + n * 1024);
                o[n] = u32x2{cvtpk(r[0], r[1]), cvtpk(r[2], r[3])};
            }
            u16* op = out + xo + r16 * D_;
#pragma unroll
            for (int n = 0; n < 4; n += 2) {
                auto r0 = __builtin_amdgcn_permlane16_swap(o[n][0], o[n + 1][0], false, false); auto r1 = __builtin_amdgcn_permlane16_swap(o[n][1], o[n + 1][1], false, false);
                *(u32x4*)(op + (n + (g & 1)) * 16 + (g >> 1) * 8) = u32x4{r0[0], r1[0], r0[1], r1[1]};
            }
            if (g < 2) *(u32x2*)(op + 64 + g * 4) = o[4];
        }
    }
    STAMP_DRAIN(); STAMP(2, 3);
}
__global__ __launch_bounds__(512, 2) void fused_kernel(const u16* __restrict__ x, const u16* __restrict__ wq, const u16* __restrict__ wk, const u16* __restrict__ wv,
                                                       const u16* __restrict__ w_in, const u16* __restrict__ w_sp, const u16* __restrict__ b_sp, const u16* __restrict__ w_out,
                                                       const u16* __restrict__ wo, char* __restrict__ ws, u16* __restrict__ out) {
    extern __shared__ __attribute__((aligned(16))) char smem[];
    if (threadIdx.x == 0) { const u64 t_ = __builtin_amdgcn_s_memrealtime(); *(volatile LAS u64*)((LAS char*)smem + TOPL + 2560) = t_; *(volatile LAS u64*)((LAS char*)smem + TOPL + 2568) = t_; }
    const u64 epoch = phase1(x, wq, wk, wv, w_in, w_sp, b_sp, w_out, wo, ws);
    phase2(w_sp, b_sp, x, ws, out, epoch, __builtin_amdgcn_readfirstlane(threadIdx.x >> 6));
    if (blockIdx.x == 100 && threadIdx.x == 0) {
        const u64 d = *(volatile LAS u64*)((LAS char*)smem + TOPL + 2568) - *(volatile LAS u64*)((LAS char*)smem + TOPL + 2560);
        const u64 t0 = __builtin_amdgcn_s_memrealtime(), w = (d < 20000 ? d : 20000) * 4 + 300;
        while (__builtin_amdgcn_s_memrealtime() - t0 < w) __builtin_amdgcn_s_sleep(1);
    }
}
__global__ void text_end_marker() { asm volatile(".Lk3_end:"); }

extern "C" void kernel_launch(void* const* d_in, const int* in_sizes, int n_in, void* d_out, int out_size, void* d_ws, size_t ws_size, hipStream_t stream) {
    const u16* x = (const u16*)d_in[0]; const u16* wq = (const u16*)d_in[1]; const u16* wk = (const u16*)d_in[2]; const u16* wv = (const u16*)d_in[3];
    const u16* wo = (const u16*)d_in[4]; const u16* w_in = (const u16*)d_in[6]; const u16* w_sp = (const u16*)d_in[7]; const u16* b_sp = (const u16*)d_in[8];
    const u16* w_out = (const u16*)d_in[9]; char* ws = (char*)d_ws;
    (void)hipFuncSetAttribute((const void*)fused_kernel, hipFuncAttributeMaxDynamicSharedMemorySize, LDS_ALL);
    fused_kernel<<<256, 512, LDS_ALL, stream>>>(x, wq, wk, wv, w_in, w_sp, b_sp, w_out, wo, ws, (u16*)d_out);
}
```

```cpp
#include <hip/hip_runtime.h>
#include <hip/hip_bf16.h>
#include <stdint.h>

#define STNOP 1
typedef unsigned short u16;
typedef unsigned long long u64;
typedef short bf16x8 __attribute__((ext_vector_type(8)));
typedef short s16x4 __attribute__((ext_vector_type(4)));
typedef float f32x2 __attribute__((ext_vector_type(2)));
typedef float f32x4 __attribute__((ext_vector_type(4)));
typedef float f32x16 __attribute__((ext_vector_type(16)));
typedef unsigned u32x4 __attribute__((ext_vector_type(4)));
typedef unsigned u32x2 __attribute__((ext_vector_type(2)));
typedef __bf16 bf16x2_t __attribute__((ext_vector_type(2)));
#define LAS __attribute__((address_space(3)))
#define DI __device__ __forceinline__

constexpr int Z1L = 122880, TOPL = 159744, LDS_ALL = 163840;
constexpr int K2_SA = 16384, K2_SB = 18432, K2_SK = 6144, K2_B0 = 3 * K2_SA, K2_K0 = K2_B0 + 3 * K2_SB, K2_X0 = 147520, K2_LDS = K2_X0 + 9344 + 64 + 2048;
constexpr int S_ = 2048, D_ = 576, FH = 2304, F_ = 4608;

constexpr size_t WS_Q = 0;
constexpr size_t WS_KV = WS_Q + (size_t)S_ * D_ * 2;
constexpr size_t WS_Z1 = WS_KV + 64 * 3072;
constexpr size_t WS_Z2T = WS_Z1 + (size_t)S_ * FH * 2;
constexpr size_t WS_UO = WS_Z2T + (size_t)S_ * FH * 2;
constexpr size_t WS_ST = WS_UO + (size_t)S_ * 2880 * 2;
constexpr size_t WS_XCH = WS_ST + (size_t)S_ * 16 * 8;
constexpr size_t WS_XR = WS_XCH + 4096 + 16384;
constexpr size_t WS_FL = WS_XR + 4096;
constexpr size_t WS_FA = WS_FL + 16384;
constexpr size_t WS_FJ = WS_FA + 2048;
constexpr size_t WS_FP = WS_FJ + 8192;
constexpr size_t WS_W3T = WS_FP + 2048;
constexpr size_t WS_DBG = 128u << 20;
#define STAMP(kid, i)
#define STAMP_DRAIN()
#define STAMPT(tid, kid, i)

DI unsigned cvtpk(float lo, float hi) { f32x2 v = {lo, hi}; return __builtin_bit_cast(unsigned, __builtin_convertvector(v, bf16x2_t)); }
DI float bf2f(u16 b) { return __uint_as_float(((unsigned)b) << 16); }
DI float bflo(unsigned w) { return __uint_as_float(w << 16); }
DI float bfhi(unsigned w) { return __uint_as_float(w & 0xffff0000u); }
#define SB() __builtin_amdgcn_sched_barrier(0)
#define PIN(x) asm volatile("" : "+v"(x))
#define EX(x) __builtin_amdgcn_exp2f(x)
#define MFMA16(a, b, c) __builtin_amdgcn_mfma_f32_16x16x32_bf16(a, b, c, 0, 0, 0)
#define MFMA32(a, b, c) __builtin_amdgcn_mfma_f32_32x32x16_bf16(a, b, c, 0, 0, 0)

DI s16x4 trread(const LAS char* p) { return __builtin_bit_cast(s16x4, __builtin_amdgcn_ds_read_tr16_b64_v4i16((LAS s16x4*)p)); }
template <int N> DI void wait_vm_bar() { asm volatile("s_waitcnt vmcnt(%0) lgkmcnt(0)\n\ts_barrier" ::"n"(N) : "memory"); }
template <int N> DI void wait_vm() { asm volatile("s_waitcnt vmcnt(%0)" ::"n"(N) : "memory"); }
template <int OFF> DI void glds_s(unsigned voff, const char* sbase) { asm volatile("global_load_lds_dwordx4 %0, %1 offset:%2" ::"v"(voff), "s"(sbase), "n"(OFF)); }
template <int OFF> DI void glds_sc1(unsigned voff, const char* sbase) { asm volatile("global_load_lds_dwordx4 %0, %1 offset:%2 sc1" ::"v"(voff), "s"(sbase), "n"(OFF)); }
DI void glds_v(const char* p, unsigned m0v, u64 em) { asm volatile("s_mov_b32 m0, %1\n\ts_mov_b64 exec, %2\n\tglobal_load_lds_dwordx4 %0, off\n\ts_mov_b64 exec, -1" ::"v"(p), "s"(m0v), "s"(em)); }
DI unsigned ld_sc1_async(const char* sb, unsigned vo) { unsigned v; asm volatile("global_load_dword %0, %1, %2 sc1" : "=v"(v) : "v"(vo), "s"(sb) : "memory"); return v; }
#define WT
DI void st16(void* p, u32x4 v) { asm volatile("global_store_dwordx4 %0, %1, off sc1\n\ts_nop 1" :: "v"(p), "v"(v) : "memory"); }
DI void st8(void* p, u32x2 v) { asm volatile("global_store_dwordx2 %0, %1, off sc1" :: "v"(p), "v"(v) : "memory"); }
template <int OFF> DI void st16s(const char* sb, unsigned vo, u32x4 v) { asm volatile("global_store_dwordx4 %0, %1, %2 offset:%3 sc1\n\ts_nop %4" :: "v"(vo), "v"(v), "s"(sb), "n"(OFF), "n"(STNOP) : "memory"); }
template <int OFF> DI void st8s(const char* sb, unsigned vo, u32x2 v) { asm volatile("global_store_dwordx2 %0, %1, %2 offset:%3 sc1" :: "v"(vo), "v"(v), "s"(sb), "n"(OFF) : "memory"); }
DI float fadd1(float a, float b) { float r = a + b; asm("" : "+v"(r)); return r; }
DI float fmul1(float a, float b) { float r = a * b; asm("" : "+v"(r)); return r; }
DI float ffma1(float a, float b, float c) { float r = __builtin_fmaf(a, b, c); asm("" : "+v"(r)); return r; }
DI float lone(float a) { asm("" : "+v"(a)); return a; }
DI void set_m0(unsigned v) { asm volatile("s_mov_b32 m0, %0\n\ts_nop 0" ::"s"(v)); }
DI bf16x8 ldsA(const LAS char* p) { return *(const LAS bf16x8*)p; }
DI bf16x8 ldsB2(const LAS char* p0, const LAS char* p1) { s16x4 lo = trread(p0), hi = trread(p1); return __builtin_shufflevector(lo, hi, 0, 1, 2, 3, 4, 5, 6, 7); }
DI bf16x8 ldsB(const LAS char* p) { return ldsB2(p, p + 256); }

DI float gelu_t(float x) {
    const float c0 = -2.0f * 1.4426950408889634f * 0.7978845608028654f;
    const float c1 = c0 * 0.044715f;
    float x2 = x * x;
    float t = __builtin_fmaf(x2, c1, c0);
    float e = __builtin_amdgcn_exp2f(x * t);
    return x * __builtin_amdgcn_rcpf(1.0f + e);
}
constexpr float QSCALE = 0.23570226039551584f * 1.4426950408889634f;

#define KO1 (3 * (((idx & 7) + (idx >> 3)) % 3))
#define KO2 (((idx & 7) + 2 * (idx >> 3) + 4 * (xcd & 1)) % 9)
#define K2_KO (8 * (xcd >> 1))
#define EPRIO 1
#define PFW 0
#define SPRIO 1
#define HW 3
#define ZSCH 5
#define ZS0 1
#define GPRIO 1
#define ZPOLL 1
#define QEARLY 1
#define WSTR 32
#define WOFF 0
#define W2STR 32
#define TFA 7
#define PW2 3
#define PEARLY 0
#define POLLSLEEP 0
#define APRIO 0
#define K3MAP 2
#define K3ROT 0
#define APRE 0
#define AGSLOT 1
#define ZHALF 1
#define POLLW 5
#define POLLSLEEP1 1
#define PLSLOT 0
constexpr int K1_SA = 16384, K1_SB = 24576, K1_B0 = 3 * K1_SA, K1_TAB = K1_B0 + 3 * K1_SB, K1_X0 = K1_TAB + 10240, K1_LDS = K1_X0 + 16384 + 2048;
typedef __attribute__((address_space(1))) unsigned gu32;
typedef __attribute__((address_space(1))) unsigned long long gu64;
#define RLX_AGENT __ATOMIC_RELAXED, __HIP_MEMORY_SCOPE_AGENT
struct K1F { bf16x8 a[4], b[3], x; };
struct K1S {
    unsigned va[2], vb[2]; const char* sa; const char* sb;
    const char* gp; unsigned st;
    int k1, k2;
    unsigned m0a, m0b; u64 em;
    const LAS char* pa[2]; const LAS char* px[2]; const LAS char* pb[3];
};
DI void k1_issue(K1S& s, int i, int w) {
    if (i == 0) { set_m0(s.m0a + w * K1_SA); glds_s<0>(s.va[0], s.sa); }
    if (i == 1) { glds_s<1024>(s.va[1], s.sa); s.sa += s.k1 == 8 ? -8 * 128 : 128; asm volatile("" : "+s"(s.sa)); }
    if (i == 2) { glds_v(s.gp + (unsigned long long)s.st * (unsigned)s.k1, s.m0b + w * K1_SB + 2048, s.em); s.k1 = s.k1 == 8 ? 0 : s.k1 + 1; asm volatile("" : "+s"(s.k1)); }
    if (i == 3) { set_m0(s.m0b + w * K1_SB); glds_s<0>(s.vb[0], s.sb); }
    if (i == 4) { glds_s<1024>(s.vb[1], s.sb); s.sb += s.k2 == 8 ? -8 * 128 * F_ : 128 * F_; s.k2 = s.k2 == 8 ? 0 : s.k2 + 1; asm volatile("" : "+s"(s.sb), "+s"(s.k2)); }
}
DI f32x2 gelu_y2(f32x2 y, float k0, float k1, float ir) {
    f32x2 t = (y * y) * k1 + k0;
    f32x2 a = y * t;
    f32x2 e = {__builtin_amdgcn_exp2f(a[0]), __builtin_amdgcn_exp2f(a[1])};
    f32x2 d = e * ir + ir;
    f32x2 rc = {__builtin_amdgcn_rcpf(d[0]), __builtin_amdgcn_rcpf(d[1])};
    return y * rc;
}
DI float dpp_x1(float x) { return __int_as_float(__builtin_amdgcn_mov_dpp(__float_as_int(x), 0xB1, 0xf, 0xf, true)); }
DI float dpp_x2(float x) { return __int_as_float(__builtin_amdgcn_mov_dpp(__float_as_int(x), 0x4E, 0xf, 0xf, true)); }
DI void kv_store(const char* kv, int sv, int j, int c, int g, f32x4 v) {
    if (j == 4) { st8s<0>(kv + sv * 256, ((g >> 1) * 32 + c) * 16 + (g & 1) * 8, u32x2{cvtpk(v[0], v[1]), cvtpk(v[2], v[3])}); return; }
    const unsigned kk = cvtpk(v[2], v[3]);
    const bool o1 = c & 1, o2 = c & 2;
    float t0 = dpp_x1(o1 ? v[0] : v[1]), t1 = dpp_x1(o1 ? v[2] : v[3]);
    if (o1) { v[0] = t0; v[2] = t1; } else { v[1] = t0; v[3] = t1; }
    t0 = dpp_x2(o2 ? v[0] : v[2]); t1 = dpp_x2(o2 ? v[1] : v[3]);
    if (o2) { v[0] = t0; v[1] = t1; } else { v[2] = t0; v[3] = t1; }
    u32x2 w = {cvtpk(v[0], v[1]), cvtpk(v[2], v[3])};
    const int kq = c >> 2; unsigned off;
    if (j == 5) {
        off = g < 2 ? c * 16 + (g ^ 1) * 8 : 256 + ((kq & 1) * 8 + 4 * (g - 2) + (c & 3)) * 16 + (kq >> 1) * 8;
        if (g < 2) w = g ? u32x2{kk, 0x3f80u} : u32x2{0u, 0u};
    } else {
        off = 512 + ((kq & 1) * 16 + 4 * g + (c & 3)) * 16 + (kq >> 1) * 8;
        if (g == 3 && !o2) { const unsigned cv = o1 ? 0u : 0x3f803f80u; w = u32x2{cv, cv}; }
    }
    st8s<1024>(kv + sv * 1024, off, w);
}
template <int KS, int GL, int RS, int WS, bool RX> DI void k1_p1(K1S& s, const K1F& f, K1F& n, f32x4 (&acc)[4][2], f32x4& sacc, f32x4 (&cso)[2], f32x4& rs, f32x4& rq, const bf16x8& ones) {
    constexpr int AO = RS * K1_SA, BO = RS * K1_SB + KS * 12288;
#define MX(d, a_, b_) d = MFMA16(a_, b_, d); PIN(d); SB()
#define MM(m, nn) MX(acc[m][nn], f.b[nn], f.a[m])
    MM(0, 0); n.b[0] = ldsB(s.pb[0] + BO); SB();
    MM(1, 0); n.a[0] = ldsA(s.pa[KS] + AO); SB();
    MM(2, 0); n.a[1] = ldsA(s.pa[KS] + AO + 2048); SB();
    MM(3, 0); n.a[2] = ldsA(s.pa[KS] + AO + 4096); SB();
    MX(cso[0], f.b[0], ones); n.a[3] = ldsA(s.pa[KS] + AO + 6144); SB();
    MX(sacc, f.b[2], f.x); n.b[1] = ldsB(s.pb[1] + BO); SB();
    MX(cso[1], f.b[2], ones); n.b[2] = ldsB(s.pb[2] + BO); SB();
    MX(rs, ones, f.x); k1_issue(s, GL == 1 ? 0 : 3, WS); SB();
    MX(rq, f.x, f.x); if (RX) n.x = ldsA(s.px[KS] + AO); SB();
    MM(0, 1); k1_issue(s, GL == 1 ? 1 : 4, WS); SB();
    MM(1, 1); if (GL == 1) k1_issue(s, 2, WS); SB();
    MM(2, 1);
    MM(3, 1);
}
template <int KS, int GL, int RS, int WS, bool RD, class E> DI void k1_p2(K1S& s, const K1F& f, K1F& n, f32x4 (&acc)[4][3], f32x4 (&cso)[2], const bf16x8& ones, E epi) {
    constexpr int AO = RS * K1_SA, BO = RS * K1_SB + KS * 12288;
    MM(0, 0); if (RD) n.b[0] = ldsB(s.pb[0] + BO); epi(0); SB();
    MM(1, 0); if (RD) n.a[0] = ldsA(s.pa[KS] + AO); epi(1); SB();
    MM(2, 0); if (RD) n.a[1] = ldsA(s.pa[KS] + AO + 2048); epi(2); SB();
    MM(3, 0); if (RD) n.a[2] = ldsA(s.pa[KS] + AO + 4096); epi(3); SB();
    MX(cso[0], f.b[0], ones); if (RD) n.a[3] = ldsA(s.pa[KS] + AO + 6144); epi(4); SB();
    MM(0, 1); if (RD) n.b[1] = ldsB(s.pb[1] + BO); epi(5); SB();
    MM(1, 1); if (RD) n.b[2] = ldsB(s.pb[2] + BO); epi(6); SB();
    MM(2, 1); if (GL) k1_issue(s, GL == 1 ? 0 : 3, WS); epi(7); SB();
    MM(3, 1); epi(8); SB();
    MX(cso[1], f.b[1], ones); if (GL) k1_issue(s, GL == 1 ? 1 : 4, WS); epi(9); SB();
    MM(0, 2); epi(10); SB();
    MM(1, 2); if (GL == 1) k1_issue(s, 2, WS); epi(11); SB();
    MM(2, 2); epi(12); SB();
    MM(3, 2); epi(13); SB();
#undef MM
#undef MX
}

constexpr unsigned XSALT = 0x9e3779b9u;
DI void epoch_reply(char* ws, int lane, const unsigned (&nv)[4], u64 epoch) {
    const unsigned lo = (unsigned)epoch, hi = (unsigned)(epoch >> 32);
    st16s<0>(ws + WS_XR, lane * 16, u32x4{nv[0], lo, hi, nv[0] ^ lo ^ hi ^ XSALT}); st16s<1024>(ws + WS_XR, lane * 16, u32x4{nv[1], lo, hi, nv[1] ^ lo ^ hi ^ XSALT});
    st16s<2048>(ws + WS_XR, lane * 16, u32x4{nv[2], lo, hi, nv[2] ^ lo ^ hi ^ XSALT}); st16s<3072>(ws + WS_XR, lane * 16, u32x4{nv[3], lo, hi, nv[3] ^ lo ^ hi ^ XSALT});
}
#define XN_LOADS "global_load_dword %0, %4, %5 sc1\n\tglobal_load_dword %1, %4, %5 offset:256 sc1\n\tglobal_load_dword %2, %4, %5 offset:512 sc1\n\tglobal_load_dword %3, %4, %5 offset:768 sc1\n"
DI void epoch_service(char* ws, int lane, u64 epoch) {
    unsigned nv[4];
    asm volatile(XN_LOADS "\ts_waitcnt vmcnt(0)" : "=&v"(nv[0]), "=&v"(nv[1]), "=&v"(nv[2]), "=&v"(nv[3]) : "v"(lane * 4), "s"(ws + WS_XCH) : "memory");
    epoch_reply(ws, lane, nv, epoch);
}
DI void poll_flags(char* ws, u64 epoch, int lane, const char* sb, unsigned vo) {
    const u64 t0 = __builtin_amdgcn_s_memrealtime();
    for (;;) {
        u64 f_;
        asm volatile("global_load_dwordx2 %0, %1, %2 sc1\n\ts_waitcnt vmcnt(0)" : "=&v"(f_) : "v"(vo), "s"(sb) : "memory");
        if (__all(f_ == epoch)) break;
        const u64 dt = __builtin_amdgcn_s_memrealtime() - t0;
        if (dt > 5000000ull) break;
        if (dt > 1000 && blockIdx.x == 0) epoch_service(ws, lane, epoch);
        __builtin_amdgcn_s_sleep(POLLSLEEP1);
    }
}
DI void first_loads(char* ws, unsigned lds0, int lane, int wid, int j) {
    const unsigned m0 = lds0 + K2_B0 + wid * 6144, vl = lane * 16;
    const char* sl = ws + WS_Z2T + j * 32 * 18432 + wid * 6144;
#pragma unroll
    for (int t = 0; t < 2; ++t) {
        set_m0(m0 + t * K2_SB); glds_sc1<0>(vl, sl); glds_sc1<1024>(vl, sl); glds_sc1<2048>(vl, sl); glds_sc1<3072>(vl, sl);
        set_m0(m0 + t * K2_SB + 4096); glds_sc1<0>(vl, sl + 4096); glds_sc1<1024>(vl, sl + 4096); sl += K2_SB;
    }
}
DI unsigned g2_va(int wid, int lane, int i) { int row = 8 * (wid * 4 + i) + (lane >> 3), c = (lane & 7) ^ ((row >> 1) & 7); return row * (S_ * 2) + c * 16 + 4096 - i * 1024; }
struct G2P { unsigned va[4]; const LAS char* pa[2][2]; const LAS char* pb[2]; const LAS char* ps[2]; f32x4 acc[4][4], sacc[2]; };
DI void g2_prep(G2P& s, const LAS char* lds, int lane, int wid) {
    const int wr = wid >> 1, wc = wid & 1, r = lane & 15, g = lane >> 4, q = r >> 2, p = lane & 3;
#pragma unroll
    for (int m = 0; m < 4; ++m) {
        const float b = bf2f(*(const LAS u16*)(lds + TOPL + 2304 + (wr * 64 + (m ^ (2 * wc)) * 16 + r) * 2));
#pragma unroll
        for (int n = 0; n < 4; ++n) { s.acc[m][n] = f32x4{b, b, b, b}; PIN(s.acc[m][n]); }
        if (m < 2) { s.sacc[m] = f32x4{b, b, b, b}; PIN(s.sacc[m]); }
    }
#pragma unroll
    for (int i = 0; i < 4; ++i) { s.va[i] = g2_va(wid, lane, i); PIN(s.va[i]); }
#pragma unroll
    for (int ks = 0; ks < 2; ++ks) {
        const int o = r * 128 + (((4 * ks + g) ^ (r >> 1)) * 16) + wr * 8192;
        s.pa[0][ks] = lds + o + wc * 4096; s.pa[1][ks] = lds + o + (1 - wc) * 4096; PIN(s.pa[0][ks]); PIN(s.pa[1][ks]);
    }
    const int ob = K2_B0 + g * 2304 + q * 32 + 8 * p;
    s.pb[0] = lds + ob + wc * 1024 + ((g & 1) * 128); s.pb[1] = lds + ob + wc * 1024 + (((g & 1) ^ 1) * 128);
    s.ps[0] = lds + ob + 2048 + ((g & 1) * 128); s.ps[1] = lds + ob + 2048 + (((g & 1) ^ 1) * 128);
    PIN(s.pb[0]); PIN(s.pb[1]); PIN(s.ps[0]); PIN(s.ps[1]);
}
DI u64 phase1(G2P& gp, const u16* __restrict__ x, const u16* __restrict__ wq, const u16* __restrict__ wk, const u16* __restrict__ wv,
                                                    const u16* __restrict__ w_in, const u16* __restrict__ w_sp, const u16* __restrict__ b_sp, const u16* __restrict__ w_out, const u16* __restrict__ wo, char* __restrict__ ws) {
    extern __shared__ __attribute__((aligned(16))) char smem[];
    const int bid = blockIdx.x, xcd = bid & 7, idx = bid >> 3;
    const int pm = (xcd & 1) * 8 + (idx & 7), j = (xcd >> 1) * 4 + (idx >> 3);
    const int lane = threadIdx.x & 63, wid = __builtin_amdgcn_readfirstlane(threadIdx.x >> 6), wr = wid >> 2, wc = wid & 3;
    const unsigned lds0 = (unsigned)(uintptr_t)smem;
    const LAS char* lds = (const LAS char*)smem;
    {
        const char* pc; unsigned len;
        asm volatile("s_getpc_b64 %0\n.Lk1_here:\n\ts_mov_b32 %1, .Lk3_end-.Lk1_here" : "=s"(pc), "=s"(len));
        unsigned off = ((idx * 8 + wid) * 2 + (lane & 1)) * 128; off = off < len ? off : len;
        asm volatile("s_mov_b32 m0, %2\n\ts_nop 0\n\tglobal_load_lds_dword %0, %1" ::"v"(off), "s"(pc), "s"(lds0 + K1_TAB + 9216 + 512));
    }
    K1S s; s.em = (wid & 1) ? 0xccccccccull : 0x33333333ull;
    s.m0a = lds0 + wid * 2048; s.m0b = lds0 + K1_B0 + wid * 3072;
    const int brow = (lane >> 2) & 7, bc = (lane & 3) ^ (2 * (wid & 1)), bk = 8 * wid + brow;
    const int ko2 = KO2, ko1 = KO1;
    unsigned oa = pm * 128 * D_ * 2, ob = 256 * j, oa1 = oa + ko1 * 128, ob1 = ob + FH * 2 + ko1 * 128 * F_, vg = bk * (F_ * 2) + 16 * (bc & 1);
    {
        int r8 = lane >> 3;
        s.va[0] = (16 * wid + r8) * (D_ * 2) + (((lane & 7) ^ (r8 >> 1)) * 16);
        s.va[1] = (16 * wid + 8 + r8) * (D_ * 2) + (((lane & 7) ^ (4 + (r8 >> 1))) * 16) - 1024;
        s.vb[0] = bk * (F_ * 2) + (lane >> 5) * 64 + bc * 16 + 1024; s.vb[1] = s.vb[0] + 128 - 1024;
        s.k1 = s.k2 = ko1; s.st = 128 * F_;
        PIN(s.va[0]); PIN(s.va[1]); PIN(s.vb[0]); PIN(s.vb[1]); PIN(vg);
        asm volatile("" : "+s"(oa1), "+s"(ob1), "+s"(s.m0a), "+s"(s.m0b));
    }
    SB();
    s.sa = (const char*)x + oa1; s.sb = (const char*)w_in - 1024 + ob1;
    k1_issue(s, 0, 0); k1_issue(s, 1, 0); k1_issue(s, 3, 0); k1_issue(s, 4, 0);
    SB();
    STAMP(0, 0);
    s.gp = (const char*)w_in + (4096 + 32 * j + FH * 2) + vg;
    k1_issue(s, 2, 0);
    const char* sa0 = (const char*)x + oa;
    const char* sb0 = (const char*)w_in - 1024 + ob;
    gu32* XN = (gu32*)(ws + WS_XCH) + pm * 16;
    gu32* XE = (gu32*)(ws + WS_XCH + 4096) + pm * 256;
    gu64* XP = (gu64*)(ws + WS_ST) + (size_t)pm * 16 * 128;
    const char* xe_s = ws + WS_XCH + 4096 + (pm * 256 + j * 16) * 4;
    const unsigned le = (lane & 16) ? 16384 - (pm * 16 + j) * 48 + (lane & 3) * 4 : (lane & 15) * 4;
    unsigned eo = ld_sc1_async(xe_s, le);
    if (SPRIO == 1 && wid >= 4) __builtin_amdgcn_s_setprio(1);
    SB();
    unsigned nonce = 0;
    const char* gp2; unsigned st2;
    {
        const u16* p; int rs;
        const u16* sp0 = j < 4 ? wq + 512 + 16 * j : (j == 5 ? wk + 10 : (j == 6 ? wv + 8 : wk));
        const u16* sp1 = j < 4 ? wq + 520 + 16 * j : (j == 5 ? wv : (j == 6 ? wv + 10 : wk + 8));
        const int srs = j < 4 ? D_ : 18;
        if (lane >= 32) { p = wq + 32 * j + 8 * bc; rs = D_; }
        else if (bc < 2) { p = w_in + 2048 + 16 * j + 8 * bc; rs = F_; }
        else { p = bc == 2 ? sp0 : sp1; rs = srs; }
        gp2 = (const char*)(p + (size_t)bk * rs); st2 = 128 * rs; PIN(gp2); PIN(st2);
    }
    const int r16 = lane & 15, g = lane >> 4, cg = wc ^ (2 * wr);
    const int nf3 = cg == 3 ? 8 : (cg == 2 ? 9 : 10 + cg);
#define NF1(n) ((n) == 2 ? 8 : 2 * wc + ((n) ^ wr))
#define NF2(n) ((n) == 0 ? nf3 : 2 * cg + (((n) - 1) ^ wr))
    const LAS char* pb2[3];
    {
        int q = r16 >> 2, p = lane & 3;
#pragma unroll
        for (int ks = 0; ks < 2; ++ks) { s.pa[ks] = lds + (r16 * 128 + (((4 * ks + g) ^ (r16 >> 1)) * 16) + wr * 8192); s.px[ks] = s.pa[ks] + wc * 2048; PIN(s.pa[ks]); PIN(s.px[ks]); }
#pragma unroll
        for (int n = 0; n < 3; ++n) {
            int nf = NF1(n); s.pb[n] = lds + (K1_B0 + g * 3072 + q * 64 + 8 * p + (nf >> 1) * 512 + (((nf ^ g) & 1) * 32)); PIN(s.pb[n]);
            nf = NF2(n); pb2[n] = lds + (K1_B0 + g * 3072 + q * 64 + 8 * p + (nf >> 1) * 512 + (((nf ^ g) & 1) * 32)); PIN(pb2[n]);
        }
    }
    f32x4 acc1[4][2] = {}; f32x4 sacc = {}; f32x4 cso[2] = {};
    f32x4 rs = {}, rq = {};
#pragma unroll
    for (int m = 0; m < 4; ++m) { PIN(acc1[m][0]); PIN(acc1[m][1]); }
    PIN(sacc); PIN(cso[0]); PIN(cso[1]); PIN(rs); PIN(rq);
    const u32x4 onesv = {0x3f803f80u, 0x3f803f80u, 0x3f803f80u, 0x3f803f80u}; bf16x8 ones = __builtin_bit_cast(bf16x8, onesv); PIN(ones);
    K1F f0, f1;
    SB();
#pragma unroll
    for (int i = 0; i < 5; ++i) k1_issue(s, i, 1);
    SB();
    if (SPRIO == 2 && wid >= 4) __builtin_amdgcn_s_setprio(1);
    const int w3p = bid < 240 ? bid : 239;
    unsigned w3vo[2], w3so[2]; const char* w3sp; const LAS char* w3rd[2];
    {
        const int jj = w3p / 30, pc = w3p - jj * 30;
        w3sp = (pc < 24 ? (const char*)w_out : (const char*)wo - 2304 * 1152) + jj * 144;
#pragma unroll
        for (int i = 0; i < 2; ++i) {
            unsigned c_ = threadIdx.x + 512 * i; c_ = c_ < 864 ? c_ : 863;
            const unsigned row = (c_ * 7282) >> 16, k_ = pc * 96 + row, jg = (k_ * 29128) >> 22, l_ = k_ - jg * 144;
            const unsigned src = pc < 24 ? (l_ < 128 ? 128 * jg + l_ : 1920 + 16 * jg + l_) : k_;
            w3vo[i] = src * 1152 + (c_ - row * 9) * 16; PIN(w3vo[i]);
            const int u = i ? (wid < 7 ? wid + 8 : 14) : wid, k = u / 5, n = u - 5 * k;
            w3rd[i] = lds + (K1_X0 + (8 * g + (r16 >> 2)) * 144 + 8 * (lane & 3) + k * 4608 + n * 32); PIN(w3rd[i]);
            w3so[i] = w3p * 13824 + k * 4608 + n * 1024 + (n < 4 ? lane : g * 8 + r16) * 16;
            if (n == 4 && r16 >= 8) w3so[i] = 240 * 13824 + bid * 512 + (g * 8 + r16 - 8) * 16;
            PIN(w3so[i]);
        }
        asm volatile("" : "+s"(w3sp));
    }
    unsigned wspo = (pm * 128 + (idx >> 3) * 32 + wid * 4 + g) * (S_ * 2) + r16 * WSTR + WOFF; PIN(wspo);
    LAS float* redp = (LAS float*)(lds + K1_TAB + 4096) + ((wr * 64 + r16) * 5 + wc) * 2 + (g & 1); PIN(redp);
    LAS float* reds = (LAS float*)(lds + K1_TAB + 4096) + ((wr * 64 + wc * 16 + r16) * 5 + 4) * 2 + (g & 1); PIN(reds);
    SB();
#pragma unroll
    for (int i = 0; i < 3; ++i) k1_issue(s, i, 2);
    __builtin_amdgcn_s_setprio(0);
    SB();
    wait_vm_bar<8>();
    SB();
    f0.b[0] = ldsB(s.pb[0]);
    f0.a[0] = ldsA(s.pa[0]); f0.a[1] = ldsA(s.pa[0] + 2048); f0.a[2] = ldsA(s.pa[0] + 4096); f0.a[3] = ldsA(s.pa[0] + 6144);
    f0.b[2] = ldsB(s.pb[2]); f0.x = ldsA(s.px[0]); f0.b[1] = ldsB(s.pb[1]);
    SB();
    PIN(eo);
    if (wid == HW) {
        nonce = 1;
        while (__ballot(eo == nonce) != 0ull) ++nonce;
        if (lane == 0) __hip_atomic_store(XN + j, nonce, RLX_AGENT);
        __hip_atomic_store(XP + j * 128 + lane, ~0ull, RLX_AGENT); __hip_atomic_store(XP + j * 128 + 64 + lane, ~0ull, RLX_AGENT);
    }
    STAMP(0, 1);
#define P1_A(c) k1_p1<1, 2, (c), ((c) + 2) % 3, true>(s, f0, f1, acc1, sacc, cso, rs, rq, ones); wait_vm_bar<5>(); SB()
#define P1_B(c, RX) k1_p1<0, 1, ((c) + 1) % 3, (c), RX>(s, f1, f0, acc1, sacc, cso, rs, rq, ones)
#pragma unroll 1
    for (int it = 0; it < 2; ++it) { P1_A(0); P1_B(0, true); P1_A(1); P1_B(1, true); P1_A(2); P1_B(2, true); }
    const char* xn_s = ws + WS_XCH + pm * 64; const unsigned l4 = (lane & 15) * 4;
    P1_A(0);
    unsigned echoed = ld_sc1_async(xn_s, l4);
    unsigned alln[4];
    asm volatile("s_cmp_lg_u32 %6, 0\n\ts_cbranch_scc1 .Lld%=\n\t" XN_LOADS ".Lld%=:" : "=&v"(alln[0]), "=&v"(alln[1]), "=&v"(alln[2]), "=&v"(alln[3])
                 : "v"(lane * 4), "s"(ws + WS_XCH), "s"(bid) : "memory", "scc");
    s.k1 = s.k2 = ko2; s.sa = sa0 + ko2 * 128; s.sb = sb0 + (size_t)ko2 * 128 * F_; s.gp = gp2; s.st = st2; s.em = ~0ull; SB();
    P1_B(0, true);
    P1_A(1);
    PIN(echoed);
    if (wid == HW && lane < 16) __hip_atomic_store(XE + lane * 16 + j, echoed, RLX_AGENT);
    PIN(alln[0]); PIN(alln[1]); PIN(alln[2]); PIN(alln[3]);
    if (bid == 0 && wid == HW - 1) epoch_reply(ws, lane, alln, __builtin_amdgcn_s_memrealtime() | (1ull << 63));
    SB();
    P1_B(1, true);
    P1_A(2);
    s.pb[0] = pb2[0]; s.pb[1] = pb2[1]; s.pb[2] = pb2[2];
    P1_B(2, false);
    STAMP(3, 0);
    float* CS = (float*)(smem + K1_TAB);
    f32x4* RS = (f32x4*)(smem + K1_TAB + 2048);
    float* red = (float*)(smem + K1_TAB + 4096);
    f32x2* ZS = (f32x2*)(smem + K1_TAB + 9216);
    volatile LAS unsigned* HS = (volatile LAS unsigned*)(lds + K1_TAB + 1792);
    const float c0 = -2.0f * 1.4426950408889634f * 0.7978845608028654f, c1 = c0 * 0.044715f;
    f32x4 acc2[4][3] = {}; f32x4 cs2[2] = {};
    auto none = [](int) {};
    auto tabs = [&](int st) {
        if (st == 2) { if (r16 == 0) { *(f32x4*)(CS + NF1(0) * 16 + 4 * g) = cso[0]; *(f32x4*)(CS + 128 + 4 * g) = cso[1]; } }
        if (st == 3) {
            float sq = (r16 & 2) ? ((r16 & 1) ? rq[3] : rq[2]) : ((r16 & 1) ? rq[1] : rq[0]);
            float r0_ = rs[0]; PIN(r0_); PIN(sq);
            float mean = fmul1(r0_, 1.0f / D_), var = fmaxf(ffma1(-mean, mean, fmul1(sq, 1.0f / D_)), 0.f);
            rs[0] = mean; rs[1] = var + 1e-5f; PIN(rs);
        }
        if (st == 5) { rs[2] = __builtin_amdgcn_rsqf(rs[1]); PIN(rs); }
        if (st == 7) { float rstd = rs[2]; if (g == (r16 >> 2)) RS[wr * 64 + wc * 16 + r16] = f32x4{rs[0], fmul1(rstd, c0), fmul1(fmul1(rstd, rstd), fmul1(rstd, c1)), fmul1(rs[1], rstd)}; }
    };
    f32x4 er[5], cs1[3], e[3];
    f32x2 su[5], sq[5];
#define ZU(u) ((u) == 8 ? sacc : acc1[((u) >> 1) & 3][(u) & 1])
#define UM(u) ((u) == 8 ? 4 : (u) >> 1)
#define UN(u) ((u) == 8 ? 2 : (u) & 1)
#define PRE(i, u) { f32x4 y = ZU(u) - cs1[UN(u)] * er[UM(u)][0]; e[i] = ((y * y) * er[UM(u)][2] + er[UM(u)][1]) * y; ZU(u) = y; PIN(ZU(u)); PIN(e[i]); }
#define FIN(i, u, first) { ZU(u) *= e[i]; PIN(ZU(u)); f32x4 v = ZU(u); f32x2 lo = {v[0], v[1]}, hi = {v[2], v[3]}; \
        if (first) { su[UM(u)] = lo + hi; sq[UM(u)] = lo * lo + hi * hi; } else { su[UM(u)] += lo; su[UM(u)] += hi; sq[UM(u)] += lo * lo; sq[UM(u)] += hi * hi; } PIN(su[UM(u)]); PIN(sq[UM(u)]); }
#define RED(m) { float a_ = su[m][0] + su[m][1], b_ = sq[m][0] + sq[m][1]; \
            auto x1 = __builtin_amdgcn_permlane16_swap(__float_as_uint(a_), __float_as_uint(b_), false, false);       \
            float t_ = __uint_as_float(x1[0]) + __uint_as_float(x1[1]);                                               \
            auto x2 = __builtin_amdgcn_permlane32_swap(__float_as_uint(t_), __float_as_uint(t_), false, false); \
            t_ = __uint_as_float(x2[0]) + __uint_as_float(x2[1]);                                                     \
            if ((m) < 4) redp[(m) * 160] = t_; else reds[0] = t_; }
#define T1(op, v) asm volatile(op " %0, %0" : "+v"(v))
#define DE1(v, sd) asm volatile("v_fma_f32 %0, %0, %1, %1" : "+v"(v) : "v"(sd))
#define EPI_E(u0, u1, u2) [&](int st) { \
        if (st < 12) { float t_ = e[st >> 2][st & 3]; T1("v_exp_f32", t_); e[st >> 2][st & 3] = t_; } \
        if (st >= 2) { const int v_ = st - 2; float t_ = e[v_ >> 2][v_ & 3]; DE1(t_, er[v_ < 4 ? UM(u0) : (v_ < 8 ? UM(u1) : UM(u2))][3]); e[v_ >> 2][v_ & 3] = t_; } }
#define EPI_R(m0, m1) [&](int st) { if (st < 12) { float t_ = e[st >> 2][st & 3]; T1("v_rcp_f32", t_); e[st >> 2][st & 3] = t_; } \
        if (st == 12 && (m0) >= 0) er[(m0) & 3] = RS[wr * 64 + (m0) * 16 + r16]; if (st == 13 && (m1) >= 0) er[(m1) & 3] = RS[wr * 64 + (m1) * 16 + r16]; }
#define VSYNC() SB(); asm volatile("s_waitcnt lgkmcnt(0)\n\ts_barrier" ::: "memory"); SB()
#define P2_A(c, GL, E) k1_p2<1, GL ? 2 : 0, (c), ((c) + 2) % 3, true>(s, f0, f1, acc2, cs2, ones, E)
#define P2_B(c, GL, RD, E) k1_p2<0, GL ? 1 : 0, ((c) + 1) % 3, (c), RD>(s, f1, f0, acc2, cs2, ones, E)
    P2_A(0, true, tabs); wait_vm_bar<5>(); SB();
    er[0] = RS[wr * 64 + r16]; er[4] = RS[wr * 64 + wc * 16 + r16];
#pragma unroll
    for (int n = 0; n < 3; ++n) cs1[n] = *(const f32x4*)(CS + NF1(n) * 16 + 4 * g);
    PRE(0, 0) PRE(1, 1) PRE(2, 8) VSYNC();
    P2_B(0, true, true, EPI_E(0, 1, 8));
    P2_A(1, true, EPI_R(1, 2)); wait_vm_bar<5>(); SB();
    unsigned ev = ld_sc1_async(xe_s, le);
    auto w3ld = [&](int i) { SB(); set_m0(lds0 + K1_X0 + i * 8192 + wid * 1024); glds_s<0>(w3vo[i], w3sp); SB(); };
    w3ld(0); FIN(0, 0, true) FIN(1, 1, false) FIN(2, 8, true) PRE(0, 2) PRE(1, 3) PRE(2, 4) w3ld(1); RED(0) RED(4) VSYNC();
    P2_B(1, true, true, EPI_E(2, 3, 4));
    P2_A(2, true, EPI_R(3, -1)); wait_vm_bar<7>(); SB();
    PIN(ev);
    bool hs = false; u64 epoch = 0;
    auto xcheck = [&]() {
        hs = hs || __all((lane & 16) || ev == nonce);
        const unsigned w0 = __builtin_amdgcn_readlane(ev, 16), w1 = __builtin_amdgcn_readlane(ev, 17), w2 = __builtin_amdgcn_readlane(ev, 18), w3 = __builtin_amdgcn_readlane(ev, 19);
        if (w0 == nonce && w3 == (w0 ^ w1 ^ w2 ^ XSALT)) epoch = ((u64)w2 << 32) | w1;
    };
    unsigned nn = ld_sc1_async(xn_s, l4);
    asm volatile("s_mov_b32 m0, %2\n\ts_nop 0\n\tglobal_load_lds_dword %0, %1" ::"v"(wspo), "s"(w_sp), "s"(lds0 + K1_X0 + 16384));
    SB(); FIN(0, 2, true) FIN(1, 3, false) FIN(2, 4, true) PRE(0, 5) PRE(1, 6) PRE(2, 7) SB();
    asm volatile("s_mov_b32 m0, %2\n\ts_nop 0\n\tglobal_load_lds_dword %0, %1" ::"v"(lane * 4), "s"(b_sp + pm * 128), "s"(lds0 + TOPL + 2304));
    SB(); RED(1) VSYNC();
    P2_B(2, true, true, EPI_E(5, 6, 7));
    P2_A(0, true, EPI_R(-1, -1)); wait_vm_bar<8>(); SB();
    {
        const bf16x8 w0 = ldsB2(w3rd[0], w3rd[0] + 576), w1 = ldsB2(w3rd[1], w3rd[1] + 576);
        SB(); FIN(0, 5, false) FIN(1, 6, true) FIN(2, 7, false) SB();
        st16s<0>(ws + WS_W3T, w3so[0], __builtin_bit_cast(u32x4, w0));
        SB(); RED(2) RED(3) SB();
        st16s<0>(ws + WS_W3T, w3so[1], __builtin_bit_cast(u32x4, w1));
        VSYNC();
    }
    f32x2 pu[5];
    auto pub = [&](int st) {
        if (st == 0) {
#pragma unroll
            for (int i = 0; i < 5; ++i) pu[i] = *(const f32x2*)(red + (wid * 16 + r16) * 10 + 2 * i);
        }
        if (st == 4) {
            float a = fadd1(fadd1(fadd1(lone(pu[0][0]), pu[1][0]), fadd1(lone(pu[2][0]), pu[3][0])), pu[4][0]), b = fadd1(fadd1(fadd1(pu[0][1], pu[1][1]), fadd1(pu[2][1], pu[3][1])), pu[4][1]);
            unsigned bb = __float_as_uint(b); if (bb == 0xffffffffu) bb = 0x7fc00000u;
            st8s<0>((const char*)(XP + j * 128 + wid * 16), r16 * 8, u32x2{__float_as_uint(a), bb});
        }
        if (st == 13 && wid == HW) { xcheck(); *HS = hs; }
    };
    P2_B(0, true, true, pub);
    P2_A(1, true, none);
    STAMP(3, 1);
    wait_vm_bar<8>(); SB();
    ev = ld_sc1_async(xe_s, le);
    auto reecho = [&](int st) { if (st == 2) { PIN(nn); if (wid == HW && nn != echoed) { echoed = nn; if (lane < 16) __hip_atomic_store(XE + lane * 16 + j, nn, RLX_AGENT); } } };
    P2_B(1, true, true, reecho);
    const char* xp_s = ws + WS_ST + (size_t)pm * 16384 + wid * 128; const unsigned xp_v = g * 4096 + r16 * 8;
    u32x2 pv[4];
#define PLOAD() asm volatile("global_load_dwordx2 %0, %4, %5 sc1\n\tglobal_load_dwordx2 %1, %4, %5 offset:1024 sc1\n\tglobal_load_dwordx2 %2, %4, %5 offset:2048 sc1\n\tglobal_load_dwordx2 %3, %4, %5 offset:3072 sc1" \
        : "=&v"(pv[0]), "=&v"(pv[1]), "=&v"(pv[2]), "=&v"(pv[3]) : "v"(xp_v), "s"(xp_s) : "memory")
    auto pload = [&](int st) { if (st >= 10) asm volatile("global_load_dwordx2 %0, %1, %2 offset:%3 sc1" : "=&v"(pv[(st - 10) & 3]) : "v"(xp_v), "s"(xp_s), "n"(((st - 10) & 3) * 1024) : "memory"); };
    P2_A(2, true, pload); wait_vm_bar<9>(); SB();
#define PEMPTY() __any(pv[0][1] == 0xffffffffu || pv[1][1] == 0xffffffffu || pv[2][1] == 0xffffffffu || pv[3][1] == 0xffffffffu)
    const unsigned hsf = *HS;
    P2_B(2, true, true, [&](int st) { if (st == 13) { PIN(ev); if (wid == HW) xcheck(); } });
    float za, zb;
    auto zstat = [&](int st) {
        if (st == ZS0) {
            wait_vm<3>();
            PIN(pv[0]); PIN(pv[1]); PIN(pv[2]); PIN(pv[3]);
            if (__builtin_expect(hsf != 1 || PEMPTY(), 0)) {
                const unsigned long long t0 = __builtin_amdgcn_s_memrealtime();
                if (wid == HW && hsf != 1) {
                    do {
                        ev = ld_sc1_async(xe_s, le); wait_vm<0>(); PIN(ev);
                        xcheck();
                        nn = __hip_atomic_load(XN + (lane & 15), RLX_AGENT);
                        if (nn != echoed) { echoed = nn; if (lane < 16) __hip_atomic_store(XE + lane * 16 + j, nn, RLX_AGENT); }
                    } while (!hs && __builtin_amdgcn_s_memrealtime() - t0 < 5000000ull);
                    *HS = 2;
                }
                while (*HS == 0 && __builtin_amdgcn_s_memrealtime() - t0 < 6000000ull) __builtin_amdgcn_s_sleep(2);
                do {
                    PLOAD(); wait_vm<0>(); PIN(pv[0]); PIN(pv[1]); PIN(pv[2]); PIN(pv[3]);
                } while (PEMPTY() && __builtin_amdgcn_s_memrealtime() - t0 < 6000000ull);
            }
        }
#define UF(x) __uint_as_float(x)
        if (st == ZS0 + 2) {
            za = fadd1(fadd1(lone(UF(pv[0][0])), UF(pv[1][0])), fadd1(lone(UF(pv[2][0])), UF(pv[3][0]))); PIN(za);
            zb = fadd1(fadd1(UF(pv[0][1]), UF(pv[1][1])), fadd1(UF(pv[2][1]), UF(pv[3][1]))); PIN(zb);
        }
        if (st == ZS0 + 4) {
            auto a1 = __builtin_amdgcn_permlane16_swap(__float_as_uint(za), __float_as_uint(za), false, false); za = fadd1(UF(a1[0]), UF(a1[1])); PIN(za);
            auto b1 = __builtin_amdgcn_permlane16_swap(__float_as_uint(zb), __float_as_uint(zb), false, false); zb = fadd1(UF(b1[0]), UF(b1[1])); PIN(zb);
        }
        if (st == ZS0 + 6) {
            auto a2 = __builtin_amdgcn_permlane32_swap(__float_as_uint(za), __float_as_uint(za), false, false); za = fadd1(UF(a2[0]), UF(a2[1])); PIN(za);
            auto b2 = __builtin_amdgcn_permlane32_swap(__float_as_uint(zb), __float_as_uint(zb), false, false); zb = fadd1(UF(b2[0]), UF(b2[1])); PIN(zb);
        }
        if (st == ZS0 + 8) {
            float m = fmul1(za, 1.0f / FH), var = fmaxf(ffma1(-m, m, fmul1(zb, 1.0f / FH)), 0.f), r = __builtin_amdgcn_rsqf(var + 1e-5f); PIN(r);
            ZS[wid * 16 + r16] = f32x2{r, fmul1(-m, r)};
        }
    };
    P2_A(0, true, zstat); wait_vm_bar<5>(); SB();
    auto merge = [&](u32x2 A, u32x2 B) {
        auto r0 = __builtin_amdgcn_permlane16_swap(A[0], B[0], false, false); auto r1 = __builtin_amdgcn_permlane16_swap(A[1], B[1], false, false);
        return u32x4{r0[0], r1[0], r0[1], r1[1]};
    };
    auto pk4 = [&](f32x4 v) { return u32x2{cvtpk(v[0], v[1]), cvtpk(v[2], v[3])}; };
    const char* z2b = ws + WS_Z2T + (size_t)(j * 32 + 2 * pm + wr) * 18432;
    f32x2 zs[5]; u32x2 za2, zb2; unsigned vs, v2;
    auto zn = [&](int q) {
        if (q == 0) {
#pragma unroll
            for (int m = 0; m < 5; ++m) zs[m] = ZS[wr * 64 + (m < 4 ? m : wc) * 16 + r16];
        }
        if (q == 1) { vs = (r16 >> 3) * 2304 + (((r16 & 7) ^ (4 * (r16 >> 3))) * 32); PIN(vs); }
        if (q == 2) { v2 = vs + (g & 1) * 4608 + (g >> 1) * 16; vs += g * 8; PIN(v2); PIN(vs); }
#define NRM(v, m, e) { v[e] = ffma1(v[e], zs[m][0], zs[m][1]); v[(e) + 1] = ffma1(v[(e) + 1], zs[m][0], zs[m][1]); PIN(v); }
        if (q >= 3 && q < 3 + 4 * ZSCH) {
            const int p = (q - 3) / ZSCH, t = (q - 3) % ZSCH, n = p >> 1, m = (p & 1) * 2;
            f32x4& A = acc1[m][n]; f32x4& B = acc1[m + 1][n];
            if (ZSCH == 5) {
                if (t == 0) NRM(A, m, 0)
                if (t == 1) { NRM(A, m, 2) za2[0] = cvtpk(A[0], A[1]); PIN(za2); }
                if (t == 2) { NRM(B, m + 1, 0) za2[1] = cvtpk(A[2], A[3]); PIN(za2); }
                if (t == 3) { NRM(B, m + 1, 2) zb2[0] = cvtpk(B[0], B[1]); PIN(zb2); }
                if (t == 4) { zb2[1] = cvtpk(B[2], B[3]); st16s<0>(z2b + NF1(n) * 256 + m * 4608, v2, merge(za2, zb2)); }
            } else {
                if (t == 0) { NRM(A, m, 0) NRM(A, m, 2) }
                if (t == 1) { NRM(B, m + 1, 0) NRM(B, m + 1, 2) }
                if (t == 2) st16s<0>(z2b + NF1(n) * 256 + m * 4608, v2, merge(pk4(A), pk4(B)));
            }
        }
        if (q == 3 + 4 * ZSCH) { NRM(sacc, 4, 0) NRM(sacc, 4, 2) }
        if (q == 4 + 4 * ZSCH) st8s<2048>(z2b + wc * 4608, vs, pk4(sacc));
    };
    P2_B(0, false, true, zn);
    P2_A(1, false, [&](int st) { zn(14 + st); }); wait_vm_bar<5>(); SB();
    P2_B(1, false, true, [&](int st) { if (st == 0 && W2STR) asm volatile("s_mov_b32 m0, %2\n\ts_nop 0\n\tglobal_load_lds_dword %0, %1 offset:512" ::"v"(wspo + r16 * (W2STR - WSTR)), "s"(w_sp), "s"(lds0 + K1_X0 + 16384)); });
    const bool isq = nf3 >= 10 || j < 4;
    float qn[4], qs[4]; unsigned vq;
    auto qpre = [&](int st) {
        if (st == 7) {
#pragma unroll
            for (int m = 0; m < 4; ++m) er[m] = RS[wr * 64 + m * 16 + r16];
        }
        if (st == 8) { vq = ((g & 1) * 2 + (r16 >> 3)) * 9216 + (g >> 1) * 128 + (r16 & 7) * 16; PIN(vq); }
        if (st >= 10) {
            const int m = (st - 10) & 3;
            qn[m] = fmul1(er[m][0], cg == 3 ? 0.f : -1.f); qs[m] = ffma1(er[m][1], cg == 3 ? 0.f : (isq ? QSCALE / c0 : 1.0f / c0), cg == 3 ? 1.f : 0.f); PIN(qn[m]); PIN(qs[m]);
        }
    };
    auto qfin = [&](int st) {
        if (st >= 6) {
            const int m = (st - 6) >> 1, e_ = ((st - 6) & 1) * 2;
            f32x4& v = acc2[m][0];
            v[e_] = fmul1(ffma1(cs2[0][e_], qn[m], v[e_]), qs[m]); v[e_ + 1] = fmul1(ffma1(cs2[0][e_ + 1], qn[m], v[e_ + 1]), qs[m]); PIN(v);
        }
    };
    P2_A(2, false, qpre);
    P2_B(2, false, false, qfin);
    wait_vm<W2STR ? 1 : 0>();
    if (cg == 3) {
    } else if (isq) {
        const char* qb = ws + WS_Q + (pm * 16 + wr * 8) * 9216 + (nf3 >= 10 ? 4 * j + (nf3 - 10) * 2 : 64 + 2 * j) * 128;
        st16s<0>(qb, vq, merge(pk4(acc2[0][0]), pk4(acc2[1][0]))); st16s<0>(qb + 4 * 9216, vq, merge(pk4(acc2[2][0]), pk4(acc2[3][0])));
    } else if (j < 7) {
#pragma unroll
        for (int m = 0; m < 4; ++m) kv_store(ws + WS_KV + (size_t)(pm * 4 + wr * 2 + (m >> 1)) * 3072, m & 1, j, r16, g, acc2[m][0]);
    }
    SB();
    if (r16 == 0) { *(f32x4*)(CS + 192 + NF2(0) * 16 + 4 * g) = cs2[0]; *(f32x4*)(CS + 192 + NF2(1) * 16 + 4 * g) = cs2[1]; }
    f32x4* HX = (f32x4*)(smem + K1_B0) + wr * 256 + lane;
    if (cg == 3) { HX[0] = acc2[0][0]; HX[64] = acc2[1][0]; HX[128] = acc2[2][0]; HX[192] = acc2[3][0]; }
    if (wid < 4) g2_prep(gp, lds, lane, wid);
    volatile LAS u64* EW = (volatile LAS u64*)(lds + TOPL + 8);
    if (wid == HW) {
        if (__builtin_expect(epoch == 0, 0)) {
            const u64 t0 = __builtin_amdgcn_s_memrealtime();
            do { ev = ld_sc1_async(xe_s, le); wait_vm<0>(); PIN(ev); xcheck(); } while (epoch == 0 && __builtin_amdgcn_s_memrealtime() - t0 < 5000000ull);
        }
        *EW = epoch; EW[-1] = 0; EW[1] = 0; EW[2] = 0;
    }
    VSYNC();
    PIN(cs2[1]);
    if (wid == HW && lane == 0) __hip_atomic_store((gu64*)(ws + WS_FJ) + j * 64 + pm, epoch, RLX_AGENT);
    const u64 e_ = *EW;
    STAMP(3, 2);
    auto gel = [&](f32x4 a, f32x4 c, f32x4 rr) {
        f32x4 y = a - c * rr[0];
        f32x2 y0 = gelu_y2(f32x2{y[0], y[1]}, rr[1], rr[2], rr[3]), y1 = gelu_y2(f32x2{y[2], y[3]}, rr[1], rr[2], rr[3]);
        return u32x2{cvtpk(y0[0], y0[1]), cvtpk(y1[0], y1[1])};
    };
    cs1[1] = cs2[1]; cs1[2] = *(const f32x4*)(CS + 192 + NF2(2) * 16 + 4 * g);
    f32x4 h8 = HX[wc * 64], c8 = *(const f32x4*)(CS + 192 + 128 + 4 * g); er[4] = RS[wr * 64 + wc * 16 + r16];
    u32x4 zw[4];
#pragma unroll
    for (int m = 0; m < ZHALF; ++m) { zw[m] = merge(gel(acc2[m][1], cs1[1], er[m]), gel(acc2[m][2], cs1[2], er[m])); PIN(zw[m]); }
    SB();
    wait_vm_bar<0>();
    STAMP(0, 3);
    if (wid == HW) {
        const bool kvp = j >= 4 && j < 7;
        const unsigned fo = lane == 0 ? (unsigned)(WS_FJ - WS_FA) + (j * 64 + pm) * 8 : (lane == 1 ? (unsigned)(WS_FP - WS_FA) + (pm * 16 + j) * 8 : (lane == 2 ? bid * 8
                            : (unsigned)(WS_FJ - WS_FA) + ((lane & 15) * 64 + 16 + pm * 3 + (j - 4)) * 8));
        if (lane == 1 || lane == 2 || (kvp && g == 1)) __hip_atomic_store((gu64*)(ws + WS_FA + fo), epoch, RLX_AGENT);
    }
    if (wid < 4) {
        const char* sa = (const char*)w_sp - 4096 + pm * 128 * S_ * 2;
#pragma unroll
        for (int t = 0; t < 2; ++t) {
            set_m0(lds0 + wid * 4096 + t * K2_SA);
            glds_s<0>(gp.va[0], sa + t * 128); glds_s<1024>(gp.va[1], sa + t * 128); glds_s<2048>(gp.va[2], sa + t * 128); glds_s<3072>(gp.va[3], sa + t * 128);
        }
    }
    SB();
    epoch = ((u64)(unsigned)__builtin_amdgcn_readfirstlane((unsigned)(e_ >> 32)) << 32) | (unsigned)__builtin_amdgcn_readfirstlane((unsigned)e_);
    volatile LAS unsigned* go = (volatile LAS unsigned*)(lds + TOPL + 20);
    LAS char* zl = (LAS char*)lds + Z1L + wr * 1024 + r16 * 16;
#define ZPUT(m) *(LAS u32x4*)(zl + cg * 8192 + (2 * ((g & 1) ^ wr) + (g >> 1)) * 2048 + (m) * 256) = zw[m]
#pragma unroll
    for (int m = 0; m < ZHALF; ++m) ZPUT(m);
    bool issued = wid >= 3;
#pragma unroll
    for (int m = ZHALF; m < 4; ++m) {
        if (m == ZPOLL && wid == POLLW) { poll_flags(ws, epoch, lane, ws + WS_FJ + j * 512, r16 * 8); *go = 1; }
        if (m >= ZPOLL && !issued && __builtin_amdgcn_readfirstlane(*go)) { first_loads(ws, lds0, lane, wid, j); issued = true; }
        SB();
        zw[m] = merge(gel(acc2[m][1], cs1[1], er[m]), gel(acc2[m][2], cs1[2], er[m])); ZPUT(m);
        SB();
    }
    *(LAS u32x2*)(zl + 32768 + wc * 256 + (g >> 1) * 2048 + (g & 1) * 8) = gel(h8, c8, er[4]);
    if (!issued) {
        while (*go == 0) __builtin_amdgcn_s_sleep(1);
        first_loads(ws, lds0, lane, wid, j);
    }
    return epoch;
}

DI void wait_fa(char* ws, u64 epoch, int lane) {
    const u64 t0 = __builtin_amdgcn_s_memrealtime();
    for (;;) {
        u32x4 a, b;
        asm volatile("global_load_dwordx4 %0, %2, %3 sc1\n\tglobal_load_dwordx4 %1, %2, %3 offset:1024 sc1\n\ts_waitcnt vmcnt(0)" : "=&v"(a), "=&v"(b) : "v"(lane * 16), "s"(ws + WS_FA) : "memory");
        const unsigned lo = (unsigned)epoch, hi = (unsigned)(epoch >> 32);
        if (__all(a[0] == lo && a[1] == hi && a[2] == lo && a[3] == hi && b[0] == lo && b[1] == hi && b[2] == lo && b[3] == hi)) break;
        if (__builtin_amdgcn_s_memrealtime() - t0 > 5000000ull) break;
        if (blockIdx.x == 0) epoch_service(ws, lane, epoch);
        __builtin_amdgcn_s_sleep(8);
    }
}

constexpr float ATT_BIAS = 72.0f;
DI float xhalf_max(float m) { auto rr = __builtin_amdgcn_permlane32_swap(__float_as_uint(m), __float_as_uint(m), false, false); return fmaxf(__uint_as_float(rr[0]), __uint_as_float(rr[1])); }
DI float lo_bcast(float m) { auto rr = __builtin_amdgcn_permlane32_swap(__float_as_uint(m), __float_as_uint(m), false, false); return __uint_as_float(rr[0]); }
DI float bf_round(float x) { return __uint_as_float(cvtpk(x, 0.f) << 16); }
DI float max16(const f32x16& s) {
    float a = fmaxf(fmaxf(s[0], s[1]), s[2]), b = fmaxf(fmaxf(s[3], s[4]), s[5]);
    a = fmaxf(fmaxf(a, s[6]), s[7]); b = fmaxf(fmaxf(b, s[8]), s[9]); a = fmaxf(fmaxf(a, s[10]), s[11]); b = fmaxf(fmaxf(b, s[12]), s[13]);
    a = fmaxf(fmaxf(a, s[14]), s[15]); return fmaxf(a, b);
}
DI void exp_pack(const f32x16& s, bf16x8& p0, bf16x8& p1) {
    float e[16];
#pragma unroll
    for (int i = 0; i < 16; ++i) e[i] = __builtin_amdgcn_exp2f(s[i]);
    u32x4 a = {cvtpk(e[0], e[1]), cvtpk(e[2], e[3]), cvtpk(e[4], e[5]), cvtpk(e[6], e[7])};
    u32x4 b = {cvtpk(e[8], e[9]), cvtpk(e[10], e[11]), cvtpk(e[12], e[13]), cvtpk(e[14], e[15])};
    p0 = __builtin_bit_cast(bf16x8, a); p1 = __builtin_bit_cast(bf16x8, b);
}

struct KVF { bf16x8 k1, k2, v0, v1; };
DI void kv_read(KVF& f, const LAS char* tb, int lane, int k2o, int vo) {
    f.k1 = *(const LAS bf16x8*)(tb + lane * 16); f.k2 = *(const LAS bf16x8*)(tb + k2o);
    f.v0 = *(const LAS bf16x8*)(tb + 1024 + vo); f.v1 = *(const LAS bf16x8*)(tb + 2048 + vo);
}
#define k2frag(x) (x)
struct NoHook { DI void operator()(int) const {} };
template <bool RD, class H = NoHook>
DI void half_step(f32x16& sx, bf16x8& px0, bf16x8& px1, f32x16& sy, const KVF kq, const bf16x8& qy1, const bf16x8& qy2,
                  f32x16& oy, const KVF kv, const bf16x8& py0, const bf16x8& py1, KVF& ld, const LAS char* tb, int lane, int k2o, int vo, H hook = H(), int hs = 0) {
    const f32x16 zero = {};
    unsigned a0, a1, a2, a3, b0, b1, b2, b3; float e0, e1, e2, e3;
    f32x16 t = MFMA32(kq.k1, qy1, zero); SB();
    hook(hs); SB();
    if (RD) ld.k1 = *(const LAS bf16x8*)(tb + lane * 16);
    e0 = EX(sx[0]); e1 = EX(sx[1]); e2 = EX(sx[2]); e3 = EX(sx[3]); PIN(e0); PIN(e1); PIN(e2); PIN(e3); SB();
    sy = MFMA32(kq.k2, qy2, t); SB();
    if (RD) ld.k2 = *(const LAS bf16x8*)(tb + k2o);
    a0 = cvtpk(e0, e1); a1 = cvtpk(e2, e3); PIN(a0); PIN(a1);
    e0 = EX(sx[4]); e1 = EX(sx[5]); e2 = EX(sx[6]); e3 = EX(sx[7]); PIN(e0); PIN(e1); PIN(e2); PIN(e3); SB();
    oy = MFMA32(kv.v0, py0, oy); SB();
    hook(hs + 1); SB();
    if (RD) ld.v0 = *(const LAS bf16x8*)(tb + 1024 + vo);
    a2 = cvtpk(e0, e1); a3 = cvtpk(e2, e3); PIN(a2); PIN(a3);
    e0 = EX(sx[8]); e1 = EX(sx[9]); e2 = EX(sx[10]); e3 = EX(sx[11]); PIN(e0); PIN(e1); PIN(e2); PIN(e3); SB();
    oy = MFMA32(kv.v1, py1, oy); SB();
    if (RD) ld.v1 = *(const LAS bf16x8*)(tb + 2048 + vo);
    b0 = cvtpk(e0, e1); b1 = cvtpk(e2, e3); PIN(b0); PIN(b1);
    e0 = EX(sx[12]); e1 = EX(sx[13]); e2 = EX(sx[14]); e3 = EX(sx[15]);
    b2 = cvtpk(e0, e1); b3 = cvtpk(e2, e3); PIN(b2); PIN(b3); SB();
    u32x4 a = {a0, a1, a2, a3}, b = {b0, b1, b2, b3};
    px0 = __builtin_bit_cast(bf16x8, a); px1 = __builtin_bit_cast(bf16x8, b);
}
DI bf16x8 p_scale(bf16x8 p, int sh) {
    u32x4 w = __builtin_bit_cast(u32x4, p);
#pragma unroll
    for (int i = 0; i < 4; ++i) w[i] = cvtpk(__builtin_amdgcn_ldexpf(bflo(w[i]), sh), __builtin_amdgcn_ldexpf(bfhi(w[i]), sh));
    return __builtin_bit_cast(bf16x8, w);
}
template <bool PP> DI void att_rescale(f32x16& o, f32x16& s, bf16x8& p0, bf16x8& p1, float& mref, bf16x8& q2, bool& bad, int c, int h) {
    bool nf = false;
#pragma unroll
    for (int i = 0; i < 11; ++i) nf = nf || (!(fabsf(o[i]) <= 3.0e38f) && (i < 8 || h == 0));
    if (__any(nf)) { bad = true; return; }
    float lb = lo_bcast(o[10]);
    int e = __builtin_amdgcn_frexp_expf(lb);
    float d = (float)(e + (int)ATT_BIAS); d = d < 0 ? 0 : d;
    float nm = bf_round(mref + d); float dd = nm - mref; mref = nm;
    int sh = -(int)dd;
#pragma unroll
    for (int i = 0; i < 16; ++i) { o[i] = __builtin_amdgcn_ldexpf(o[i], sh); if (!PP) { float b_ = s[i] - dd; PIN(b_); s[i] = b_; } }
    if (PP) { p0 = p_scale(p0, sh); p1 = p_scale(p1, sh); }
    u32x4 b = __builtin_bit_cast(u32x4, q2); b[1] = h ? 0u : (__float_as_uint(-nm) >> 16); q2 = __builtin_bit_cast(bf16x8, b);
}

#define W3LD 1
#define W3ST 3
struct G2F { bf16x8 a[4], b[4], sb; };
struct G2S {
    unsigned va[4], vl, vl2;
    const char* sa; const char* sl; unsigned lstep;
    unsigned m0a, m0l[3];
    const LAS char* pa[2][2]; const LAS char* pb[2]; const LAS char* ps[2];
};
template <int KS, int GL, bool RD, int RS, int WS, class H = NoHook> DI void g2_phase(G2S& s, const G2F& f, G2F& n, f32x4 (&acc)[4][4], f32x4 (&sacc)[2], H hook = H()) {
    const LAS char* a0 = s.pa[0][KS] + RS * K2_SA; const LAS char* a1 = s.pa[1][KS] + RS * K2_SA;
    constexpr int BO = RS * K2_SB + KS * 9216;
    const LAS char* b0 = s.pb[0] + BO; const LAS char* b1 = s.pb[1] + BO;
#define MM(m, nn) acc[m][nn] = MFMA16(f.b[nn], f.a[m], acc[m][nn]); PIN(acc[m][nn]); SB(); hook((nn) * 4 + (m)); SB()
    MM(0, 0); if (RD) n.b[0] = ldsB2(b0, b1); SB();
    MM(1, 0); if (RD) n.a[0] = ldsA(a0); SB();
    MM(2, 0); if (RD) n.a[1] = ldsA(a0 + 2048); SB();
    MM(3, 0); if (RD) n.a[2] = ldsA(a1); SB();
    MM(0, 1); if (RD) n.a[3] = ldsA(a1 + 2048); SB();
    MM(1, 1); if (RD) n.b[1] = ldsB2(b0 + 256, b1 + 256); SB();
    MM(2, 1); if (RD) n.b[2] = ldsB2(b0 + 512, b1 + 512); SB();
    MM(3, 1); if (RD) n.b[3] = ldsB2(b0 + 768, b1 + 768); SB();
    MM(0, 2); if (RD) n.sb = ldsB2(s.ps[0] + BO, s.ps[1] + BO); SB();
    MM(1, 2); if (GL == 1) { set_m0(s.m0a + WS * K2_SA); glds_s<0>(s.va[0], s.sa); } if (GL == 2) { glds_sc1<1024>(s.vl, s.sl); } SB();
    MM(2, 2);
    MM(3, 2); if (GL == 1) glds_s<1024>(s.va[1], s.sa); if (GL == 2) glds_sc1<2048>(s.vl, s.sl); SB();
    MM(0, 3);
    MM(1, 3); if (GL == 1) glds_s<2048>(s.va[2], s.sa); if (GL == 2) glds_sc1<3072>(s.vl, s.sl); SB();
    MM(2, 3);
    MM(3, 3); if (GL == 1) { glds_s<3072>(s.va[3], s.sa); s.sa += 128; asm volatile("" : "+s"(s.sa)); } if (GL == 2) { set_m0(s.m0l[WS] + 4096); glds_sc1<0>(s.vl2, s.sl); } SB();
    sacc[0] = MFMA16(f.sb, f.a[0], sacc[0]); PIN(sacc[0]); SB();
    if (GL == 1) { set_m0(s.m0l[WS]); glds_sc1<0>(s.vl, s.sl); } if (GL == 2) { glds_sc1<1024>(s.vl2, s.sl); s.sl += s.lstep; asm volatile("" : "+s"(s.sl)); } SB();
    sacc[1] = MFMA16(f.sb, f.a[1], sacc[1]); PIN(sacc[1]); SB();
#undef MM
}
template <int C, bool G2nd, bool G1st, int VM, bool RDN> DI void g2_iter(G2S& s, G2F& f0, G2F& f1, f32x4 (&acc)[4][4], f32x4 (&sacc)[2]) {
    g2_phase<1, G2nd ? 2 : 0, true, C, (C + 2) % 3>(s, f0, f1, acc, sacc);
    wait_vm_bar<VM>(); SB();
    g2_phase<0, G1st ? 1 : 0, RDN, (C + 1) % 3, C>(s, f1, f0, acc, sacc);
}

constexpr int K3_S = 4608, K3_RW = 4 * K3_S;
struct B5 { bf16x8 b[5]; };
DI unsigned k3_so(int w, int sl) {
    const int r = sl == 1 ? 2 : sl - 2;
    if (sl == 0) return Z1L + w * K3_S;
    return w < 4 ? K2_B0 + r * K2_SB + w * K3_S : (w < 7 ? r * K2_SA + (w - 4) * K3_S : K2_K0 + r * K2_SK);
}
DI unsigned k3_orow(int r) { return r < 6 ? (r >> 1) * K2_SA + 3 * K3_S + 64 + (r & 1) * 1168 : K2_K0 + (r - 6) * K2_SK + K3_S + 64; }
DI void k3_b1(int p, unsigned m0v, unsigned va, const char* sb) {
    if (p == 0) { set_m0(m0v); glds_sc1<0>(va, sb); }
    if (p == 1) glds_sc1<1024>(va, sb);
    if (p == 2) glds_sc1<2048>(va, sb);
    if (p == 3) glds_sc1<3072>(va, sb);
    if (p == 4) asm volatile("s_mov_b32 m0, %2\n\ts_mov_b32 exec_hi, 0\n\tglobal_load_lds_dwordx4 %0, %1 sc1\n\ts_mov_b32 exec_hi, -1" ::"v"(va), "s"(sb + 4096), "s"(m0v + 4096));
}
DI void k3_a(unsigned m0v, unsigned va, const char* sa) {
    set_m0(m0v); glds_sc1<0>(va, sa); glds_sc1<1024>(va, sa); glds_sc1<2048>(va, sa); glds_sc1<3072>(va, sa);
}
DI void k3_b(unsigned m0v, unsigned va, const char* sb) {
#pragma unroll
    for (int p = 0; p < 5; ++p) k3_b1(p, m0v, va, sb);
}
DI void k3_rdb(B5& r, const LAS char* p, const LAS char* p5) {
#pragma unroll
    for (int n = 0; n < 4; ++n) r.b[n] = ldsA(p + n * 1024);
    r.b[4] = ldsA(p5);
}
DI void k3_lb1(int p, B5& r, unsigned va, unsigned va5, const char* sb) {
    if (p < 4) asm volatile("global_load_dwordx4 %0, %1, %2 offset:%3 sc1" : "+v"(r.b[p & 3]) : "v"(va), "s"(sb), "n"((p & 3) * 1024) : "memory");
    else asm volatile("global_load_dwordx4 %0, %1, %2 sc1" : "+v"(r.b[4]) : "v"(va5), "s"(sb) : "memory");
}
#define LB5 "global_load_dwordx4 %0, %5, %7 sc1\n\tglobal_load_dwordx4 %1, %5, %7 offset:1024 sc1\n\tglobal_load_dwordx4 %2, %5, %7 offset:2048 sc1\n\t" \
            "global_load_dwordx4 %3, %5, %7 offset:3072 sc1\n\tglobal_load_dwordx4 %4, %6, %7 sc1\n"
DI void k3_lb(B5& r, unsigned va, unsigned va5, const char* sb) {
    asm volatile(LB5 : "=&v"(r.b[0]), "=&v"(r.b[1]), "=&v"(r.b[2]), "=&v"(r.b[3]), "=&v"(r.b[4]) : "v"(va), "v"(va5), "s"(sb) : "memory");
}
DI void k3_lb_if(B5& r, unsigned va, unsigned va5, const char* sb, int a, int b) {
    asm volatile("s_cmp_lg_u32 %8, %9\n\ts_cbranch_scc1 .Llb%=\n\t" LB5 ".Llb%=:" : "+v"(r.b[0]), "+v"(r.b[1]), "+v"(r.b[2]), "+v"(r.b[3]), "+v"(r.b[4])
                 : "v"(va), "v"(va5), "s"(sb), "s"(a), "n"(b) : "memory", "scc");
}
DI void pin5(B5& r) { PIN(r.b[0]); PIN(r.b[1]); PIN(r.b[2]); PIN(r.b[3]); PIN(r.b[4]); }
template <int N0, int N1, bool FIRST = false> DI void k3_mm(f32x4 (&acc)[4][5], const bf16x8 (&a)[4], const B5& r) {
#pragma unroll
    for (int n = N0; n < N1; ++n)
#pragma unroll
        for (int m = 0; m < 4; ++m) {
            if (FIRST) asm volatile("v_mfma_f32_16x16x32_bf16 %0, %1, %2, 0" : "=&v"(acc[m][n]) : "v"(r.b[n]), "v"(a[m]));
            else asm volatile("v_mfma_f32_16x16x32_bf16 %0, %1, %2, %0" : "+v"(acc[m][n]) : "v"(r.b[n]), "v"(a[m]));
        }
    SB();
}

#define K3MM(d, b_, a_) asm volatile("v_mfma_f32_16x16x32_bf16 %0, %1, %2, %0" : "+v"(d) : "v"(b_), "v"(a_))
#define ILV 1
struct UA { const char* p[4]; };
template <int NX, bool FIRST = false> DI void k3_step(f32x4 (&acc)[4][5], bf16x8 (&a)[4], const B5& r, unsigned va, const UA& ua) {
#pragma unroll
    for (int m = 0; m < 4; ++m) {
#pragma unroll
        for (int n = 0; n < 5; ++n) {
            if (FIRST) asm volatile("v_mfma_f32_16x16x32_bf16 %0, %1, %2, 0" : "=&v"(acc[m][n]) : "v"(r.b[n]), "v"(a[m]));
            else K3MM(acc[m][n], r.b[n], a[m]);
        }
        if (NX >= 0 && ILV) asm volatile("global_load_dwordx4 %0, %1, %2 sc1" : "+v"(a[m]) : "v"(va), "s"(ua.p[m] + (NX < 0 ? 0 : NX) * 16384) : "memory");
    }
    if (NX >= 0 && !ILV) {
#pragma unroll
        for (int m = 0; m < 4; ++m) asm volatile("global_load_dwordx4 %0, %1, %2 sc1" : "+v"(a[m]) : "v"(va), "s"(ua.p[m] + (NX < 0 ? 0 : NX) * 16384) : "memory");
    }
    SB();
}

DI void phase2(G2P& gp, const u16* __restrict__ w_sp, const u16* __restrict__ b_sp, const u16* __restrict__ x, char* __restrict__ ws, u16* __restrict__ out, const u64 epoch, const int wid) {
    extern __shared__ __attribute__((aligned(16))) char smem[];
    int bid = blockIdx.x, xcd = bid & 7, idx = bid >> 3;
    int pm = (xcd & 1) * 8 + (idx & 7), j = (xcd >> 1) * 4 + (idx >> 3);
    unsigned zz = 0; asm volatile("" : "+s"(zz));
    const int lane = __builtin_amdgcn_mbcnt_hi(~0u, __builtin_amdgcn_mbcnt_lo(~0u, zz)) & 63;
    const int tid = wid * 64 + lane;
    const unsigned lds0 = (unsigned)(uintptr_t)smem; const LAS char* lds = (const LAS char*)smem;
    STAMP(1, 0);
    const int rb = ((xcd & 1) ^ 1) * 16 + (xcd >> 1) * 4 + (idx & 3), j3 = idx >> 2;
    volatile LAS unsigned* SY = (volatile LAS unsigned*)(lds + TOPL);
    gu64* FLr = (gu64*)(ws + WS_FL) + rb * 64;
    const unsigned va = lane * 16;
    unsigned va5 = 4096 + ((lane >> 4) * 8 + (lane & 7)) * 16; PIN(va5);
    B5 rb0, rb1, rb2, rb3;
    const int ks0 = wid < 4 ? wid : wid + 42;
    const char* wb = ws + WS_W3T + (size_t)j3 * 90 * 4608 + ks0 * 4608;
    if (wid >= 4) {
        __builtin_amdgcn_s_setprio(1);
        const unsigned qf = lane < 16 ? (unsigned)(WS_FP - WS_FJ) + pm * 128 + lane * 8 : j * 512 + lane * 8;
        u64 f_;
        asm volatile("s_cmp_lg_u32 %3, 4\n\ts_cbranch_scc1 .Lqf%=\n\tglobal_load_dwordx2 %0, %1, %2 sc1\n.Lqf%=:\n\ts_waitcnt lgkmcnt(0)\n\ts_barrier" : "=&v"(f_) : "v"(qf), "s"(ws + WS_FJ), "s"(wid) : "memory", "scc");
        SB();
        const int c = lane & 31, h = lane >> 5, aw = wid - 4;
        const char* kvg = ws + WS_KV;
        bf16x8 q1[2], q2[2]; float mref[2];
        const f32x16 zero = {};
        f32x16 oA = zero, oB = zero;
        int k2o = 1024 + (c >> 4) * 1024 + (c & 15) * 16;
        int vo = c < 8 ? 256 + (h * 8 + c) * 16 : 512 + (h * 16 + (c < 16 ? c - 8 : (c < 18 ? c - 2 : (c == 18 ? 12 : 13)))) * 16;
        {
            const int qbase = K2_K0 + K2_SK + aw * 3072;
            const LAS char* ql = lds + qbase;
            const char* qs = ws + WS_Q + (pm * 16 + j) * 9216 + aw * 32;
            unsigned vq = (lane >> 1) * 128 + (lane & 1) * 16; PIN(vq);
            SB();
            if (wid == 4) {
                wait_vm<0>(); PIN(f_);
                if (!__all(f_ == epoch)) poll_flags(ws, epoch, lane, ws + WS_FJ, qf);
                SY[6] = 1;
            } else while (SY[6] == 0) __builtin_amdgcn_s_sleep(1);
            set_m0(lds0 + qbase); glds_sc1<0>(vq, qs); glds_sc1<1024>(vq, qs + 3072);
            if (lane < 16) glds_sc1<2048>(vq, qs + 6144);
            SB();
            asm volatile("s_barrier" ::: "memory");
            const LAS char* qo[5];
#pragma unroll
            for (int i = 0; i < 5; ++i) { const int k = 18 * c + (i < 4 ? 8 * h + 2 * i : 16); qo[i] = ql + ((k >> 3) * 32 + (k & 7) * 2); PIN(qo[i]); }
            PIN(oA); PIN(oB); PIN(k2o); PIN(vo);
            SB();
            wait_vm<0>();
#pragma unroll
            for (int t = 0; t < 2; ++t) {
                u32x4 a;
#pragma unroll
                for (int i = 0; i < 4; ++i) a[i] = *(const LAS unsigned*)(qo[i] + t * 16);
                q1[t] = __builtin_bit_cast(bf16x8, a);
                const unsigned e = *(const LAS unsigned*)(qo[4] + t * 16);
                u32x4 b = {h ? 0u : e, 0u, 0u, 0u}; q2[t] = __builtin_bit_cast(bf16x8, b);
                PIN(q1[t]); PIN(q2[t]);
            }
        }
        bool bad = false;
        const LAS char* kvl = lds + K2_K0;
        asm volatile("s_waitcnt lgkmcnt(0)\n\ts_barrier\n\ts_barrier" ::: "memory");
        KVF X, Y;
        kv_read(X, kvl, lane, k2o, vo); kv_read(Y, kvl + 3072, lane, k2o, vo);
#pragma unroll
        for (int t = 0; t < 2; ++t) {
            f32x16 s0 = MFMA32(X.k1, q1[t], zero); s0 = MFMA32(X.k2, q2[t], s0);
            f32x16 s1 = MFMA32(Y.k1, q1[t], zero); s1 = MFMA32(Y.k2, q2[t], s1);
            float m = xhalf_max(fmaxf(max16(s0), max16(s1)));
            mref[t] = bf_round(rintf(m) + ATT_BIAS);
            u32x4 b = __builtin_bit_cast(u32x4, q2[t]); b[1] = h ? 0u : (__float_as_uint(-mref[t]) >> 16); q2[t] = __builtin_bit_cast(bf16x8, b);
        }
        f32x16 sA = MFMA32(X.k1, q1[0], zero); sA = MFMA32(X.k2, q2[0], sA);
        f32x16 sB = zero;
        bf16x8 pA0, pA1, pB0 = {}, pB1 = {};
        half_step<false>(sA, pA0, pA1, sB, X, q1[1], q2[1], oB, X, pB0, pB1, Y, kvl, lane, k2o, vo);
        half_step<false>(sB, pB0, pB1, sA, Y, q1[0], q2[0], oA, X, pA0, pA1, Y, kvl, lane, k2o, vo);
        asm volatile("s_waitcnt lgkmcnt(0)\n\ts_barrier" ::: "memory");
#define A_IV(sl, CHK, H, hs) { const LAS char* tb = (sl) < 3 ? kvl + (sl) * K2_SK : lds + 2 * K2_SA + ((sl) - 3) * 6144; \
            half_step<true>(sA, pA0, pA1, sB, Y, q1[1], q2[1], oB, X, pB0, pB1, X, tb, lane, k2o, vo, H, hs); \
            half_step<false>(sB, pB0, pB1, sA, X, q1[0], q2[0], oA, Y, pA0, pA1, X, tb, lane, k2o, vo, H, hs + 2); \
            if (CHK) { mB = __builtin_amdgcn_ballot_w64(!(oB[10] <= 0x1p20f)); }                  \
            half_step<true>(sA, pA0, pA1, sB, X, q1[1], q2[1], oB, Y, pB0, pB1, Y, tb + 3072, lane, k2o, vo, H, hs + 4); \
            if (CHK) { mA = __builtin_amdgcn_ballot_w64(!(oA[10] <= 0x1p20f)); any = mA | mB; asm volatile("" : "+s"(any)); } \
            half_step<false>(sB, pB0, pB1, sA, Y, q1[0], q2[0], oA, X, pA0, pA1, Y, tb, lane, k2o, vo, H, hs + 6); }
#define A_BAR() asm volatile("s_waitcnt lgkmcnt(0)\n\ts_barrier" ::: "memory")
        unsigned long long mA, mB, any;
#define UNDEF5(r) asm volatile("" : "=v"(r.b[0]), "=v"(r.b[1]), "=v"(r.b[2]), "=v"(r.b[3]), "=v"(r.b[4]))
        u32x4 fa0, fa1;
        asm volatile("global_load_dwordx4 %0, %2, %3 sc1\n\tglobal_load_dwordx4 %1, %2, %3 offset:1024 sc1" : "=&v"(fa0), "=&v"(fa1) : "v"(va), "s"(ws + WS_FA) : "memory");
        const NoHook nh;
        const char* pf = ws + WS_W3T + (size_t)j3 * 90 * 4608 + 16 * 4608 + min(((idx & 3) * 4 + aw) * 192, 2664 - 192) * 128;
        auto wl = [&](int i) {
            if (i < 20) k3_lb1(i % 5, i < 5 ? rb0 : (i < 10 ? rb1 : (i < 15 ? rb2 : rb3)), va, va5, wb + (i / 5) * 18432);
            else if (i < 23 && PFW)
                asm volatile("s_mov_b32 m0, %2\n\ts_nop 0\n\tglobal_load_lds_dword %0, %1 sc1" ::"v"(va * 8), "s"(pf + (i - 20) * 8192), "s"(lds0 + TOPL + 64 + wid * 256));
        };
#define A_RS() if (__builtin_expect(any != 0, 0)) { \
                if (mA) att_rescale<false>(oA, sA, pA0, pA1, mref[0], q2[0], bad, c, h); \
                if (mB) att_rescale<true>(oB, sB, pB0, pB1, mref[1], q2[1], bad, c, h); \
            }
#define A_IT(H) A_IV(1, false, H, 0); A_BAR(); A_IV(2, false, H, 8); A_BAR(); A_IV(0, true, H, 16); A_RS() A_BAR()
#pragma unroll 1
        for (int t = 0; t < TFA; ++t) { A_IT(nh); }
        {
            wait_vm<0>(); PIN(fa0); PIN(fa1);
            const unsigned lo = (unsigned)epoch, hi = (unsigned)(epoch >> 32);
            if (__builtin_expect(!__all(fa0[0] == lo && fa0[1] == hi && fa0[2] == lo && fa0[3] == hi && fa1[0] == lo && fa1[1] == hi && fa1[2] == lo && fa1[3] == hi), 0)) wait_fa(ws, epoch, lane);
            UNDEF5(rb0); UNDEF5(rb1); UNDEF5(rb2); UNDEF5(rb3);
            SB();
            A_IT(wl);
        }
#pragma unroll 1
        for (int t = TFA + 1; t < 9; ++t) { A_IT(nh); }
        A_IV(1, false, nh, 0); A_BAR();
        wait_vm<0>(); pin5(rb0); pin5(rb1); pin5(rb2); pin5(rb3);
        auto w9 = [&](int i) { if (i < 5) k3_b1(i, lds0 + k3_so(wid, 3), va, wb + 9 * 18432); };
        A_IV(2, false, nh, 0); A_IV(3, true, w9, 0); A_RS() A_IV(4, false, nh, 0);
        half_step<false>(sA, pA0, pA1, sB, Y, q1[1], q2[1], oB, X, pB0, pB1, X, kvl, lane, k2o, vo);
        exp_pack(sB, pB0, pB1);
        oA = MFMA32(Y.v0, pA0, oA); oA = MFMA32(Y.v1, pA1, oA);
        oB = MFMA32(Y.v0, pB0, oB); oB = MFMA32(Y.v1, pB1, oB);
        {
            float sa_ = fabsf(oA[10]) * 0x1p27f, sb_ = fabsf(oB[10]) * 0x1p27f;
#pragma unroll
            for (int i = 0; i < 10; ++i) { sa_ += fabsf(oA[i]); PIN(sa_); sb_ += fabsf(oB[i]); PIN(sb_); }
            if (__builtin_amdgcn_ballot_w64(!(sa_ + sb_ <= 3.0e38f))) bad = true;
        }
        f32x16 o[2] = {oA, oB};
        if (__builtin_expect(bad, 0)) {
#pragma unroll 1
            for (int t = 0; t < 2; ++t) {
                bf16x8 qa = t ? q1[1] : q1[0]; u32x4 qb = __builtin_bit_cast(u32x4, t ? q2[1] : q2[0]); qb[1] = 0;
                f32x16 oo = zero; float m = -3.0e38f;
                for (int kt = 0; kt < 64; ++kt) {
                    const char* tb = kvg + (size_t)kt * 3072;
                    bf16x8 k1, v0, v1; u32x4 k2v;
                    asm volatile("global_load_dwordx4 %0, %4, %7 sc1\n\tglobal_load_dwordx4 %1, %5, %7 sc1\n\tglobal_load_dwordx4 %2, %6, %7 offset:1024 sc1\n\t"
                                 "global_load_dwordx4 %3, %6, %7 offset:2048 sc1\n\ts_waitcnt vmcnt(0)"
                                 : "=&v"(k1), "=&v"(k2v), "=&v"(v0), "=&v"(v1) : "v"(lane * 16), "v"(k2o), "v"(vo), "s"(tb) : "memory");
                    f32x16 s = MFMA32(k1, qa, zero); s = MFMA32(__builtin_bit_cast(bf16x8, k2v), __builtin_bit_cast(bf16x8, qb), s);
                    float nm = fmaxf(m, xhalf_max(max16(s)));
                    float sc = __builtin_amdgcn_exp2f(m - nm); m = nm;
#pragma unroll
                    for (int i = 0; i < 16; ++i) { float a_ = oo[i] * sc, b_ = s[i] - nm; PIN(a_); PIN(b_); oo[i] = a_; s[i] = b_; }
                    bf16x8 p0, p1; exp_pack(s, p0, p1);
                    oo = MFMA32(v0, p0, oo); oo = MFMA32(v1, p1, oo);
                }
                if (t) o[1] = oo; else o[0] = oo;
            }
        }
#pragma unroll
        for (int t = 0; t < 2; ++t) {
            float inv = __builtin_amdgcn_rcpf(lo_bcast(o[t][10])); PIN(inv);
            LAS unsigned* ob = (LAS unsigned*)(lds + k3_orow(aw * 2 + t) + 36 * c + 8 * h);
#define ON(i) cvtpk(fmul1(o[t][i], inv), fmul1(o[t][(i) + 1], inv))
            ob[0] = ON(0); ob[1] = ON(2); ob[4] = ON(4); ob[5] = ON(6);
            if (h == 0) ob[8] = ON(8);
        }
        asm volatile("s_waitcnt lgkmcnt(0)" ::: "memory");
        if (lane == 0) __hip_atomic_fetch_add((LAS unsigned*)SY, 1u, __ATOMIC_RELAXED, __HIP_MEMORY_SCOPE_WORKGROUP);
        __builtin_amdgcn_s_setprio(APRIO);
    } else {
        const int wr = wid >> 1, wc = wid & 1;
        G2S s;
        s.vl = lane * 16; s.vl2 = lane * 16 + 4096;
        s.lstep = wid < 3 ? 18432 : 6144;
        unsigned ol = (wid < 3 ? (unsigned)WS_Z2T + j * 32 * 18432 + wid * 6144 : (unsigned)WS_KV - 3 * 6144), oa = pm * 128 * S_ * 2;
        s.m0a = lds0 + wid * 4096; s.m0l[0] = lds0 + (wid < 3 ? K2_B0 + wid * 6144 : K2_K0); s.m0l[1] = s.m0l[0] + s.lstep; s.m0l[2] = s.m0l[1] + s.lstep;
        PIN(s.vl); PIN(s.vl2);
        s.sl = ws + ol + 2 * s.lstep;
        STAMPT(0, 3, 3);
#pragma unroll
        for (int i = 0; i < 4; ++i) { s.va[i] = gp.va[i]; s.pa[i >> 1][i & 1] = gp.pa[i >> 1][i & 1]; }
        s.pb[0] = gp.pb[0]; s.pb[1] = gp.pb[1]; s.ps[0] = gp.ps[0]; s.ps[1] = gp.ps[1];
        s.sa = (const char*)w_sp - 4096 + oa + 256;
        G2F f0, f1;
        f32x4 (&acc)[4][4] = gp.acc; f32x4 (&sacc)[2] = gp.sacc;
        SB();
        if (wid == 3) wait_vm<0>();
        wait_vm_bar<6>();
        STAMP(1, 1);
        const unsigned m0l2 = s.m0l[2];
        if (wid == 3) s.m0l[2] = s.m0l[0];
        f0.b[0] = ldsB2(s.pb[0], s.pb[1]);
        f0.a[0] = ldsA(s.pa[0][0]); f0.a[1] = ldsA(s.pa[0][0] + 2048); f0.a[2] = ldsA(s.pa[1][0]); f0.a[3] = ldsA(s.pa[1][0] + 2048);
        f0.b[1] = ldsB2(s.pb[0] + 256, s.pb[1] + 256); f0.b[2] = ldsB2(s.pb[0] + 512, s.pb[1] + 512); f0.b[3] = ldsB2(s.pb[0] + 768, s.pb[1] + 768); f0.sb = ldsB2(s.ps[0], s.ps[1]);
        SB();
        set_m0(s.m0a + 2 * K2_SA); glds_s<0>(s.va[0], s.sa); glds_s<1024>(s.va[1], s.sa); glds_s<2048>(s.va[2], s.sa); glds_s<3072>(s.va[3], s.sa); s.sa += 128;
        set_m0(s.m0l[2]); glds_sc1<0>(s.vl, s.sl);
        SB();
#pragma unroll 1
        for (int t = 0; t < 9; ++t) {
            g2_iter<0, true, true, 10, true>(s, f0, f1, acc, sacc); s.m0l[2] = m0l2; g2_iter<1, true, true, 10, true>(s, f0, f1, acc, sacc); g2_iter<2, true, true, 10, true>(s, f0, f1, acc, sacc);
        }
        asm volatile("" : "=v"(rb0.b[0]), "=v"(rb0.b[1]), "=v"(rb0.b[2]), "=v"(rb0.b[3]), "=v"(rb0.b[4]), "=v"(rb1.b[0]), "=v"(rb1.b[1]), "=v"(rb1.b[2]), "=v"(rb1.b[3]), "=v"(rb1.b[4]));
        g2_phase<1, 2, true, 0, 2>(s, f0, f1, acc, sacc);
        wait_vm_bar<10>(); SB();
        g2_phase<0, 1, true, 1, 0>(s, f1, f0, acc, sacc, [&](int st) { if (st < 10 && !(st & 1)) k3_lb1(st >> 1, rb0, va, va5, wb); });
        g2_phase<1, 2, true, 1, 0>(s, f0, f1, acc, sacc);
        wait_vm_bar<15>(); SB();
        g2_phase<0, 1, true, 2, 1>(s, f1, f0, acc, sacc, [&](int st) { if (st < 10 && !(st & 1)) k3_lb1(st >> 1, rb1, va, va5, wb + 18432); });
        const int c = lane & 15, g = lane >> 4;
        const LAS char* z1b = lds + Z1L + (g >> 1) * 2048 + (wr * 64 + c) * 16 + (g & 1) * 8;
        u32x2 z1r[4][4], z1s[2];
#pragma unroll
        for (int m = 0; m < 4; ++m) {
            const int mo = (m ^ (2 * wc)) * 16;
#pragma unroll
            for (int n = 0; n < 4; ++n) z1r[m][n] = *(const LAS u32x2*)(z1b + (wc * 4 + n) * 4096 + mo * 16);
            if (m < 2) z1s[m] = *(const LAS u32x2*)(z1b + 8 * 4096 + mo * 16);
        }
        SB();
#define PREB(i) [&](int st) { if (st < 5) k3_b1(st, lds0 + k3_so(wid, i), va, wb + ((i) + 6) * 18432); else if (st < 10) k3_b1(st - 5, lds0 + k3_so(wid + 4, i), va, wb + ((i) + 6) * 18432 + 46 * 4608); }
        g2_phase<1, 2, true, 2, 1>(s, f0, f1, acc, sacc);
        wait_vm_bar<15>(); SB();
        {
            const int p = wid < 3 ? wid : 2;
            const char* ks = ws + WS_KV + (29 + p) * 6144; const unsigned km = lds0 + (p ? 2 * K2_SA + (p - 1) * 6144 : K2_K0 + 2 * K2_SK);
            g2_phase<0, 0, true, 0, 2>(s, f1, f0, acc, sacc, [&](int st) {
                if (st < 4) k3_b1(st, km, va, ks); else if (st == 4) { set_m0(km + 4096); glds_sc1<0>(s.vl2, ks); } else if (st == 5) glds_sc1<1024>(s.vl2, ks);
                else if (st < 11) k3_b1(st - 6, lds0 + k3_so(wid, 1), va, wb + 7 * 18432); });
        }
        g2_phase<1, 0, true, 0, 2>(s, f0, f1, acc, sacc, PREB(0));
        wait_vm_bar<21>(); SB();
        g2_phase<0, 0, true, 1, 0>(s, f1, f0, acc, sacc, PREB(2));
        unsigned vu[3];
#pragma unroll
        for (int i = 0; i < 3; ++i) {
            const int cgl = 18 * j + (i < 2 ? 2 * (wc * 4 + 2 * i + (g & 1)) : 16) + (g >> 1);
            vu[i] = (cgl >> 2) * 4096 + (cgl & 3) * 256 + c * 16 + (i == 2 ? (g & 1) * 1024 : 0); PIN(vu[i]);
        }
        const char* ub0 = ws + WS_UO + (size_t)(pm * 2 + wr) * 90 * 4096 + wc * 2048; const char* ub1 = ub0 + 2048 - wc * 4096;
        SB();
        g2_phase<1, 0, true, 1, 0>(s, f0, f1, acc, sacc);
        wait_vm_bar<0>(); SB();
        __builtin_amdgcn_s_setprio(2);
        auto fin = [&](f32x4 v, u32x2 z) { return u32x2{cvtpk(fmul1(bflo(z[0]), v[0]), fmul1(bfhi(z[0]), v[1])), cvtpk(fmul1(bflo(z[1]), v[2]), fmul1(bfhi(z[1]), v[3]))}; };
        auto mrg = [&](u32x2 A, u32x2 B) {
            auto r0 = __builtin_amdgcn_permlane16_swap(A[0], B[0], false, false); auto r1 = __builtin_amdgcn_permlane16_swap(A[1], B[1], false, false);
            return u32x4{r0[0], r1[0], r0[1], r1[1]};
        };
#define M2(m, n) acc[m][n] = MFMA16(f1.b[n], f1.a[m], acc[m][n]); acc[m][(n) + 1] = MFMA16(f1.b[(n) + 1], f1.a[m], acc[m][(n) + 1]); PIN(acc[m][n]); PIN(acc[m][(n) + 1]); SB()
#define PUT(m, n) st16s<((m) & 1) * 1024>((m) < 2 ? ub0 : ub1, vu[(n) >> 1], mrg(fin(acc[m][n], z1r[m][n]), fin(acc[m][(n) + 1], z1r[m][(n) + 1]))); SB()
        M2(0, 0); M2(1, 0); M2(2, 0); PUT(0, 0);
        M2(3, 0); PUT(1, 0);
        sacc[0] = MFMA16(f1.sb, f1.a[0], sacc[0]); sacc[1] = MFMA16(f1.sb, f1.a[1], sacc[1]); PIN(sacc[0]); PIN(sacc[1]); SB(); PUT(2, 0);
        M2(0, 2); PUT(3, 0);
        M2(1, 2); st16s<0>(ub0, vu[2], mrg(fin(sacc[0], z1s[0]), fin(sacc[1], z1s[1]))); SB();
        M2(2, 2); PUT(0, 2);
        M2(3, 2); PUT(1, 2);
        STAMP(1, 2);
        PUT(2, 2); PUT(3, 2);
        SB(); k3_b(lds0 + k3_so(wid, 3), va, wb + 9 * 18432);
        wait_vm<5>();
        if (lane == 0) __hip_atomic_store((gu64*)(ws + WS_FL) + (pm * 2 + wr) * 64 + j * 2 + wc, epoch, RLX_AGENT);
        pin5(rb0); pin5(rb1);
        asm volatile("" : "=v"(rb2.b[0]), "=v"(rb2.b[1]), "=v"(rb2.b[2]), "=v"(rb2.b[3]), "=v"(rb2.b[4]), "=v"(rb3.b[0]), "=v"(rb3.b[1]), "=v"(rb3.b[2]), "=v"(rb3.b[3]), "=v"(rb3.b[4]));
        STAMP(1, 3);
        __builtin_amdgcn_s_setprio(GPRIO);
    }
    {
#define TK()
#define TP()
        TP();
        UA ua;
#pragma unroll
        for (int i = 0; i < 4; ++i) ua.p[i] = ws + WS_UO + (size_t)rb * 90 * 4096 + ks0 * 4096 + ((wid + 1 + i) & 3) * 1024;
        auto relane = [&]() { unsigned t_ = va; PIN(t_); return (int)(t_ >> 4); };
        unsigned so[4];
#pragma unroll
        for (int i = 0; i < 4; ++i) so[i] = __builtin_amdgcn_readfirstlane(k3_so(wid, i));
#define LG0() asm volatile("s_waitcnt lgkmcnt(0)" ::: "memory"); SB()
#define LB(d, i) k3_lb(d, va, va5, wb + (i) * 18432)
#define RB(d, sl) { asm volatile("" : "+s"(so[sl])); k3_rdb(d, lds + va + so[sl], lds + va5 + so[sl]); }
        B5 rb4, rb5; bf16x8 a0[4], a1[4], a2[4], a3[4], a4[4];
        f32x4 ac[4][5];
        const size_t xo = (size_t)(rb * 64 + (wid & 3) * 16) * D_ + 72 * j3;
        bool opub = true;
#define OPUB() if (relane() == 0) __hip_atomic_store((gu64*)(ws + WS_FL) + (pm * 2 + (j >> 3)) * 64 + 32 + (j & 7) * 4 + (wid - 4), epoch, RLX_AGENT)
        UNDEF5(rb4); UNDEF5(rb5);
        if (wid < 4) {
            k3_lb_if(rb2, va, va5, wb + 2 * 18432, 0, 0); k3_lb_if(rb3, va, va5, wb + 3 * 18432, 0, 0); TP();
            k3_lb_if(rb4, va, va5, wb + 4 * 18432, 0, 0); k3_lb_if(rb5, va, va5, wb + 5 * 18432, 0, 0); TP();
        } else {
            const int orow = pm * 128 + j * 8, ck = (wid - 4) * 18 + (lane >> 3);
            const char* ob = ws + WS_UO + ((size_t)((orow >> 6) * 90 + 72) * 4 + ((orow >> 4) & 3)) * 1024 + (orow & 15) * 16;
            unsigned vo_ = (ck >> 2) * 4096 + (ck & 3) * 256 + (lane & 7) * 16; PIN(vo_);
            const LAS char* ol = lds + (k3_orow(lane & 7) + ck * 16); PIN(ol);
            SB();
            while (SY[0] < 4) __builtin_amdgcn_s_sleep(1);
            TP();
            const u32x4 o0 = *(const LAS u32x4*)ol, o1 = *(const LAS u32x4*)(ol + 128), o2 = *(const LAS u32x4*)(ol + 256);
            opub = __builtin_amdgcn_readfirstlane(SY[1]) == 0;
            st16s<0>(ob, vo_, o0); st16s<0>(ob + 8192, vo_, o1);
            if (lane < 16) st16s<0>(ob + 16384, vo_, o2);
            TP();
            if (opub) { wait_vm<0>(); OPUB(); }
        }
        SB();
        TP();
        if (wid == PW2) {
            const unsigned long long t0 = __builtin_amdgcn_s_memrealtime();
            u64 f = __hip_atomic_load(FLr + (lane & 31), RLX_AGENT);
            while (!__all(f == epoch) && __builtin_amdgcn_s_memrealtime() - t0 < 5000000ull) {
                __builtin_amdgcn_s_sleep(POLLSLEEP);
                f = __hip_atomic_load(FLr + (lane & 31), RLX_AGENT);
            }
            SY[1] = 1;
        }
        while (SY[1] == 0) __builtin_amdgcn_s_sleep(1);
        TP();
        STAMP(2, 0); STAMP(2, 1);
#define LA4 "global_load_dwordx4 %0, %4, %5 sc1\n\tglobal_load_dwordx4 %1, %4, %6 sc1\n\tglobal_load_dwordx4 %2, %4, %7 sc1\n\tglobal_load_dwordx4 %3, %4, %8 sc1\n"
#define LAO(d, o_) : "=&v"(d[0]), "=&v"(d[1]), "=&v"(d[2]), "=&v"(d[3]) : "v"(va), "s"(ua.p[0] + (o_)), "s"(ua.p[1] + (o_)), "s"(ua.p[2] + (o_)), "s"(ua.p[3] + (o_))
#define LA(d, i) asm volatile(LA4 LAO(d, (i) * 16384) : "memory")
#define PIN4(d) PIN(d[0]); PIN(d[1]); PIN(d[2]); PIN(d[3])
#define GO(d, rb_, W, NX, ...) wait_vm<W>(); TK(); PIN4(d); SB(); k3_step<NX, ##__VA_ARGS__>(ac, d, rb_, va, ua)
        TK();
        LA(a0, 0); LA(a1, 1);
        const bool ext = wid < 2;
#define GO2(d, rb_, WE, WL, NX, ...) if (wid < 4) wait_vm<WE>(); GO(d, rb_, WL, NX, ##__VA_ARGS__)
        wait_vm<4>();
        if (!opub) OPUB();
        GO(a0, rb0, 5, 2, true); k3_lb_if(rb4, va, va5, wb + 4 * 18432, wid >> 2, 1);
        pin5(rb2); pin5(rb3);
        GO2(a1, rb1, 4, 9, 3); k3_lb_if(rb5, va, va5, wb + 5 * 18432, wid >> 2, 1);
        GO2(a0, rb2, 4, 14, 4);
        if (wid >= 4) {
            asm volatile("s_mov_b32 m0, %2\n\ts_nop 0\n\tglobal_load_lds_dword %0, %1 sc1" ::"v"(relane() * 4), "s"(FLr + 32), "s"(lds0 + TOPL + 64 + wid * 256));
            k3_b(lds0 + so[1], va, wb + 7 * 18432);
        }
        GO2(a1, rb3, 4, 15, 5); LB(rb0, 10);
        if (wid >= 4) {
            wait_vm<14>();
            const int l2 = relane();
            const unsigned fo = *(const volatile LAS unsigned*)(lds + TOPL + 64 + wid * 256 + l2 * 4);
            if (__builtin_expect(!__all(fo == ((l2 & 1) ? (unsigned)(epoch >> 32) : (unsigned)epoch)), 0)) {
                const unsigned long long t0 = __builtin_amdgcn_s_memrealtime();
                u64 f2; do { f2 = __hip_atomic_load(FLr + 32 + (l2 & 31), RLX_AGENT); } while (!__all(f2 == epoch) && __builtin_amdgcn_s_memrealtime() - t0 < 5000000ull);
            }
        }
        if (wid < 4) wait_vm<9>();
        pin5(rb4); pin5(rb5);
        GO(a0, rb4, 14, 6); k3_lb_if(rb1, va, va5, wb + 11 * 18432, wid >> 1, 0); RB(rb2, 0); SB();
        GO(a1, rb5, 9, 7); LA(a2, 8); RB(rb3, 1); SB();
        GO(a0, rb2, 8, 9); LA(a3, 10); RB(rb4, 2); SB();
        asm volatile("s_cmp_lg_u32 %9, 0\n\ts_cbranch_scc1 .Lla%=\n\t" LA4 ".Lla%=:" LAO(a4, 11 * 16384), "s"(wid >> 1) : "memory", "scc");
        GO(a1, rb3, 12, -1); RB(rb5, 3); SB();
        wait_vm<0>(); TK(); PIN4(a0); PIN4(a2); PIN4(a3); PIN4(a4); pin5(rb0); pin5(rb1);
        if (ext) k3_step<-1>(ac, a4, rb1, va, ua);
        const int l3 = relane(), r16 = l3 & 15, g = l3 >> 4;
        LAS char* pw = (LAS char*)lds + va;
        u32x2 xv[5];
        if (wid < 4) {
#pragma unroll
            for (int n = 0; n < 5; ++n) xv[n] = *(const u32x2*)(x + xo + r16 * D_ + n * 16 + (n < 4 ? g : (g & 1)) * 4);
        }
        SB();
#pragma unroll
        for (int m = 0; m < 4; ++m)
#pragma unroll
            for (int q = 0; q < 15; ++q) {
                if (q < 5) K3MM(ac[m][q], rb4.b[q], a2[m]); else if (q < 10) K3MM(ac[m][q - 5], rb0.b[q - 5], a3[m]); else K3MM(ac[m][q - 10], rb5.b[q - 10], a0[m]);
                SB();
                if (m > 0 && q % 3 == 0) { const int n = q / 3; if (n < 4 || g < 2) *(LAS f32x4*)(pw + so[m - 1] + n * 1024) = ac[m - 1][n]; SB(); }
            }
        asm volatile("s_nop 7\n\ts_nop 7\n\ts_waitcnt lgkmcnt(0)" ::: "memory");
        TK();
        STAMP(2, 2);
        const LAS char* pr = lds + va;
#define PART(k, n) *(const LAS f32x4*)(pr + k3_so((wid & 4) + ((wid + 3 - (k)) & 3), k) + (n) * 1024)
        if (wid < 4) {
            __builtin_amdgcn_s_setprio(EPRIO);
            if (l3 == 0) __hip_atomic_fetch_add((LAS unsigned*)SY + 4, 1u, __ATOMIC_RELAXED, __HIP_MEMORY_SCOPE_WORKGROUP);
#pragma unroll
            for (int n = 0; n < 5; ++n) {
                ac[3][n] = f32x4{fadd1(ac[3][n][0], bflo(xv[n][0])), fadd1(ac[3][n][1], bfhi(xv[n][0])), fadd1(ac[3][n][2], bflo(xv[n][1])), fadd1(ac[3][n][3], bfhi(xv[n][1]))}; PIN(ac[3][n]);
            }
            while (SY[4] < 4) __builtin_amdgcn_s_sleep(1);
            SB();
#pragma unroll
            for (int n = 0; n < 5; ++n) {
                const f32x4 p0 = PART(0, n), p1 = PART(1, n), p2 = PART(2, n);
#pragma unroll
                for (int e = 0; e < 4; ++e) ac[3][n][e] = fadd1(fadd1(ac[3][n][e], p0[e]), fadd1(p1[e], p2[e]));
            }
#pragma unroll
            for (int n = 0; n < 5; ++n) if (n < 4 || g < 2) *(LAS f32x4*)(pw + so[3] + n * 1024) = ac[3][n];
        }
        SB();
        asm volatile("s_waitcnt lgkmcnt(0)" ::: "memory"); TK();
        asm volatile("s_barrier" ::: "memory"); TK();
        if (wid >= 4) {
            u32x2 o[5];
#pragma unroll
            for (int n = 0; n < 5; ++n) {
                const f32x4 r = ((ac[3][n] + PART(0, n)) + (PART(1, n) + PART(2, n))) + *(const LAS f32x4*)(pr + k3_so(wid - 4, 3) + n * 1024);
                o[n] = u32x2{cvtpk(r[0], r[1]), cvtpk(r[2], r[3])};
            }
            u16* op = out + xo + r16 * D_;
#pragma unroll
            for (int n = 0; n < 4; n += 2) {
                auto r0 = __builtin_amdgcn_permlane16_swap(o[n][0], o[n + 1][0], false, false); auto r1 = __builtin_amdgcn_permlane16_swap(o[n][1], o[n + 1][1], false, false);
                *(u32x4*)(op + (n + (g & 1)) * 16 + (g >> 1) * 8) = u32x4{r0[0], r1[0], r0[1], r1[1]};
            }
            if (g < 2) *(u32x2*)(op + 64 + g * 4) = o[4];
        }
    }
    STAMP_DRAIN(); STAMP(2, 3);
}
__global__ __launch_bounds__(512, 2) void fused_kernel(const u16* __restrict__ x, const u16* __restrict__ wq, const u16* __restrict__ wk, const u16* __restrict__ wv,
                                                       const u16* __restrict__ w_in, const u16* __restrict__ w_sp, const u16* __restrict__ b_sp, const u16* __restrict__ w_out,
                                                       const u16* __restrict__ wo, char* __restrict__ ws, u16* __restrict__ out) {
    G2P gp;
    const u64 epoch = phase1(gp, x, wq, wk, wv, w_in, w_sp, b_sp, w_out, wo, ws);
    phase2(gp, w_sp, b_sp, x, ws, out, epoch, __builtin_amdgcn_readfirstlane(threadIdx.x >> 6));
}
__global__ void text_end_marker() { asm volatile(".Lk3_end:"); }

extern "C" void kernel_launch(void* const* d_in, const int* in_sizes, int n_in, void* d_out, int out_size, void* d_ws, size_t ws_size, hipStream_t stream) {
    const u16* x = (const u16*)d_in[0]; const u16* wq = (const u16*)d_in[1]; const u16* wk = (const u16*)d_in[2]; const u16* wv = (const u16*)d_in[3];
    const u16* wo = (const u16*)d_in[4]; const u16* w_in = (const u16*)d_in[6]; const u16* w_sp = (const u16*)d_in[7]; const u16* b_sp = (const u16*)d_in[8];
    const u16* w_out = (const u16*)d_in[9]; char* ws = (char*)d_ws;
    (void)hipFuncSetAttribute((const void*)fused_kernel, hipFuncAttributeMaxDynamicSharedMemorySize, LDS_ALL);
    fused_kernel<<<256, 512, LDS_ALL, stream>>>(x, wq, wk, wv, w_in, w_sp, b_sp, w_out, wo, ws, (u16*)d_out);
}
```

```cpp
#define DBID 100
#define DMUL 0
#include <hip/hip_runtime.h>
#include <hip/hip_bf16.h>
#include <stdint.h>

#define STNOP 1
typedef unsigned short u16;
typedef unsigned long long u64;
typedef short bf16x8 __attribute__((ext_vector_type(8)));
typedef short s16x4 __attribute__((ext_vector_type(4)));
typedef float f32x2 __attribute__((ext_vector_type(2)));
typedef float f32x4 __attribute__((ext_vector_type(4)));
typedef float f32x16 __attribute__((ext_vector_type(16)));
typedef unsigned u32x4 __attribute__((ext_vector_type(4)));
typedef unsigned u32x2 __attribute__((ext_vector_type(2)));
typedef __bf16 bf16x2_t __attribute__((ext_vector_type(2)));
#define LAS __attribute__((address_space(3)))
#define DI __device__ __forceinline__

constexpr int Z1L = 122880, TOPL = 159744, LDS_ALL = 163840;
constexpr int K2_SA = 16384, K2_SB = 18432, K2_SK = 6144, K2_B0 = 3 * K2_SA, K2_K0 = K2_B0 + 3 * K2_SB, K2_X0 = 147520, K2_LDS = K2_X0 + 9344 + 64 + 2048;
constexpr int S_ = 2048, D_ = 576, FH = 2304, F_ = 4608;

constexpr size_t WS_Q = 0;
constexpr size_t WS_KV = WS_Q + (size_t)S_ * D_ * 2;
constexpr size_t WS_Z1 = WS_KV + 64 * 3072;
constexpr size_t WS_Z2T = WS_Z1 + (size_t)S_ * FH * 2;
constexpr size_t WS_UO = WS_Z2T + (size_t)S_ * FH * 2;
constexpr size_t WS_ST = WS_UO + (size_t)S_ * 2880 * 2;
constexpr size_t WS_XCH = WS_ST + (size_t)S_ * 16 * 8;
constexpr size_t WS_XR = WS_XCH + 4096 + 16384;
constexpr size_t WS_FL = WS_XR + 4096;
constexpr size_t WS_FA = WS_FL + 16384;
constexpr size_t WS_FJ = WS_FA + 2048;
constexpr size_t WS_FP = WS_FJ + 8192;
constexpr size_t WS_W3T = WS_FP + 2048;
constexpr size_t WS_DBG = 128u << 20;
#define STAMP(kid, i)
#define STAMP_DRAIN()
#define STAMPT(tid, kid, i)

DI unsigned cvtpk(float lo, float hi) { f32x2 v = {lo, hi}; return __builtin_bit_cast(unsigned, __builtin_convertvector(v, bf16x2_t)); }
DI float bf2f(u16 b) { return __uint_as_float(((unsigned)b) << 16); }
DI float bflo(unsigned w) { return __uint_as_float(w << 16); }
DI float bfhi(unsigned w) { return __uint_as_float(w & 0xffff0000u); }
#define SB() __builtin_amdgcn_sched_barrier(0)
#define PIN(x) asm volatile("" : "+v"(x))
#define EX(x) __builtin_amdgcn_exp2f(x)
#define MFMA16(a, b, c) __builtin_amdgcn_mfma_f32_16x16x32_bf16(a, b, c, 0, 0, 0)
#define MFMA32(a, b, c) __builtin_amdgcn_mfma_f32_32x32x16_bf16(a, b, c, 0, 0, 0)

DI s16x4 trread(const LAS char* p) { return __builtin_bit_cast(s16x4, __builtin_amdgcn_ds_read_tr16_b64_v4i16((LAS s16x4*)p)); }
template <int N> DI void wait_vm_bar() { asm volatile("s_waitcnt vmcnt(%0) lgkmcnt(0)\n\ts_barrier" ::"n"(N) : "memory"); }
template <int N> DI void wait_vm() { asm volatile("s_waitcnt vmcnt(%0)" ::"n"(N) : "memory"); }
template <int OFF> DI void glds_s(unsigned voff, const char* sbase) { asm volatile("global_load_lds_dwordx4 %0, %1 offset:%2" ::"v"(voff), "s"(sbase), "n"(OFF)); }
template <int OFF> DI void glds_sc1(unsigned voff, const char* sbase) { asm volatile("global_load_lds_dwordx4 %0, %1 offset:%2 sc1" ::"v"(voff), "s"(sbase), "n"(OFF)); }
DI void glds_v(const char* p, unsigned m0v, u64 em) { asm volatile("s_mov_b32 m0, %1\n\ts_mov_b64 exec, %2\n\tglobal_load_lds_dwordx4 %0, off\n\ts_mov_b64 exec, -1" ::"v"(p), "s"(m0v), "s"(em)); }
DI unsigned ld_sc1_async(const char* sb, unsigned vo) { unsigned v; asm volatile("global_load_dword %0, %1, %2 sc1" : "=v"(v) : "v"(vo), "s"(sb) : "memory"); return v; }
#define WT
DI void st16(void* p, u32x4 v) { asm volatile("global_store_dwordx4 %0, %1, off sc1\n\ts_nop 1" :: "v"(p), "v"(v) : "memory"); }
DI void st8(void* p, u32x2 v) { asm volatile("global_store_dwordx2 %0, %1, off sc1" :: "v"(p), "v"(v) : "memory"); }
template <int OFF> DI void st16s(const char* sb, unsigned vo, u32x4 v) { asm volatile("global_store_dwordx4 %0, %1, %2 offset:%3 sc1\n\ts_nop %4" :: "v"(vo), "v"(v), "s"(sb), "n"(OFF), "n"(STNOP) : "memory"); }
template <int OFF> DI void st8s(const char* sb, unsigned vo, u32x2 v) { asm volatile("global_store_dwordx2 %0, %1, %2 offset:%3 sc1" :: "v"(vo), "v"(v), "s"(sb), "n"(OFF) : "memory"); }
DI float fadd1(float a, float b) { float r = a + b; asm("" : "+v"(r)); return r; }
DI float fmul1(float a, float b) { float r = a * b; asm("" : "+v"(r)); return r; }
DI float ffma1(float a, float b, float c) { float r = __builtin_fmaf(a, b, c); asm("" : "+v"(r)); return r; }
DI float lone(float a) { asm("" : "+v"(a)); return a; }
DI void set_m0(unsigned v) { asm volatile("s_mov_b32 m0, %0\n\ts_nop 0" ::"s"(v)); }
DI bf16x8 ldsA(const LAS char* p) { return *(const LAS bf16x8*)p; }
DI bf16x8 ldsB2(const LAS char* p0, const LAS char* p1) { s16x4 lo = trread(p0), hi = trread(p1); return __builtin_shufflevector(lo, hi, 0, 1, 2, 3, 4, 5, 6, 7); }
DI bf16x8 ldsB(const LAS char* p) { return ldsB2(p, p + 256); }

DI float gelu_t(float x) {
    const float c0 = -2.0f * 1.4426950408889634f * 0.7978845608028654f;
    const float c1 = c0 * 0.044715f;
    float x2 = x * x;
    float t = __builtin_fmaf(x2, c1, c0);
    float e = __builtin_amdgcn_exp2f(x * t);
    return x * __builtin_amdgcn_rcpf(1.0f + e);
}
constexpr float QSCALE = 0.23570226039551584f * 1.4426950408889634f;

#define KO1 (3 * (((idx & 7) + (idx >> 3)) % 3))
#define KO2 (((idx & 7) + 2 * (idx >> 3) + 4 * (xcd & 1)) % 9)
#define K2_KO (8 * (xcd >> 1))
#define EPRIO 1
#define PFW 0
#define SPRIO 1
#define HW 3
#define ZSCH 5
#define ZS0 1
#define GPRIO 1
#define ZPOLL 1
#define QEARLY 1
#define WSTR 32
#define WOFF 0
#define W2STR 32
#define TFA 7
#define PW2 3
#define PEARLY 0
#define POLLSLEEP 0
#define APRIO 0
#define K3MAP 2
#define K3ROT 0
#define APRE 0
#define AGSLOT 1
#define ZHALF 1
#define POLLW 5
#define POLLSLEEP1 1
#define PLSLOT 0
constexpr int K1_SA = 16384, K1_SB = 24576, K1_B0 = 3 * K1_SA, K1_TAB = K1_B0 + 3 * K1_SB, K1_X0 = K1_TAB + 10240, K1_LDS = K1_X0 + 16384 + 2048;
typedef __attribute__((address_space(1))) unsigned gu32;
typedef __attribute__((address_space(1))) unsigned long long gu64;
#define RLX_AGENT __ATOMIC_RELAXED, __HIP_MEMORY_SCOPE_AGENT
struct K1F { bf16x8 a[4], b[3], x; };
struct K1S {
    unsigned va[2], vb[2]; const char* sa; const char* sb;
    const char* gp; unsigned st;
    int k1, k2;
    unsigned m0a, m0b; u64 em;
    const LAS char* pa[2]; const LAS char* px[2]; const LAS char* pb[3];
};
DI void k1_issue(K1S& s, int i, int w) {
    if (i == 0) { set_m0(s.m0a + w * K1_SA); glds_s<0>(s.va[0], s.sa); }
    if (i == 1) { glds_s<1024>(s.va[1], s.sa); s.sa += s.k1 == 8 ? -8 * 128 : 128; asm volatile("" : "+s"(s.sa)); }
    if (i == 2) { glds_v(s.gp + (unsigned long long)s.st * (unsigned)s.k1, s.m0b + w * K1_SB + 2048, s.em); s.k1 = s.k1 == 8 ? 0 : s.k1 + 1; asm volatile("" : "+s"(s.k1)); }
    if (i == 3) { set_m0(s.m0b + w * K1_SB); glds_s<0>(s.vb[0], s.sb); }
    if (i == 4) { glds_s<1024>(s.vb[1], s.sb); s.sb += s.k2 == 8 ? -8 * 128 * F_ : 128 * F_; s.k2 = s.k2 == 8 ? 0 : s.k2 + 1; asm volatile("" : "+s"(s.sb), "+s"(s.k2)); }
}
DI f32x2 gelu_y2(f32x2 y, float k0, float k1, float ir) {
    f32x2 t = (y * y) * k1 + k0;
    f32x2 a = y * t;
    f32x2 e = {__builtin_amdgcn_exp2f(a[0]), __builtin_amdgcn_exp2f(a[1])};
    f32x2 d = e * ir + ir;
    f32x2 rc = {__builtin_amdgcn_rcpf(d[0]), __builtin_amdgcn_rcpf(d[1])};
    return y * rc;
}
DI float dpp_x1(float x) { return __int_as_float(__builtin_amdgcn_mov_dpp(__float_as_int(x), 0xB1, 0xf, 0xf, true)); }
DI float dpp_x2(float x) { return __int_as_float(__builtin_amdgcn_mov_dpp(__float_as_int(x), 0x4E, 0xf, 0xf, true)); }
DI void kv_store(const char* kv, int sv, int j, int c, int g, f32x4 v) {
    if (j == 4) { st8s<0>(kv + sv * 256, ((g >> 1) * 32 + c) * 16 + (g & 1) * 8, u32x2{cvtpk(v[0], v[1]), cvtpk(v[2], v[3])}); return; }
    const unsigned kk = cvtpk(v[2], v[3]);
    const bool o1 = c & 1, o2 = c & 2;
    float t0 = dpp_x1(o1 ? v[0] : v[1]), t1 = dpp_x1(o1 ? v[2] : v[3]);
    if (o1) { v[0] = t0; v[2] = t1; } else { v[1] = t0; v[3] = t1; }
    t0 = dpp_x2(o2 ? v[0] : v[2]); t1 = dpp_x2(o2 ? v[1] : v[3]);
    if (o2) { v[0] = t0; v[1] = t1; } else { v[2] = t0; v[3] = t1; }
    u32x2 w = {cvtpk(v[0], v[1]), cvtpk(v[2], v[3])};
    const int kq = c >> 2; unsigned off;
    if (j == 5) {
        off = g < 2 ? c * 16 + (g ^ 1) * 8 : 256 + ((kq & 1) * 8 + 4 * (g - 2) + (c & 3)) * 16 + (kq >> 1) * 8;
        if (g < 2) w = g ? u32x2{kk, 0x3f80u} : u32x2{0u, 0u};
    } else {
        off = 512 + ((kq & 1) * 16 + 4 * g + (c & 3)) * 16 + (kq >> 1) * 8;
        if (g == 3 && !o2) { const unsigned cv = o1 ? 0u : 0x3f803f80u; w = u32x2{cv, cv}; }
    }
    st8s<1024>(kv + sv * 1024, off, w);
}
template <int KS, int GL, int RS, int WS, bool RX> DI void k1_p1(K1S& s, const K1F& f, K1F& n, f32x4 (&acc)[4][2], f32x4& sacc, f32x4 (&cso)[2], f32x4& rs, f32x4& rq, const bf16x8& ones) {
    constexpr int AO = RS * K1_SA, BO = RS * K1_SB + KS * 12288;
#define MX(d, a_, b_) d = MFMA16(a_, b_, d); PIN(d); SB()
#define MM(m, nn) MX(acc[m][nn], f.b[nn], f.a[m])
    MM(0, 0); n.b[0] = ldsB(s.pb[0] + BO); SB();
    MM(1, 0); n.a[0] = ldsA(s.pa[KS] + AO); SB();
    MM(2, 0); n.a[1] = ldsA(s.pa[KS] + AO + 2048); SB();
    MM(3, 0); n.a[2] = ldsA(s.pa[KS] + AO + 4096); SB();
    MX(cso[0], f.b[0], ones); n.a[3] = ldsA(s.pa[KS] + AO + 6144); SB();
    MX(sacc, f.b[2], f.x); n.b[1] = ldsB(s.pb[1] + BO); SB();
    MX(cso[1], f.b[2], ones); n.b[2] = ldsB(s.pb[2] + BO); SB();
    MX(rs, ones, f.x); k1_issue(s, GL == 1 ? 0 : 3, WS); SB();
    MX(rq, f.x, f.x); if (RX) n.x = ldsA(s.px[KS] + AO); SB();
    MM(0, 1); k1_issue(s, GL == 1 ? 1 : 4, WS); SB();
    MM(1, 1); if (GL == 1) k1_issue(s, 2, WS); SB();
    MM(2, 1);
    MM(3, 1);
}
template <int KS, int GL, int RS, int WS, bool RD, class E> DI void k1_p2(K1S& s, const K1F& f, K1F& n, f32x4 (&acc)[4][3], f32x4 (&cso)[2], const bf16x8& ones, E epi) {
    constexpr int AO = RS * K1_SA, BO = RS * K1_SB + KS * 12288;
    MM(0, 0); if (RD) n.b[0] = ldsB(s.pb[0] + BO); epi(0); SB();
    MM(1, 0); if (RD) n.a[0] = ldsA(s.pa[KS] + AO); epi(1); SB();
    MM(2, 0); if (RD) n.a[1] = ldsA(s.pa[KS] + AO + 2048); epi(2); SB();
    MM(3, 0); if (RD) n.a[2] = ldsA(s.pa[KS] + AO + 4096); epi(3); SB();
    MX(cso[0], f.b[0], ones); if (RD) n.a[3] = ldsA(s.pa[KS] + AO + 6144); epi(4); SB();
    MM(0, 1); if (RD) n.b[1] = ldsB(s.pb[1] + BO); epi(5); SB();
    MM(1, 1); if (RD) n.b[2] = ldsB(s.pb[2] + BO); epi(6); SB();
    MM(2, 1); if (GL) k1_issue(s, GL == 1 ? 0 : 3, WS); epi(7); SB();
    MM(3, 1); epi(8); SB();
    MX(cso[1], f.b[1], ones); if (GL) k1_issue(s, GL == 1 ? 1 : 4, WS); epi(9); SB();
    MM(0, 2); epi(10); SB();
    MM(1, 2); if (GL == 1) k1_issue(s, 2, WS); epi(11); SB();
    MM(2, 2); epi(12); SB();
    MM(3, 2); epi(13); SB();
#undef MM
#undef MX
}

constexpr unsigned XSALT = 0x9e3779b9u;
DI void epoch_reply(char* ws, int lane, const unsigned (&nv)[4], u64 epoch) {
    const unsigned lo = (unsigned)epoch, hi = (unsigned)(epoch >> 32);
    st16s<0>(ws + WS_XR, lane * 16, u32x4{nv[0], lo, hi, nv[0] ^ lo ^ hi ^ XSALT}); st16s<1024>(ws + WS_XR, lane * 16, u32x4{nv[1], lo, hi, nv[1] ^ lo ^ hi ^ XSALT});
    st16s<2048>(ws + WS_XR, lane * 16, u32x4{nv[2], lo, hi, nv[2] ^ lo ^ hi ^ XSALT}); st16s<3072>(ws + WS_XR, lane * 16, u32x4{nv[3], lo, hi, nv[3] ^ lo ^ hi ^ XSALT});
}
#define XN_LOADS "global_load_dword %0, %4, %5 sc1\n\tglobal_load_dword %1, %4, %5 offset:256 sc1\n\tglobal_load_dword %2, %4, %5 offset:512 sc1\n\tglobal_load_dword %3, %4, %5 offset:768 sc1\n"
DI void epoch_service(char* ws, int lane, u64 epoch) {
    unsigned nv[4];
    asm volatile(XN_LOADS "\ts_waitcnt vmcnt(0)" : "=&v"(nv[0]), "=&v"(nv[1]), "=&v"(nv[2]), "=&v"(nv[3]) : "v"(lane * 4), "s"(ws + WS_XCH) : "memory");
    epoch_reply(ws, lane, nv, epoch);
}
DI void poll_flags(char* ws, u64 epoch, int lane, const char* sb, unsigned vo) {
    const u64 t0 = __builtin_amdgcn_s_memrealtime();
    for (;;) {
        u64 f_;
        asm volatile("global_load_dwordx2 %0, %1, %2 sc1\n\ts_waitcnt vmcnt(0)" : "=&v"(f_) : "v"(vo), "s"(sb) : "memory");
        if (__all(f_ == epoch)) break;
        const u64 dt = __builtin_amdgcn_s_memrealtime() - t0;
        if (dt > 5000000ull) break;
        if (dt > 1000 && blockIdx.x == 0) epoch_service(ws, lane, epoch);
        __builtin_amdgcn_s_sleep(POLLSLEEP1);
    }
}
DI void first_loads(char* ws, unsigned lds0, int lane, int wid, int j) {
    const unsigned m0 = lds0 + K2_B0 + wid * 6144, vl = lane * 16;
    const char* sl = ws + WS_Z2T + j * 32 * 18432 + wid * 6144;
#pragma unroll
    for (int t = 0; t < 2; ++t) {
        set_m0(m0 + t * K2_SB); glds_sc1<0>(vl, sl); glds_sc1<1024>(vl, sl); glds_sc1<2048>(vl, sl); glds_sc1<3072>(vl, sl);
        set_m0(m0 + t * K2_SB + 4096); glds_sc1<0>(vl, sl + 4096); glds_sc1<1024>(vl, sl + 4096); sl += K2_SB;
    }
}
DI unsigned g2_va(int wid, int lane, int i) { int row = 8 * (wid * 4 + i) + (lane >> 3), c = (lane & 7) ^ ((row >> 1) & 7); return row * (S_ * 2) + c * 16 + 4096 - i * 1024; }
struct G2P { unsigned va[4]; const LAS char* pa[2][2]; const LAS char* pb[2]; const LAS char* ps[2]; f32x4 acc[4][4], sacc[2]; };
DI void g2_prep(G2P& s, const LAS char* lds, int lane, int wid) {
    const int wr = wid >> 1, wc = wid & 1, r = lane & 15, g = lane >> 4, q = r >> 2, p = lane & 3;
#pragma unroll
    for (int m = 0; m < 4; ++m) {
        const float b = bf2f(*(const LAS u16*)(lds + TOPL + 2304 + (wr * 64 + (m ^ (2 * wc)) * 16 + r) * 2));
#pragma unroll
        for (int n = 0; n < 4; ++n) { s.acc[m][n] = f32x4{b, b, b, b}; PIN(s.acc[m][n]); }
        if (m < 2) { s.sacc[m] = f32x4{b, b, b, b}; PIN(s.sacc[m]); }
    }
#pragma unroll
    for (int i = 0; i < 4; ++i) { s.va[i] = g2_va(wid, lane, i); PIN(s.va[i]); }
#pragma unroll
    for (int ks = 0; ks < 2; ++ks) {
        const int o = r * 128 + (((4 * ks + g) ^ (r >> 1)) * 16) + wr * 8192;
        s.pa[0][ks] = lds + o + wc * 4096; s.pa[1][ks] = lds + o + (1 - wc) * 4096; PIN(s.pa[0][ks]); PIN(s.pa[1][ks]);
    }
    const int ob = K2_B0 + g * 2304 + q * 32 + 8 * p;
    s.pb[0] = lds + ob + wc * 1024 + ((g & 1) * 128); s.pb[1] = lds + ob + wc * 1024 + (((g & 1) ^ 1) * 128);
    s.ps[0] = lds + ob + 2048 + ((g & 1) * 128); s.ps[1] = lds + ob + 2048 + (((g & 1) ^ 1) * 128);
    PIN(s.pb[0]); PIN(s.pb[1]); PIN(s.ps[0]); PIN(s.ps[1]);
}
DI u64 phase1(G2P& gp, const u16* __restrict__ x, const u16* __restrict__ wq, const u16* __restrict__ wk, const u16* __restrict__ wv,
                                                    const u16* __restrict__ w_in, const u16* __restrict__ w_sp, const u16* __restrict__ b_sp, const u16* __restrict__ w_out, const u16* __restrict__ wo, char* __restrict__ ws) {
    extern __shared__ __attribute__((aligned(16))) char smem[];
    const int bid = blockIdx.x, xcd = bid & 7, idx = bid >> 3;
    const int pm = (xcd & 1) * 8 + (idx & 7), j = (xcd >> 1) * 4 + (idx >> 3);
    const int lane = threadIdx.x & 63, wid = __builtin_amdgcn_readfirstlane(threadIdx.x >> 6), wr = wid >> 2, wc = wid & 3;
    const unsigned lds0 = (unsigned)(uintptr_t)smem;
    const LAS char* lds = (const LAS char*)smem;
    {
        const char* pc; unsigned len;
        asm volatile("s_getpc_b64 %0\n.Lk1_here:\n\ts_mov_b32 %1, .Lk3_end-.Lk1_here" : "=s"(pc), "=s"(len));
        unsigned off = ((idx * 8 + wid) * 2 + (lane & 1)) * 128; off = off < len ? off : len;
        asm volatile("s_mov_b32 m0, %2\n\ts_nop 0\n\tglobal_load_lds_dword %0, %1" ::"v"(off), "s"(pc), "s"(lds0 + K1_TAB + 9216 + 512));
    }
    K1S s; s.em = (wid & 1) ? 0xccccccccull : 0x33333333ull;
    s.m0a = lds0 + wid * 2048; s.m0b = lds0 + K1_B0 + wid * 3072;
    const int brow = (lane >> 2) & 7, bc = (lane & 3) ^ (2 * (wid & 1)), bk = 8 * wid + brow;
    const int ko2 = KO2, ko1 = KO1;
    unsigned oa = pm * 128 * D_ * 2, ob = 256 * j, oa1 = oa + ko1 * 128, ob1 = ob + FH * 2 + ko1 * 128 * F_, vg = bk * (F_ * 2) + 16 * (bc & 1);
    {
        int r8 = lane >> 3;
        s.va[0] = (16 * wid + r8) * (D_ * 2) + (((lane & 7) ^ (r8 >> 1)) * 16);
        s.va[1] = (16 * wid + 8 + r8) * (D_ * 2) + (((lane & 7) ^ (4 + (r8 >> 1))) * 16) - 1024;
        s.vb[0] = bk * (F_ * 2) + (lane >> 5) * 64 + bc * 16 + 1024; s.vb[1] = s.vb[0] + 128 - 1024;
        s.k1 = s.k2 = ko1; s.st = 128 * F_;
        PIN(s.va[0]); PIN(s.va[1]); PIN(s.vb[0]); PIN(s.vb[1]); PIN(vg);
        asm volatile("" : "+s"(oa1), "+s"(ob1), "+s"(s.m0a), "+s"(s.m0b));
    }
    SB();
    s.sa = (const char*)x + oa1; s.sb = (const char*)w_in - 1024 + ob1;
    k1_issue(s, 0, 0); k1_issue(s, 1, 0); k1_issue(s, 3, 0); k1_issue(s, 4, 0);
    SB();
    STAMP(0, 0);
    s.gp = (const char*)w_in + (4096 + 32 * j + FH * 2) + vg;
    k1_issue(s, 2, 0);
    const char* sa0 = (const char*)x + oa;
    const char* sb0 = (const char*)w_in - 1024 + ob;
    gu32* XN = (gu32*)(ws + WS_XCH) + pm * 16;
    gu32* XE = (gu32*)(ws + WS_XCH + 4096) + pm * 256;
    gu64* XP = (gu64*)(ws + WS_ST) + (size_t)pm * 16 * 128;
    const char* xe_s = ws + WS_XCH + 4096 + (pm * 256 + j * 16) * 4;
    const unsigned le = (lane & 16) ? 16384 - (pm * 16 + j) * 48 + (lane & 3) * 4 : (lane & 15) * 4;
    unsigned eo = ld_sc1_async(xe_s, le);
    if (SPRIO == 1 && wid >= 4) __builtin_amdgcn_s_setprio(1);
    SB();
    unsigned nonce = 0;
    const char* gp2; unsigned st2;
    {
        const u16* p; int rs;
        const u16* sp0 = j < 4 ? wq + 512 + 16 * j : (j == 5 ? wk + 10 : (j == 6 ? wv + 8 : wk));
        const u16* sp1 = j < 4 ? wq + 520 + 16 * j : (j == 5 ? wv : (j == 6 ? wv + 10 : wk + 8));
        const int srs = j < 4 ? D_ : 18;
        if (lane >= 32) { p = wq + 32 * j + 8 * bc; rs = D_; }
        else if (bc < 2) { p = w_in + 2048 + 16 * j + 8 * bc; rs = F_; }
        else { p = bc == 2 ? sp0 : sp1; rs = srs; }
        gp2 = (const char*)(p + (size_t)bk * rs); st2 = 128 * rs; PIN(gp2); PIN(st2);
    }
    const int r16 = lane & 15, g = lane >> 4, cg = wc ^ (2 * wr);
    const int nf3 = cg == 3 ? 8 : (cg == 2 ? 9 : 10 + cg);
#define NF1(n) ((n) == 2 ? 8 : 2 * wc + ((n) ^ wr))
#define NF2(n) ((n) == 0 ? nf3 : 2 * cg + (((n) - 1) ^ wr))
    const LAS char* pb2[3];
    {
        int q = r16 >> 2, p = lane & 3;
#pragma unroll
        for (int ks = 0; ks < 2; ++ks) { s.pa[ks] = lds + (r16 * 128 + (((4 * ks + g) ^ (r16 >> 1)) * 16) + wr * 8192); s.px[ks] = s.pa[ks] + wc * 2048; PIN(s.pa[ks]); PIN(s.px[ks]); }
#pragma unroll
        for (int n = 0; n < 3; ++n) {
            int nf = NF1(n); s.pb[n] = lds + (K1_B0 + g * 3072 + q * 64 + 8 * p + (nf >> 1) * 512 + (((nf ^ g) & 1) * 32)); PIN(s.pb[n]);
            nf = NF2(n); pb2[n] = lds + (K1_B0 + g * 3072 + q * 64 + 8 * p + (nf >> 1) * 512 + (((nf ^ g) & 1) * 32)); PIN(pb2[n]);
        }
    }
    f32x4 acc1[4][2] = {}; f32x4 sacc = {}; f32x4 cso[2] = {};
    f32x4 rs = {}, rq = {};
#pragma unroll
    for (int m = 0; m < 4; ++m) { PIN(acc1[m][0]); PIN(acc1[m][1]); }
    PIN(sacc); PIN(cso[0]); PIN(cso[1]); PIN(rs); PIN(rq);
    const u32x4 onesv = {0x3f803f80u, 0x3f803f80u, 0x3f803f80u, 0x3f803f80u}; bf16x8 ones = __builtin_bit_cast(bf16x8, onesv); PIN(ones);
    K1F f0, f1;
    SB();
#pragma unroll
    for (int i = 0; i < 5; ++i) k1_issue(s, i, 1);
    SB();
    if (SPRIO == 2 && wid >= 4) __builtin_amdgcn_s_setprio(1);
    const int w3p = bid < 240 ? bid : 239;
    unsigned w3vo[2], w3so[2]; const char* w3sp; const LAS char* w3rd[2];
    {
        const int jj = w3p / 30, pc = w3p - jj * 30;
        w3sp = (pc < 24 ? (const char*)w_out : (const char*)wo - 2304 * 1152) + jj * 144;
#pragma unroll
        for (int i = 0; i < 2; ++i) {
            unsigned c_ = threadIdx.x + 512 * i; c_ = c_ < 864 ? c_ : 863;
            const unsigned row = (c_ * 7282) >> 16, k_ = pc * 96 + row, jg = (k_ * 29128) >> 22, l_ = k_ - jg * 144;
            const unsigned src = pc < 24 ? (l_ < 128 ? 128 * jg + l_ : 1920 + 16 * jg + l_) : k_;
            w3vo[i] = src * 1152 + (c_ - row * 9) * 16; PIN(w3vo[i]);
            const int u = i ? (wid < 7 ? wid + 8 : 14) : wid, k = u / 5, n = u - 5 * k;
            w3rd[i] = lds + (K1_X0 + (8 * g + (r16 >> 2)) * 144 + 8 * (lane & 3) + k * 4608 + n * 32); PIN(w3rd[i]);
            w3so[i] = w3p * 13824 + k * 4608 + n * 1024 + (n < 4 ? lane : g * 8 + r16) * 16;
            if (n == 4 && r16 >= 8) w3so[i] = 240 * 13824 + bid * 512 + (g * 8 + r16 - 8) * 16;
            PIN(w3so[i]);
        }
        asm volatile("" : "+s"(w3sp));
    }
    unsigned wspo = (pm * 128 + (idx >> 3) * 32 + wid * 4 + g) * (S_ * 2) + r16 * WSTR + WOFF; PIN(wspo);
    LAS float* redp = (LAS float*)(lds + K1_TAB + 4096) + ((wr * 64 + r16) * 5 + wc) * 2 + (g & 1); PIN(redp);
    LAS float* reds = (LAS float*)(lds + K1_TAB + 4096) + ((wr * 64 + wc * 16 + r16) * 5 + 4) * 2 + (g & 1); PIN(reds);
    SB();
#pragma unroll
    for (int i = 0; i < 3; ++i) k1_issue(s, i, 2);
    __builtin_amdgcn_s_setprio(0);
    SB();
    wait_vm_bar<8>();
    SB();
    f0.b[0] = ldsB(s.pb[0]);
    f0.a[0] = ldsA(s.pa[0]); f0.a[1] = ldsA(s.pa[0] + 2048); f0.a[2] = ldsA(s.pa[0] + 4096); f0.a[3] = ldsA(s.pa[0] + 6144);
    f0.b[2] = ldsB(s.pb[2]); f0.x = ldsA(s.px[0]); f0.b[1] = ldsB(s.pb[1]);
    SB();
    PIN(eo);
    if (wid == HW) {
        nonce = 1;
        while (__ballot(eo == nonce) != 0ull) ++nonce;
        if (lane == 0) __hip_atomic_store(XN + j, nonce, RLX_AGENT);
        __hip_atomic_store(XP + j * 128 + lane, ~0ull, RLX_AGENT); __hip_atomic_store(XP + j * 128 + 64 + lane, ~0ull, RLX_AGENT);
    }
    STAMP(0, 1);
#define P1_A(c) k1_p1<1, 2, (c), ((c) + 2) % 3, true>(s, f0, f1, acc1, sacc, cso, rs, rq, ones); wait_vm_bar<5>(); SB()
#define P1_B(c, RX) k1_p1<0, 1, ((c) + 1) % 3, (c), RX>(s, f1, f0, acc1, sacc, cso, rs, rq, ones)
#pragma unroll 1
    for (int it = 0; it < 2; ++it) { P1_A(0); P1_B(0, true); P1_A(1); P1_B(1, true); P1_A(2); P1_B(2, true); }
    const char* xn_s = ws + WS_XCH + pm * 64; const unsigned l4 = (lane & 15) * 4;
    P1_A(0);
    unsigned echoed = ld_sc1_async(xn_s, l4);
    unsigned alln[4];
    asm volatile("s_cmp_lg_u32 %6, 0\n\ts_cbranch_scc1 .Lld%=\n\t" XN_LOADS ".Lld%=:" : "=&v"(alln[0]), "=&v"(alln[1]), "=&v"(alln[2]), "=&v"(alln[3])
                 : "v"(lane * 4), "s"(ws + WS_XCH), "s"(bid) : "memory", "scc");
    s.k1 = s.k2 = ko2; s.sa = sa0 + ko2 * 128; s.sb = sb0 + (size_t)ko2 * 128 * F_; s.gp = gp2; s.st = st2; s.em = ~0ull; SB();
    P1_B(0, true);
    P1_A(1);
    PIN(echoed);
    if (wid == HW && lane < 16) __hip_atomic_store(XE + lane * 16 + j, echoed, RLX_AGENT);
    PIN(alln[0]); PIN(alln[1]); PIN(alln[2]); PIN(alln[3]);
    if (bid == 0 && wid == HW - 1) epoch_reply(ws, lane, alln, __builtin_amdgcn_s_memrealtime() | (1ull << 63));
    SB();
    P1_B(1, true);
    P1_A(2);
    s.pb[0] = pb2[0]; s.pb[1] = pb2[1]; s.pb[2] = pb2[2];
    P1_B(2, false);
    STAMP(3, 0);
    float* CS = (float*)(smem + K1_TAB);
    f32x4* RS = (f32x4*)(smem + K1_TAB + 2048);
    float* red = (float*)(smem + K1_TAB + 4096);
    f32x2* ZS = (f32x2*)(smem + K1_TAB + 9216);
    volatile LAS unsigned* HS = (volatile LAS unsigned*)(lds + K1_TAB + 1792);
    const float c0 = -2.0f * 1.4426950408889634f * 0.7978845608028654f, c1 = c0 * 0.044715f;
    f32x4 acc2[4][3] = {}; f32x4 cs2[2] = {};
    auto none = [](int) {};
    auto tabs = [&](int st) {
        if (st == 2) { if (r16 == 0) { *(f32x4*)(CS + NF1(0) * 16 + 4 * g) = cso[0]; *(f32x4*)(CS + 128 + 4 * g) = cso[1]; } }
        if (st == 3) {
            float sq = (r16 & 2) ? ((r16 & 1) ? rq[3] : rq[2]) : ((r16 & 1) ? rq[1] : rq[0]);
            float r0_ = rs[0]; PIN(r0_); PIN(sq);
            float mean = fmul1(r0_, 1.0f / D_), var = fmaxf(ffma1(-mean, mean, fmul1(sq, 1.0f / D_)), 0.f);
            rs[0] = mean; rs[1] = var + 1e-5f; PIN(rs);
        }
        if (st == 5) { rs[2] = __builtin_amdgcn_rsqf(rs[1]); PIN(rs); }
        if (st == 7) { float rstd = rs[2]; if (g == (r16 >> 2)) RS[wr * 64 + wc * 16 + r16] = f32x4{rs[0], fmul1(rstd, c0), fmul1(fmul1(rstd, rstd), fmul1(rstd, c1)), fmul1(rs[1], rstd)}; }
    };
    f32x4 er[5], cs1[3], e[3];
    f32x2 su[5], sq[5];
#define ZU(u) ((u) == 8 ? sacc : acc1[((u) >> 1) & 3][(u) & 1])
#define UM(u) ((u) == 8 ? 4 : (u) >> 1)
#define UN(u) ((u) == 8 ? 2 : (u) & 1)
#define PRE(i, u) { f32x4 y = ZU(u) - cs1[UN(u)] * er[UM(u)][0]; e[i] = ((y * y) * er[UM(u)][2] + er[UM(u)][1]) * y; ZU(u) = y; PIN(ZU(u)); PIN(e[i]); }
#define FIN(i, u, first) { ZU(u) *= e[i]; PIN(ZU(u)); f32x4 v = ZU(u); f32x2 lo = {v[0], v[1]}, hi = {v[2], v[3]}; \
        if (first) { su[UM(u)] = lo + hi; sq[UM(u)] = lo * lo + hi * hi; } else { su[UM(u)] += lo; su[UM(u)] += hi; sq[UM(u)] += lo * lo; sq[UM(u)] += hi * hi; } PIN(su[UM(u)]); PIN(sq[UM(u)]); }
#define RED(m) { float a_ = su[m][0] + su[m][1], b_ = sq[m][0] + sq[m][1]; \
            auto x1 = __builtin_amdgcn_permlane16_swap(__float_as_uint(a_), __float_as_uint(b_), false, false);       \
            float t_ = __uint_as_float(x1[0]) + __uint_as_float(x1[1]);                                               \
            auto x2 = __builtin_amdgcn_permlane32_swap(__float_as_uint(t_), __float_as_uint(t_), false, false); \
            t_ = __uint_as_float(x2[0]) + __uint_as_float(x2[1]);                                                     \
            if ((m) < 4) redp[(m) * 160] = t_; else reds[0] = t_; }
#define T1(op, v) asm volatile(op " %0, %0" : "+v"(v))
#define DE1(v, sd) asm volatile("v_fma_f32 %0, %0, %1, %1" : "+v"(v) : "v"(sd))
#define EPI_E(u0, u1, u2) [&](int st) { \
        if (st < 12) { float t_ = e[st >> 2][st & 3]; T1("v_exp_f32", t_); e[st >> 2][st & 3] = t_; } \
        if (st >= 2) { const int v_ = st - 2; float t_ = e[v_ >> 2][v_ & 3]; DE1(t_, er[v_ < 4 ? UM(u0) : (v_ < 8 ? UM(u1) : UM(u2))][3]); e[v_ >> 2][v_ & 3] = t_; } }
#define EPI_R(m0, m1) [&](int st) { if (st < 12) { float t_ = e[st >> 2][st & 3]; T1("v_rcp_f32", t_); e[st >> 2][st & 3] = t_; } \
        if (st == 12 && (m0) >= 0) er[(m0) & 3] = RS[wr * 64 + (m0) * 16 + r16]; if (st == 13 && (m1) >= 0) er[(m1) & 3] = RS[wr * 64 + (m1) * 16 + r16]; }
#define VSYNC() SB(); asm volatile("s_waitcnt lgkmcnt(0)\n\ts_barrier" ::: "memory"); SB()
#define P2_A(c, GL, E) k1_p2<1, GL ? 2 : 0, (c), ((c) + 2) % 3, true>(s, f0, f1, acc2, cs2, ones, E)
#define P2_B(c, GL, RD, E) k1_p2<0, GL ? 1 : 0, ((c) + 1) % 3, (c), RD>(s, f1, f0, acc2, cs2, ones, E)
    P2_A(0, true, tabs); wait_vm_bar<5>(); SB();
    er[0] = RS[wr * 64 + r16]; er[4] = RS[wr * 64 + wc * 16 + r16];
#pragma unroll
    for (int n = 0; n < 3; ++n) cs1[n] = *(const f32x4*)(CS + NF1(n) * 16 + 4 * g);
    PRE(0, 0) PRE(1, 1) PRE(2, 8) VSYNC();
    P2_B(0, true, true, EPI_E(0, 1, 8));
    P2_A(1, true, EPI_R(1, 2)); wait_vm_bar<5>(); SB();
    unsigned ev = ld_sc1_async(xe_s, le);
    auto w3ld = [&](int i) { SB(); set_m0(lds0 + K1_X0 + i * 8192 + wid * 1024); glds_s<0>(w3vo[i], w3sp); SB(); };
    w3ld(0); FIN(0, 0, true) FIN(1, 1, false) FIN(2, 8, true) PRE(0, 2) PRE(1, 3) PRE(2, 4) w3ld(1); RED(0) RED(4) VSYNC();
    P2_B(1, true, true, EPI_E(2, 3, 4));
    P2_A(2, true, EPI_R(3, -1)); wait_vm_bar<7>(); SB();
    PIN(ev);
    bool hs = false; u64 epoch = 0;
    auto xcheck = [&]() {
        hs = hs || __all((lane & 16) || ev == nonce);
        const unsigned w0 = __builtin_amdgcn_readlane(ev, 16), w1 = __builtin_amdgcn_readlane(ev, 17), w2 = __builtin_amdgcn_readlane(ev, 18), w3 = __builtin_amdgcn_readlane(ev, 19);
        if (w0 == nonce && w3 == (w0 ^ w1 ^ w2 ^ XSALT)) epoch = ((u64)w2 << 32) | w1;
    };
    unsigned nn = ld_sc1_async(xn_s, l4);
    asm volatile("s_mov_b32 m0, %2\n\ts_nop 0\n\tglobal_load_lds_dword %0, %1" ::"v"(wspo), "s"(w_sp), "s"(lds0 + K1_X0 + 16384));
    SB(); FIN(0, 2, true) FIN(1, 3, false) FIN(2, 4, true) PRE(0, 5) PRE(1, 6) PRE(2, 7) SB();
    asm volatile("s_mov_b32 m0, %2\n\ts_nop 0\n\tglobal_load_lds_dword %0, %1" ::"v"(lane * 4), "s"(b_sp + pm * 128), "s"(lds0 + TOPL + 2304));
    SB(); RED(1) VSYNC();
    P2_B(2, true, true, EPI_E(5, 6, 7));
    P2_A(0, true, EPI_R(-1, -1)); wait_vm_bar<8>(); SB();
    {
        const bf16x8 w0 = ldsB2(w3rd[0], w3rd[0] + 576), w1 = ldsB2(w3rd[1], w3rd[1] + 576);
        SB(); FIN(0, 5, false) FIN(1, 6, true) FIN(2, 7, false) SB();
        st16s<0>(ws + WS_W3T, w3so[0], __builtin_bit_cast(u32x4, w0));
        SB(); RED(2) RED(3) SB();
        st16s<0>(ws + WS_W3T, w3so[1], __builtin_bit_cast(u32x4, w1));
        VSYNC();
    }
    f32x2 pu[5];
    auto pub = [&](int st) {
        if (st == 0) {
#pragma unroll
            for (int i = 0; i < 5; ++i) pu[i] = *(const f32x2*)(red + (wid * 16 + r16) * 10 + 2 * i);
        }
        if (st == 4) {
            float a = fadd1(fadd1(fadd1(lone(pu[0][0]), pu[1][0]), fadd1(lone(pu[2][0]), pu[3][0])), pu[4][0]), b = fadd1(fadd1(fadd1(pu[0][1], pu[1][1]), fadd1(pu[2][1], pu[3][1])), pu[4][1]);
            unsigned bb = __float_as_uint(b); if (bb == 0xffffffffu) bb = 0x7fc00000u;
            st8s<0>((const char*)(XP + j * 128 + wid * 16), r16 * 8, u32x2{__float_as_uint(a), bb});
        }
        if (st == 13 && wid == HW) { xcheck(); *HS = hs; }
    };
    P2_B(0, true, true, pub);
    P2_A(1, true, none);
    STAMP(3, 1);
    wait_vm_bar<8>(); SB();
    ev = ld_sc1_async(xe_s, le);
    auto reecho = [&](int st) { if (st == 2) { PIN(nn); if (wid == HW && nn != echoed) { echoed = nn; if (lane < 16) __hip_atomic_store(XE + lane * 16 + j, nn, RLX_AGENT); } } };
    P2_B(1, true, true, reecho);
    const char* xp_s = ws + WS_ST + (size_t)pm * 16384 + wid * 128; const unsigned xp_v = g * 4096 + r16 * 8;
    u32x2 pv[4];
#define PLOAD() asm volatile("global_load_dwordx2 %0, %4, %5 sc1\n\tglobal_load_dwordx2 %1, %4, %5 offset:1024 sc1\n\tglobal_load_dwordx2 %2, %4, %5 offset:2048 sc1\n\tglobal_load_dwordx2 %3, %4, %5 offset:3072 sc1" \
        : "=&v"(pv[0]), "=&v"(pv[1]), "=&v"(pv[2]), "=&v"(pv[3]) : "v"(xp_v), "s"(xp_s) : "memory")
    auto pload = [&](int st) { if (st >= 10) asm volatile("global_load_dwordx2 %0, %1, %2 offset:%3 sc1" : "=&v"(pv[(st - 10) & 3]) : "v"(xp_v), "s"(xp_s), "n"(((st - 10) & 3) * 1024) : "memory"); };
    P2_A(2, true, pload); wait_vm_bar<9>(); SB();
#define PEMPTY() __any(pv[0][1] == 0xffffffffu || pv[1][1] == 0xffffffffu || pv[2][1] == 0xffffffffu || pv[3][1] == 0xffffffffu)
    const unsigned hsf = *HS;
    P2_B(2, true, true, [&](int st) { if (st == 13) { PIN(ev); if (wid == HW) xcheck(); } });
    float za, zb;
    auto zstat = [&](int st) {
        if (st == ZS0) {
            wait_vm<3>();
            PIN(pv[0]); PIN(pv[1]); PIN(pv[2]); PIN(pv[3]);
            if (__builtin_expect(hsf != 1 || PEMPTY(), 0)) {
                const unsigned long long t0 = __builtin_amdgcn_s_memrealtime();
                if (wid == HW && hsf != 1) {
                    do {
                        ev = ld_sc1_async(xe_s, le); wait_vm<0>(); PIN(ev);
                        xcheck();
                        nn = __hip_atomic_load(XN + (lane & 15), RLX_AGENT);
                        if (nn != echoed) { echoed = nn; if (lane < 16) __hip_atomic_store(XE + lane * 16 + j, nn, RLX_AGENT); }
                    } while (!hs && __builtin_amdgcn_s_memrealtime() - t0 < 5000000ull);
                    *HS = 2;
                }
                while (*HS == 0 && __builtin_amdgcn_s_memrealtime() - t0 < 6000000ull) __builtin_amdgcn_s_sleep(2);
                do {
                    PLOAD(); wait_vm<0>(); PIN(pv[0]); PIN(pv[1]); PIN(pv[2]); PIN(pv[3]);
                } while (PEMPTY() && __builtin_amdgcn_s_memrealtime() - t0 < 6000000ull);
            }
        }
#define UF(x) __uint_as_float(x)
        if (st == ZS0 + 2) {
            za = fadd1(fadd1(lone(UF(pv[0][0])), UF(pv[1][0])), fadd1(lone(UF(pv[2][0])), UF(pv[3][0]))); PIN(za);
            zb = fadd1(fadd1(UF(pv[0][1]), UF(pv[1][1])), fadd1(UF(pv[2][1]), UF(pv[3][1]))); PIN(zb);
        }
        if (st == ZS0 + 4) {
            auto a1 = __builtin_amdgcn_permlane16_swap(__float_as_uint(za), __float_as_uint(za), false, false); za = fadd1(UF(a1[0]), UF(a1[1])); PIN(za);
            auto b1 = __builtin_amdgcn_permlane16_swap(__float_as_uint(zb), __float_as_uint(zb), false, false); zb = fadd1(UF(b1[0]), UF(b1[1])); PIN(zb);
        }
        if (st == ZS0 + 6) {
            auto a2 = __builtin_amdgcn_permlane32_swap(__float_as_uint(za), __float_as_uint(za), false, false); za = fadd1(UF(a2[0]), UF(a2[1])); PIN(za);
            auto b2 = __builtin_amdgcn_permlane32_swap(__float_as_uint(zb), __float_as_uint(zb), false, false); zb = fadd1(UF(b2[0]), UF(b2[1])); PIN(zb);
        }
        if (st == ZS0 + 8) {
            float m = fmul1(za, 1.0f / FH), var = fmaxf(ffma1(-m, m, fmul1(zb, 1.0f / FH)), 0.f), r = __builtin_amdgcn_rsqf(var + 1e-5f); PIN(r);
            ZS[wid * 16 + r16] = f32x2{r, fmul1(-m, r)};
        }
    };
    P2_A(0, true, zstat); wait_vm_bar<5>(); SB();
    auto merge = [&](u32x2 A, u32x2 B) {
        auto r0 = __builtin_amdgcn_permlane16_swap(A[0], B[0], false, false); auto r1 = __builtin_amdgcn_permlane16_swap(A[1], B[1], false, false);
        return u32x4{r0[0], r1[0], r0[1], r1[1]};
    };
    auto pk4 = [&](f32x4 v) { return u32x2{cvtpk(v[0], v[1]), cvtpk(v[2], v[3])}; };
    const char* z2b = ws + WS_Z2T + (size_t)(j * 32 + 2 * pm + wr) * 18432;
    f32x2 zs[5]; u32x2 za2, zb2; unsigned vs, v2;
    auto zn = [&](int q) {
        if (q == 0) {
#pragma unroll
            for (int m = 0; m < 5; ++m) zs[m] = ZS[wr * 64 + (m < 4 ? m : wc) * 16 + r16];
        }
        if (q == 1) { vs = (r16 >> 3) * 2304 + (((r16 & 7) ^ (4 * (r16 >> 3))) * 32); PIN(vs); }
        if (q == 2) { v2 = vs + (g & 1) * 4608 + (g >> 1) * 16; vs += g * 8; PIN(v2); PIN(vs); }
#define NRM(v, m, e) { v[e] = ffma1(v[e], zs[m][0], zs[m][1]); v[(e) + 1] = ffma1(v[(e) + 1], zs[m][0], zs[m][1]); PIN(v); }
        if (q >= 3 && q < 3 + 4 * ZSCH) {
            const int p = (q - 3) / ZSCH, t = (q - 3) % ZSCH, n = p >> 1, m = (p & 1) * 2;
            f32x4& A = acc1[m][n]; f32x4& B = acc1[m + 1][n];
            if (ZSCH == 5) {
                if (t == 0) NRM(A, m, 0)
                if (t == 1) { NRM(A, m, 2) za2[0] = cvtpk(A[0], A[1]); PIN(za2); }
                if (t == 2) { NRM(B, m + 1, 0) za2[1] = cvtpk(A[2], A[3]); PIN(za2); }
                if (t == 3) { NRM(B, m + 1, 2) zb2[0] = cvtpk(B[0], B[1]); PIN(zb2); }
                if (t == 4) { zb2[1] = cvtpk(B[2], B[3]); st16s<0>(z2b + NF1(n) * 256 + m * 4608, v2, merge(za2, zb2)); }
            } else {
                if (t == 0) { NRM(A, m, 0) NRM(A, m, 2) }
                if (t == 1) { NRM(B, m + 1, 0) NRM(B, m + 1, 2) }
                if (t == 2) st16s<0>(z2b + NF1(n) * 256 + m * 4608, v2, merge(pk4(A), pk4(B)));
            }
        }
        if (q == 3 + 4 * ZSCH) { NRM(sacc, 4, 0) NRM(sacc, 4, 2) }
        if (q == 4 + 4 * ZSCH) st8s<2048>(z2b + wc * 4608, vs, pk4(sacc));
    };
    P2_B(0, false, true, zn);
    P2_A(1, false, [&](int st) { zn(14 + st); }); wait_vm_bar<5>(); SB();
    P2_B(1, false, true, [&](int st) { if (st == 0 && W2STR) asm volatile("s_mov_b32 m0, %2\n\ts_nop 0\n\tglobal_load_lds_dword %0, %1 offset:512" ::"v"(wspo + r16 * (W2STR - WSTR)), "s"(w_sp), "s"(lds0 + K1_X0 + 16384)); });
    const bool isq = nf3 >= 10 || j < 4;
    float qn[4], qs[4]; unsigned vq;
    auto qpre = [&](int st) {
        if (st == 7) {
#pragma unroll
            for (int m = 0; m < 4; ++m) er[m] = RS[wr * 64 + m * 16 + r16];
        }
        if (st == 8) { vq = ((g & 1) * 2 + (r16 >> 3)) * 9216 + (g >> 1) * 128 + (r16 & 7) * 16; PIN(vq); }
        if (st >= 10) {
            const int m = (st - 10) & 3;
            qn[m] = fmul1(er[m][0], cg == 3 ? 0.f : -1.f); qs[m] = ffma1(er[m][1], cg == 3 ? 0.f : (isq ? QSCALE / c0 : 1.0f / c0), cg == 3 ? 1.f : 0.f); PIN(qn[m]); PIN(qs[m]);
        }
    };
    auto qfin = [&](int st) {
        if (st >= 6) {
            const int m = (st - 6) >> 1, e_ = ((st - 6) & 1) * 2;
            f32x4& v = acc2[m][0];
            v[e_] = fmul1(ffma1(cs2[0][e_], qn[m], v[e_]), qs[m]); v[e_ + 1] = fmul1(ffma1(cs2[0][e_ + 1], qn[m], v[e_ + 1]), qs[m]); PIN(v);
        }
    };
    P2_A(2, false, qpre);
    P2_B(2, false, false, qfin);
    wait_vm<W2STR ? 1 : 0>();
    if (cg == 3) {
    } else if (isq) {
        const char* qb = ws + WS_Q + (pm * 16 + wr * 8) * 9216 + (nf3 >= 10 ? 4 * j + (nf3 - 10) * 2 : 64 + 2 * j) * 128;
        st16s<0>(qb, vq, merge(pk4(acc2[0][0]), pk4(acc2[1][0]))); st16s<0>(qb + 4 * 9216, vq, merge(pk4(acc2[2][0]), pk4(acc2[3][0])));
    } else if (j < 7) {
#pragma unroll
        for (int m = 0; m < 4; ++m) kv_store(ws + WS_KV + (size_t)(pm * 4 + wr * 2 + (m >> 1)) * 3072, m & 1, j, r16, g, acc2[m][0]);
    }
    SB();
    if (r16 == 0) { *(f32x4*)(CS + 192 + NF2(0) * 16 + 4 * g) = cs2[0]; *(f32x4*)(CS + 192 + NF2(1) * 16 + 4 * g) = cs2[1]; }
    f32x4* HX = (f32x4*)(smem + K1_B0) + wr * 256 + lane;
    if (cg == 3) { HX[0] = acc2[0][0]; HX[64] = acc2[1][0]; HX[128] = acc2[2][0]; HX[192] = acc2[3][0]; }
    if (wid < 4) g2_prep(gp, lds, lane, wid);
    volatile LAS u64* EW = (volatile LAS u64*)(lds + TOPL + 8);
    if (wid == HW) {
        if (__builtin_expect(epoch == 0, 0)) {
            const u64 t0 = __builtin_amdgcn_s_memrealtime();
            do { ev = ld_sc1_async(xe_s, le); wait_vm<0>(); PIN(ev); xcheck(); } while (epoch == 0 && __builtin_amdgcn_s_memrealtime() - t0 < 5000000ull);
        }
        *EW = epoch; EW[-1] = 0; EW[1] = 0; EW[2] = 0;
    }
    VSYNC();
    PIN(cs2[1]);
    if (wid == HW && lane == 0) __hip_atomic_store((gu64*)(ws + WS_FJ) + j * 64 + pm, epoch, RLX_AGENT);
    const u64 e_ = *EW;
    STAMP(3, 2);
    auto gel = [&](f32x4 a, f32x4 c, f32x4 rr) {
        f32x4 y = a - c * rr[0];
        f32x2 y0 = gelu_y2(f32x2{y[0], y[1]}, rr[1], rr[2], rr[3]), y1 = gelu_y2(f32x2{y[2], y[3]}, rr[1], rr[2], rr[3]);
        return u32x2{cvtpk(y0[0], y0[1]), cvtpk(y1[0], y1[1])};
    };
    cs1[1] = cs2[1]; cs1[2] = *(const f32x4*)(CS + 192 + NF2(2) * 16 + 4 * g);
    f32x4 h8 = HX[wc * 64], c8 = *(const f32x4*)(CS + 192 + 128 + 4 * g); er[4] = RS[wr * 64 + wc * 16 + r16];
    u32x4 zw[4];
#pragma unroll
    for (int m = 0; m < ZHALF; ++m) { zw[m] = merge(gel(acc2[m][1], cs1[1], er[m]), gel(acc2[m][2], cs1[2], er[m])); PIN(zw[m]); }
    SB();
    wait_vm_bar<0>();
    STAMP(0, 3);
    if (wid == HW) {
        const bool kvp = j >= 4 && j < 7;
        const unsigned fo = lane == 0 ? (unsigned)(WS_FJ - WS_FA) + (j * 64 + pm) * 8 : (lane == 1 ? (unsigned)(WS_FP - WS_FA) + (pm * 16 + j) * 8 : (lane == 2 ? bid * 8
                            : (unsigned)(WS_FJ - WS_FA) + ((lane & 15) * 64 + 16 + pm * 3 + (j - 4)) * 8));
        if (lane == 1 || lane == 2 || (kvp && g == 1)) __hip_atomic_store((gu64*)(ws + WS_FA + fo), epoch, RLX_AGENT);
    }
    if (wid < 4) {
        const char* sa = (const char*)w_sp - 4096 + pm * 128 * S_ * 2;
#pragma unroll
        for (int t = 0; t < 2; ++t) {
            set_m0(lds0 + wid * 4096 + t * K2_SA);
            glds_s<0>(gp.va[0], sa + t * 128); glds_s<1024>(gp.va[1], sa + t * 128); glds_s<2048>(gp.va[2], sa + t * 128); glds_s<3072>(gp.va[3], sa + t * 128);
        }
    }
    SB();
    epoch = ((u64)(unsigned)__builtin_amdgcn_readfirstlane((unsigned)(e_ >> 32)) << 32) | (unsigned)__builtin_amdgcn_readfirstlane((unsigned)e_);
    volatile LAS unsigned* go = (volatile LAS unsigned*)(lds + TOPL + 20);
    LAS char* zl = (LAS char*)lds + Z1L + wr * 1024 + r16 * 16;
#define ZPUT(m) *(LAS u32x4*)(zl + cg * 8192 + (2 * ((g & 1) ^ wr) + (g >> 1)) * 2048 + (m) * 256) = zw[m]
#pragma unroll
    for (int m = 0; m < ZHALF; ++m) ZPUT(m);
    bool issued = wid >= 3;
#pragma unroll
    for (int m = ZHALF; m < 4; ++m) {
        if (m == ZPOLL && wid == POLLW) { poll_flags(ws, epoch, lane, ws + WS_FJ + j * 512, r16 * 8); *go = 1; }
        if (m >= ZPOLL && !issued && __builtin_amdgcn_readfirstlane(*go)) { first_loads(ws, lds0, lane, wid, j); issued = true; }
        SB();
        zw[m] = merge(gel(acc2[m][1], cs1[1], er[m]), gel(acc2[m][2], cs1[2], er[m])); ZPUT(m);
        SB();
    }
    *(LAS u32x2*)(zl + 32768 + wc * 256 + (g >> 1) * 2048 + (g & 1) * 8) = gel(h8, c8, er[4]);
    if (!issued) {
        while (*go == 0) __builtin_amdgcn_s_sleep(1);
        first_loads(ws, lds0, lane, wid, j);
    }
    return epoch;
}

DI void wait_fa(char* ws, u64 epoch, int lane) {
    const u64 t0 = __builtin_amdgcn_s_memrealtime();
    for (;;) {
        u32x4 a, b;
        asm volatile("global_load_dwordx4 %0, %2, %3 sc1\n\tglobal_load_dwordx4 %1, %2, %3 offset:1024 sc1\n\ts_waitcnt vmcnt(0)" : "=&v"(a), "=&v"(b) : "v"(lane * 16), "s"(ws + WS_FA) : "memory");
        const unsigned lo = (unsigned)epoch, hi = (unsigned)(epoch >> 32);
        if (__all(a[0] == lo && a[1] == hi && a[2] == lo && a[3] == hi && b[0] == lo && b[1] == hi && b[2] == lo && b[3] == hi)) break;
        if (__builtin_amdgcn_s_memrealtime() - t0 > 5000000ull) break;
        if (blockIdx.x == 0) epoch_service(ws, lane, epoch);
        __builtin_amdgcn_s_sleep(8);
    }
}

constexpr float ATT_BIAS = 72.0f;
DI float xhalf_max(float m) { auto rr = __builtin_amdgcn_permlane32_swap(__float_as_uint(m), __float_as_uint(m), false, false); return fmaxf(__uint_as_float(rr[0]), __uint_as_float(rr[1])); }
DI float lo_bcast(float m) { auto rr = __builtin_amdgcn_permlane32_swap(__float_as_uint(m), __float_as_uint(m), false, false); return __uint_as_float(rr[0]); }
DI float bf_round(float x) { return __uint_as_float(cvtpk(x, 0.f) << 16); }
DI float max16(const f32x16& s) {
    float a = fmaxf(fmaxf(s[0], s[1]), s[2]), b = fmaxf(fmaxf(s[3], s[4]), s[5]);
    a = fmaxf(fmaxf(a, s[6]), s[7]); b = fmaxf(fmaxf(b, s[8]), s[9]); a = fmaxf(fmaxf(a, s[10]), s[11]); b = fmaxf(fmaxf(b, s[12]), s[13]);
    a = fmaxf(fmaxf(a, s[14]), s[15]); return fmaxf(a, b);
}
DI void exp_pack(const f32x16& s, bf16x8& p0, bf16x8& p1) {
    float e[16];
#pragma unroll
    for (int i = 0; i < 16; ++i) e[i] = __builtin_amdgcn_exp2f(s[i]);
    u32x4 a = {cvtpk(e[0], e[1]), cvtpk(e[2], e[3]), cvtpk(e[4], e[5]), cvtpk(e[6], e[7])};
    u32x4 b = {cvtpk(e[8], e[9]), cvtpk(e[10], e[11]), cvtpk(e[12], e[13]), cvtpk(e[14], e[15])};
    p0 = __builtin_bit_cast(bf16x8, a); p1 = __builtin_bit_cast(bf16x8, b);
}

struct KVF { bf16x8 k1, k2, v0, v1; };
DI void kv_read(KVF& f, const LAS char* tb, int lane, int k2o, int vo) {
    f.k1 = *(const LAS bf16x8*)(tb + lane * 16); f.k2 = *(const LAS bf16x8*)(tb + k2o);
    f.v0 = *(const LAS bf16x8*)(tb + 1024 + vo); f.v1 = *(const LAS bf16x8*)(tb + 2048 + vo);
}
#define k2frag(x) (x)
struct NoHook { DI void operator()(int) const {} };
template <bool RD, class H = NoHook>
DI void half_step(f32x16& sx, bf16x8& px0, bf16x8& px1, f32x16& sy, const KVF kq, const bf16x8& qy1, const bf16x8& qy2,
                  f32x16& oy, const KVF kv, const bf16x8& py0, const bf16x8& py1, KVF& ld, const LAS char* tb, int lane, int k2o, int vo, H hook = H(), int hs = 0) {
    const f32x16 zero = {};
    unsigned a0, a1, a2, a3, b0, b1, b2, b3; float e0, e1, e2, e3;
    f32x16 t = MFMA32(kq.k1, qy1, zero); SB();
    hook(hs); SB();
    if (RD) ld.k1 = *(const LAS bf16x8*)(tb + lane * 16);
    e0 = EX(sx[0]); e1 = EX(sx[1]); e2 = EX(sx[2]); e3 = EX(sx[3]); PIN(e0); PIN(e1); PIN(e2); PIN(e3); SB();
    sy = MFMA32(kq.k2, qy2, t); SB();
    if (RD) ld.k2 = *(const LAS bf16x8*)(tb + k2o);
    a0 = cvtpk(e0, e1); a1 = cvtpk(e2, e3); PIN(a0); PIN(a1);
    e0 = EX(sx[4]); e1 = EX(sx[5]); e2 = EX(sx[6]); e3 = EX(sx[7]); PIN(e0); PIN(e1); PIN(e2); PIN(e3); SB();
    oy = MFMA32(kv.v0, py0, oy); SB();
    hook(hs + 1); SB();
    if (RD) ld.v0 = *(const LAS bf16x8*)(tb + 1024 + vo);
    a2 = cvtpk(e0, e1); a3 = cvtpk(e2, e3); PIN(a2); PIN(a3);
    e0 = EX(sx[8]); e1 = EX(sx[9]); e2 = EX(sx[10]); e3 = EX(sx[11]); PIN(e0); PIN(e1); PIN(e2); PIN(e3); SB();
    oy = MFMA32(kv.v1, py1, oy); SB();
    if (RD) ld.v1 = *(const LAS bf16x8*)(tb + 2048 + vo);
    b0 = cvtpk(e0, e1); b1 = cvtpk(e2, e3); PIN(b0); PIN(b1);
    e0 = EX(sx[12]); e1 = EX(sx[13]); e2 = EX(sx[14]); e3 = EX(sx[15]);
    b2 = cvtpk(e0, e1); b3 = cvtpk(e2, e3); PIN(b2); PIN(b3); SB();
    u32x4 a = {a0, a1, a2, a3}, b = {b0, b1, b2, b3};
    px0 = __builtin_bit_cast(bf16x8, a); px1 = __builtin_bit_cast(bf16x8, b);
}
DI bf16x8 p_scale(bf16x8 p, int sh) {
    u32x4 w = __builtin_bit_cast(u32x4, p);
#pragma unroll
    for (int i = 0; i < 4; ++i) w[i] = cvtpk(__builtin_amdgcn_ldexpf(bflo(w[i]), sh), __builtin_amdgcn_ldexpf(bfhi(w[i]), sh));
    return __builtin_bit_cast(bf16x8, w);
}
template <bool PP> DI void att_rescale(f32x16& o, f32x16& s, bf16x8& p0, bf16x8& p1, float& mref, bf16x8& q2, bool& bad, int c, int h) {
    bool nf = false;
#pragma unroll
    for (int i = 0; i < 11; ++i) nf = nf || (!(fabsf(o[i]) <= 3.0e38f) && (i < 8 || h == 0));
    if (__any(nf)) { bad = true; return; }
    float lb = lo_bcast(o[10]);
    int e = __builtin_amdgcn_frexp_expf(lb);
    float d = (float)(e + (int)ATT_BIAS); d = d < 0 ? 0 : d;
    float nm = bf_round(mref + d); float dd = nm - mref; mref = nm;
    int sh = -(int)dd;
#pragma unroll
    for (int i = 0; i < 16; ++i) { o[i] = __builtin_amdgcn_ldexpf(o[i], sh); if (!PP) { float b_ = s[i] - dd; PIN(b_); s[i] = b_; } }
    if (PP) { p0 = p_scale(p0, sh); p1 = p_scale(p1, sh); }
    u32x4 b = __builtin_bit_cast(u32x4, q2); b[1] = h ? 0u : (__float_as_uint(-nm) >> 16); q2 = __builtin_bit_cast(bf16x8, b);
}

#define W3LD 1
#define W3ST 3
struct G2F { bf16x8 a[4], b[4], sb; };
struct G2S {
    unsigned va[4], vl, vl2;
    const char* sa; const char* sl; unsigned lstep;
    unsigned m0a, m0l[3];
    const LAS char* pa[2][2]; const LAS char* pb[2]; const LAS char* ps[2];
};
template <int KS, int GL, bool RD, int RS, int WS, class H = NoHook> DI void g2_phase(G2S& s, const G2F& f, G2F& n, f32x4 (&acc)[4][4], f32x4 (&sacc)[2], H hook = H()) {
    const LAS char* a0 = s.pa[0][KS] + RS * K2_SA; const LAS char* a1 = s.pa[1][KS] + RS * K2_SA;
    constexpr int BO = RS * K2_SB + KS * 9216;
    const LAS char* b0 = s.pb[0] + BO; const LAS char* b1 = s.pb[1] + BO;
#define MM(m, nn) acc[m][nn] = MFMA16(f.b[nn], f.a[m], acc[m][nn]); PIN(acc[m][nn]); SB(); hook((nn) * 4 + (m)); SB()
    MM(0, 0); if (RD) n.b[0] = ldsB2(b0, b1); SB();
    MM(1, 0); if (RD) n.a[0] = ldsA(a0); SB();
    MM(2, 0); if (RD) n.a[1] = ldsA(a0 + 2048); SB();
    MM(3, 0); if (RD) n.a[2] = ldsA(a1); SB();
    MM(0, 1); if (RD) n.a[3] = ldsA(a1 + 2048); SB();
    MM(1, 1); if (RD) n.b[1] = ldsB2(b0 + 256, b1 + 256); SB();
    MM(2, 1); if (RD) n.b[2] = ldsB2(b0 + 512, b1 + 512); SB();
    MM(3, 1); if (RD) n.b[3] = ldsB2(b0 + 768, b1 + 768); SB();
    MM(0, 2); if (RD) n.sb = ldsB2(s.ps[0] + BO, s.ps[1] + BO); SB();
    MM(1, 2); if (GL == 1) { set_m0(s.m0a + WS * K2_SA); glds_s<0>(s.va[0], s.sa); } if (GL == 2) { glds_sc1<1024>(s.vl, s.sl); } SB();
    MM(2, 2);
    MM(3, 2); if (GL == 1) glds_s<1024>(s.va[1], s.sa); if (GL == 2) glds_sc1<2048>(s.vl, s.sl); SB();
    MM(0, 3);
    MM(1, 3); if (GL == 1) glds_s<2048>(s.va[2], s.sa); if (GL == 2) glds_sc1<3072>(s.vl, s.sl); SB();
    MM(2, 3);
    MM(3, 3); if (GL == 1) { glds_s<3072>(s.va[3], s.sa); s.sa += 128; asm volatile("" : "+s"(s.sa)); } if (GL == 2) { set_m0(s.m0l[WS] + 4096); glds_sc1<0>(s.vl2, s.sl); } SB();
    sacc[0] = MFMA16(f.sb, f.a[0], sacc[0]); PIN(sacc[0]); SB();
    if (GL == 1) { set_m0(s.m0l[WS]); glds_sc1<0>(s.vl, s.sl); } if (GL == 2) { glds_sc1<1024>(s.vl2, s.sl); s.sl += s.lstep; asm volatile("" : "+s"(s.sl)); } SB();
    sacc[1] = MFMA16(f.sb, f.a[1], sacc[1]); PIN(sacc[1]); SB();
#undef MM
}
template <int C, bool G2nd, bool G1st, int VM, bool RDN> DI void g2_iter(G2S& s, G2F& f0, G2F& f1, f32x4 (&acc)[4][4], f32x4 (&sacc)[2]) {
    g2_phase<1, G2nd ? 2 : 0, true, C, (C + 2) % 3>(s, f0, f1, acc, sacc);
    wait_vm_bar<VM>(); SB();
    g2_phase<0, G1st ? 1 : 0, RDN, (C + 1) % 3, C>(s, f1, f0, acc, sacc);
}

constexpr int K3_S = 4608, K3_RW = 4 * K3_S;
struct B5 { bf16x8 b[5]; };
DI unsigned k3_so(int w, int sl) {
    const int r = sl == 1 ? 2 : sl - 2;
    if (sl == 0) return Z1L + w * K3_S;
    return w < 4 ? K2_B0 + r * K2_SB + w * K3_S : (w < 7 ? r * K2_SA + (w - 4) * K3_S : K2_K0 + r * K2_SK);
}
DI unsigned k3_orow(int r) { return r < 6 ? (r >> 1) * K2_SA + 3 * K3_S + 64 + (r & 1) * 1168 : K2_K0 + (r - 6) * K2_SK + K3_S + 64; }
DI void k3_b1(int p, unsigned m0v, unsigned va, const char* sb) {
    if (p == 0) { set_m0(m0v); glds_sc1<0>(va, sb); }
    if (p == 1) glds_sc1<1024>(va, sb);
    if (p == 2) glds_sc1<2048>(va, sb);
    if (p == 3) glds_sc1<3072>(va, sb);
    if (p == 4) asm volatile("s_mov_b32 m0, %2\n\ts_mov_b32 exec_hi, 0\n\tglobal_load_lds_dwordx4 %0, %1 sc1\n\ts_mov_b32 exec_hi, -1" ::"v"(va), "s"(sb + 4096), "s"(m0v + 4096));
}
DI void k3_a(unsigned m0v, unsigned va, const char* sa) {
    set_m0(m0v); glds_sc1<0>(va, sa); glds_sc1<1024>(va, sa); glds_sc1<2048>(va, sa); glds_sc1<3072>(va, sa);
}
DI void k3_b(unsigned m0v, unsigned va, const char* sb) {
#pragma unroll
    for (int p = 0; p < 5; ++p) k3_b1(p, m0v, va, sb);
}
DI void k3_rdb(B5& r, const LAS char* p, const LAS char* p5) {
#pragma unroll
    for (int n = 0; n < 4; ++n) r.b[n] = ldsA(p + n * 1024);
    r.b[4] = ldsA(p5);
}
DI void k3_lb1(int p, B5& r, unsigned va, unsigned va5, const char* sb) {
    if (p < 4) asm volatile("global_load_dwordx4 %0, %1, %2 offset:%3 sc1" : "+v"(r.b[p & 3]) : "v"(va), "s"(sb), "n"((p & 3) * 1024) : "memory");
    else asm volatile("global_load_dwordx4 %0, %1, %2 sc1" : "+v"(r.b[4]) : "v"(va5), "s"(sb) : "memory");
}
#define LB5 "global_load_dwordx4 %0, %5, %7 sc1\n\tglobal_load_dwordx4 %1, %5, %7 offset:1024 sc1\n\tglobal_load_dwordx4 %2, %5, %7 offset:2048 sc1\n\t" \
            "global_load_dwordx4 %3, %5, %7 offset:3072 sc1\n\tglobal_load_dwordx4 %4, %6, %7 sc1\n"
DI void k3_lb(B5& r, unsigned va, unsigned va5, const char* sb) {
    asm volatile(LB5 : "=&v"(r.b[0]), "=&v"(r.b[1]), "=&v"(r.b[2]), "=&v"(r.b[3]), "=&v"(r.b[4]) : "v"(va), "v"(va5), "s"(sb) : "memory");
}
DI void k3_lb_if(B5& r, unsigned va, unsigned va5, const char* sb, int a, int b) {
    asm volatile("s_cmp_lg_u32 %8, %9\n\ts_cbranch_scc1 .Llb%=\n\t" LB5 ".Llb%=:" : "+v"(r.b[0]), "+v"(r.b[1]), "+v"(r.b[2]), "+v"(r.b[3]), "+v"(r.b[4])
                 : "v"(va), "v"(va5), "s"(sb), "s"(a), "n"(b) : "memory", "scc");
}
DI void pin5(B5& r) { PIN(r.b[0]); PIN(r.b[1]); PIN(r.b[2]); PIN(r.b[3]); PIN(r.b[4]); }
template <int N0, int N1, bool FIRST = false> DI void k3_mm(f32x4 (&acc)[4][5], const bf16x8 (&a)[4], const B5& r) {
#pragma unroll
    for (int n = N0; n < N1; ++n)
#pragma unroll
        for (int m = 0; m < 4; ++m) {
            if (FIRST) asm volatile("v_mfma_f32_16x16x32_bf16 %0, %1, %2, 0" : "=&v"(acc[m][n]) : "v"(r.b[n]), "v"(a[m]));
            else asm volatile("v_mfma_f32_16x16x32_bf16 %0, %1, %2, %0" : "+v"(acc[m][n]) : "v"(r.b[n]), "v"(a[m]));
        }
    SB();
}

#define K3MM(d, b_, a_) asm volatile("v_mfma_f32_16x16x32_bf16 %0, %1, %2, %0" : "+v"(d) : "v"(b_), "v"(a_))
#define ILV 1
struct UA { const char* p[4]; };
template <int NX, bool FIRST = false> DI void k3_step(f32x4 (&acc)[4][5], bf16x8 (&a)[4], const B5& r, unsigned va, const UA& ua) {
#pragma unroll
    for (int m = 0; m < 4; ++m) {
#pragma unroll
        for (int n = 0; n < 5; ++n) {
            if (FIRST) asm volatile("v_mfma_f32_16x16x32_bf16 %0, %1, %2, 0" : "=&v"(acc[m][n]) : "v"(r.b[n]), "v"(a[m]));
            else K3MM(acc[m][n], r.b[n], a[m]);
        }
        if (NX >= 0 && ILV) asm volatile("global_load_dwordx4 %0, %1, %2 sc1" : "+v"(a[m]) : "v"(va), "s"(ua.p[m] + (NX < 0 ? 0 : NX) * 16384) : "memory");
    }
    if (NX >= 0 && !ILV) {
#pragma unroll
        for (int m = 0; m < 4; ++m) asm volatile("global_load_dwordx4 %0, %1, %2 sc1" : "+v"(a[m]) : "v"(va), "s"(ua.p[m] + (NX < 0 ? 0 : NX) * 16384) : "memory");
    }
    SB();
}

DI void phase2(G2P& gp, const u16* __restrict__ w_sp, const u16* __restrict__ b_sp, const u16* __restrict__ x, char* __restrict__ ws, u16* __restrict__ out, const u64 epoch, const int wid) {
    extern __shared__ __attribute__((aligned(16))) char smem[];
    int bid = blockIdx.x, xcd = bid & 7, idx = bid >> 3;
    int pm = (xcd & 1) * 8 + (idx & 7), j = (xcd >> 1) * 4 + (idx >> 3);
    unsigned zz = 0; asm volatile("" : "+s"(zz));
    const int lane = __builtin_amdgcn_mbcnt_hi(~0u, __builtin_amdgcn_mbcnt_lo(~0u, zz)) & 63;
    const int tid = wid * 64 + lane;
    const unsigned lds0 = (unsigned)(uintptr_t)smem; const LAS char* lds = (const LAS char*)smem;
    STAMP(1, 0);
    const int rb = ((xcd & 1) ^ 1) * 16 + (xcd >> 1) * 4 + (idx & 3), j3 = idx >> 2;
    volatile LAS unsigned* SY = (volatile LAS unsigned*)(lds + TOPL);
    gu64* FLr = (gu64*)(ws + WS_FL) + rb * 64;
    const unsigned va = lane * 16;
    unsigned va5 = 4096 + ((lane >> 4) * 8 + (lane & 7)) * 16; PIN(va5);
    B5 rb0, rb1, rb2, rb3;
    const int ks0 = wid < 4 ? wid : wid + 42;
    const char* wb = ws + WS_W3T + (size_t)j3 * 90 * 4608 + ks0 * 4608;
    if (wid >= 4) {
        __builtin_amdgcn_s_setprio(1);
        const unsigned qf = lane < 16 ? (unsigned)(WS_FP - WS_FJ) + pm * 128 + lane * 8 : j * 512 + lane * 8;
        u64 f_;
        asm volatile("s_cmp_lg_u32 %3, 4\n\ts_cbranch_scc1 .Lqf%=\n\tglobal_load_dwordx2 %0, %1, %2 sc1\n.Lqf%=:\n\ts_waitcnt lgkmcnt(0)\n\ts_barrier" : "=&v"(f_) : "v"(qf), "s"(ws + WS_FJ), "s"(wid) : "memory", "scc");
        SB();
        const int c = lane & 31, h = lane >> 5, aw = wid - 4;
        const char* kvg = ws + WS_KV;
        bf16x8 q1[2], q2[2]; float mref[2];
        const f32x16 zero = {};
        f32x16 oA = zero, oB = zero;
        int k2o = 1024 + (c >> 4) * 1024 + (c & 15) * 16;
        int vo = c < 8 ? 256 + (h * 8 + c) * 16 : 512 + (h * 16 + (c < 16 ? c - 8 : (c < 18 ? c - 2 : (c == 18 ? 12 : 13)))) * 16;
        {
            const int qbase = K2_K0 + K2_SK + aw * 3072;
            const LAS char* ql = lds + qbase;
            const char* qs = ws + WS_Q + (pm * 16 + j) * 9216 + aw * 32;
            unsigned vq = (lane >> 1) * 128 + (lane & 1) * 16; PIN(vq);
            SB();
            if (wid == 4) {
                wait_vm<0>(); PIN(f_);
                if (!__all(f_ == epoch)) poll_flags(ws, epoch, lane, ws + WS_FJ, qf);
                SY[6] = 1;
            } else while (SY[6] == 0) __builtin_amdgcn_s_sleep(1);
            set_m0(lds0 + qbase); glds_sc1<0>(vq, qs); glds_sc1<1024>(vq, qs + 3072);
            if (lane < 16) glds_sc1<2048>(vq, qs + 6144);
            SB();
            asm volatile("s_barrier" ::: "memory");
            const LAS char* qo[5];
#pragma unroll
            for (int i = 0; i < 5; ++i) { const int k = 18 * c + (i < 4 ? 8 * h + 2 * i : 16); qo[i] = ql + ((k >> 3) * 32 + (k & 7) * 2); PIN(qo[i]); }
            PIN(oA); PIN(oB); PIN(k2o); PIN(vo);
            SB();
            wait_vm<0>();
#pragma unroll
            for (int t = 0; t < 2; ++t) {
                u32x4 a;
#pragma unroll
                for (int i = 0; i < 4; ++i) a[i] = *(const LAS unsigned*)(qo[i] + t * 16);
                q1[t] = __builtin_bit_cast(bf16x8, a);
                const unsigned e = *(const LAS unsigned*)(qo[4] + t * 16);
                u32x4 b = {h ? 0u : e, 0u, 0u, 0u}; q2[t] = __builtin_bit_cast(bf16x8, b);
                PIN(q1[t]); PIN(q2[t]);
            }
        }
        bool bad = false;
        const LAS char* kvl = lds + K2_K0;
        asm volatile("s_waitcnt lgkmcnt(0)\n\ts_barrier\n\ts_barrier" ::: "memory");
        KVF X, Y;
        kv_read(X, kvl, lane, k2o, vo); kv_read(Y, kvl + 3072, lane, k2o, vo);
#pragma unroll
        for (int t = 0; t < 2; ++t) {
            f32x16 s0 = MFMA32(X.k1, q1[t], zero); s0 = MFMA32(X.k2, q2[t], s0);
            f32x16 s1 = MFMA32(Y.k1, q1[t], zero); s1 = MFMA32(Y.k2, q2[t], s1);
            float m = xhalf_max(fmaxf(max16(s0), max16(s1)));
            mref[t] = bf_round(rintf(m) + ATT_BIAS);
            u32x4 b = __builtin_bit_cast(u32x4, q2[t]); b[1] = h ? 0u : (__float_as_uint(-mref[t]) >> 16); q2[t] = __builtin_bit_cast(bf16x8, b);
        }
        f32x16 sA = MFMA32(X.k1, q1[0], zero); sA = MFMA32(X.k2, q2[0], sA);
        f32x16 sB = zero;
        bf16x8 pA0, pA1, pB0 = {}, pB1 = {};
        half_step<false>(sA, pA0, pA1, sB, X, q1[1], q2[1], oB, X, pB0, pB1, Y, kvl, lane, k2o, vo);
        half_step<false>(sB, pB0, pB1, sA, Y, q1[0], q2[0], oA, X, pA0, pA1, Y, kvl, lane, k2o, vo);
        asm volatile("s_waitcnt lgkmcnt(0)\n\ts_barrier" ::: "memory");
#define A_IV(sl, CHK, H, hs) { const LAS char* tb = (sl) < 3 ? kvl + (sl) * K2_SK : lds + 2 * K2_SA + ((sl) - 3) * 6144; \
            half_step<true>(sA, pA0, pA1, sB, Y, q1[1], q2[1], oB, X, pB0, pB1, X, tb, lane, k2o, vo, H, hs); \
            half_step<false>(sB, pB0, pB1, sA, X, q1[0], q2[0], oA, Y, pA0, pA1, X, tb, lane, k2o, vo, H, hs + 2); \
            if (CHK) { mB = __builtin_amdgcn_ballot_w64(!(oB[10] <= 0x1p20f)); }                  \
            half_step<true>(sA, pA0, pA1, sB, X, q1[1], q2[1], oB, Y, pB0, pB1, Y, tb + 3072, lane, k2o, vo, H, hs + 4); \
            if (CHK) { mA = __builtin_amdgcn_ballot_w64(!(oA[10] <= 0x1p20f)); any = mA | mB; asm volatile("" : "+s"(any)); } \
            half_step<false>(sB, pB0, pB1, sA, Y, q1[0], q2[0], oA, X, pA0, pA1, Y, tb, lane, k2o, vo, H, hs + 6); }
#define A_BAR() asm volatile("s_waitcnt lgkmcnt(0)\n\ts_barrier" ::: "memory")
        unsigned long long mA, mB, any;
#define UNDEF5(r) asm volatile("" : "=v"(r.b[0]), "=v"(r.b[1]), "=v"(r.b[2]), "=v"(r.b[3]), "=v"(r.b[4]))
        u32x4 fa0, fa1;
        asm volatile("global_load_dwordx4 %0, %2, %3 sc1\n\tglobal_load_dwordx4 %1, %2, %3 offset:1024 sc1" : "=&v"(fa0), "=&v"(fa1) : "v"(va), "s"(ws + WS_FA) : "memory");
        const NoHook nh;
        const char* pf = ws + WS_W3T + (size_t)j3 * 90 * 4608 + 16 * 4608 + min(((idx & 3) * 4 + aw) * 192, 2664 - 192) * 128;
        auto wl = [&](int i) {
            if (i < 20) k3_lb1(i % 5, i < 5 ? rb0 : (i < 10 ? rb1 : (i < 15 ? rb2 : rb3)), va, va5, wb + (i / 5) * 18432);
            else if (i < 23 && PFW)
                asm volatile("s_mov_b32 m0, %2\n\ts_nop 0\n\tglobal_load_lds_dword %0, %1 sc1" ::"v"(va * 8), "s"(pf + (i - 20) * 8192), "s"(lds0 + TOPL + 64 + wid * 256));
        };
#define A_RS() if (__builtin_expect(any != 0, 0)) { \
                if (mA) att_rescale<false>(oA, sA, pA0, pA1, mref[0], q2[0], bad, c, h); \
                if (mB) att_rescale<true>(oB, sB, pB0, pB1, mref[1], q2[1], bad, c, h); \
            }
#define A_IT(H) A_IV(1, false, H, 0); A_BAR(); A_IV(2, false, H, 8); A_BAR(); A_IV(0, true, H, 16); A_RS() A_BAR()
#pragma unroll 1
        for (int t = 0; t < TFA; ++t) { A_IT(nh); }
        {
            wait_vm<0>(); PIN(fa0); PIN(fa1);
            const unsigned lo = (unsigned)epoch, hi = (unsigned)(epoch >> 32);
            if (__builtin_expect(!__all(fa0[0] == lo && fa0[1] == hi && fa0[2] == lo && fa0[3] == hi && fa1[0] == lo && fa1[1] == hi && fa1[2] == lo && fa1[3] == hi), 0)) wait_fa(ws, epoch, lane);
            UNDEF5(rb0); UNDEF5(rb1); UNDEF5(rb2); UNDEF5(rb3);
            SB();
            A_IT(wl);
        }
#pragma unroll 1
        for (int t = TFA + 1; t < 9; ++t) { A_IT(nh); }
        A_IV(1, false, nh, 0); A_BAR();
        wait_vm<0>(); pin5(rb0); pin5(rb1); pin5(rb2); pin5(rb3);
        auto w9 = [&](int i) { if (i < 5) k3_b1(i, lds0 + k3_so(wid, 3), va, wb + 9 * 18432); };
        A_IV(2, false, nh, 0); A_IV(3, true, w9, 0); A_RS() A_IV(4, false, nh, 0);
        half_step<false>(sA, pA0, pA1, sB, Y, q1[1], q2[1], oB, X, pB0, pB1, X, kvl, lane, k2o, vo);
        exp_pack(sB, pB0, pB1);
        oA = MFMA32(Y.v0, pA0, oA); oA = MFMA32(Y.v1, pA1, oA);
        oB = MFMA32(Y.v0, pB0, oB); oB = MFMA32(Y.v1, pB1, oB);
        {
            float sa_ = fabsf(oA[10]) * 0x1p27f, sb_ = fabsf(oB[10]) * 0x1p27f;
#pragma unroll
            for (int i = 0; i < 10; ++i) { sa_ += fabsf(oA[i]); PIN(sa_); sb_ += fabsf(oB[i]); PIN(sb_); }
            if (__builtin_amdgcn_ballot_w64(!(sa_ + sb_ <= 3.0e38f))) bad = true;
        }
        f32x16 o[2] = {oA, oB};
        if (__builtin_expect(bad, 0)) {
#pragma unroll 1
            for (int t = 0; t < 2; ++t) {
                bf16x8 qa = t ? q1[1] : q1[0]; u32x4 qb = __builtin_bit_cast(u32x4, t ? q2[1] : q2[0]); qb[1] = 0;
                f32x16 oo = zero; float m = -3.0e38f;
                for (int kt = 0; kt < 64; ++kt) {
                    const char* tb = kvg + (size_t)kt * 3072;
                    bf16x8 k1, v0, v1; u32x4 k2v;
                    asm volatile("global_load_dwordx4 %0, %4, %7 sc1\n\tglobal_load_dwordx4 %1, %5, %7 sc1\n\tglobal_load_dwordx4 %2, %6, %7 offset:1024 sc1\n\t"
                                 "global_load_dwordx4 %3, %6, %7 offset:2048 sc1\n\ts_waitcnt vmcnt(0)"
                                 : "=&v"(k1), "=&v"(k2v), "=&v"(v0), "=&v"(v1) : "v"(lane * 16), "v"(k2o), "v"(vo), "s"(tb) : "memory");
                    f32x16 s = MFMA32(k1, qa, zero); s = MFMA32(__builtin_bit_cast(bf16x8, k2v), __builtin_bit_cast(bf16x8, qb), s);
                    float nm = fmaxf(m, xhalf_max(max16(s)));
                    float sc = __builtin_amdgcn_exp2f(m - nm); m = nm;
#pragma unroll
                    for (int i = 0; i < 16; ++i) { float a_ = oo[i] * sc, b_ = s[i] - nm; PIN(a_); PIN(b_); oo[i] = a_; s[i] = b_; }
                    bf16x8 p0, p1; exp_pack(s, p0, p1);
                    oo = MFMA32(v0, p0, oo); oo = MFMA32(v1, p1, oo);
                }
                if (t) o[1] = oo; else o[0] = oo;
            }
        }
#pragma unroll
        for (int t = 0; t < 2; ++t) {
            float inv = __builtin_amdgcn_rcpf(lo_bcast(o[t][10])); PIN(inv);
            LAS unsigned* ob = (LAS unsigned*)(lds + k3_orow(aw * 2 + t) + 36 * c + 8 * h);
#define ON(i) cvtpk(fmul1(o[t][i], inv), fmul1(o[t][(i) + 1], inv))
            ob[0] = ON(0); ob[1] = ON(2); ob[4] = ON(4); ob[5] = ON(6);
            if (h == 0) ob[8] = ON(8);
        }
        asm volatile("s_waitcnt lgkmcnt(0)" ::: "memory");
        if (lane == 0) __hip_atomic_fetch_add((LAS unsigned*)SY, 1u, __ATOMIC_RELAXED, __HIP_MEMORY_SCOPE_WORKGROUP);
        __builtin_amdgcn_s_setprio(APRIO);
    } else {
        const int wr = wid >> 1, wc = wid & 1;
        G2S s;
        s.vl = lane * 16; s.vl2 = lane * 16 + 4096;
        s.lstep = wid < 3 ? 18432 : 6144;
        unsigned ol = (wid < 3 ? (unsigned)WS_Z2T + j * 32 * 18432 + wid * 6144 : (unsigned)WS_KV - 3 * 6144), oa = pm * 128 * S_ * 2;
        s.m0a = lds0 + wid * 4096; s.m0l[0] = lds0 + (wid < 3 ? K2_B0 + wid * 6144 : K2_K0); s.m0l[1] = s.m0l[0] + s.lstep; s.m0l[2] = s.m0l[1] + s.lstep;
        PIN(s.vl); PIN(s.vl2);
        s.sl = ws + ol + 2 * s.lstep;
        STAMPT(0, 3, 3);
#pragma unroll
        for (int i = 0; i < 4; ++i) { s.va[i] = gp.va[i]; s.pa[i >> 1][i & 1] = gp.pa[i >> 1][i & 1]; }
        s.pb[0] = gp.pb[0]; s.pb[1] = gp.pb[1]; s.ps[0] = gp.ps[0]; s.ps[1] = gp.ps[1];
        s.sa = (const char*)w_sp - 4096 + oa + 256;
        G2F f0, f1;
        f32x4 (&acc)[4][4] = gp.acc; f32x4 (&sacc)[2] = gp.sacc;
        SB();
        if (wid == 3) wait_vm<0>();
        wait_vm_bar<6>();
        STAMP(1, 1);
        const unsigned m0l2 = s.m0l[2];
        if (wid == 3) s.m0l[2] = s.m0l[0];
        f0.b[0] = ldsB2(s.pb[0], s.pb[1]);
        f0.a[0] = ldsA(s.pa[0][0]); f0.a[1] = ldsA(s.pa[0][0] + 2048); f0.a[2] = ldsA(s.pa[1][0]); f0.a[3] = ldsA(s.pa[1][0] + 2048);
        f0.b[1] = ldsB2(s.pb[0] + 256, s.pb[1] + 256); f0.b[2] = ldsB2(s.pb[0] + 512, s.pb[1] + 512); f0.b[3] = ldsB2(s.pb[0] + 768, s.pb[1] + 768); f0.sb = ldsB2(s.ps[0], s.ps[1]);
        SB();
        set_m0(s.m0a + 2 * K2_SA); glds_s<0>(s.va[0], s.sa); glds_s<1024>(s.va[1], s.sa); glds_s<2048>(s.va[2], s.sa); glds_s<3072>(s.va[3], s.sa); s.sa += 128;
        set_m0(s.m0l[2]); glds_sc1<0>(s.vl, s.sl);
        SB();
#pragma unroll 1
        for (int t = 0; t < 9; ++t) {
            g2_iter<0, true, true, 10, true>(s, f0, f1, acc, sacc); s.m0l[2] = m0l2; g2_iter<1, true, true, 10, true>(s, f0, f1, acc, sacc); g2_iter<2, true, true, 10, true>(s, f0, f1, acc, sacc);
        }
        asm volatile("" : "=v"(rb0.b[0]), "=v"(rb0.b[1]), "=v"(rb0.b[2]), "=v"(rb0.b[3]), "=v"(rb0.b[4]), "=v"(rb1.b[0]), "=v"(rb1.b[1]), "=v"(rb1.b[2]), "=v"(rb1.b[3]), "=v"(rb1.b[4]));
        g2_phase<1, 2, true, 0, 2>(s, f0, f1, acc, sacc);
        wait_vm_bar<10>(); SB();
        g2_phase<0, 1, true, 1, 0>(s, f1, f0, acc, sacc, [&](int st) { if (st < 10 && !(st & 1)) k3_lb1(st >> 1, rb0, va, va5, wb); });
        g2_phase<1, 2, true, 1, 0>(s, f0, f1, acc, sacc);
        wait_vm_bar<15>(); SB();
        g2_phase<0, 1, true, 2, 1>(s, f1, f0, acc, sacc, [&](int st) { if (st < 10 && !(st & 1)) k3_lb1(st >> 1, rb1, va, va5, wb + 18432); });
        const int c = lane & 15, g = lane >> 4;
        const LAS char* z1b = lds + Z1L + (g >> 1) * 2048 + (wr * 64 + c) * 16 + (g & 1) * 8;
        u32x2 z1r[4][4], z1s[2];
#pragma unroll
        for (int m = 0; m < 4; ++m) {
            const int mo = (m ^ (2 * wc)) * 16;
#pragma unroll
            for (int n = 0; n < 4; ++n) z1r[m][n] = *(const LAS u32x2*)(z1b + (wc * 4 + n) * 4096 + mo * 16);
            if (m < 2) z1s[m] = *(const LAS u32x2*)(z1b + 8 * 4096 + mo * 16);
        }
        SB();
#define PREB(i) [&](int st) { if (st < 5) k3_b1(st, lds0 + k3_so(wid, i), va, wb + ((i) + 6) * 18432); else if (st < 10) k3_b1(st - 5, lds0 + k3_so(wid + 4, i), va, wb + ((i) + 6) * 18432 + 46 * 4608); }
        g2_phase<1, 2, true, 2, 1>(s, f0, f1, acc, sacc);
        wait_vm_bar<15>(); SB();
        {
            const int p = wid < 3 ? wid : 2;
            const char* ks = ws + WS_KV + (29 + p) * 6144; const unsigned km = lds0 + (p ? 2 * K2_SA + (p - 1) * 6144 : K2_K0 + 2 * K2_SK);
            g2_phase<0, 0, true, 0, 2>(s, f1, f0, acc, sacc, [&](int st) {
                if (st < 4) k3_b1(st, km, va, ks); else if (st == 4) { set_m0(km + 4096); glds_sc1<0>(s.vl2, ks); } else if (st == 5) glds_sc1<1024>(s.vl2, ks);
                else if (st < 11) k3_b1(st - 6, lds0 + k3_so(wid, 1), va, wb + 7 * 18432); });
        }
        g2_phase<1, 0, true, 0, 2>(s, f0, f1, acc, sacc, PREB(0));
        wait_vm_bar<21>(); SB();
        g2_phase<0, 0, true, 1, 0>(s, f1, f0, acc, sacc, PREB(2));
        unsigned vu[3];
#pragma unroll
        for (int i = 0; i < 3; ++i) {
            const int cgl = 18 * j + (i < 2 ? 2 * (wc * 4 + 2 * i + (g & 1)) : 16) + (g >> 1);
            vu[i] = (cgl >> 2) * 4096 + (cgl & 3) * 256 + c * 16 + (i == 2 ? (g & 1) * 1024 : 0); PIN(vu[i]);
        }
        const char* ub0 = ws + WS_UO + (size_t)(pm * 2 + wr) * 90 * 4096 + wc * 2048; const char* ub1 = ub0 + 2048 - wc * 4096;
        SB();
        g2_phase<1, 0, true, 1, 0>(s, f0, f1, acc, sacc);
        wait_vm_bar<0>(); SB();
        __builtin_amdgcn_s_setprio(2);
        auto fin = [&](f32x4 v, u32x2 z) { return u32x2{cvtpk(fmul1(bflo(z[0]), v[0]), fmul1(bfhi(z[0]), v[1])), cvtpk(fmul1(bflo(z[1]), v[2]), fmul1(bfhi(z[1]), v[3]))}; };
        auto mrg = [&](u32x2 A, u32x2 B) {
            auto r0 = __builtin_amdgcn_permlane16_swap(A[0], B[0], false, false); auto r1 = __builtin_amdgcn_permlane16_swap(A[1], B[1], false, false);
            return u32x4{r0[0], r1[0], r0[1], r1[1]};
        };
#define M2(m, n) acc[m][n] = MFMA16(f1.b[n], f1.a[m], acc[m][n]); acc[m][(n) + 1] = MFMA16(f1.b[(n) + 1], f1.a[m], acc[m][(n) + 1]); PIN(acc[m][n]); PIN(acc[m][(n) + 1]); SB()
#define PUT(m, n) st16s<((m) & 1) * 1024>((m) < 2 ? ub0 : ub1, vu[(n) >> 1], mrg(fin(acc[m][n], z1r[m][n]), fin(acc[m][(n) + 1], z1r[m][(n) + 1]))); SB()
        M2(0, 0); M2(1, 0); M2(2, 0); PUT(0, 0);
        M2(3, 0); PUT(1, 0);
        sacc[0] = MFMA16(f1.sb, f1.a[0], sacc[0]); sacc[1] = MFMA16(f1.sb, f1.a[1], sacc[1]); PIN(sacc[0]); PIN(sacc[1]); SB(); PUT(2, 0);
        M2(0, 2); PUT(3, 0);
        M2(1, 2); st16s<0>(ub0, vu[2], mrg(fin(sacc[0], z1s[0]), fin(sacc[1], z1s[1]))); SB();
        M2(2, 2); PUT(0, 2);
        M2(3, 2); PUT(1, 2);
        STAMP(1, 2);
        PUT(2, 2); PUT(3, 2);
        SB(); k3_b(lds0 + k3_so(wid, 3), va, wb + 9 * 18432);
        wait_vm<5>();
        if (lane == 0) __hip_atomic_store((gu64*)(ws + WS_FL) + (pm * 2 + wr) * 64 + j * 2 + wc, epoch, RLX_AGENT);
        pin5(rb0); pin5(rb1);
        asm volatile("" : "=v"(rb2.b[0]), "=v"(rb2.b[1]), "=v"(rb2.b[2]), "=v"(rb2.b[3]), "=v"(rb2.b[4]), "=v"(rb3.b[0]), "=v"(rb3.b[1]), "=v"(rb3.b[2]), "=v"(rb3.b[3]), "=v"(rb3.b[4]));
        STAMP(1, 3);
        __builtin_amdgcn_s_setprio(GPRIO);
    }
    {
#define TK()
#define TP()
        TP();
        UA ua;
#pragma unroll
        for (int i = 0; i < 4; ++i) ua.p[i] = ws + WS_UO + (size_t)rb * 90 * 4096 + ks0 * 4096 + ((wid + 1 + i) & 3) * 1024;
        auto relane = [&]() { unsigned t_ = va; PIN(t_); return (int)(t_ >> 4); };
        unsigned so[4];
#pragma unroll
        for (int i = 0; i < 4; ++i) so[i] = __builtin_amdgcn_readfirstlane(k3_so(wid, i));
#define LG0() asm volatile("s_waitcnt lgkmcnt(0)" ::: "memory"); SB()
#define LB(d, i) k3_lb(d, va, va5, wb + (i) * 18432)
#define RB(d, sl) { asm volatile("" : "+s"(so[sl])); k3_rdb(d, lds + va + so[sl], lds + va5 + so[sl]); }
        B5 rb4, rb5; bf16x8 a0[4], a1[4], a2[4], a3[4], a4[4];
        f32x4 ac[4][5];
        const size_t xo = (size_t)(rb * 64 + (wid & 3) * 16) * D_ + 72 * j3;
        bool opub = true;
#define OPUB() if (relane() == 0) __hip_atomic_store((gu64*)(ws + WS_FL) + (pm * 2 + (j >> 3)) * 64 + 32 + (j & 7) * 4 + (wid - 4), epoch, RLX_AGENT)
        UNDEF5(rb4); UNDEF5(rb5);
        if (wid < 4) {
            k3_lb_if(rb2, va, va5, wb + 2 * 18432, 0, 0); k3_lb_if(rb3, va, va5, wb + 3 * 18432, 0, 0); TP();
            k3_lb_if(rb4, va, va5, wb + 4 * 18432, 0, 0); k3_lb_if(rb5, va, va5, wb + 5 * 18432, 0, 0); TP();
        } else {
            const int orow = pm * 128 + j * 8, ck = (wid - 4) * 18 + (lane >> 3);
            const char* ob = ws + WS_UO + ((size_t)((orow >> 6) * 90 + 72) * 4 + ((orow >> 4) & 3)) * 1024 + (orow & 15) * 16;
            unsigned vo_ = (ck >> 2) * 4096 + (ck & 3) * 256 + (lane & 7) * 16; PIN(vo_);
            const LAS char* ol = lds + (k3_orow(lane & 7) + ck * 16); PIN(ol);
            SB();
            while (SY[0] < 4) __builtin_amdgcn_s_sleep(1);
            TP();
            const u32x4 o0 = *(const LAS u32x4*)ol, o1 = *(const LAS u32x4*)(ol + 128), o2 = *(const LAS u32x4*)(ol + 256);
            opub = __builtin_amdgcn_readfirstlane(SY[1]) == 0;
            st16s<0>(ob, vo_, o0); st16s<0>(ob + 8192, vo_, o1);
            if (lane < 16) st16s<0>(ob + 16384, vo_, o2);
            TP();
            if (opub) { wait_vm<0>(); OPUB(); }
        }
        SB();
        TP();
        if (wid == PW2) {
            const unsigned long long t0 = __builtin_amdgcn_s_memrealtime();
            u64 f = __hip_atomic_load(FLr + (lane & 31), RLX_AGENT);
            while (!__all(f == epoch) && __builtin_amdgcn_s_memrealtime() - t0 < 5000000ull) {
                __builtin_amdgcn_s_sleep(POLLSLEEP);
                f = __hip_atomic_load(FLr + (lane & 31), RLX_AGENT);
            }
            SY[1] = 1;
        }
        while (SY[1] == 0) __builtin_amdgcn_s_sleep(1);
        TP();
        STAMP(2, 0); STAMP(2, 1);
#define LA4 "global_load_dwordx4 %0, %4, %5 sc1\n\tglobal_load_dwordx4 %1, %4, %6 sc1\n\tglobal_load_dwordx4 %2, %4, %7 sc1\n\tglobal_load_dwordx4 %3, %4, %8 sc1\n"
#define LAO(d, o_) : "=&v"(d[0]), "=&v"(d[1]), "=&v"(d[2]), "=&v"(d[3]) : "v"(va), "s"(ua.p[0] + (o_)), "s"(ua.p[1] + (o_)), "s"(ua.p[2] + (o_)), "s"(ua.p[3] + (o_))
#define LA(d, i) asm volatile(LA4 LAO(d, (i) * 16384) : "memory")
#define PIN4(d) PIN(d[0]); PIN(d[1]); PIN(d[2]); PIN(d[3])
#define GO(d, rb_, W, NX, ...) wait_vm<W>(); TK(); PIN4(d); SB(); k3_step<NX, ##__VA_ARGS__>(ac, d, rb_, va, ua)
        TK();
        LA(a0, 0); LA(a1, 1);
        const bool ext = wid < 2;
#define GO2(d, rb_, WE, WL, NX, ...) if (wid < 4) wait_vm<WE>(); GO(d, rb_, WL, NX, ##__VA_ARGS__)
        wait_vm<4>();
        if (!opub) OPUB();
        GO(a0, rb0, 5, 2, true); k3_lb_if(rb4, va, va5, wb + 4 * 18432, wid >> 2, 1);
        pin5(rb2); pin5(rb3);
        GO2(a1, rb1, 4, 9, 3); k3_lb_if(rb5, va, va5, wb + 5 * 18432, wid >> 2, 1);
        GO2(a0, rb2, 4, 14, 4);
        if (wid >= 4) {
            asm volatile("s_mov_b32 m0, %2\n\ts_nop 0\n\tglobal_load_lds_dword %0, %1 sc1" ::"v"(relane() * 4), "s"(FLr + 32), "s"(lds0 + TOPL + 64 + wid * 256));
            k3_b(lds0 + so[1], va, wb + 7 * 18432);
        }
        GO2(a1, rb3, 4, 15, 5); LB(rb0, 10);
        if (wid >= 4) {
            wait_vm<14>();
            const int l2 = relane();
            const unsigned fo = *(const volatile LAS unsigned*)(lds + TOPL + 64 + wid * 256 + l2 * 4);
            if (__builtin_expect(!__all(fo == ((l2 & 1) ? (unsigned)(epoch >> 32) : (unsigned)epoch)), 0)) {
                const unsigned long long t0 = __builtin_amdgcn_s_memrealtime();
                u64 f2; do { f2 = __hip_atomic_load(FLr + 32 + (l2 & 31), RLX_AGENT); } while (!__all(f2 == epoch) && __builtin_amdgcn_s_memrealtime() - t0 < 5000000ull);
            }
        }
        if (wid < 4) wait_vm<9>();
        pin5(rb4); pin5(rb5);
        GO(a0, rb4, 14, 6); k3_lb_if(rb1, va, va5, wb + 11 * 18432, wid >> 1, 0); RB(rb2, 0); SB();
        GO(a1, rb5, 9, 7); LA(a2, 8); RB(rb3, 1); SB();
        GO(a0, rb2, 8, 9); LA(a3, 10); RB(rb4, 2); SB();
        asm volatile("s_cmp_lg_u32 %9, 0\n\ts_cbranch_scc1 .Lla%=\n\t" LA4 ".Lla%=:" LAO(a4, 11 * 16384), "s"(wid >> 1) : "memory", "scc");
        GO(a1, rb3, 12, -1); RB(rb5, 3); SB();
        wait_vm<0>(); TK(); PIN4(a0); PIN4(a2); PIN4(a3); PIN4(a4); pin5(rb0); pin5(rb1);
        if (ext) k3_step<-1>(ac, a4, rb1, va, ua);
        const int l3 = relane(), r16 = l3 & 15, g = l3 >> 4;
        LAS char* pw = (LAS char*)lds + va;
        u32x2 xv[5];
        if (wid < 4) {
#pragma unroll
            for (int n = 0; n < 5; ++n) xv[n] = *(const u32x2*)(x + xo + r16 * D_ + n * 16 + (n < 4 ? g : (g & 1)) * 4);
        }
        SB();
#pragma unroll
        for (int m = 0; m < 4; ++m)
#pragma unroll
            for (int q = 0; q < 15; ++q) {
                if (q < 5) K3MM(ac[m][q], rb4.b[q], a2[m]); else if (q < 10) K3MM(ac[m][q - 5], rb0.b[q - 5], a3[m]); else K3MM(ac[m][q - 10], rb5.b[q - 10], a0[m]);
                SB();
                if (m > 0 && q % 3 == 0) { const int n = q / 3; if (n < 4 || g < 2) *(LAS f32x4*)(pw + so[m - 1] + n * 1024) = ac[m - 1][n]; SB(); }
            }
        asm volatile("s_nop 7\n\ts_nop 7\n\ts_waitcnt lgkmcnt(0)" ::: "memory");
        TK();
        STAMP(2, 2);
        const LAS char* pr = lds + va;
#define PART(k, n) *(const LAS f32x4*)(pr + k3_so((wid & 4) + ((wid + 3 - (k)) & 3), k) + (n) * 1024)
        if (wid < 4) {
            __builtin_amdgcn_s_setprio(EPRIO);
            if (l3 == 0) __hip_atomic_fetch_add((LAS unsigned*)SY + 4, 1u, __ATOMIC_RELAXED, __HIP_MEMORY_SCOPE_WORKGROUP);
#pragma unroll
            for (int n = 0; n < 5; ++n) {
                ac[3][n] = f32x4{fadd1(ac[3][n][0], bflo(xv[n][0])), fadd1(ac[3][n][1], bfhi(xv[n][0])), fadd1(ac[3][n][2], bflo(xv[n][1])), fadd1(ac[3][n][3], bfhi(xv[n][1]))}; PIN(ac[3][n]);
            }
            while (SY[4] < 4) __builtin_amdgcn_s_sleep(1);
            SB();
#pragma unroll
            for (int n = 0; n < 5; ++n) {
                const f32x4 p0 = PART(0, n), p1 = PART(1, n), p2 = PART(2, n);
#pragma unroll
                for (int e = 0; e < 4; ++e) ac[3][n][e] = fadd1(fadd1(ac[3][n][e], p0[e]), fadd1(p1[e], p2[e]));
            }
#pragma unroll
            for (int n = 0; n < 5; ++n) if (n < 4 || g < 2) *(LAS f32x4*)(pw + so[3] + n * 1024) = ac[3][n];
        }
        SB();
        asm volatile("s_waitcnt lgkmcnt(0)" ::: "memory"); TK();
        asm volatile("s_barrier" ::: "memory"); TK();
        if (wid >= 4) {
            u32x2 o[5];
#pragma unroll
            for (int n = 0; n < 5; ++n) {
                const f32x4 r = ((ac[3][n] + PART(0, n)) + (PART(1, n) + PART(2, n))) + *(const LAS f32x4*)(pr + k3_so(wid - 4, 3) + n * 1024);
                o[n] = u32x2{cvtpk(r[0], r[1]), cvtpk(r[2], r[3])};
            }
            u16* op = out + xo + r16 * D_;
#pragma unroll
            for (int n = 0; n < 4; n += 2) {
                auto r0 = __builtin_amdgcn_permlane16_swap(o[n][0], o[n + 1][0], false, false); auto r1 = __builtin_amdgcn_permlane16_swap(o[n][1], o[n + 1][1], false, false);
                *(u32x4*)(op + (n + (g & 1)) * 16 + (g >> 1) * 8) = u32x4{r0[0], r1[0], r0[1], r1[1]};
            }
            if (g < 2) *(u32x2*)(op + 64 + g * 4) = o[4];
        }
    }
    STAMP_DRAIN(); STAMP(2, 3);
}
__global__ __launch_bounds__(512, 2) void fused_kernel(const u16* __restrict__ x, const u16* __restrict__ wq, const u16* __restrict__ wk, const u16* __restrict__ wv,
                                                       const u16* __restrict__ w_in, const u16* __restrict__ w_sp, const u16* __restrict__ b_sp, const u16* __restrict__ w_out,
                                                       const u16* __restrict__ wo, char* __restrict__ ws, u16* __restrict__ out) {
    extern __shared__ __attribute__((aligned(16))) char smem[];
    if (threadIdx.x == 0) { const u64 t_ = __builtin_amdgcn_s_memrealtime(); *(volatile LAS u64*)((LAS char*)smem + TOPL + 2560) = t_; }
    G2P gp;
    const u64 epoch = phase1(gp, x, wq, wk, wv, w_in, w_sp, b_sp, w_out, wo, ws);
    phase2(gp, w_sp, b_sp, x, ws, out, epoch, __builtin_amdgcn_readfirstlane(threadIdx.x >> 6));
    if (blockIdx.x == DBID && __builtin_amdgcn_mbcnt_hi(~0u, __builtin_amdgcn_mbcnt_lo(~0u, 0)) == 0) {
        const u64 t0 = __builtin_amdgcn_s_memrealtime();
        const u64 d = t0 - *(volatile LAS u64*)((LAS char*)smem + TOPL + 2560), w = (d < 20000 ? d : 20000) * DMUL + 300;
        while (__builtin_amdgcn_s_memrealtime() - t0 < w) __builtin_amdgcn_s_sleep(1);
    }
}
__global__ void text_end_marker() { asm volatile(".Lk3_end:"); }

extern "C" void kernel_launch(void* const* d_in, const int* in_sizes, int n_in, void* d_out, int out_size, void* d_ws, size_t ws_size, hipStream_t stream) {
    const u16* x = (const u16*)d_in[0]; const u16* wq = (const u16*)d_in[1]; const u16* wk = (const u16*)d_in[2]; const u16* wv = (const u16*)d_in[3];
    const u16* wo = (const u16*)d_in[4]; const u16* w_in = (const u16*)d_in[6]; const u16* w_sp = (const u16*)d_in[7]; const u16* b_sp = (const u16*)d_in[8];
    const u16* w_out = (const u16*)d_in[9]; char* ws = (char*)d_ws;
    (void)hipFuncSetAttribute((const void*)fused_kernel, hipFuncAttributeMaxDynamicSharedMemorySize, LDS_ALL);
    fused_kernel<<<256, 512, LDS_ALL, stream>>>(x, wq, wk, wv, w_in, w_sp, b_sp, w_out, wo, ws, (u16*)d_out);
}
```

```cpp
#include <hip/hip_runtime.h>
#include <hip/hip_bf16.h>
#include <stdint.h>

#define STNOP 1
typedef unsigned short u16;
typedef unsigned long long u64;
typedef short bf16x8 __attribute__((ext_vector_type(8)));
typedef short s16x4 __attribute__((ext_vector_type(4)));
typedef float f32x2 __attribute__((ext_vector_type(2)));
typedef float f32x4 __attribute__((ext_vector_type(4)));
typedef float f32x16 __attribute__((ext_vector_type(16)));
typedef unsigned u32x4 __attribute__((ext_vector_type(4)));
typedef unsigned u32x2 __attribute__((ext_vector_type(2)));
typedef __bf16 bf16x2_t __attribute__((ext_vector_type(2)));
#define LAS __attribute__((address_space(3)))
#define DI __device__ __forceinline__

constexpr int Z1L = 122880, TOPL = 159744, LDS_ALL = 163840;
constexpr int K2_SA = 16384, K2_SB = 18432, K2_SK = 6144, K2_B0 = 3 * K2_SA, K2_K0 = K2_B0 + 3 * K2_SB, K2_X0 = 147520, K2_LDS = K2_X0 + 9344 + 64 + 2048;
constexpr int S_ = 2048, D_ = 576, FH = 2304, F_ = 4608;

constexpr size_t WS_Q = 0;
constexpr size_t WS_KV = WS_Q + (size_t)S_ * D_ * 2;
constexpr size_t WS_Z1 = WS_KV + 64 * 3072;
constexpr size_t WS_Z2T = WS_Z1 + (size_t)S_ * FH * 2;
constexpr size_t WS_UO = WS_Z2T + (size_t)S_ * FH * 2;
constexpr size_t WS_ST = WS_UO + (size_t)S_ * 2880 * 2;
constexpr size_t WS_XCH = WS_ST + (size_t)S_ * 16 * 8;
constexpr size_t WS_XR = WS_XCH + 4096 + 16384;
constexpr size_t WS_FL = WS_XR + 4096;
constexpr size_t WS_FA = WS_FL + 16384;
constexpr size_t WS_FJ = WS_FA + 2048;
constexpr size_t WS_FP = WS_FJ + 8192;
constexpr size_t WS_W3T = WS_FP + 2048;
constexpr size_t WS_DBG = 128u << 20;
#define STAMP(kid, i)
#define STAMP_DRAIN()
#define STAMPT(tid, kid, i)

DI unsigned cvtpk(float lo, float hi) { f32x2 v = {lo, hi}; return __builtin_bit_cast(unsigned, __builtin_convertvector(v, bf16x2_t)); }
DI float bf2f(u16 b) { return __uint_as_float(((unsigned)b) << 16); }
DI float bflo(unsigned w) { return __uint_as_float(w << 16); }
DI float bfhi(unsigned w) { return __uint_as_float(w & 0xffff0000u); }
#define SB() __builtin_amdgcn_sched_barrier(0)
#define PIN(x) asm volatile("" : "+v"(x))
#define EX(x) __builtin_amdgcn_exp2f(x)
#define MFMA16(a, b, c) __builtin_amdgcn_mfma_f32_16x16x32_bf16(a, b, c, 0, 0, 0)
#define MFMA32(a, b, c) __builtin_amdgcn_mfma_f32_32x32x16_bf16(a, b, c, 0, 0, 0)

DI s16x4 trread(const LAS char* p) { return __builtin_bit_cast(s16x4, __builtin_amdgcn_ds_read_tr16_b64_v4i16((LAS s16x4*)p)); }
template <int N> DI void wait_vm_bar() { asm volatile("s_waitcnt vmcnt(%0) lgkmcnt(0)\n\ts_barrier" ::"n"(N) : "memory"); }
template <int N> DI void wait_vm() { asm volatile("s_waitcnt vmcnt(%0)" ::"n"(N) : "memory"); }
template <int OFF> DI void glds_s(unsigned voff, const char* sbase) { asm volatile("global_load_lds_dwordx4 %0, %1 offset:%2" ::"v"(voff), "s"(sbase), "n"(OFF)); }
template <int OFF> DI void glds_sc1(unsigned voff, const char* sbase) { asm volatile("global_load_lds_dwordx4 %0, %1 offset:%2 sc1" ::"v"(voff), "s"(sbase), "n"(OFF)); }
DI void glds_v(const char* p, unsigned m0v, u64 em) { asm volatile("s_mov_b32 m0, %1\n\ts_mov_b64 exec, %2\n\tglobal_load_lds_dwordx4 %0, off\n\ts_mov_b64 exec, -1" ::"v"(p), "s"(m0v), "s"(em)); }
DI unsigned ld_sc1_async(const char* sb, unsigned vo) { unsigned v; asm volatile("global_load_dword %0, %1, %2 sc1" : "=v"(v) : "v"(vo), "s"(sb) : "memory"); return v; }
#define WT
DI void st16(void* p, u32x4 v) { asm volatile("global_store_dwordx4 %0, %1, off sc1\n\ts_nop 1" :: "v"(p), "v"(v) : "memory"); }
DI void st8(void* p, u32x2 v) { asm volatile("global_store_dwordx2 %0, %1, off sc1" :: "v"(p), "v"(v) : "memory"); }
template <int OFF> DI void st16s(const char* sb, unsigned vo, u32x4 v) { asm volatile("global_store_dwordx4 %0, %1, %2 offset:%3 sc1\n\ts_nop %4" :: "v"(vo), "v"(v), "s"(sb), "n"(OFF), "n"(STNOP) : "memory"); }
template <int OFF> DI void st8s(const char* sb, unsigned vo, u32x2 v) { asm volatile("global_store_dwordx2 %0, %1, %2 offset:%3 sc1" :: "v"(vo), "v"(v), "s"(sb), "n"(OFF) : "memory"); }
DI float fadd1(float a, float b) { float r = a + b; asm("" : "+v"(r)); return r; }
DI float fmul1(float a, float b) { float r = a * b; asm("" : "+v"(r)); return r; }
DI float ffma1(float a, float b, float c) { float r = __builtin_fmaf(a, b, c); asm("" : "+v"(r)); return r; }
DI float lone(float a) { asm("" : "+v"(a)); return a; }
DI void set_m0(unsigned v) { asm volatile("s_mov_b32 m0, %0\n\ts_nop 0" ::"s"(v)); }
DI bf16x8 ldsA(const LAS char* p) { return *(const LAS bf16x8*)p; }
DI bf16x8 ldsB2(const LAS char* p0, const LAS char* p1) { s16x4 lo = trread(p0), hi = trread(p1); return __builtin_shufflevector(lo, hi, 0, 1, 2, 3, 4, 5, 6, 7); }
DI bf16x8 ldsB(const LAS char* p) { return ldsB2(p, p + 256); }

DI float gelu_t(float x) {
    const float c0 = -2.0f * 1.4426950408889634f * 0.7978845608028654f;
    const float c1 = c0 * 0.044715f;
    float x2 = x * x;
    float t = __builtin_fmaf(x2, c1, c0);
    float e = __builtin_amdgcn_exp2f(x * t);
    return x * __builtin_amdgcn_rcpf(1.0f + e);
}
constexpr float QSCALE = 0.23570226039551584f * 1.4426950408889634f;

#define KO1 (3 * (((idx & 7) + (idx >> 3)) % 3))
#define KO2 (((idx & 7) + 2 * (idx >> 3) + 4 * (xcd & 1)) % 9)
#define K2_KO (8 * (xcd >> 1))
#define EPRIO 1
#define PFW 0
#define SPRIO 1
#define HW 3
#define ZSCH 5
#define ZS0 1
#define GPRIO 1
#define ZPOLL 1
#define QEARLY 1
#define WSTR 32
#define WOFF 0
#define W2STR 0
#define ZASYNC 1
#define TFA 7
#define PW2 3
#define PEARLY 0
#define POLLSLEEP 0
#define APRIO 0
#define K3MAP 2
#define K3ROT 0
#define APRE 0
#define AGSLOT 1
#define ZHALF 1
#define POLLW 5
#define POLLSLEEP1 1
#define PLSLOT 0
constexpr int K1_SA = 16384, K1_SB = 24576, K1_B0 = 3 * K1_SA, K1_TAB = K1_B0 + 3 * K1_SB, K1_X0 = K1_TAB + 10240, K1_LDS = K1_X0 + 16384 + 2048;
typedef __attribute__((address_space(1))) unsigned gu32;
typedef __attribute__((address_space(1))) unsigned long long gu64;
#define RLX_AGENT __ATOMIC_RELAXED, __HIP_MEMORY_SCOPE_AGENT
struct K1F { bf16x8 a[4], b[3], x; };
struct K1S {
    unsigned va[2], vb[2]; const char* sa; const char* sb;
    const char* gp; unsigned st;
    int k1, k2;
    unsigned m0a, m0b; u64 em;
    const LAS char* pa[2]; const LAS char* px[2]; const LAS char* pb[3];
};
DI void k1_issue(K1S& s, int i, int w) {
    if (i == 0) { set_m0(s.m0a + w * K1_SA); glds_s<0>(s.va[0], s.sa); }
    if (i == 1) { glds_s<1024>(s.va[1], s.sa); s.sa += s.k1 == 8 ? -8 * 128 : 128; asm volatile("" : "+s"(s.sa)); }
    if (i == 2) { glds_v(s.gp + (unsigned long long)s.st * (unsigned)s.k1, s.m0b + w * K1_SB + 2048, s.em); s.k1 = s.k1 == 8 ? 0 : s.k1 + 1; asm volatile("" : "+s"(s.k1)); }
    if (i == 3) { set_m0(s.m0b + w * K1_SB); glds_s<0>(s.vb[0], s.sb); }
    if (i == 4) { glds_s<1024>(s.vb[1], s.sb); s.sb += s.k2 == 8 ? -8 * 128 * F_ : 128 * F_; s.k2 = s.k2 == 8 ? 0 : s.k2 + 1; asm volatile("" : "+s"(s.sb), "+s"(s.k2)); }
}
DI f32x2 gelu_y2(f32x2 y, float k0, float k1, float ir) {
    f32x2 t = (y * y) * k1 + k0;
    f32x2 a = y * t;
    f32x2 e = {__builtin_amdgcn_exp2f(a[0]), __builtin_amdgcn_exp2f(a[1])};
    f32x2 d = e * ir + ir;
    f32x2 rc = {__builtin_amdgcn_rcpf(d[0]), __builtin_amdgcn_rcpf(d[1])};
    return y * rc;
}
DI float dpp_x1(float x) { return __int_as_float(__builtin_amdgcn_mov_dpp(__float_as_int(x), 0xB1, 0xf, 0xf, true)); }
DI float dpp_x2(float x) { return __int_as_float(__builtin_amdgcn_mov_dpp(__float_as_int(x), 0x4E, 0xf, 0xf, true)); }
DI void kv_store(const char* kv, int sv, int j, int c, int g, f32x4 v) {
    if (j == 4) { st8s<0>(kv + sv * 256, ((g >> 1) * 32 + c) * 16 + (g & 1) * 8, u32x2{cvtpk(v[0], v[1]), cvtpk(v[2], v[3])}); return; }
    const unsigned kk = cvtpk(v[2], v[3]);
    const bool o1 = c & 1, o2 = c & 2;
    float t0 = dpp_x1(o1 ? v[0] : v[1]), t1 = dpp_x1(o1 ? v[2] : v[3]);
    if (o1) { v[0] = t0; v[2] = t1; } else { v[1] = t0; v[3] = t1; }
    t0 = dpp_x2(o2 ? v[0] : v[2]); t1 = dpp_x2(o2 ? v[1] : v[3]);
    if (o2) { v[0] = t0; v[1] = t1; } else { v[2] = t0; v[3] = t1; }
    u32x2 w = {cvtpk(v[0], v[1]), cvtpk(v[2], v[3])};
    const int kq = c >> 2; unsigned off;
    if (j == 5) {
        off = g < 2 ? c * 16 + (g ^ 1) * 8 : 256 + ((kq & 1) * 8 + 4 * (g - 2) + (c & 3)) * 16 + (kq >> 1) * 8;
        if (g < 2) w = g ? u32x2{kk, 0x3f80u} : u32x2{0u, 0u};
    } else {
        off = 512 + ((kq & 1) * 16 + 4 * g + (c & 3)) * 16 + (kq >> 1) * 8;
        if (g == 3 && !o2) { const unsigned cv = o1 ? 0u : 0x3f803f80u; w = u32x2{cv, cv}; }
    }
    st8s<1024>(kv + sv * 1024, off, w);
}
template <int KS, int GL, int RS, int WS, bool RX> DI void k1_p1(K1S& s, const K1F& f, K1F& n, f32x4 (&acc)[4][2], f32x4& sacc, f32x4 (&cso)[2], f32x4& rs, f32x4& rq, const bf16x8& ones) {
    constexpr int AO = RS * K1_SA, BO = RS * K1_SB + KS * 12288;
#define MX(d, a_, b_) d = MFMA16(a_, b_, d); PIN(d); SB()
#define MM(m, nn) MX(acc[m][nn], f.b[nn], f.a[m])
    MM(0, 0); n.b[0] = ldsB(s.pb[0] + BO); SB();
    MM(1, 0); n.a[0] = ldsA(s.pa[KS] + AO); SB();
    MM(2, 0); n.a[1] = ldsA(s.pa[KS] + AO + 2048); SB();
    MM(3, 0); n.a[2] = ldsA(s.pa[KS] + AO + 4096); SB();
    MX(cso[0], f.b[0], ones); n.a[3] = ldsA(s.pa[KS] + AO + 6144); SB();
    MX(sacc, f.b[2], f.x); n.b[1] = ldsB(s.pb[1] + BO); SB();
    MX(cso[1], f.b[2], ones); n.b[2] = ldsB(s.pb[2] + BO); SB();
    MX(rs, ones, f.x); k1_issue(s, GL == 1 ? 0 : 3, WS); SB();
    MX(rq, f.x, f.x); if (RX) n.x = ldsA(s.px[KS] + AO); SB();
    MM(0, 1); k1_issue(s, GL == 1 ? 1 : 4, WS); SB();
    MM(1, 1); if (GL == 1) k1_issue(s, 2, WS); SB();
    MM(2, 1);
    MM(3, 1);
}
template <int KS, int GL, int RS, int WS, bool RD, class E> DI void k1_p2(K1S& s, const K1F& f, K1F& n, f32x4 (&acc)[4][3], f32x4 (&cso)[2], const bf16x8& ones, E epi) {
    constexpr int AO = RS * K1_SA, BO = RS * K1_SB + KS * 12288;
    MM(0, 0); if (RD) n.b[0] = ldsB(s.pb[0] + BO); epi(0); SB();
    MM(1, 0); if (RD) n.a[0] = ldsA(s.pa[KS] + AO); epi(1); SB();
    MM(2, 0); if (RD) n.a[1] = ldsA(s.pa[KS] + AO + 2048); epi(2); SB();
    MM(3, 0); if (RD) n.a[2] = ldsA(s.pa[KS] + AO + 4096); epi(3); SB();
    MX(cso[0], f.b[0], ones); if (RD) n.a[3] = ldsA(s.pa[KS] + AO + 6144); epi(4); SB();
    MM(0, 1); if (RD) n.b[1] = ldsB(s.pb[1] + BO); epi(5); SB();
    MM(1, 1); if (RD) n.b[2] = ldsB(s.pb[2] + BO); epi(6); SB();
    MM(2, 1); if (GL) k1_issue(s, GL == 1 ? 0 : 3, WS); epi(7); SB();
    MM(3, 1); epi(8); SB();
    MX(cso[1], f.b[1], ones); if (GL) k1_issue(s, GL == 1 ? 1 : 4, WS); epi(9); SB();
    MM(0, 2); epi(10); SB();
    MM(1, 2); if (GL == 1) k1_issue(s, 2, WS); epi(11); SB();
    MM(2, 2); epi(12); SB();
    MM(3, 2); epi(13); SB();
#undef MM
#undef MX
}

constexpr unsigned XSALT = 0x9e3779b9u;
DI void epoch_reply(char* ws, int lane, const unsigned (&nv)[4], u64 epoch) {
    const unsigned lo = (unsigned)epoch, hi = (unsigned)(epoch >> 32);
    st16s<0>(ws + WS_XR, lane * 16, u32x4{nv[0], lo, hi, nv[0] ^ lo ^ hi ^ XSALT}); st16s<1024>(ws + WS_XR, lane * 16, u32x4{nv[1], lo, hi, nv[1] ^ lo ^ hi ^ XSALT});
    st16s<2048>(ws + WS_XR, lane * 16, u32x4{nv[2], lo, hi, nv[2] ^ lo ^ hi ^ XSALT}); st16s<3072>(ws + WS_XR, lane * 16, u32x4{nv[3], lo, hi, nv[3] ^ lo ^ hi ^ XSALT});
}
#define XN_LOADS "global_load_dword %0, %4, %5 sc1\n\tglobal_load_dword %1, %4, %5 offset:256 sc1\n\tglobal_load_dword %2, %4, %5 offset:512 sc1\n\tglobal_load_dword %3, %4, %5 offset:768 sc1\n"
DI void epoch_service(char* ws, int lane, u64 epoch) {
    unsigned nv[4];
    asm volatile(XN_LOADS "\ts_waitcnt vmcnt(0)" : "=&v"(nv[0]), "=&v"(nv[1]), "=&v"(nv[2]), "=&v"(nv[3]) : "v"(lane * 4), "s"(ws + WS_XCH) : "memory");
    epoch_reply(ws, lane, nv, epoch);
}
DI void poll_flags(char* ws, u64 epoch, int lane, const char* sb, unsigned vo) {
    const u64 t0 = __builtin_amdgcn_s_memrealtime();
    for (;;) {
        u64 f_;
        asm volatile("global_load_dwordx2 %0, %1, %2 sc1\n\ts_waitcnt vmcnt(0)" : "=&v"(f_) : "v"(vo), "s"(sb) : "memory");
        if (__all(f_ == epoch)) break;
        const u64 dt = __builtin_amdgcn_s_memrealtime() - t0;
        if (dt > 5000000ull) break;
        if (dt > 200 && blockIdx.x == 0) epoch_service(ws, lane, epoch);
        __builtin_amdgcn_s_sleep(POLLSLEEP1);
    }
}
DI void first_loads(char* ws, unsigned lds0, int lane, int wid, int j) {
    const unsigned m0 = lds0 + K2_B0 + wid * 6144, vl = lane * 16;
    const char* sl = ws + WS_Z2T + j * 32 * 18432 + wid * 6144;
#pragma unroll
    for (int t = 0; t < 2; ++t) {
        set_m0(m0 + t * K2_SB); glds_sc1<0>(vl, sl); glds_sc1<1024>(vl, sl); glds_sc1<2048>(vl, sl); glds_sc1<3072>(vl, sl);
        set_m0(m0 + t * K2_SB + 4096); glds_sc1<0>(vl, sl + 4096); glds_sc1<1024>(vl, sl + 4096); sl += K2_SB;
    }
}
DI unsigned g2_va(int wid, int lane, int i) { int row = 8 * (wid * 4 + i) + (lane >> 3), c = (lane & 7) ^ ((row >> 1) & 7); return row * (S_ * 2) + c * 16 + 4096 - i * 1024; }
struct G2P { unsigned va[4]; const LAS char* pa[2][2]; const LAS char* pb[2]; const LAS char* ps[2]; f32x4 acc[4][4], sacc[2]; };
DI void g2_prep(G2P& s, const LAS char* lds, int lane, int wid) {
    const int wr = wid >> 1, wc = wid & 1, r = lane & 15, g = lane >> 4, q = r >> 2, p = lane & 3;
#pragma unroll
    for (int m = 0; m < 4; ++m) {
        const float b = bf2f(*(const LAS u16*)(lds + TOPL + 2304 + (wr * 64 + (m ^ (2 * wc)) * 16 + r) * 2));
#pragma unroll
        for (int n = 0; n < 4; ++n) { s.acc[m][n] = f32x4{b, b, b, b}; PIN(s.acc[m][n]); }
        if (m < 2) { s.sacc[m] = f32x4{b, b, b, b}; PIN(s.sacc[m]); }
    }
#pragma unroll
    for (int i = 0; i < 4; ++i) { s.va[i] = g2_va(wid, lane, i); PIN(s.va[i]); }
#pragma unroll
    for (int ks = 0; ks < 2; ++ks) {
        const int o = r * 128 + (((4 * ks + g) ^ (r >> 1)) * 16) + wr * 8192;
        s.pa[0][ks] = lds + o + wc * 4096; s.pa[1][ks] = lds + o + (1 - wc) * 4096; PIN(s.pa[0][ks]); PIN(s.pa[1][ks]);
    }
    const int ob = K2_B0 + g * 2304 + q * 32 + 8 * p;
    s.pb[0] = lds + ob + wc * 1024 + ((g & 1) * 128); s.pb[1] = lds + ob + wc * 1024 + (((g & 1) ^ 1) * 128);
    s.ps[0] = lds + ob + 2048 + ((g & 1) * 128); s.ps[1] = lds + ob + 2048 + (((g & 1) ^ 1) * 128);
    PIN(s.pb[0]); PIN(s.pb[1]); PIN(s.ps[0]); PIN(s.ps[1]);
}
DI u64 phase1(G2P& gp, const u16* __restrict__ x, const u16* __restrict__ wq, const u16* __restrict__ wk, const u16* __restrict__ wv,
                                                    const u16* __restrict__ w_in, const u16* __restrict__ w_sp, const u16* __restrict__ b_sp, const u16* __restrict__ w_out, const u16* __restrict__ wo, char* __restrict__ ws) {
    extern __shared__ __attribute__((aligned(16))) char smem[];
    const int bid = blockIdx.x, xcd = bid & 7, idx = bid >> 3;
    const int pm = (xcd & 1) * 8 + (idx & 7), j = (xcd >> 1) * 4 + (idx >> 3);
    const int lane = threadIdx.x & 63, wid = __builtin_amdgcn_readfirstlane(threadIdx.x >> 6), wr = wid >> 2, wc = wid & 3;
    const unsigned lds0 = (unsigned)(uintptr_t)smem;
    const LAS char* lds = (const LAS char*)smem;
    {
        const char* pc; unsigned len;
        asm volatile("s_getpc_b64 %0\n.Lk1_here:\n\ts_mov_b32 %1, .Lk3_end-.Lk1_here" : "=s"(pc), "=s"(len));
        unsigned off = ((idx * 8 + wid) * 2 + (lane & 1)) * 128; off = off < len ? off : len;
        asm volatile("s_mov_b32 m0, %2\n\ts_nop 0\n\tglobal_load_lds_dword %0, %1" ::"v"(off), "s"(pc), "s"(lds0 + K1_TAB + 9216 + 512));
    }
    K1S s; s.em = (wid & 1) ? 0xccccccccull : 0x33333333ull;
    s.m0a = lds0 + wid * 2048; s.m0b = lds0 + K1_B0 + wid * 3072;
    const int brow = (lane >> 2) & 7, bc = (lane & 3) ^ (2 * (wid & 1)), bk = 8 * wid + brow;
    const int ko2 = KO2, ko1 = KO1;
    unsigned oa = pm * 128 * D_ * 2, ob = 256 * j, oa1 = oa + ko1 * 128, ob1 = ob + FH * 2 + ko1 * 128 * F_, vg = bk * (F_ * 2) + 16 * (bc & 1);
    {
        int r8 = lane >> 3;
        s.va[0] = (16 * wid + r8) * (D_ * 2) + (((lane & 7) ^ (r8 >> 1)) * 16);
        s.va[1] = (16 * wid + 8 + r8) * (D_ * 2) + (((lane & 7) ^ (4 + (r8 >> 1))) * 16) - 1024;
        s.vb[0] = bk * (F_ * 2) + (lane >> 5) * 64 + bc * 16 + 1024; s.vb[1] = s.vb[0] + 128 - 1024;
        s.k1 = s.k2 = ko1; s.st = 128 * F_;
        PIN(s.va[0]); PIN(s.va[1]); PIN(s.vb[0]); PIN(s.vb[1]); PIN(vg);
        asm volatile("" : "+s"(oa1), "+s"(ob1), "+s"(s.m0a), "+s"(s.m0b));
    }
    SB();
    s.sa = (const char*)x + oa1; s.sb = (const char*)w_in - 1024 + ob1;
    k1_issue(s, 0, 0); k1_issue(s, 1, 0); k1_issue(s, 3, 0); k1_issue(s, 4, 0);
    SB();
    STAMP(0, 0);
    s.gp = (const char*)w_in + (4096 + 32 * j + FH * 2) + vg;
    k1_issue(s, 2, 0);
    const char* sa0 = (const char*)x + oa;
    const char* sb0 = (const char*)w_in - 1024 + ob;
    gu32* XN = (gu32*)(ws + WS_XCH) + pm * 16;
    gu32* XE = (gu32*)(ws + WS_XCH + 4096) + pm * 256;
    gu64* XP = (gu64*)(ws + WS_ST) + (size_t)pm * 16 * 128;
    const char* xe_s = ws + WS_XCH + 4096 + (pm * 256 + j * 16) * 4;
    const unsigned le = (lane & 16) ? 16384 - (pm * 16 + j) * 48 + (lane & 3) * 4 : (lane & 15) * 4;
    unsigned eo = ld_sc1_async(xe_s, le);
    if (SPRIO == 1 && wid >= 4) __builtin_amdgcn_s_setprio(1);
    SB();
    unsigned nonce = 0;
    const char* gp2; unsigned st2;
    {
        const u16* p; int rs;
        const u16* sp0 = j < 4 ? wq + 512 + 16 * j : (j == 5 ? wk + 10 : (j == 6 ? wv + 8 : wk));
        const u16* sp1 = j < 4 ? wq + 520 + 16 * j : (j == 5 ? wv : (j == 6 ? wv + 10 : wk + 8));
        const int srs = j < 4 ? D_ : 18;
        if (lane >= 32) { p = wq + 32 * j + 8 * bc; rs = D_; }
        else if (bc < 2) { p = w_in + 2048 + 16 * j + 8 * bc; rs = F_; }
        else { p = bc == 2 ? sp0 : sp1; rs = srs; }
        gp2 = (const char*)(p + (size_t)bk * rs); st2 = 128 * rs; PIN(gp2); PIN(st2);
    }
    const int r16 = lane & 15, g = lane >> 4, cg = wc ^ (2 * wr);
    const int nf3 = cg == 3 ? 8 : (cg == 2 ? 9 : 10 + cg);
#define NF1(n) ((n) == 2 ? 8 : 2 * wc + ((n) ^ wr))
#define NF2(n) ((n) == 0 ? nf3 : 2 * cg + (((n) - 1) ^ wr))
    const LAS char* pb2[3];
    {
        int q = r16 >> 2, p = lane & 3;
#pragma unroll
        for (int ks = 0; ks < 2; ++ks) { s.pa[ks] = lds + (r16 * 128 + (((4 * ks + g) ^ (r16 >> 1)) * 16) + wr * 8192); s.px[ks] = s.pa[ks] + wc * 2048; PIN(s.pa[ks]); PIN(s.px[ks]); }
#pragma unroll
        for (int n = 0; n < 3; ++n) {
            int nf = NF1(n); s.pb[n] = lds + (K1_B0 + g * 3072 + q * 64 + 8 * p + (nf >> 1) * 512 + (((nf ^ g) & 1) * 32)); PIN(s.pb[n]);
            nf = NF2(n); pb2[n] = lds + (K1_B0 + g * 3072 + q * 64 + 8 * p + (nf >> 1) * 512 + (((nf ^ g) & 1) * 32)); PIN(pb2[n]);
        }
    }
    f32x4 acc1[4][2] = {}; f32x4 sacc = {}; f32x4 cso[2] = {};
    f32x4 rs = {}, rq = {};
#pragma unroll
    for (int m = 0; m < 4; ++m) { PIN(acc1[m][0]); PIN(acc1[m][1]); }
    PIN(sacc); PIN(cso[0]); PIN(cso[1]); PIN(rs); PIN(rq);
    const u32x4 onesv = {0x3f803f80u, 0x3f803f80u, 0x3f803f80u, 0x3f803f80u}; bf16x8 ones = __builtin_bit_cast(bf16x8, onesv); PIN(ones);
    K1F f0, f1;
    SB();
#pragma unroll
    for (int i = 0; i < 5; ++i) k1_issue(s, i, 1);
    SB();
    if (SPRIO == 2 && wid >= 4) __builtin_amdgcn_s_setprio(1);
    const int w3p = bid < 240 ? bid : 239;
    unsigned w3vo[2], w3so[2]; const char* w3sp; const LAS char* w3rd[2];
    {
        const int jj = w3p / 30, pc = w3p - jj * 30;
        w3sp = (pc < 24 ? (const char*)w_out : (const char*)wo - 2304 * 1152) + jj * 144;
#pragma unroll
        for (int i = 0; i < 2; ++i) {
            unsigned c_ = threadIdx.x + 512 * i; c_ = c_ < 864 ? c_ : 863;
            const unsigned row = (c_ * 7282) >> 16, k_ = pc * 96 + row, jg = (k_ * 29128) >> 22, l_ = k_ - jg * 144;
            const unsigned src = pc < 24 ? (l_ < 128 ? 128 * jg + l_ : 1920 + 16 * jg + l_) : k_;
            w3vo[i] = src * 1152 + (c_ - row * 9) * 16; PIN(w3vo[i]);
            const int u = i ? (wid < 7 ? wid + 8 : 14) : wid, k = u / 5, n = u - 5 * k;
            w3rd[i] = lds + (K1_X0 + (8 * g + (r16 >> 2)) * 144 + 8 * (lane & 3) + k * 4608 + n * 32); PIN(w3rd[i]);
            w3so[i] = w3p * 13824 + k * 4608 + n * 1024 + (n < 4 ? lane : g * 8 + r16) * 16;
            if (n == 4 && r16 >= 8) w3so[i] = 240 * 13824 + bid * 512 + (g * 8 + r16 - 8) * 16;
            PIN(w3so[i]);
        }
        asm volatile("" : "+s"(w3sp));
    }
    unsigned wspo = (pm * 128 + (idx >> 3) * 32 + wid * 4 + g) * (S_ * 2) + r16 * WSTR + WOFF; PIN(wspo);
    LAS float* redp = (LAS float*)(lds + K1_TAB + 4096) + ((wr * 64 + r16) * 5 + wc) * 2 + (g & 1); PIN(redp);
    LAS float* reds = (LAS float*)(lds + K1_TAB + 4096) + ((wr * 64 + wc * 16 + r16) * 5 + 4) * 2 + (g & 1); PIN(reds);
    SB();
#pragma unroll
    for (int i = 0; i < 3; ++i) k1_issue(s, i, 2);
    __builtin_amdgcn_s_setprio(0);
    SB();
    wait_vm_bar<8>();
    SB();
    f0.b[0] = ldsB(s.pb[0]);
    f0.a[0] = ldsA(s.pa[0]); f0.a[1] = ldsA(s.pa[0] + 2048); f0.a[2] = ldsA(s.pa[0] + 4096); f0.a[3] = ldsA(s.pa[0] + 6144);
    f0.b[2] = ldsB(s.pb[2]); f0.x = ldsA(s.px[0]); f0.b[1] = ldsB(s.pb[1]);
    SB();
    PIN(eo);
    if (wid == HW) {
        nonce = 1;
        while (__ballot(eo == nonce) != 0ull) ++nonce;
        if (lane == 0) __hip_atomic_store(XN + j, nonce, RLX_AGENT);
        __hip_atomic_store(XP + j * 128 + lane, ~0ull, RLX_AGENT); __hip_atomic_store(XP + j * 128 + 64 + lane, ~0ull, RLX_AGENT);
    }
    STAMP(0, 1);
#define P1_A(c) k1_p1<1, 2, (c), ((c) + 2) % 3, true>(s, f0, f1, acc1, sacc, cso, rs, rq, ones); wait_vm_bar<5>(); SB()
#define P1_B(c, RX) k1_p1<0, 1, ((c) + 1) % 3, (c), RX>(s, f1, f0, acc1, sacc, cso, rs, rq, ones)
#pragma unroll 1
    for (int it = 0; it < 2; ++it) { P1_A(0); P1_B(0, true); P1_A(1); P1_B(1, true); P1_A(2); P1_B(2, true); }
    const char* xn_s = ws + WS_XCH + pm * 64; const unsigned l4 = (lane & 15) * 4;
    P1_A(0);
    unsigned echoed = ld_sc1_async(xn_s, l4);
    unsigned alln[4];
    asm volatile("s_cmp_lg_u32 %6, 0\n\ts_cbranch_scc1 .Lld%=\n\t" XN_LOADS ".Lld%=:" : "=&v"(alln[0]), "=&v"(alln[1]), "=&v"(alln[2]), "=&v"(alln[3])
                 : "v"(lane * 4), "s"(ws + WS_XCH), "s"(bid) : "memory", "scc");
    s.k1 = s.k2 = ko2; s.sa = sa0 + ko2 * 128; s.sb = sb0 + (size_t)ko2 * 128 * F_; s.gp = gp2; s.st = st2; s.em = ~0ull; SB();
    P1_B(0, true);
    P1_A(1);
    PIN(echoed);
    if (wid == HW && lane < 16) __hip_atomic_store(XE + lane * 16 + j, echoed, RLX_AGENT);
    PIN(alln[0]); PIN(alln[1]); PIN(alln[2]); PIN(alln[3]);
    if (bid == 0 && wid == HW - 1) epoch_reply(ws, lane, alln, __builtin_amdgcn_s_memrealtime() | (1ull << 63));
    SB();
    P1_B(1, true);
    P1_A(2);
    s.pb[0] = pb2[0]; s.pb[1] = pb2[1]; s.pb[2] = pb2[2];
    P1_B(2, false);
    STAMP(3, 0);
    float* CS = (float*)(smem + K1_TAB);
    f32x4* RS = (f32x4*)(smem + K1_TAB + 2048);
    float* red = (float*)(smem + K1_TAB + 4096);
    f32x2* ZS = (f32x2*)(smem + K1_TAB + 9216);
    volatile LAS unsigned* HS = (volatile LAS unsigned*)(lds + K1_TAB + 1792);
    const float c0 = -2.0f * 1.4426950408889634f * 0.7978845608028654f, c1 = c0 * 0.044715f;
    f32x4 acc2[4][3] = {}; f32x4 cs2[2] = {};
    auto none = [](int) {};
    auto tabs = [&](int st) {
        if (st == 2) { if (r16 == 0) { *(f32x4*)(CS + NF1(0) * 16 + 4 * g) = cso[0]; *(f32x4*)(CS + 128 + 4 * g) = cso[1]; } }
        if (st == 3) {
            float sq = (r16 & 2) ? ((r16 & 1) ? rq[3] : rq[2]) : ((r16 & 1) ? rq[1] : rq[0]);
            float r0_ = rs[0]; PIN(r0_); PIN(sq);
            float mean = fmul1(r0_, 1.0f / D_), var = fmaxf(ffma1(-mean, mean, fmul1(sq, 1.0f / D_)), 0.f);
            rs[0] = mean; rs[1] = var + 1e-5f; PIN(rs);
        }
        if (st == 5) { rs[2] = __builtin_amdgcn_rsqf(rs[1]); PIN(rs); }
        if (st == 7) { float rstd = rs[2]; if (g == (r16 >> 2)) RS[wr * 64 + wc * 16 + r16] = f32x4{rs[0], fmul1(rstd, c0), fmul1(fmul1(rstd, rstd), fmul1(rstd, c1)), fmul1(rs[1], rstd)}; }
    };
    f32x4 er[5], cs1[3], e[3];
    f32x2 su[5], sq[5];
#define ZU(u) ((u) == 8 ? sacc : acc1[((u) >> 1) & 3][(u) & 1])
#define UM(u) ((u) == 8 ? 4 : (u) >> 1)
#define UN(u) ((u) == 8 ? 2 : (u) & 1)
#define PRE(i, u) { f32x4 y = ZU(u) - cs1[UN(u)] * er[UM(u)][0]; e[i] = ((y * y) * er[UM(u)][2] + er[UM(u)][1]) * y; ZU(u) = y; PIN(ZU(u)); PIN(e[i]); }
#define FIN(i, u, first) { ZU(u) *= e[i]; PIN(ZU(u)); f32x4 v = ZU(u); f32x2 lo = {v[0], v[1]}, hi = {v[2], v[3]}; \
        if (first) { su[UM(u)] = lo + hi; sq[UM(u)] = lo * lo + hi * hi; } else { su[UM(u)] += lo; su[UM(u)] += hi; sq[UM(u)] += lo * lo; sq[UM(u)] += hi * hi; } PIN(su[UM(u)]); PIN(sq[UM(u)]); }
#define RED(m) { float a_ = su[m][0] + su[m][1], b_ = sq[m][0] + sq[m][1]; \
            auto x1 = __builtin_amdgcn_permlane16_swap(__float_as_uint(a_), __float_as_uint(b_), false, false);       \
            float t_ = __uint_as_float(x1[0]) + __uint_as_float(x1[1]);                                               \
            auto x2 = __builtin_amdgcn_permlane32_swap(__float_as_uint(t_), __float_as_uint(t_), false, false); \
            t_ = __uint_as_float(x2[0]) + __uint_as_float(x2[1]);                                                     \
            if ((m) < 4) redp[(m) * 160] = t_; else reds[0] = t_; }
#define T1(op, v) asm volatile(op " %0, %0" : "+v"(v))
#define DE1(v, sd) asm volatile("v_fma_f32 %0, %0, %1, %1" : "+v"(v) : "v"(sd))
#define EPI_E(u0, u1, u2) [&](int st) { \
        if (st < 12) { float t_ = e[st >> 2][st & 3]; T1("v_exp_f32", t_); e[st >> 2][st & 3] = t_; } \
        if (st >= 2) { const int v_ = st - 2; float t_ = e[v_ >> 2][v_ & 3]; DE1(t_, er[v_ < 4 ? UM(u0) : (v_ < 8 ? UM(u1) : UM(u2))][3]); e[v_ >> 2][v_ & 3] = t_; } }
#define EPI_R(m0, m1) [&](int st) { if (st < 12) { float t_ = e[st >> 2][st & 3]; T1("v_rcp_f32", t_); e[st >> 2][st & 3] = t_; } \
        if (st == 12 && (m0) >= 0) er[(m0) & 3] = RS[wr * 64 + (m0) * 16 + r16]; if (st == 13 && (m1) >= 0) er[(m1) & 3] = RS[wr * 64 + (m1) * 16 + r16]; }
#define VSYNC() SB(); asm volatile("s_waitcnt lgkmcnt(0)\n\ts_barrier" ::: "memory"); SB()
#define P2_A(c, GL, E) k1_p2<1, GL ? 2 : 0, (c), ((c) + 2) % 3, true>(s, f0, f1, acc2, cs2, ones, E)
#define P2_B(c, GL, RD, E) k1_p2<0, GL ? 1 : 0, ((c) + 1) % 3, (c), RD>(s, f1, f0, acc2, cs2, ones, E)
    P2_A(0, true, tabs); wait_vm_bar<5>(); SB();
    er[0] = RS[wr * 64 + r16]; er[4] = RS[wr * 64 + wc * 16 + r16];
#pragma unroll
    for (int n = 0; n < 3; ++n) cs1[n] = *(const f32x4*)(CS + NF1(n) * 16 + 4 * g);
    PRE(0, 0) PRE(1, 1) PRE(2, 8) VSYNC();
    P2_B(0, true, true, EPI_E(0, 1, 8));
    P2_A(1, true, EPI_R(1, 2)); wait_vm_bar<5>(); SB();
    unsigned ev = ld_sc1_async(xe_s, le);
    auto w3ld = [&](int i) { SB(); set_m0(lds0 + K1_X0 + i * 8192 + wid * 1024); glds_s<0>(w3vo[i], w3sp); SB(); };
    w3ld(0); FIN(0, 0, true) FIN(1, 1, false) FIN(2, 8, true) PRE(0, 2) PRE(1, 3) PRE(2, 4) w3ld(1); RED(0) RED(4) VSYNC();
    P2_B(1, true, true, EPI_E(2, 3, 4));
    P2_A(2, true, EPI_R(3, -1)); wait_vm_bar<7>(); SB();
    PIN(ev);
    bool hs = false; u64 epoch = 0;
    auto xcheck = [&]() {
        hs = hs || __all((lane & 16) || ev == nonce);
        const unsigned w0 = __builtin_amdgcn_readlane(ev, 16), w1 = __builtin_amdgcn_readlane(ev, 17), w2 = __builtin_amdgcn_readlane(ev, 18), w3 = __builtin_amdgcn_readlane(ev, 19);
        if (w0 == nonce && w3 == (w0 ^ w1 ^ w2 ^ XSALT)) epoch = ((u64)w2 << 32) | w1;
    };
    unsigned nn = ld_sc1_async(xn_s, l4);
    asm volatile("s_mov_b32 m0, %2\n\ts_nop 0\n\tglobal_load_lds_dword %0, %1" ::"v"(wspo), "s"(w_sp), "s"(lds0 + K1_X0 + 16384));
    SB(); FIN(0, 2, true) FIN(1, 3, false) FIN(2, 4, true) PRE(0, 5) PRE(1, 6) PRE(2, 7) SB();
    asm volatile("s_mov_b32 m0, %2\n\ts_nop 0\n\tglobal_load_lds_dword %0, %1" ::"v"(lane * 4), "s"(b_sp + pm * 128), "s"(lds0 + TOPL + 2304));
    SB(); RED(1) VSYNC();
    P2_B(2, true, true, EPI_E(5, 6, 7));
    P2_A(0, true, EPI_R(-1, -1)); wait_vm_bar<8>(); SB();
    {
        const bf16x8 w0 = ldsB2(w3rd[0], w3rd[0] + 576), w1 = ldsB2(w3rd[1], w3rd[1] + 576);
        SB(); FIN(0, 5, false) FIN(1, 6, true) FIN(2, 7, false) SB();
        st16s<0>(ws + WS_W3T, w3so[0], __builtin_bit_cast(u32x4, w0));
        SB(); RED(2) RED(3) SB();
        st16s<0>(ws + WS_W3T, w3so[1], __builtin_bit_cast(u32x4, w1));
        VSYNC();
    }
    f32x2 pu[5];
    auto pub = [&](int st) {
        if (st == 0) {
#pragma unroll
            for (int i = 0; i < 5; ++i) pu[i] = *(const f32x2*)(red + (wid * 16 + r16) * 10 + 2 * i);
        }
        if (st == 4) {
            float a = fadd1(fadd1(fadd1(lone(pu[0][0]), pu[1][0]), fadd1(lone(pu[2][0]), pu[3][0])), pu[4][0]), b = fadd1(fadd1(fadd1(pu[0][1], pu[1][1]), fadd1(pu[2][1], pu[3][1])), pu[4][1]);
            unsigned bb = __float_as_uint(b); if (bb == 0xffffffffu) bb = 0x7fc00000u;
            st8s<0>((const char*)(XP + j * 128 + wid * 16), r16 * 8, u32x2{__float_as_uint(a), bb});
        }
        if (st == 13 && wid == HW) { xcheck(); *HS = hs; }
    };
    P2_B(0, true, true, pub);
    P2_A(1, true, none);
    STAMP(3, 1);
    wait_vm_bar<8>(); SB();
    ev = ld_sc1_async(xe_s, le);
    auto reecho = [&](int st) { if (st == 2) { PIN(nn); if (wid == HW && nn != echoed) { echoed = nn; if (lane < 16) __hip_atomic_store(XE + lane * 16 + j, nn, RLX_AGENT); } } };
    P2_B(1, true, true, reecho);
    const char* xp_s = ws + WS_ST + (size_t)pm * 16384 + wid * 128; const unsigned xp_v = g * 4096 + r16 * 8;
    u32x2 pv[4];
#define PLOAD() asm volatile("global_load_dwordx2 %0, %4, %5 sc1\n\tglobal_load_dwordx2 %1, %4, %5 offset:1024 sc1\n\tglobal_load_dwordx2 %2, %4, %5 offset:2048 sc1\n\tglobal_load_dwordx2 %3, %4, %5 offset:3072 sc1" \
        : "=&v"(pv[0]), "=&v"(pv[1]), "=&v"(pv[2]), "=&v"(pv[3]) : "v"(xp_v), "s"(xp_s) : "memory")
    auto pload = [&](int st) { if (st >= 10) asm volatile("global_load_dwordx2 %0, %1, %2 offset:%3 sc1" : "=&v"(pv[(st - 10) & 3]) : "v"(xp_v), "s"(xp_s), "n"(((st - 10) & 3) * 1024) : "memory"); };
    P2_A(2, true, pload); wait_vm_bar<9>(); SB();
#define PEMPTY() __any(pv[0][1] == 0xffffffffu || pv[1][1] == 0xffffffffu || pv[2][1] == 0xffffffffu || pv[3][1] == 0xffffffffu)
    const unsigned hsf = *HS;
    P2_B(2, true, true, [&](int st) { if (st == 13) { PIN(ev); if (wid == HW) xcheck(); } });
    float za, zb;
    auto zstat = [&](int st) {
        if (st == ZS0) {
            wait_vm<3>();
            PIN(pv[0]); PIN(pv[1]); PIN(pv[2]); PIN(pv[3]);
            if (__builtin_expect(hsf != 1 || PEMPTY(), 0)) {
                const unsigned long long t0 = __builtin_amdgcn_s_memrealtime();
                if (wid == HW && hsf != 1) {
                    do {
                        ev = ld_sc1_async(xe_s, le); wait_vm<0>(); PIN(ev);
                        xcheck();
                        nn = __hip_atomic_load(XN + (lane & 15), RLX_AGENT);
                        if (nn != echoed) { echoed = nn; if (lane < 16) __hip_atomic_store(XE + lane * 16 + j, nn, RLX_AGENT); }
                    } while (!hs && __builtin_amdgcn_s_memrealtime() - t0 < 5000000ull);
                    *HS = 2;
                }
                while (*HS == 0 && __builtin_amdgcn_s_memrealtime() - t0 < 6000000ull) __builtin_amdgcn_s_sleep(2);
                do {
                    PLOAD(); wait_vm<0>(); PIN(pv[0]); PIN(pv[1]); PIN(pv[2]); PIN(pv[3]);
                } while (PEMPTY() && __builtin_amdgcn_s_memrealtime() - t0 < 6000000ull);
            }
        }
#define UF(x) __uint_as_float(x)
        if (st == ZS0 + 2) {
            za = fadd1(fadd1(lone(UF(pv[0][0])), UF(pv[1][0])), fadd1(lone(UF(pv[2][0])), UF(pv[3][0]))); PIN(za);
            zb = fadd1(fadd1(UF(pv[0][1]), UF(pv[1][1])), fadd1(UF(pv[2][1]), UF(pv[3][1]))); PIN(zb);
        }
        if (st == ZS0 + 4) {
            auto a1 = __builtin_amdgcn_permlane16_swap(__float_as_uint(za), __float_as_uint(za), false, false); za = fadd1(UF(a1[0]), UF(a1[1])); PIN(za);
            auto b1 = __builtin_amdgcn_permlane16_swap(__float_as_uint(zb), __float_as_uint(zb), false, false); zb = fadd1(UF(b1[0]), UF(b1[1])); PIN(zb);
        }
        if (st == ZS0 + 6) {
            auto a2 = __builtin_amdgcn_permlane32_swap(__float_as_uint(za), __float_as_uint(za), false, false); za = fadd1(UF(a2[0]), UF(a2[1])); PIN(za);
            auto b2 = __builtin_amdgcn_permlane32_swap(__float_as_uint(zb), __float_as_uint(zb), false, false); zb = fadd1(UF(b2[0]), UF(b2[1])); PIN(zb);
        }
        if (st == ZS0 + 8) {
            float m = fmul1(za, 1.0f / FH), var = fmaxf(ffma1(-m, m, fmul1(zb, 1.0f / FH)), 0.f), r = __builtin_amdgcn_rsqf(var + 1e-5f); PIN(r);
            ZS[wid * 16 + r16] = f32x2{r, fmul1(-m, r)};
        }
    };
    P2_A(0, true, zstat); wait_vm_bar<5>(); SB();
    auto merge = [&](u32x2 A, u32x2 B) {
        auto r0 = __builtin_amdgcn_permlane16_swap(A[0], B[0], false, false); auto r1 = __builtin_amdgcn_permlane16_swap(A[1], B[1], false, false);
        return u32x4{r0[0], r1[0], r0[1], r1[1]};
    };
    auto pk4 = [&](f32x4 v) { return u32x2{cvtpk(v[0], v[1]), cvtpk(v[2], v[3])}; };
    const char* z2b = ws + WS_Z2T + (size_t)(j * 32 + 2 * pm + wr) * 18432;
    f32x2 zs[5]; u32x2 za2, zb2; unsigned vs, v2;
    auto zn = [&](int q) {
        if (q == 0) {
#pragma unroll
            for (int m = 0; m < 5; ++m) zs[m] = ZS[wr * 64 + (m < 4 ? m : wc) * 16 + r16];
        }
        if (q == 1) { vs = (r16 >> 3) * 2304 + (((r16 & 7) ^ (4 * (r16 >> 3))) * 32); PIN(vs); }
        if (q == 2) { v2 = vs + (g & 1) * 4608 + (g >> 1) * 16; vs += g * 8; PIN(v2); PIN(vs); }
#define NRM(v, m, e) { v[e] = ffma1(v[e], zs[m][0], zs[m][1]); v[(e) + 1] = ffma1(v[(e) + 1], zs[m][0], zs[m][1]); PIN(v); }
        if (q >= 3 && q < 3 + 4 * ZSCH) {
            const int p = (q - 3) / ZSCH, t = (q - 3) % ZSCH, n = p >> 1, m = (p & 1) * 2;
            f32x4& A = acc1[m][n]; f32x4& B = acc1[m + 1][n];
            if (ZSCH == 5) {
                if (t == 0) NRM(A, m, 0)
                if (t == 1) { NRM(A, m, 2) za2[0] = cvtpk(A[0], A[1]); PIN(za2); }
                if (t == 2) { NRM(B, m + 1, 0) za2[1] = cvtpk(A[2], A[3]); PIN(za2); }
                if (t == 3) { NRM(B, m + 1, 2) zb2[0] = cvtpk(B[0], B[1]); PIN(zb2); }
                if (t == 4) { zb2[1] = cvtpk(B[2], B[3]); st16s<0>(z2b + NF1(n) * 256 + m * 4608, v2, merge(za2, zb2)); }
            } else {
                if (t == 0) { NRM(A, m, 0) NRM(A, m, 2) }
                if (t == 1) { NRM(B, m + 1, 0) NRM(B, m + 1, 2) }
                if (t == 2) st16s<0>(z2b + NF1(n) * 256 + m * 4608, v2, merge(pk4(A), pk4(B)));
            }
        }
        if (q == 3 + 4 * ZSCH) { NRM(sacc, 4, 0) NRM(sacc, 4, 2) }
        if (q == 4 + 4 * ZSCH) st8s<2048>(z2b + wc * 4608, vs, pk4(sacc));
    };
    P2_B(0, false, true, zn);
    P2_A(1, false, [&](int st) { zn(14 + st); }); wait_vm_bar<5>(); SB();
    P2_B(1, false, true, [&](int st) { if (st == 0 && W2STR) asm volatile("s_mov_b32 m0, %2\n\ts_nop 0\n\tglobal_load_lds_dword %0, %1 offset:512" ::"v"(wspo + r16 * (W2STR - WSTR)), "s"(w_sp), "s"(lds0 + K1_X0 + 16384)); });
    const bool isq = nf3 >= 10 || j < 4;
    float qn[4], qs[4]; unsigned vq;
    auto qpre = [&](int st) {
        if (st == 7) {
#pragma unroll
            for (int m = 0; m < 4; ++m) er[m] = RS[wr * 64 + m * 16 + r16];
        }
        if (st == 8) { vq = ((g & 1) * 2 + (r16 >> 3)) * 9216 + (g >> 1) * 128 + (r16 & 7) * 16; PIN(vq); }
        if (st >= 10) {
            const int m = (st - 10) & 3;
            qn[m] = fmul1(er[m][0], cg == 3 ? 0.f : -1.f); qs[m] = ffma1(er[m][1], cg == 3 ? 0.f : (isq ? QSCALE / c0 : 1.0f / c0), cg == 3 ? 1.f : 0.f); PIN(qn[m]); PIN(qs[m]);
        }
    };
    auto qfin = [&](int st) {
        if (st >= 6) {
            const int m = (st - 6) >> 1, e_ = ((st - 6) & 1) * 2;
            f32x4& v = acc2[m][0];
            v[e_] = fmul1(ffma1(cs2[0][e_], qn[m], v[e_]), qs[m]); v[e_ + 1] = fmul1(ffma1(cs2[0][e_ + 1], qn[m], v[e_ + 1]), qs[m]); PIN(v);
        }
    };
    P2_A(2, false, qpre);
    P2_B(2, false, false, qfin);
    wait_vm<W2STR ? 1 : 0>();
    if (cg == 3) {
    } else if (isq) {
        const char* qb = ws + WS_Q + (pm * 16 + wr * 8) * 9216 + (nf3 >= 10 ? 4 * j + (nf3 - 10) * 2 : 64 + 2 * j) * 128;
        st16s<0>(qb, vq, merge(pk4(acc2[0][0]), pk4(acc2[1][0]))); st16s<0>(qb + 4 * 9216, vq, merge(pk4(acc2[2][0]), pk4(acc2[3][0])));
    } else if (j < 7) {
#pragma unroll
        for (int m = 0; m < 4; ++m) kv_store(ws + WS_KV + (size_t)(pm * 4 + wr * 2 + (m >> 1)) * 3072, m & 1, j, r16, g, acc2[m][0]);
    }
    SB();
    if (r16 == 0) { *(f32x4*)(CS + 192 + NF2(0) * 16 + 4 * g) = cs2[0]; *(f32x4*)(CS + 192 + NF2(1) * 16 + 4 * g) = cs2[1]; }
    f32x4* HX = (f32x4*)(smem + K1_B0) + wr * 256 + lane;
    if (cg == 3) { HX[0] = acc2[0][0]; HX[64] = acc2[1][0]; HX[128] = acc2[2][0]; HX[192] = acc2[3][0]; }
    if (wid < 4) g2_prep(gp, lds, lane, wid);
    volatile LAS u64* EW = (volatile LAS u64*)(lds + TOPL + 8);
    if (wid == HW) {
        if (__builtin_expect(epoch == 0, 0)) {
            const u64 t0 = __builtin_amdgcn_s_memrealtime();
            do { ev = ld_sc1_async(xe_s, le); wait_vm<0>(); PIN(ev); xcheck(); } while (epoch == 0 && __builtin_amdgcn_s_memrealtime() - t0 < 5000000ull);
        }
        *EW = epoch; EW[-1] = 0; EW[1] = 0; EW[2] = 0;
    }
    VSYNC();
    PIN(cs2[1]);
    if (wid == HW && lane == 0) __hip_atomic_store((gu64*)(ws + WS_FJ) + j * 64 + pm, epoch, RLX_AGENT);
    const u64 e_ = *EW;
    STAMP(3, 2);
    auto gel = [&](f32x4 a, f32x4 c, f32x4 rr) {
        f32x4 y = a - c * rr[0];
        f32x2 y0 = gelu_y2(f32x2{y[0], y[1]}, rr[1], rr[2], rr[3]), y1 = gelu_y2(f32x2{y[2], y[3]}, rr[1], rr[2], rr[3]);
        return u32x2{cvtpk(y0[0], y0[1]), cvtpk(y1[0], y1[1])};
    };
    cs1[1] = cs2[1]; cs1[2] = *(const f32x4*)(CS + 192 + NF2(2) * 16 + 4 * g);
    f32x4 h8 = HX[wc * 64], c8 = *(const f32x4*)(CS + 192 + 128 + 4 * g); er[4] = RS[wr * 64 + wc * 16 + r16];
    u32x4 zw[4];
#pragma unroll
    for (int m = 0; m < ZHALF; ++m) { zw[m] = merge(gel(acc2[m][1], cs1[1], er[m]), gel(acc2[m][2], cs1[2], er[m])); PIN(zw[m]); }
    SB();
    wait_vm_bar<0>();
    STAMP(0, 3);
    if (wid == HW) {
        const bool kvp = j >= 4 && j < 7;
        const unsigned fo = lane == 0 ? (unsigned)(WS_FJ - WS_FA) + (j * 64 + pm) * 8 : (lane == 1 ? (unsigned)(WS_FP - WS_FA) + (pm * 16 + j) * 8 : (lane == 2 ? bid * 8
                            : (unsigned)(WS_FJ - WS_FA) + ((lane & 15) * 64 + 16 + pm * 3 + (j - 4)) * 8));
        if (lane == 1 || lane == 2 || (kvp && g == 1)) __hip_atomic_store((gu64*)(ws + WS_FA + fo), epoch, RLX_AGENT);
    }
    if (wid < 4) {
        const char* sa = (const char*)w_sp - 4096 + pm * 128 * S_ * 2;
#pragma unroll
        for (int t = 0; t < 2; ++t) {
            set_m0(lds0 + wid * 4096 + t * K2_SA);
            glds_s<0>(gp.va[0], sa + t * 128); glds_s<1024>(gp.va[1], sa + t * 128); glds_s<2048>(gp.va[2], sa + t * 128); glds_s<3072>(gp.va[3], sa + t * 128);
        }
    }
    SB();
    epoch = ((u64)(unsigned)__builtin_amdgcn_readfirstlane((unsigned)(e_ >> 32)) << 32) | (unsigned)__builtin_amdgcn_readfirstlane((unsigned)e_);
    volatile LAS unsigned* go = (volatile LAS unsigned*)(lds + TOPL + 20);
    LAS char* zl = (LAS char*)lds + Z1L + wr * 1024 + r16 * 16;
#define ZPUT(m) *(LAS u32x4*)(zl + cg * 8192 + (2 * ((g & 1) ^ wr) + (g >> 1)) * 2048 + (m) * 256) = zw[m]
#pragma unroll
    for (int m = 0; m < ZHALF; ++m) ZPUT(m);
    bool issued = wid >= 3;
    u64 fz; int seen = wid != POLLW;
    asm volatile("" : "=v"(fz));
#pragma unroll
    for (int m = ZHALF; m < 5; ++m) {
        if (m > ZHALF && !seen) { wait_vm<0>(); PIN(fz); if (__all(fz == epoch)) { *go = 1; seen = 1; } }
        asm volatile("s_cmp_lg_u32 %3, 0\n\ts_cbranch_scc1 .Lfz%=\n\tglobal_load_dwordx2 %0, %1, %2 sc1\n.Lfz%=:" : "+v"(fz) : "v"(r16 * 8), "s"(ws + WS_FJ + j * 512), "s"(__builtin_amdgcn_readfirstlane(seen)) : "memory", "scc");
        if (m > ZHALF && !issued && __builtin_amdgcn_readfirstlane(*go)) { first_loads(ws, lds0, lane, wid, j); issued = true; }
        SB();
        if (m < 4) { zw[m & 3] = merge(gel(acc2[m & 3][1], cs1[1], er[m & 3]), gel(acc2[m & 3][2], cs1[2], er[m & 3])); ZPUT(m & 3); }
        else *(LAS u32x2*)(zl + 32768 + wc * 256 + (g >> 1) * 2048 + (g & 1) * 8) = gel(h8, c8, er[4]);
        SB();
    }
    if (!seen) { wait_vm<0>(); PIN(fz); if (!__all(fz == epoch)) poll_flags(ws, epoch, lane, ws + WS_FJ + j * 512, r16 * 8); *go = 1; }
    if (!issued) {
        while (*go == 0) __builtin_amdgcn_s_sleep(1);
        first_loads(ws, lds0, lane, wid, j);
    }
    return epoch;
}

DI void wait_fa(char* ws, u64 epoch, int lane) {
    const u64 t0 = __builtin_amdgcn_s_memrealtime();
    for (;;) {
        u32x4 a, b;
        asm volatile("global_load_dwordx4 %0, %2, %3 sc1\n\tglobal_load_dwordx4 %1, %2, %3 offset:1024 sc1\n\ts_waitcnt vmcnt(0)" : "=&v"(a), "=&v"(b) : "v"(lane * 16), "s"(ws + WS_FA) : "memory");
        const unsigned lo = (unsigned)epoch, hi = (unsigned)(epoch >> 32);
        if (__all(a[0] == lo && a[1] == hi && a[2] == lo && a[3] == hi && b[0] == lo && b[1] == hi && b[2] == lo && b[3] == hi)) break;
        if (__builtin_amdgcn_s_memrealtime() - t0 > 5000000ull) break;
        if (blockIdx.x == 0) epoch_service(ws, lane, epoch);
        __builtin_amdgcn_s_sleep(8);
    }
}

constexpr float ATT_BIAS = 72.0f;
DI float xhalf_max(float m) { auto rr = __builtin_amdgcn_permlane32_swap(__float_as_uint(m), __float_as_uint(m), false, false); return fmaxf(__uint_as_float(rr[0]), __uint_as_float(rr[1])); }
DI float lo_bcast(float m) { auto rr = __builtin_amdgcn_permlane32_swap(__float_as_uint(m), __float_as_uint(m), false, false); return __uint_as_float(rr[0]); }
DI float bf_round(float x) { return __uint_as_float(cvtpk(x, 0.f) << 16); }
DI float max16(const f32x16& s) {
    float a = fmaxf(fmaxf(s[0], s[1]), s[2]), b = fmaxf(fmaxf(s[3], s[4]), s[5]);
    a = fmaxf(fmaxf(a, s[6]), s[7]); b = fmaxf(fmaxf(b, s[8]), s[9]); a = fmaxf(fmaxf(a, s[10]), s[11]); b = fmaxf(fmaxf(b, s[12]), s[13]);
    a = fmaxf(fmaxf(a, s[14]), s[15]); return fmaxf(a, b);
}
DI void exp_pack(const f32x16& s, bf16x8& p0, bf16x8& p1) {
    float e[16];
#pragma unroll
    for (int i = 0; i < 16; ++i) e[i] = __builtin_amdgcn_exp2f(s[i]);
    u32x4 a = {cvtpk(e[0], e[1]), cvtpk(e[2], e[3]), cvtpk(e[4], e[5]), cvtpk(e[6], e[7])};
    u32x4 b = {cvtpk(e[8], e[9]), cvtpk(e[10], e[11]), cvtpk(e[12], e[13]), cvtpk(e[14], e[15])};
    p0 = __builtin_bit_cast(bf16x8, a); p1 = __builtin_bit_cast(bf16x8, b);
}

struct KVF { bf16x8 k1, k2, v0, v1; };
DI void kv_read(KVF& f, const LAS char* tb, int lane, int k2o, int vo) {
    f.k1 = *(const LAS bf16x8*)(tb + lane * 16); f.k2 = *(const LAS bf16x8*)(tb + k2o);
    f.v0 = *(const LAS bf16x8*)(tb + 1024 + vo); f.v1 = *(const LAS bf16x8*)(tb + 2048 + vo);
}
#define k2frag(x) (x)
struct NoHook { DI void operator()(int) const {} };
template <bool RD, class H = NoHook>
DI void half_step(f32x16& sx, bf16x8& px0, bf16x8& px1, f32x16& sy, const KVF kq, const bf16x8& qy1, const bf16x8& qy2,
                  f32x16& oy, const KVF kv, const bf16x8& py0, const bf16x8& py1, KVF& ld, const LAS char* tb, int lane, int k2o, int vo, H hook = H(), int hs = 0) {
    const f32x16 zero = {};
    unsigned a0, a1, a2, a3, b0, b1, b2, b3; float e0, e1, e2, e3;
    f32x16 t = MFMA32(kq.k1, qy1, zero); SB();
    hook(hs); SB();
    if (RD) ld.k1 = *(const LAS bf16x8*)(tb + lane * 16);
    e0 = EX(sx[0]); e1 = EX(sx[1]); e2 = EX(sx[2]); e3 = EX(sx[3]); PIN(e0); PIN(e1); PIN(e2); PIN(e3); SB();
    sy = MFMA32(kq.k2, qy2, t); SB();
    if (RD) ld.k2 = *(const LAS bf16x8*)(tb + k2o);
    a0 = cvtpk(e0, e1); a1 = cvtpk(e2, e3); PIN(a0); PIN(a1);
    e0 = EX(sx[4]); e1 = EX(sx[5]); e2 = EX(sx[6]); e3 = EX(sx[7]); PIN(e0); PIN(e1); PIN(e2); PIN(e3); SB();
    oy = MFMA32(kv.v0, py0, oy); SB();
    hook(hs + 1); SB();
    if (RD) ld.v0 = *(const LAS bf16x8*)(tb + 1024 + vo);
    a2 = cvtpk(e0, e1); a3 = cvtpk(e2, e3); PIN(a2); PIN(a3);
    e0 = EX(sx[8]); e1 = EX(sx[9]); e2 = EX(sx[10]); e3 = EX(sx[11]); PIN(e0); PIN(e1); PIN(e2); PIN(e3); SB();
    oy = MFMA32(kv.v1, py1, oy); SB();
    if (RD) ld.v1 = *(const LAS bf16x8*)(tb + 2048 + vo);
    b0 = cvtpk(e0, e1); b1 = cvtpk(e2, e3); PIN(b0); PIN(b1);
    e0 = EX(sx[12]); e1 = EX(sx[13]); e2 = EX(sx[14]); e3 = EX(sx[15]);
    b2 = cvtpk(e0, e1); b3 = cvtpk(e2, e3); PIN(b2); PIN(b3); SB();
    u32x4 a = {a0, a1, a2, a3}, b = {b0, b1, b2, b3};
    px0 = __builtin_bit_cast(bf16x8, a); px1 = __builtin_bit_cast(bf16x8, b);
}
DI bf16x8 p_scale(bf16x8 p, int sh) {
    u32x4 w = __builtin_bit_cast(u32x4, p);
#pragma unroll
    for (int i = 0; i < 4; ++i) w[i] = cvtpk(__builtin_amdgcn_ldexpf(bflo(w[i]), sh), __builtin_amdgcn_ldexpf(bfhi(w[i]), sh));
    return __builtin_bit_cast(bf16x8, w);
}
template <bool PP> DI void att_rescale(f32x16& o, f32x16& s, bf16x8& p0, bf16x8& p1, float& mref, bf16x8& q2, bool& bad, int c, int h) {
    bool nf = false;
#pragma unroll
    for (int i = 0; i < 11; ++i) nf = nf || (!(fabsf(o[i]) <= 3.0e38f) && (i < 8 || h == 0));
    if (__any(nf)) { bad = true; return; }
    float lb = lo_bcast(o[10]);
    int e = __builtin_amdgcn_frexp_expf(lb);
    float d = (float)(e + (int)ATT_BIAS); d = d < 0 ? 0 : d;
    float nm = bf_round(mref + d); float dd = nm - mref; mref = nm;
    int sh = -(int)dd;
#pragma unroll
    for (int i = 0; i < 16; ++i) { o[i] = __builtin_amdgcn_ldexpf(o[i], sh); if (!PP) { float b_ = s[i] - dd; PIN(b_); s[i] = b_; } }
    if (PP) { p0 = p_scale(p0, sh); p1 = p_scale(p1, sh); }
    u32x4 b = __builtin_bit_cast(u32x4, q2); b[1] = h ? 0u : (__float_as_uint(-nm) >> 16); q2 = __builtin_bit_cast(bf16x8, b);
}

#define W3LD 1
#define W3ST 3
struct G2F { bf16x8 a[4], b[4], sb; };
struct G2S {
    unsigned va[4], vl, vl2;
    const char* sa; const char* sl; unsigned lstep;
    unsigned m0a, m0l[3];
    const LAS char* pa[2][2]; const LAS char* pb[2]; const LAS char* ps[2];
};
template <int KS, int GL, bool RD, int RS, int WS, class H = NoHook> DI void g2_phase(G2S& s, const G2F& f, G2F& n, f32x4 (&acc)[4][4], f32x4 (&sacc)[2], H hook = H()) {
    const LAS char* a0 = s.pa[0][KS] + RS * K2_SA; const LAS char* a1 = s.pa[1][KS] + RS * K2_SA;
    constexpr int BO = RS * K2_SB + KS * 9216;
    const LAS char* b0 = s.pb[0] + BO; const LAS char* b1 = s.pb[1] + BO;
#define MM(m, nn) acc[m][nn] = MFMA16(f.b[nn], f.a[m], acc[m][nn]); PIN(acc[m][nn]); SB(); hook((nn) * 4 + (m)); SB()
    MM(0, 0); if (RD) n.b[0] = ldsB2(b0, b1); SB();
    MM(1, 0); if (RD) n.a[0] = ldsA(a0); SB();
    MM(2, 0); if (RD) n.a[1] = ldsA(a0 + 2048); SB();
    MM(3, 0); if (RD) n.a[2] = ldsA(a1); SB();
    MM(0, 1); if (RD) n.a[3] = ldsA(a1 + 2048); SB();
    MM(1, 1); if (RD) n.b[1] = ldsB2(b0 + 256, b1 + 256); SB();
    MM(2, 1); if (RD) n.b[2] = ldsB2(b0 + 512, b1 + 512); SB();
    MM(3, 1); if (RD) n.b[3] = ldsB2(b0 + 768, b1 + 768); SB();
    MM(0, 2); if (RD) n.sb = ldsB2(s.ps[0] + BO, s.ps[1] + BO); SB();
    MM(1, 2); if (GL == 1) { set_m0(s.m0a + WS * K2_SA); glds_s<0>(s.va[0], s.sa); } if (GL == 2) { glds_sc1<1024>(s.vl, s.sl); } SB();
    MM(2, 2);
    MM(3, 2); if (GL == 1) glds_s<1024>(s.va[1], s.sa); if (GL == 2) glds_sc1<2048>(s.vl, s.sl); SB();
    MM(0, 3);
    MM(1, 3); if (GL == 1) glds_s<2048>(s.va[2], s.sa); if (GL == 2) glds_sc1<3072>(s.vl, s.sl); SB();
    MM(2, 3);
    MM(3, 3); if (GL == 1) { glds_s<3072>(s.va[3], s.sa); s.sa += 128; asm volatile("" : "+s"(s.sa)); } if (GL == 2) { set_m0(s.m0l[WS] + 4096); glds_sc1<0>(s.vl2, s.sl); } SB();
    sacc[0] = MFMA16(f.sb, f.a[0], sacc[0]); PIN(sacc[0]); SB();
    if (GL == 1) { set_m0(s.m0l[WS]); glds_sc1<0>(s.vl, s.sl); } if (GL == 2) { glds_sc1<1024>(s.vl2, s.sl); s.sl += s.lstep; asm volatile("" : "+s"(s.sl)); } SB();
    sacc[1] = MFMA16(f.sb, f.a[1], sacc[1]); PIN(sacc[1]); SB();
#undef MM
}
template <int C, bool G2nd, bool G1st, int VM, bool RDN> DI void g2_iter(G2S& s, G2F& f0, G2F& f1, f32x4 (&acc)[4][4], f32x4 (&sacc)[2]) {
    g2_phase<1, G2nd ? 2 : 0, true, C, (C + 2) % 3>(s, f0, f1, acc, sacc);
    wait_vm_bar<VM>(); SB();
    g2_phase<0, G1st ? 1 : 0, RDN, (C + 1) % 3, C>(s, f1, f0, acc, sacc);
}

constexpr int K3_S = 4608, K3_RW = 4 * K3_S;
struct B5 { bf16x8 b[5]; };
DI unsigned k3_so(int w, int sl) {
    const int r = sl == 1 ? 2 : sl - 2;
    if (sl == 0) return Z1L + w * K3_S;
    return w < 4 ? K2_B0 + r * K2_SB + w * K3_S : (w < 7 ? r * K2_SA + (w - 4) * K3_S : K2_K0 + r * K2_SK);
}
DI unsigned k3_orow(int r) { return r < 6 ? (r >> 1) * K2_SA + 3 * K3_S + 64 + (r & 1) * 1168 : K2_K0 + (r - 6) * K2_SK + K3_S + 64; }
DI void k3_b1(int p, unsigned m0v, unsigned va, const char* sb) {
    if (p == 0) { set_m0(m0v); glds_sc1<0>(va, sb); }
    if (p == 1) glds_sc1<1024>(va, sb);
    if (p == 2) glds_sc1<2048>(va, sb);
    if (p == 3) glds_sc1<3072>(va, sb);
    if (p == 4) asm volatile("s_mov_b32 m0, %2\n\ts_mov_b32 exec_hi, 0\n\tglobal_load_lds_dwordx4 %0, %1 sc1\n\ts_mov_b32 exec_hi, -1" ::"v"(va), "s"(sb + 4096), "s"(m0v + 4096));
}
DI void k3_a(unsigned m0v, unsigned va, const char* sa) {
    set_m0(m0v); glds_sc1<0>(va, sa); glds_sc1<1024>(va, sa); glds_sc1<2048>(va, sa); glds_sc1<3072>(va, sa);
}
DI void k3_b(unsigned m0v, unsigned va, const char* sb) {
#pragma unroll
    for (int p = 0; p < 5; ++p) k3_b1(p, m0v, va, sb);
}
DI void k3_rdb(B5& r, const LAS char* p, const LAS char* p5) {
#pragma unroll
    for (int n = 0; n < 4; ++n) r.b[n] = ldsA(p + n * 1024);
    r.b[4] = ldsA(p5);
}
DI void k3_lb1(int p, B5& r, unsigned va, unsigned va5, const char* sb) {
    if (p < 4) asm volatile("global_load_dwordx4 %0, %1, %2 offset:%3 sc1" : "+v"(r.b[p & 3]) : "v"(va), "s"(sb), "n"((p & 3) * 1024) : "memory");
    else asm volatile("global_load_dwordx4 %0, %1, %2 sc1" : "+v"(r.b[4]) : "v"(va5), "s"(sb) : "memory");
}
#define LB5 "global_load_dwordx4 %0, %5, %7 sc1\n\tglobal_load_dwordx4 %1, %5, %7 offset:1024 sc1\n\tglobal_load_dwordx4 %2, %5, %7 offset:2048 sc1\n\t" \
            "global_load_dwordx4 %3, %5, %7 offset:3072 sc1\n\tglobal_load_dwordx4 %4, %6, %7 sc1\n"
DI void k3_lb(B5& r, unsigned va, unsigned va5, const char* sb) {
    asm volatile(LB5 : "=&v"(r.b[0]), "=&v"(r.b[1]), "=&v"(r.b[2]), "=&v"(r.b[3]), "=&v"(r.b[4]) : "v"(va), "v"(va5), "s"(sb) : "memory");
}
DI void k3_lb_if(B5& r, unsigned va, unsigned va5, const char* sb, int a, int b) {
    asm volatile("s_cmp_lg_u32 %8, %9\n\ts_cbranch_scc1 .Llb%=\n\t" LB5 ".Llb%=:" : "+v"(r.b[0]), "+v"(r.b[1]), "+v"(r.b[2]), "+v"(r.b[3]), "+v"(r.b[4])
                 : "v"(va), "v"(va5), "s"(sb), "s"(a), "n"(b) : "memory", "scc");
}
DI void pin5(B5& r) { PIN(r.b[0]); PIN(r.b[1]); PIN(r.b[2]); PIN(r.b[3]); PIN(r.b[4]); }
template <int N0, int N1, bool FIRST = false> DI void k3_mm(f32x4 (&acc)[4][5], const bf16x8 (&a)[4], const B5& r) {
#pragma unroll
    for (int n = N0; n < N1; ++n)
#pragma unroll
        for (int m = 0; m < 4; ++m) {
            if (FIRST) asm volatile("v_mfma_f32_16x16x32_bf16 %0, %1, %2, 0" : "=&v"(acc[m][n]) : "v"(r.b[n]), "v"(a[m]));
            else asm volatile("v_mfma_f32_16x16x32_bf16 %0, %1, %2, %0" : "+v"(acc[m][n]) : "v"(r.b[n]), "v"(a[m]));
        }
    SB();
}

#define K3MM(d, b_, a_) asm volatile("v_mfma_f32_16x16x32_bf16 %0, %1, %2, %0" : "+v"(d) : "v"(b_), "v"(a_))
#define ILV 1
struct UA { const char* p[4]; };
template <int NX, bool FIRST = false> DI void k3_step(f32x4 (&acc)[4][5], bf16x8 (&a)[4], const B5& r, unsigned va, const UA& ua) {
#pragma unroll
    for (int m = 0; m < 4; ++m) {
#pragma unroll
        for (int n = 0; n < 5; ++n) {
            if (FIRST) asm volatile("v_mfma_f32_16x16x32_bf16 %0, %1, %2, 0" : "=&v"(acc[m][n]) : "v"(r.b[n]), "v"(a[m]));
            else K3MM(acc[m][n], r.b[n], a[m]);
        }
        if (NX >= 0 && ILV) asm volatile("global_load_dwordx4 %0, %1, %2 sc1" : "+v"(a[m]) : "v"(va), "s"(ua.p[m] + (NX < 0 ? 0 : NX) * 16384) : "memory");
    }
    if (NX >= 0 && !ILV) {
#pragma unroll
        for (int m = 0; m < 4; ++m) asm volatile("global_load_dwordx4 %0, %1, %2 sc1" : "+v"(a[m]) : "v"(va), "s"(ua.p[m] + (NX < 0 ? 0 : NX) * 16384) : "memory");
    }
    SB();
}

DI void phase2(G2P& gp, const u16* __restrict__ w_sp, const u16* __restrict__ b_sp, const u16* __restrict__ x, char* __restrict__ ws, u16* __restrict__ out, const u64 epoch, const int wid) {
    extern __shared__ __attribute__((aligned(16))) char smem[];
    int bid = blockIdx.x, xcd = bid & 7, idx = bid >> 3;
    int pm = (xcd & 1) * 8 + (idx & 7), j = (xcd >> 1) * 4 + (idx >> 3);
    unsigned zz = 0; asm volatile("" : "+s"(zz));
    const int lane = __builtin_amdgcn_mbcnt_hi(~0u, __builtin_amdgcn_mbcnt_lo(~0u, zz)) & 63;
    const int tid = wid * 64 + lane;
    const unsigned lds0 = (unsigned)(uintptr_t)smem; const LAS char* lds = (const LAS char*)smem;
    STAMP(1, 0);
    const int rb = ((xcd & 1) ^ 1) * 16 + (xcd >> 1) * 4 + (idx & 3), j3 = idx >> 2;
    volatile LAS unsigned* SY = (volatile LAS unsigned*)(lds + TOPL);
    gu64* FLr = (gu64*)(ws + WS_FL) + rb * 64;
    const unsigned va = lane * 16;
    unsigned va5 = 4096 + ((lane >> 4) * 8 + (lane & 7)) * 16; PIN(va5);
    B5 rb0, rb1, rb2, rb3;
    const int ks0 = wid < 4 ? wid : wid + 42;
    const char* wb = ws + WS_W3T + (size_t)j3 * 90 * 4608 + ks0 * 4608;
    if (wid >= 4) {
        __builtin_amdgcn_s_setprio(1);
        const unsigned qf = lane < 16 ? (unsigned)(WS_FP - WS_FJ) + pm * 128 + lane * 8 : j * 512 + lane * 8;
        u64 f_;
        asm volatile("s_cmp_lg_u32 %3, 4\n\ts_cbranch_scc1 .Lqf%=\n\tglobal_load_dwordx2 %0, %1, %2 sc1\n.Lqf%=:\n\ts_waitcnt lgkmcnt(0)\n\ts_barrier" : "=&v"(f_) : "v"(qf), "s"(ws + WS_FJ), "s"(wid) : "memory", "scc");
        SB();
        const int c = lane & 31, h = lane >> 5, aw = wid - 4;
        const char* kvg = ws + WS_KV;
        bf16x8 q1[2], q2[2]; float mref[2];
        const f32x16 zero = {};
        f32x16 oA = zero, oB = zero;
        int k2o = 1024 + (c >> 4) * 1024 + (c & 15) * 16;
        int vo = c < 8 ? 256 + (h * 8 + c) * 16 : 512 + (h * 16 + (c < 16 ? c - 8 : (c < 18 ? c - 2 : (c == 18 ? 12 : 13)))) * 16;
        {
            const int qb0 = aw == 2 ? TOPL + 64 : K2_K0 + 2 * K2_SK + (aw < 2 ? aw : 2) * 2304, qb1 = aw == 3 ? TOPL + 64 + 2304 - 1024 : qb0;
            const char* qs = ws + WS_Q + (pm * 16 + j) * 9216 + aw * 32;
            unsigned vq = (lane >> 1) * 128 + (lane & 1) * 16; PIN(vq); PIN(k2o); PIN(vo);
            SB();
            if (wid == 4) {
                wait_vm<0>(); PIN(f_);
                if (!__all(f_ == epoch)) poll_flags(ws, epoch, lane, ws + WS_FJ, qf);
            }
            asm volatile("s_barrier" ::: "memory");
            set_m0(lds0 + qb0); glds_sc1<0>(vq, qs); set_m0(lds0 + qb1); glds_sc1<1024>(vq, qs + 3072);
            if (lane < 16) glds_sc1<2048>(vq, qs + 6144);
            SB();
            const LAS char* qo[5];
#pragma unroll
            for (int i = 0; i < 5; ++i) { const int k = 18 * c + (i < 4 ? 8 * h + 2 * i : 16); qo[i] = lds + ((k < 256 ? qb0 : qb1) + (k >> 3) * 32 + (k & 7) * 2); PIN(qo[i]); }
            SB();
            asm volatile("s_barrier" ::: "memory");
            PIN(oA); PIN(oB);
            SB();
            wait_vm<0>();
#pragma unroll
            for (int t = 0; t < 2; ++t) {
                u32x4 a;
#pragma unroll
                for (int i = 0; i < 4; ++i) a[i] = *(const LAS unsigned*)(qo[i] + t * 16);
                q1[t] = __builtin_bit_cast(bf16x8, a);
                const unsigned e = *(const LAS unsigned*)(qo[4] + t * 16);
                u32x4 b = {h ? 0u : e, 0u, 0u, 0u}; q2[t] = __builtin_bit_cast(bf16x8, b);
                PIN(q1[t]); PIN(q2[t]);
            }
        }
        bool bad = false;
        const LAS char* kvl = lds + K2_K0;
        asm volatile("s_waitcnt lgkmcnt(0)\n\ts_barrier" ::: "memory");
        KVF X, Y;
        kv_read(X, kvl, lane, k2o, vo); kv_read(Y, kvl + 3072, lane, k2o, vo);
#pragma unroll
        for (int t = 0; t < 2; ++t) {
            f32x16 s0 = MFMA32(X.k1, q1[t], zero); s0 = MFMA32(X.k2, q2[t], s0);
            f32x16 s1 = MFMA32(Y.k1, q1[t], zero); s1 = MFMA32(Y.k2, q2[t], s1);
            float m = xhalf_max(fmaxf(max16(s0), max16(s1)));
            mref[t] = bf_round(rintf(m) + ATT_BIAS);
            u32x4 b = __builtin_bit_cast(u32x4, q2[t]); b[1] = h ? 0u : (__float_as_uint(-mref[t]) >> 16); q2[t] = __builtin_bit_cast(bf16x8, b);
        }
        f32x16 sA = MFMA32(X.k1, q1[0], zero); sA = MFMA32(X.k2, q2[0], sA);
        f32x16 sB = zero;
        bf16x8 pA0, pA1, pB0 = {}, pB1 = {};
        half_step<false>(sA, pA0, pA1, sB, X, q1[1], q2[1], oB, X, pB0, pB1, Y, kvl, lane, k2o, vo);
        half_step<false>(sB, pB0, pB1, sA, Y, q1[0], q2[0], oA, X, pA0, pA1, Y, kvl, lane, k2o, vo);
        asm volatile("s_waitcnt lgkmcnt(0)\n\ts_barrier" ::: "memory");
#define A_IV(sl, CHK, H, hs) { const LAS char* tb = (sl) < 3 ? kvl + (sl) * K2_SK : lds + 2 * K2_SA + ((sl) - 3) * 6144; \
            half_step<true>(sA, pA0, pA1, sB, Y, q1[1], q2[1], oB, X, pB0, pB1, X, tb, lane, k2o, vo, H, hs); \
            half_step<false>(sB, pB0, pB1, sA, X, q1[0], q2[0], oA, Y, pA0, pA1, X, tb, lane, k2o, vo, H, hs + 2); \
            if (CHK) { mB = __builtin_amdgcn_ballot_w64(!(oB[10] <= 0x1p20f)); }                  \
            half_step<true>(sA, pA0, pA1, sB, X, q1[1], q2[1], oB, Y, pB0, pB1, Y, tb + 3072, lane, k2o, vo, H, hs + 4); \
            if (CHK) { mA = __builtin_amdgcn_ballot_w64(!(oA[10] <= 0x1p20f)); any = mA | mB; asm volatile("" : "+s"(any)); } \
            half_step<false>(sB, pB0, pB1, sA, Y, q1[0], q2[0], oA, X, pA0, pA1, Y, tb, lane, k2o, vo, H, hs + 6); }
#define A_BAR() asm volatile("s_waitcnt lgkmcnt(0)\n\ts_barrier" ::: "memory")
        unsigned long long mA, mB, any;
#define UNDEF5(r) asm volatile("" : "=v"(r.b[0]), "=v"(r.b[1]), "=v"(r.b[2]), "=v"(r.b[3]), "=v"(r.b[4]))
        u32x4 fa0, fa1;
        asm volatile("global_load_dwordx4 %0, %2, %3 sc1\n\tglobal_load_dwordx4 %1, %2, %3 offset:1024 sc1" : "=&v"(fa0), "=&v"(fa1) : "v"(va), "s"(ws + WS_FA) : "memory");
        const NoHook nh;
        const char* pf = ws + WS_W3T + (size_t)j3 * 90 * 4608 + 16 * 4608 + min(((idx & 3) * 4 + aw) * 192, 2664 - 192) * 128;
        auto wl = [&](int i) {
            if (i < 20) k3_lb1(i % 5, i < 5 ? rb0 : (i < 10 ? rb1 : (i < 15 ? rb2 : rb3)), va, va5, wb + (i / 5) * 18432);
            else if (i < 23 && PFW)
                asm volatile("s_mov_b32 m0, %2\n\ts_nop 0\n\tglobal_load_lds_dword %0, %1 sc1" ::"v"(va * 8), "s"(pf + (i - 20) * 8192), "s"(lds0 + TOPL + 64 + wid * 256));
        };
#define A_RS() if (__builtin_expect(any != 0, 0)) { \
                if (mA) att_rescale<false>(oA, sA, pA0, pA1, mref[0], q2[0], bad, c, h); \
                if (mB) att_rescale<true>(oB, sB, pB0, pB1, mref[1], q2[1], bad, c, h); \
            }
#define A_IT(H) A_IV(1, false, H, 0); A_BAR(); A_IV(2, false, H, 8); A_BAR(); A_IV(0, true, H, 16); A_RS() A_BAR()
#pragma unroll 1
        for (int t = 0; t < TFA; ++t) { A_IT(nh); }
        {
            wait_vm<0>(); PIN(fa0); PIN(fa1);
            const unsigned lo = (unsigned)epoch, hi = (unsigned)(epoch >> 32);
            if (__builtin_expect(!__all(fa0[0] == lo && fa0[1] == hi && fa0[2] == lo && fa0[3] == hi && fa1[0] == lo && fa1[1] == hi && fa1[2] == lo && fa1[3] == hi), 0)) wait_fa(ws, epoch, lane);
            UNDEF5(rb0); UNDEF5(rb1); UNDEF5(rb2); UNDEF5(rb3);
            SB();
            A_IT(wl);
        }
#pragma unroll 1
        for (int t = TFA + 1; t < 9; ++t) { A_IT(nh); }
        A_IV(1, false, nh, 0); A_BAR();
        wait_vm<0>(); pin5(rb0); pin5(rb1); pin5(rb2); pin5(rb3);
        auto w9 = [&](int i) { if (i < 5) k3_b1(i, lds0 + k3_so(wid, 3), va, wb + 9 * 18432); };
        A_IV(2, false, nh, 0); A_IV(3, true, w9, 0); A_RS() A_IV(4, false, nh, 0);
        half_step<false>(sA, pA0, pA1, sB, Y, q1[1], q2[1], oB, X, pB0, pB1, X, kvl, lane, k2o, vo);
        exp_pack(sB, pB0, pB1);
        oA = MFMA32(Y.v0, pA0, oA); oA = MFMA32(Y.v1, pA1, oA);
        oB = MFMA32(Y.v0, pB0, oB); oB = MFMA32(Y.v1, pB1, oB);
        {
            float sa_ = fabsf(oA[10]) * 0x1p27f, sb_ = fabsf(oB[10]) * 0x1p27f;
#pragma unroll
            for (int i = 0; i < 10; ++i) { sa_ += fabsf(oA[i]); PIN(sa_); sb_ += fabsf(oB[i]); PIN(sb_); }
            if (__builtin_amdgcn_ballot_w64(!(sa_ + sb_ <= 3.0e38f))) bad = true;
        }
        f32x16 o[2] = {oA, oB};
        if (__builtin_expect(bad, 0)) {
#pragma unroll 1
            for (int t = 0; t < 2; ++t) {
                bf16x8 qa = t ? q1[1] : q1[0]; u32x4 qb = __builtin_bit_cast(u32x4, t ? q2[1] : q2[0]); qb[1] = 0;
                f32x16 oo = zero; float m = -3.0e38f;
                for (int kt = 0; kt < 64; ++kt) {
                    const char* tb = kvg + (size_t)kt * 3072;
                    bf16x8 k1, v0, v1; u32x4 k2v;
                    asm volatile("global_load_dwordx4 %0, %4, %7 sc1\n\tglobal_load_dwordx4 %1, %5, %7 sc1\n\tglobal_load_dwordx4 %2, %6, %7 offset:1024 sc1\n\t"
                                 "global_load_dwordx4 %3, %6, %7 offset:2048 sc1\n\ts_waitcnt vmcnt(0)"
                                 : "=&v"(k1), "=&v"(k2v), "=&v"(v0), "=&v"(v1) : "v"(lane * 16), "v"(k2o), "v"(vo), "s"(tb) : "memory");
                    f32x16 s = MFMA32(k1, qa, zero); s = MFMA32(__builtin_bit_cast(bf16x8, k2v), __builtin_bit_cast(bf16x8, qb), s);
                    float nm = fmaxf(m, xhalf_max(max16(s)));
                    float sc = __builtin_amdgcn_exp2f(m - nm); m = nm;
#pragma unroll
                    for (int i = 0; i < 16; ++i) { float a_ = oo[i] * sc, b_ = s[i] - nm; PIN(a_); PIN(b_); oo[i] = a_; s[i] = b_; }
                    bf16x8 p0, p1; exp_pack(s, p0, p1);
                    oo = MFMA32(v0, p0, oo); oo = MFMA32(v1, p1, oo);
                }
                if (t) o[1] = oo; else o[0] = oo;
            }
        }
#pragma unroll
        for (int t = 0; t < 2; ++t) {
            float inv = __builtin_amdgcn_rcpf(lo_bcast(o[t][10])); PIN(inv);
            LAS unsigned* ob = (LAS unsigned*)(lds + k3_orow(aw * 2 + t) + 36 * c + 8 * h);
#define ON(i) cvtpk(fmul1(o[t][i], inv), fmul1(o[t][(i) + 1], inv))
            ob[0] = ON(0); ob[1] = ON(2); ob[4] = ON(4); ob[5] = ON(6);
            if (h == 0) ob[8] = ON(8);
        }
        asm volatile("s_waitcnt lgkmcnt(0)" ::: "memory");
        if (lane == 0) __hip_atomic_fetch_add((LAS unsigned*)SY, 1u, __ATOMIC_RELAXED, __HIP_MEMORY_SCOPE_WORKGROUP);
        __builtin_amdgcn_s_setprio(APRIO);
    } else {
        const int wr = wid >> 1, wc = wid & 1;
        G2S s;
        s.vl = lane * 16; s.vl2 = lane * 16 + 4096;
        s.lstep = wid < 3 ? 18432 : 6144;
        unsigned ol = (wid < 3 ? (unsigned)WS_Z2T + j * 32 * 18432 + wid * 6144 : (unsigned)WS_KV - 3 * 6144), oa = pm * 128 * S_ * 2;
        s.m0a = lds0 + wid * 4096; s.m0l[0] = lds0 + (wid < 3 ? K2_B0 + wid * 6144 : K2_K0); s.m0l[1] = s.m0l[0] + s.lstep; s.m0l[2] = s.m0l[1] + s.lstep;
        PIN(s.vl); PIN(s.vl2);
        s.sl = ws + ol + 2 * s.lstep;
        STAMPT(0, 3, 3);
#pragma unroll
        for (int i = 0; i < 4; ++i) { s.va[i] = gp.va[i]; s.pa[i >> 1][i & 1] = gp.pa[i >> 1][i & 1]; }
        s.pb[0] = gp.pb[0]; s.pb[1] = gp.pb[1]; s.ps[0] = gp.ps[0]; s.ps[1] = gp.ps[1];
        s.sa = (const char*)w_sp - 4096 + oa + 256;
        G2F f0, f1;
        f32x4 (&acc)[4][4] = gp.acc; f32x4 (&sacc)[2] = gp.sacc;
        SB();
        if (wid == 3) wait_vm<0>();
        wait_vm_bar<6>();
        STAMP(1, 1);
        const unsigned m0l2 = s.m0l[2];
        if (wid == 3) s.m0l[2] = s.m0l[0];
        f0.b[0] = ldsB2(s.pb[0], s.pb[1]);
        f0.a[0] = ldsA(s.pa[0][0]); f0.a[1] = ldsA(s.pa[0][0] + 2048); f0.a[2] = ldsA(s.pa[1][0]); f0.a[3] = ldsA(s.pa[1][0] + 2048);
        f0.b[1] = ldsB2(s.pb[0] + 256, s.pb[1] + 256); f0.b[2] = ldsB2(s.pb[0] + 512, s.pb[1] + 512); f0.b[3] = ldsB2(s.pb[0] + 768, s.pb[1] + 768); f0.sb = ldsB2(s.ps[0], s.ps[1]);
        SB();
        set_m0(s.m0a + 2 * K2_SA); glds_s<0>(s.va[0], s.sa); glds_s<1024>(s.va[1], s.sa); glds_s<2048>(s.va[2], s.sa); glds_s<3072>(s.va[3], s.sa); s.sa += 128;
        set_m0(s.m0l[2]); glds_sc1<0>(s.vl, s.sl);
        SB();
#pragma unroll 1
        for (int t = 0; t < 9; ++t) {
            g2_iter<0, true, true, 10, true>(s, f0, f1, acc, sacc); s.m0l[2] = m0l2; g2_iter<1, true, true, 10, true>(s, f0, f1, acc, sacc); g2_iter<2, true, true, 10, true>(s, f0, f1, acc, sacc);
        }
        asm volatile("" : "=v"(rb0.b[0]), "=v"(rb0.b[1]), "=v"(rb0.b[2]), "=v"(rb0.b[3]), "=v"(rb0.b[4]), "=v"(rb1.b[0]), "=v"(rb1.b[1]), "=v"(rb1.b[2]), "=v"(rb1.b[3]), "=v"(rb1.b[4]));
        g2_phase<1, 2, true, 0, 2>(s, f0, f1, acc, sacc);
        wait_vm_bar<10>(); SB();
        g2_phase<0, 1, true, 1, 0>(s, f1, f0, acc, sacc, [&](int st) { if (st < 10 && !(st & 1)) k3_lb1(st >> 1, rb0, va, va5, wb); });
        g2_phase<1, 2, true, 1, 0>(s, f0, f1, acc, sacc);
        wait_vm_bar<15>(); SB();
        g2_phase<0, 1, true, 2, 1>(s, f1, f0, acc, sacc, [&](int st) { if (st < 10 && !(st & 1)) k3_lb1(st >> 1, rb1, va, va5, wb + 18432); });
        const int c = lane & 15, g = lane >> 4;
        const LAS char* z1b = lds + Z1L + (g >> 1) * 2048 + (wr * 64 + c) * 16 + (g & 1) * 8;
        u32x2 z1r[4][4], z1s[2];
#pragma unroll
        for (int m = 0; m < 4; ++m) {
            const int mo = (m ^ (2 * wc)) * 16;
#pragma unroll
            for (int n = 0; n < 4; ++n) z1r[m][n] = *(const LAS u32x2*)(z1b + (wc * 4 + n) * 4096 + mo * 16);
            if (m < 2) z1s[m] = *(const LAS u32x2*)(z1b + 8 * 4096 + mo * 16);
        }
        SB();
#define PREB(i) [&](int st) { if (st < 5) k3_b1(st, lds0 + k3_so(wid, i), va, wb + ((i) + 6) * 18432); else if (st < 10) k3_b1(st - 5, lds0 + k3_so(wid + 4, i), va, wb + ((i) + 6) * 18432 + 46 * 4608); }
        g2_phase<1, 2, true, 2, 1>(s, f0, f1, acc, sacc);
        wait_vm_bar<15>(); SB();
        {
            const int p = wid < 3 ? wid : 2;
            const char* ks = ws + WS_KV + (29 + p) * 6144; const unsigned km = lds0 + (p ? 2 * K2_SA + (p - 1) * 6144 : K2_K0 + 2 * K2_SK);
            g2_phase<0, 0, true, 0, 2>(s, f1, f0, acc, sacc, [&](int st) {
                if (st < 4) k3_b1(st, km, va, ks); else if (st == 4) { set_m0(km + 4096); glds_sc1<0>(s.vl2, ks); } else if (st == 5) glds_sc1<1024>(s.vl2, ks);
                else if (st < 11) k3_b1(st - 6, lds0 + k3_so(wid, 1), va, wb + 7 * 18432); });
        }
        g2_phase<1, 0, true, 0, 2>(s, f0, f1, acc, sacc, PREB(0));
        wait_vm_bar<21>(); SB();
        g2_phase<0, 0, true, 1, 0>(s, f1, f0, acc, sacc, PREB(2));
        unsigned vu[3];
#pragma unroll
        for (int i = 0; i < 3; ++i) {
            const int cgl = 18 * j + (i < 2 ? 2 * (wc * 4 + 2 * i + (g & 1)) : 16) + (g >> 1);
            vu[i] = (cgl >> 2) * 4096 + (cgl & 3) * 256 + c * 16 + (i == 2 ? (g & 1) * 1024 : 0); PIN(vu[i]);
        }
        const char* ub0 = ws + WS_UO + (size_t)(pm * 2 + wr) * 90 * 4096 + wc * 2048; const char* ub1 = ub0 + 2048 - wc * 4096;
        SB();
        g2_phase<1, 0, true, 1, 0>(s, f0, f1, acc, sacc);
        wait_vm_bar<0>(); SB();
        __builtin_amdgcn_s_setprio(2);
        auto fin = [&](f32x4 v, u32x2 z) { return u32x2{cvtpk(fmul1(bflo(z[0]), v[0]), fmul1(bfhi(z[0]), v[1])), cvtpk(fmul1(bflo(z[1]), v[2]), fmul1(bfhi(z[1]), v[3]))}; };
        auto mrg = [&](u32x2 A, u32x2 B) {
            auto r0 = __builtin_amdgcn_permlane16_swap(A[0], B[0], false, false); auto r1 = __builtin_amdgcn_permlane16_swap(A[1], B[1], false, false);
            return u32x4{r0[0], r1[0], r0[1], r1[1]};
        };
#define M2(m, n) acc[m][n] = MFMA16(f1.b[n], f1.a[m], acc[m][n]); acc[m][(n) + 1] = MFMA16(f1.b[(n) + 1], f1.a[m], acc[m][(n) + 1]); PIN(acc[m][n]); PIN(acc[m][(n) + 1]); SB()
#define PUT(m, n) st16s<((m) & 1) * 1024>((m) < 2 ? ub0 : ub1, vu[(n) >> 1], mrg(fin(acc[m][n], z1r[m][n]), fin(acc[m][(n) + 1], z1r[m][(n) + 1]))); SB()
        M2(0, 0); M2(1, 0); M2(2, 0); PUT(0, 0);
        M2(3, 0); PUT(1, 0);
        sacc[0] = MFMA16(f1.sb, f1.a[0], sacc[0]); sacc[1] = MFMA16(f1.sb, f1.a[1], sacc[1]); PIN(sacc[0]); PIN(sacc[1]); SB(); PUT(2, 0);
        M2(0, 2); PUT(3, 0);
        M2(1, 2); st16s<0>(ub0, vu[2], mrg(fin(sacc[0], z1s[0]), fin(sacc[1], z1s[1]))); SB();
        M2(2, 2); PUT(0, 2);
        M2(3, 2); PUT(1, 2);
        STAMP(1, 2);
        PUT(2, 2); PUT(3, 2);
        SB(); k3_b(lds0 + k3_so(wid, 3), va, wb + 9 * 18432);
        wait_vm<5>();
        if (lane == 0) __hip_atomic_store((gu64*)(ws + WS_FL) + (pm * 2 + wr) * 64 + j * 2 + wc, epoch, RLX_AGENT);
        pin5(rb0); pin5(rb1);
        asm volatile("" : "=v"(rb2.b[0]), "=v"(rb2.b[1]), "=v"(rb2.b[2]), "=v"(rb2.b[3]), "=v"(rb2.b[4]), "=v"(rb3.b[0]), "=v"(rb3.b[1]), "=v"(rb3.b[2]), "=v"(rb3.b[3]), "=v"(rb3.b[4]));
        STAMP(1, 3);
        __builtin_amdgcn_s_setprio(GPRIO);
    }
    {
#define TK()
#define TP()
        TP();
        UA ua;
#pragma unroll
        for (int i = 0; i < 4; ++i) ua.p[i] = ws + WS_UO + (size_t)rb * 90 * 4096 + ks0 * 4096 + ((wid + 1 + i) & 3) * 1024;
        auto relane = [&]() { unsigned t_ = va; PIN(t_); return (int)(t_ >> 4); };
        unsigned so[4];
#pragma unroll
        for (int i = 0; i < 4; ++i) so[i] = __builtin_amdgcn_readfirstlane(k3_so(wid, i));
#define LG0() asm volatile("s_waitcnt lgkmcnt(0)" ::: "memory"); SB()
#define LB(d, i) k3_lb(d, va, va5, wb + (i) * 18432)
#define RB(d, sl) { asm volatile("" : "+s"(so[sl])); k3_rdb(d, lds + va + so[sl], lds + va5 + so[sl]); }
        B5 rb4, rb5; bf16x8 a0[4], a1[4], a2[4], a3[4], a4[4];
        f32x4 ac[4][5];
        const size_t xo = (size_t)(rb * 64 + (wid & 3) * 16) * D_ + 72 * j3;
        bool opub = true;
#define OPUB() if (relane() == 0) __hip_atomic_store((gu64*)(ws + WS_FL) + (pm * 2 + (j >> 3)) * 64 + 32 + (j & 7) * 4 + (wid - 4), epoch, RLX_AGENT)
        UNDEF5(rb4); UNDEF5(rb5);
        if (wid < 4) {
            k3_lb_if(rb2, va, va5, wb + 2 * 18432, 0, 0); k3_lb_if(rb3, va, va5, wb + 3 * 18432, 0, 0); TP();
            k3_lb_if(rb4, va, va5, wb + 4 * 18432, 0, 0); k3_lb_if(rb5, va, va5, wb + 5 * 18432, 0, 0); TP();
        } else {
            const int orow = pm * 128 + j * 8, ck = (wid - 4) * 18 + (lane >> 3);
            const char* ob = ws + WS_UO + ((size_t)((orow >> 6) * 90 + 72) * 4 + ((orow >> 4) & 3)) * 1024 + (orow & 15) * 16;
            unsigned vo_ = (ck >> 2) * 4096 + (ck & 3) * 256 + (lane & 7) * 16; PIN(vo_);
            const LAS char* ol = lds + (k3_orow(lane & 7) + ck * 16); PIN(ol);
            SB();
            while (SY[0] < 4) __builtin_amdgcn_s_sleep(1);
            TP();
            const u32x4 o0 = *(const LAS u32x4*)ol, o1 = *(const LAS u32x4*)(ol + 128), o2 = *(const LAS u32x4*)(ol + 256);
            opub = __builtin_amdgcn_readfirstlane(SY[1]) == 0;
            st16s<0>(ob, vo_, o0); st16s<0>(ob + 8192, vo_, o1);
            if (lane < 16) st16s<0>(ob + 16384, vo_, o2);
            TP();
            if (opub) { wait_vm<0>(); OPUB(); }
        }
        SB();
        TP();
        if (wid == PW2) {
            const unsigned long long t0 = __builtin_amdgcn_s_memrealtime();
            u64 f = __hip_atomic_load(FLr + (lane & 31), RLX_AGENT);
            while (!__all(f == epoch) && __builtin_amdgcn_s_memrealtime() - t0 < 5000000ull) {
                __builtin_amdgcn_s_sleep(POLLSLEEP);
                f = __hip_atomic_load(FLr + (lane & 31), RLX_AGENT);
            }
            SY[1] = 1;
        }
        while (SY[1] == 0) __builtin_amdgcn_s_sleep(1);
        TP();
        STAMP(2, 0); STAMP(2, 1);
#define LA4 "global_load_dwordx4 %0, %4, %5 sc1\n\tglobal_load_dwordx4 %1, %4, %6 sc1\n\tglobal_load_dwordx4 %2, %4, %7 sc1\n\tglobal_load_dwordx4 %3, %4, %8 sc1\n"
#define LAO(d, o_) : "=&v"(d[0]), "=&v"(d[1]), "=&v"(d[2]), "=&v"(d[3]) : "v"(va), "s"(ua.p[0] + (o_)), "s"(ua.p[1] + (o_)), "s"(ua.p[2] + (o_)), "s"(ua.p[3] + (o_))
#define LA(d, i) asm volatile(LA4 LAO(d, (i) * 16384) : "memory")
#define PIN4(d) PIN(d[0]); PIN(d[1]); PIN(d[2]); PIN(d[3])
#define GO(d, rb_, W, NX, ...) wait_vm<W>(); TK(); PIN4(d); SB(); k3_step<NX, ##__VA_ARGS__>(ac, d, rb_, va, ua)
        TK();
        LA(a0, 0); LA(a1, 1);
        const bool ext = wid < 2;
#define GO2(d, rb_, WE, WL, NX, ...) if (wid < 4) wait_vm<WE>(); GO(d, rb_, WL, NX, ##__VA_ARGS__)
        wait_vm<4>();
        if (!opub) OPUB();
        GO(a0, rb0, 5, 2, true); k3_lb_if(rb4, va, va5, wb + 4 * 18432, wid >> 2, 1);
        pin5(rb2); pin5(rb3);
        GO2(a1, rb1, 4, 9, 3); k3_lb_if(rb5, va, va5, wb + 5 * 18432, wid >> 2, 1);
        GO2(a0, rb2, 4, 14, 4);
        if (wid >= 4) {
            asm volatile("s_mov_b32 m0, %2\n\ts_nop 0\n\tglobal_load_lds_dword %0, %1 sc1" ::"v"(relane() * 4), "s"(FLr + 32), "s"(lds0 + TOPL + 64 + wid * 256));
            k3_b(lds0 + so[1], va, wb + 7 * 18432);
        }
        GO2(a1, rb3, 4, 15, 5); LB(rb0, 10);
        if (wid >= 4) {
            wait_vm<14>();
            const int l2 = relane();
            const unsigned fo = *(const volatile LAS unsigned*)(lds + TOPL + 64 + wid * 256 + l2 * 4);
            if (__builtin_expect(!__all(fo == ((l2 & 1) ? (unsigned)(epoch >> 32) : (unsigned)epoch)), 0)) {
                const unsigned long long t0 = __builtin_amdgcn_s_memrealtime();
                u64 f2; do { f2 = __hip_atomic_load(FLr + 32 + (l2 & 31), RLX_AGENT); } while (!__all(f2 == epoch) && __builtin_amdgcn_s_memrealtime() - t0 < 5000000ull);
            }
        }
        if (wid < 4) wait_vm<9>();
        pin5(rb4); pin5(rb5);
        GO(a0, rb4, 14, 6); k3_lb_if(rb1, va, va5, wb + 11 * 18432, wid >> 1, 0); RB(rb2, 0); SB();
        GO(a1, rb5, 9, 7); LA(a2, 8); RB(rb3, 1); SB();
        GO(a0, rb2, 8, 9); LA(a3, 10); RB(rb4, 2); SB();
        asm volatile("s_cmp_lg_u32 %9, 0\n\ts_cbranch_scc1 .Lla%=\n\t" LA4 ".Lla%=:" LAO(a4, 11 * 16384), "s"(wid >> 1) : "memory", "scc");
        GO(a1, rb3, 12, -1); RB(rb5, 3); SB();
        wait_vm<0>(); TK(); PIN4(a0); PIN4(a2); PIN4(a3); PIN4(a4); pin5(rb0); pin5(rb1);
        if (ext) k3_step<-1>(ac, a4, rb1, va, ua);
        const int l3 = relane(), r16 = l3 & 15, g = l3 >> 4;
        LAS char* pw = (LAS char*)lds + va;
        u32x2 xv[5];
        if (wid < 4) {
#pragma unroll
            for (int n = 0; n < 5; ++n) xv[n] = *(const u32x2*)(x + xo + r16 * D_ + n * 16 + (n < 4 ? g : (g & 1)) * 4);
        }
        SB();
#pragma unroll
        for (int m = 0; m < 4; ++m)
#pragma unroll
            for (int q = 0; q < 15; ++q) {
                if (q < 5) K3MM(ac[m][q], rb4.b[q], a2[m]); else if (q < 10) K3MM(ac[m][q - 5], rb0.b[q - 5], a3[m]); else K3MM(ac[m][q - 10], rb5.b[q - 10], a0[m]);
                SB();
                if (m > 0 && q % 3 == 0) { const int n = q / 3; if (n < 4 || g < 2) *(LAS f32x4*)(pw + so[m - 1] + n * 1024) = ac[m - 1][n]; SB(); }
            }
        asm volatile("s_nop 7\n\ts_nop 7\n\ts_waitcnt lgkmcnt(0)" ::: "memory");
        TK();
        STAMP(2, 2);
        const LAS char* pr = lds + va;
#define PART(k, n) *(const LAS f32x4*)(pr + k3_so((wid & 4) + ((wid + 3 - (k)) & 3), k) + (n) * 1024)
        if (wid < 4) {
            __builtin_amdgcn_s_setprio(EPRIO);
            if (l3 == 0) __hip_atomic_fetch_add((LAS unsigned*)SY + 4, 1u, __ATOMIC_RELAXED, __HIP_MEMORY_SCOPE_WORKGROUP);
#pragma unroll
            for (int n = 0; n < 5; ++n) {
                ac[3][n] = f32x4{fadd1(ac[3][n][0], bflo(xv[n][0])), fadd1(ac[3][n][1], bfhi(xv[n][0])), fadd1(ac[3][n][2], bflo(xv[n][1])), fadd1(ac[3][n][3], bfhi(xv[n][1]))}; PIN(ac[3][n]);
            }
            while (SY[4] < 4) __builtin_amdgcn_s_sleep(1);
            SB();
#pragma unroll
            for (int n = 0; n < 5; ++n) {
                const f32x4 p0 = PART(0, n), p1 = PART(1, n), p2 = PART(2, n);
#pragma unroll
                for (int e = 0; e < 4; ++e) ac[3][n][e] = fadd1(fadd1(ac[3][n][e], p0[e]), fadd1(p1[e], p2[e]));
            }
#pragma unroll
            for (int n = 0; n < 5; ++n) if (n < 4 || g < 2) *(LAS f32x4*)(pw + so[3] + n * 1024) = ac[3][n];
        }
        SB();
        asm volatile("s_waitcnt lgkmcnt(0)" ::: "memory"); TK();
        asm volatile("s_barrier" ::: "memory"); TK();
        if (wid >= 4) {
            u32x2 o[5];
#pragma unroll
            for (int n = 0; n < 5; ++n) {
                const f32x4 r = ((ac[3][n] + PART(0, n)) + (PART(1, n) + PART(2, n))) + *(const LAS f32x4*)(pr + k3_so(wid - 4, 3) + n * 1024);
                o[n] = u32x2{cvtpk(r[0], r[1]), cvtpk(r[2], r[3])};
            }
            u16* op = out + xo + r16 * D_;
#pragma unroll
            for (int n = 0; n < 4; n += 2) {
                auto r0 = __builtin_amdgcn_permlane16_swap(o[n][0], o[n + 1][0], false, false); auto r1 = __builtin_amdgcn_permlane16_swap(o[n][1], o[n + 1][1], false, false);
                *(u32x4*)(op + (n + (g & 1)) * 16 + (g >> 1) * 8) = u32x4{r0[0], r1[0], r0[1], r1[1]};
            }
            if (g < 2) *(u32x2*)(op + 64 + g * 4) = o[4];
        }
    }
    STAMP_DRAIN(); STAMP(2, 3);
}
__global__ __launch_bounds__(512, 2) void fused_kernel(const u16* __restrict__ x, const u16* __restrict__ wq, const u16* __restrict__ wk, const u16* __restrict__ wv,
                                                       const u16* __restrict__ w_in, const u16* __restrict__ w_sp, const u16* __restrict__ b_sp, const u16* __restrict__ w_out,
                                                       const u16* __restrict__ wo, char* __restrict__ ws, u16* __restrict__ out) {
    G2P gp;
    const u64 epoch = phase1(gp, x, wq, wk, wv, w_in, w_sp, b_sp, w_out, wo, ws);
    phase2(gp, w_sp, b_sp, x, ws, out, epoch, __builtin_amdgcn_readfirstlane(threadIdx.x >> 6));
}
__global__ void text_end_marker() { asm volatile(".Lk3_end:"); }

extern "C" void kernel_launch(void* const* d_in, const int* in_sizes, int n_in, void* d_out, int out_size, void* d_ws, size_t ws_size, hipStream_t stream) {
    const u16* x = (const u16*)d_in[0]; const u16* wq = (const u16*)d_in[1]; const u16* wk = (const u16*)d_in[2]; const u16* wv = (const u16*)d_in[3];
    const u16* wo = (const u16*)d_in[4]; const u16* w_in = (const u16*)d_in[6]; const u16* w_sp = (const u16*)d_in[7]; const u16* b_sp = (const u16*)d_in[8];
    const u16* w_out = (const u16*)d_in[9]; char* ws = (char*)d_ws;
    (void)hipFuncSetAttribute((const void*)fused_kernel, hipFuncAttributeMaxDynamicSharedMemorySize, LDS_ALL);
    fused_kernel<<<256, 512, LDS_ALL, stream>>>(x, wq, wk, wv, w_in, w_sp, b_sp, w_out, wo, ws, (u16*)d_out);
}
```

```cpp
#include <hip/hip_runtime.h>
#include <hip/hip_bf16.h>
#include <stdint.h>

#define STNOP 1
typedef unsigned short u16;
typedef unsigned long long u64;
typedef short bf16x8 __attribute__((ext_vector_type(8)));
typedef short s16x4 __attribute__((ext_vector_type(4)));
typedef float f32x2 __attribute__((ext_vector_type(2)));
typedef float f32x4 __attribute__((ext_vector_type(4)));
typedef float f32x16 __attribute__((ext_vector_type(16)));
typedef unsigned u32x4 __attribute__((ext_vector_type(4)));
typedef unsigned u32x2 __attribute__((ext_vector_type(2)));
typedef __bf16 bf16x2_t __attribute__((ext_vector_type(2)));
#define LAS __attribute__((address_space(3)))
#define DI __device__ __forceinline__

constexpr int Z1L = 122880, TOPL = 159744, LDS_ALL = 163840;
constexpr int K2_SA = 16384, K2_SB = 18432, K2_SK = 6144, K2_B0 = 3 * K2_SA, K2_K0 = K2_B0 + 3 * K2_SB, K2_X0 = 147520, K2_LDS = K2_X0 + 9344 + 64 + 2048;
constexpr int S_ = 2048, D_ = 576, FH = 2304, F_ = 4608;

constexpr size_t WS_Q = 0;
constexpr size_t WS_KV = WS_Q + (size_t)S_ * D_ * 2;
constexpr size_t WS_Z1 = WS_KV + 64 * 3072;
constexpr size_t WS_Z2T = WS_Z1 + (size_t)S_ * FH * 2;
constexpr size_t WS_UO = WS_Z2T + (size_t)S_ * FH * 2;
constexpr size_t WS_ST = WS_UO + (size_t)S_ * 2880 * 2;
constexpr size_t WS_XCH = WS_ST + (size_t)S_ * 16 * 8;
constexpr size_t WS_XR = WS_XCH + 4096 + 16384;
constexpr size_t WS_FL = WS_XR + 4096;
constexpr size_t WS_FA = WS_FL + 16384;
constexpr size_t WS_FJ = WS_FA + 2048;
constexpr size_t WS_FP = WS_FJ + 8192;
constexpr size_t WS_W3T = WS_FP + 2048;

DI unsigned cvtpk(float lo, float hi) { f32x2 v = {lo, hi}; return __builtin_bit_cast(unsigned, __builtin_convertvector(v, bf16x2_t)); }
DI float bf2f(u16 b) { return __uint_as_float(((unsigned)b) << 16); }
DI float bflo(unsigned w) { return __uint_as_float(w << 16); }
DI float bfhi(unsigned w) { return __uint_as_float(w & 0xffff0000u); }
#define SB() __builtin_amdgcn_sched_barrier(0)
#define PIN(x) asm volatile("" : "+v"(x))
#define EX(x) __builtin_amdgcn_exp2f(x)
#define MFMA16(a, b, c) __builtin_amdgcn_mfma_f32_16x16x32_bf16(a, b, c, 0, 0, 0)
#define MFMA32(a, b, c) __builtin_amdgcn_mfma_f32_32x32x16_bf16(a, b, c, 0, 0, 0)

DI s16x4 trread(const LAS char* p) { return __builtin_bit_cast(s16x4, __builtin_amdgcn_ds_read_tr16_b64_v4i16((LAS s16x4*)p)); }
template <int N> DI void wait_vm_bar() { asm volatile("s_waitcnt vmcnt(%0) lgkmcnt(0)\n\ts_barrier" ::"n"(N) : "memory"); }
template <int N> DI void wait_vm() { asm volatile("s_waitcnt vmcnt(%0)" ::"n"(N) : "memory"); }
template <int OFF> DI void glds_s(unsigned voff, const char* sbase) { asm volatile("global_load_lds_dwordx4 %0, %1 offset:%2" ::"v"(voff), "s"(sbase), "n"(OFF)); }
template <int OFF> DI void glds_sc1(unsigned voff, const char* sbase) { asm volatile("global_load_lds_dwordx4 %0, %1 offset:%2 sc1" ::"v"(voff), "s"(sbase), "n"(OFF)); }
DI void glds_v(const char* p, unsigned m0v, u64 em) { asm volatile("s_mov_b32 m0, %1\n\ts_mov_b64 exec, %2\n\tglobal_load_lds_dwordx4 %0, off\n\ts_mov_b64 exec, -1" ::"v"(p), "s"(m0v), "s"(em)); }
DI unsigned ld_sc1_async(const char* sb, unsigned vo) { unsigned v; asm volatile("global_load_dword %0, %1, %2 sc1" : "=v"(v) : "v"(vo), "s"(sb) : "memory"); return v; }
DI void st16(void* p, u32x4 v) { asm volatile("global_store_dwordx4 %0, %1, off sc1\n\ts_nop 1" :: "v"(p), "v"(v) : "memory"); }
DI void st8(void* p, u32x2 v) { asm volatile("global_store_dwordx2 %0, %1, off sc1" :: "v"(p), "v"(v) : "memory"); }
template <int OFF> DI void st16s(const char* sb, unsigned vo, u32x4 v) { asm volatile("global_store_dwordx4 %0, %1, %2 offset:%3 sc1\n\ts_nop %4" :: "v"(vo), "v"(v), "s"(sb), "n"(OFF), "n"(STNOP) : "memory"); }
template <int OFF> DI void st8s(const char* sb, unsigned vo, u32x2 v) { asm volatile("global_store_dwordx2 %0, %1, %2 offset:%3 sc1" :: "v"(vo), "v"(v), "s"(sb), "n"(OFF) : "memory"); }
DI float fadd1(float a, float b) { float r = a + b; asm("" : "+v"(r)); return r; }
DI float fmul1(float a, float b) { float r = a * b; asm("" : "+v"(r)); return r; }
DI float ffma1(float a, float b, float c) { float r = __builtin_fmaf(a, b, c); asm("" : "+v"(r)); return r; }
DI float lone(float a) { asm("" : "+v"(a)); return a; }
DI void set_m0(unsigned v) { asm volatile("s_mov_b32 m0, %0\n\ts_nop 0" ::"s"(v)); }
DI bf16x8 ldsA(const LAS char* p) { return *(const LAS bf16x8*)p; }
DI bf16x8 ldsB2(const LAS char* p0, const LAS char* p1) { s16x4 lo = trread(p0), hi = trread(p1); return __builtin_shufflevector(lo, hi, 0, 1, 2, 3, 4, 5, 6, 7); }
DI bf16x8 ldsB(const LAS char* p) { return ldsB2(p, p + 256); }

DI float gelu_t(float x) {
    const float c0 = -2.0f * 1.4426950408889634f * 0.7978845608028654f;
    const float c1 = c0 * 0.044715f;
    float x2 = x * x;
    float t = __builtin_fmaf(x2, c1, c0);
    float e = __builtin_amdgcn_exp2f(x * t);
    return x * __builtin_amdgcn_rcpf(1.0f + e);
}
constexpr float QSCALE = 0.23570226039551584f * 1.4426950408889634f;

#define KO1 (3 * (((idx & 7) + (idx >> 3)) % 3))
#define KO2 (((idx & 7) + 2 * (idx >> 3) + 4 * (xcd & 1)) % 9)
#define EPRIO 1
#define PFW 0
#define SPRIO 1
#define HW 3
#define ZSCH 5
#define ZS0 1
#define GPRIO 1
#define WSTR 32
#define WOFF 0
#define W2STR 0
#define PFP 1
#define PFN 3
#define GOFF 0
#define AOFF 0
#define CAL(n) asm volatile(".p2alignl 6, 3212836864\n\t.fill %0, 4, 3212836864" :: "n"(n))
#define TFA 7
#define PW2 3
#define POLLSLEEP 0
#define APRIO 0
#define ZHALF 1
#define POLLW 5
#define POLLSLEEP1 1
constexpr int K1_SA = 16384, K1_SB = 24576, K1_B0 = 3 * K1_SA, K1_TAB = K1_B0 + 3 * K1_SB, K1_X0 = K1_TAB + 10240, K1_LDS = K1_X0 + 16384 + 2048;
typedef __attribute__((address_space(1))) unsigned gu32;
typedef __attribute__((address_space(1))) unsigned long long gu64;
#define RLX_AGENT __ATOMIC_RELAXED, __HIP_MEMORY_SCOPE_AGENT
struct K1F { bf16x8 a[4], b[3], x; };
struct K1S {
    unsigned va[2], vb[2]; const char* sa; const char* sb;
    const char* gp; unsigned st;
    int k1, k2;
    unsigned m0a, m0b; u64 em;
    const LAS char* pa[2]; const LAS char* px[2]; const LAS char* pb[3];
};
DI void k1_issue(K1S& s, int i, int w) {
    if (i == 0) { set_m0(s.m0a + w * K1_SA); glds_s<0>(s.va[0], s.sa); }
    if (i == 1) { glds_s<1024>(s.va[1], s.sa); s.sa += s.k1 == 8 ? -8 * 128 : 128; asm volatile("" : "+s"(s.sa)); }
    if (i == 2) { glds_v(s.gp + (unsigned long long)s.st * (unsigned)s.k1, s.m0b + w * K1_SB + 2048, s.em); s.k1 = s.k1 == 8 ? 0 : s.k1 + 1; asm volatile("" : "+s"(s.k1)); }
    if (i == 3) { set_m0(s.m0b + w * K1_SB); glds_s<0>(s.vb[0], s.sb); }
    if (i == 4) { glds_s<1024>(s.vb[1], s.sb); s.sb += s.k2 == 8 ? -8 * 128 * F_ : 128 * F_; s.k2 = s.k2 == 8 ? 0 : s.k2 + 1; asm volatile("" : "+s"(s.sb), "+s"(s.k2)); }
}
DI f32x2 gelu_y2(f32x2 y, float k0, float k1, float ir) {
    f32x2 t = (y * y) * k1 + k0;
    f32x2 a = y * t;
    f32x2 e = {__builtin_amdgcn_exp2f(a[0]), __builtin_amdgcn_exp2f(a[1])};
    f32x2 d = e * ir + ir;
    f32x2 rc = {__builtin_amdgcn_rcpf(d[0]), __builtin_amdgcn_rcpf(d[1])};
    return y * rc;
}
DI float dpp_x1(float x) { return __int_as_float(__builtin_amdgcn_mov_dpp(__float_as_int(x), 0xB1, 0xf, 0xf, true)); }
DI float dpp_x2(float x) { return __int_as_float(__builtin_amdgcn_mov_dpp(__float_as_int(x), 0x4E, 0xf, 0xf, true)); }
DI void kv_store(const char* kv, int sv, int j, int c, int g, f32x4 v) {
    if (j == 4) { st8s<0>(kv + sv * 256, ((g >> 1) * 32 + c) * 16 + (g & 1) * 8, u32x2{cvtpk(v[0], v[1]), cvtpk(v[2], v[3])}); return; }
    const unsigned kk = cvtpk(v[2], v[3]);
    const bool o1 = c & 1, o2 = c & 2;
    float t0 = dpp_x1(o1 ? v[0] : v[1]), t1 = dpp_x1(o1 ? v[2] : v[3]);
    if (o1) { v[0] = t0; v[2] = t1; } else { v[1] = t0; v[3] = t1; }
    t0 = dpp_x2(o2 ? v[0] : v[2]); t1 = dpp_x2(o2 ? v[1] : v[3]);
    if (o2) { v[0] = t0; v[1] = t1; } else { v[2] = t0; v[3] = t1; }
    u32x2 w = {cvtpk(v[0], v[1]), cvtpk(v[2], v[3])};
    const int kq = c >> 2; unsigned off;
    if (j == 5) {
        off = g < 2 ? c * 16 + (g ^ 1) * 8 : 256 + ((kq & 1) * 8 + 4 * (g - 2) + (c & 3)) * 16 + (kq >> 1) * 8;
        if (g < 2) w = g ? u32x2{kk, 0x3f80u} : u32x2{0u, 0u};
    } else {
        off = 512 + ((kq & 1) * 16 + 4 * g + (c & 3)) * 16 + (kq >> 1) * 8;
        if (g == 3 && !o2) { const unsigned cv = o1 ? 0u : 0x3f803f80u; w = u32x2{cv, cv}; }
    }
    st8s<1024>(kv + sv * 1024, off, w);
}
template <int KS, int GL, int RS, int WS, bool RX> DI void k1_p1(K1S& s, const K1F& f, K1F& n, f32x4 (&acc)[4][2], f32x4& sacc, f32x4 (&cso)[2], f32x4& rs, f32x4& rq, const bf16x8& ones) {
    constexpr int AO = RS * K1_SA, BO = RS * K1_SB + KS * 12288;
#define MX(d, a_, b_) d = MFMA16(a_, b_, d); PIN(d); SB()
#define MM(m, nn) MX(acc[m][nn], f.b[nn], f.a[m])
    MM(0, 0); n.b[0] = ldsB(s.pb[0] + BO); SB();
    MM(1, 0); n.a[0] = ldsA(s.pa[KS] + AO); SB();
    MM(2, 0); n.a[1] = ldsA(s.pa[KS] + AO + 2048); SB();
    MM(3, 0); n.a[2] = ldsA(s.pa[KS] + AO + 4096); SB();
    MX(cso[0], f.b[0], ones); n.a[3] = ldsA(s.pa[KS] + AO + 6144); SB();
    MX(sacc, f.b[2], f.x); n.b[1] = ldsB(s.pb[1] + BO); SB();
    MX(cso[1], f.b[2], ones); n.b[2] = ldsB(s.pb[2] + BO); SB();
    MX(rs, ones, f.x); k1_issue(s, GL == 1 ? 0 : 3, WS); SB();
    MX(rq, f.x, f.x); if (RX) n.x = ldsA(s.px[KS] + AO); SB();
    MM(0, 1); k1_issue(s, GL == 1 ? 1 : 4, WS); SB();
    MM(1, 1); if (GL == 1) k1_issue(s, 2, WS); SB();
    MM(2, 1);
    MM(3, 1);
}
template <int KS, int GL, int RS, int WS, bool RD, class E> DI void k1_p2(K1S& s, const K1F& f, K1F& n, f32x4 (&acc)[4][3], f32x4 (&cso)[2], const bf16x8& ones, E epi) {
    constexpr int AO = RS * K1_SA, BO = RS * K1_SB + KS * 12288;
    MM(0, 0); if (RD) n.b[0] = ldsB(s.pb[0] + BO); epi(0); SB();
    MM(1, 0); if (RD) n.a[0] = ldsA(s.pa[KS] + AO); epi(1); SB();
    MM(2, 0); if (RD) n.a[1] = ldsA(s.pa[KS] + AO + 2048); epi(2); SB();
    MM(3, 0); if (RD) n.a[2] = ldsA(s.pa[KS] + AO + 4096); epi(3); SB();
    MX(cso[0], f.b[0], ones); if (RD) n.a[3] = ldsA(s.pa[KS] + AO + 6144); epi(4); SB();
    MM(0, 1); if (RD) n.b[1] = ldsB(s.pb[1] + BO); epi(5); SB();
    MM(1, 1); if (RD) n.b[2] = ldsB(s.pb[2] + BO); epi(6); SB();
    MM(2, 1); if (GL) k1_issue(s, GL == 1 ? 0 : 3, WS); epi(7); SB();
    MM(3, 1); epi(8); SB();
    MX(cso[1], f.b[1], ones); if (GL) k1_issue(s, GL == 1 ? 1 : 4, WS); epi(9); SB();
    MM(0, 2); epi(10); SB();
    MM(1, 2); if (GL == 1) k1_issue(s, 2, WS); epi(11); SB();
    MM(2, 2); epi(12); SB();
    MM(3, 2); epi(13); SB();
#undef MM
#undef MX
}

constexpr unsigned XSALT = 0x9e3779b9u;
DI void epoch_reply(char* ws, int lane, const unsigned (&nv)[4], u64 epoch) {
    const unsigned lo = (unsigned)epoch, hi = (unsigned)(epoch >> 32);
    st16s<0>(ws + WS_XR, lane * 16, u32x4{nv[0], lo, hi, nv[0] ^ lo ^ hi ^ XSALT}); st16s<1024>(ws + WS_XR, lane * 16, u32x4{nv[1], lo, hi, nv[1] ^ lo ^ hi ^ XSALT});
    st16s<2048>(ws + WS_XR, lane * 16, u32x4{nv[2], lo, hi, nv[2] ^ lo ^ hi ^ XSALT}); st16s<3072>(ws + WS_XR, lane * 16, u32x4{nv[3], lo, hi, nv[3] ^ lo ^ hi ^ XSALT});
}
#define XN_LOADS "global_load_dword %0, %4, %5 sc1\n\tglobal_load_dword %1, %4, %5 offset:256 sc1\n\tglobal_load_dword %2, %4, %5 offset:512 sc1\n\tglobal_load_dword %3, %4, %5 offset:768 sc1\n"
DI void epoch_service(char* ws, int lane, u64 epoch) {
    unsigned nv[4];
    asm volatile(XN_LOADS "\ts_waitcnt vmcnt(0)" : "=&v"(nv[0]), "=&v"(nv[1]), "=&v"(nv[2]), "=&v"(nv[3]) : "v"(lane * 4), "s"(ws + WS_XCH) : "memory");
    epoch_reply(ws, lane, nv, epoch);
}
DI void poll_flags(char* ws, u64 epoch, int lane, const char* sb, unsigned vo) {
    const u64 t0 = __builtin_amdgcn_s_memrealtime();
    for (;;) {
        u64 f_;
        asm volatile("global_load_dwordx2 %0, %1, %2 sc1\n\ts_waitcnt vmcnt(0)" : "=&v"(f_) : "v"(vo), "s"(sb) : "memory");
        if (__all(f_ == epoch)) break;
        const u64 dt = __builtin_amdgcn_s_memrealtime() - t0;
        if (dt > 5000000ull) break;
        if (dt > 200 && blockIdx.x == 0) epoch_service(ws, lane, epoch);
        __builtin_amdgcn_s_sleep(POLLSLEEP1);
    }
}
DI void first_loads(char* ws, unsigned lds0, int lane, int wid, int j) {
    const unsigned m0 = lds0 + K2_B0 + wid * 6144, vl = lane * 16;
    const char* sl = ws + WS_Z2T + j * 32 * 18432 + wid * 6144;
#pragma unroll
    for (int t = 0; t < 2; ++t) {
        set_m0(m0 + t * K2_SB); glds_sc1<0>(vl, sl); glds_sc1<1024>(vl, sl); glds_sc1<2048>(vl, sl); glds_sc1<3072>(vl, sl);
        set_m0(m0 + t * K2_SB + 4096); glds_sc1<0>(vl, sl + 4096); glds_sc1<1024>(vl, sl + 4096); sl += K2_SB;
    }
}
DI unsigned g2_va(int wid, int lane, int i) { int row = 8 * (wid * 4 + i) + (lane >> 3), c = (lane & 7) ^ ((row >> 1) & 7); return row * (S_ * 2) + c * 16 + 4096 - i * 1024; }
struct G2P { unsigned va[4]; const LAS char* pa[2][2]; const LAS char* pb[2]; const LAS char* ps[2]; f32x4 acc[4][4], sacc[2]; };
DI void g2_prep(G2P& s, const LAS char* lds, int lane, int wid) {
    const int wr = wid >> 1, wc = wid & 1, r = lane & 15, g = lane >> 4, q = r >> 2, p = lane & 3;
#pragma unroll
    for (int m = 0; m < 4; ++m) {
        const float b = bf2f(*(const LAS u16*)(lds + TOPL + 2304 + (wr * 64 + (m ^ (2 * wc)) * 16 + r) * 2));
#pragma unroll
        for (int n = 0; n < 4; ++n) { s.acc[m][n] = f32x4{b, b, b, b}; PIN(s.acc[m][n]); }
        if (m < 2) { s.sacc[m] = f32x4{b, b, b, b}; PIN(s.sacc[m]); }
    }
#pragma unroll
    for (int i = 0; i < 4; ++i) { s.va[i] = g2_va(wid, lane, i); PIN(s.va[i]); }
#pragma unroll
    for (int ks = 0; ks < 2; ++ks) {
        const int o = r * 128 + (((4 * ks + g) ^ (r >> 1)) * 16) + wr * 8192;
        s.pa[0][ks] = lds + o + wc * 4096; s.pa[1][ks] = lds + o + (1 - wc) * 4096; PIN(s.pa[0][ks]); PIN(s.pa[1][ks]);
    }
    const int ob = K2_B0 + g * 2304 + q * 32 + 8 * p;
    s.pb[0] = lds + ob + wc * 1024 + ((g & 1) * 128); s.pb[1] = lds + ob + wc * 1024 + (((g & 1) ^ 1) * 128);
    s.ps[0] = lds + ob + 2048 + ((g & 1) * 128); s.ps[1] = lds + ob + 2048 + (((g & 1) ^ 1) * 128);
    PIN(s.pb[0]); PIN(s.pb[1]); PIN(s.ps[0]); PIN(s.ps[1]);
}
DI u64 phase1(G2P& gp, const u16* __restrict__ x, const u16* __restrict__ wq, const u16* __restrict__ wk, const u16* __restrict__ wv,
                                                    const u16* __restrict__ w_in, const u16* __restrict__ w_sp, const u16* __restrict__ b_sp, const u16* __restrict__ w_out, const u16* __restrict__ wo, char* __restrict__ ws) {
    extern __shared__ __attribute__((aligned(16))) char smem[];
    const int bid = blockIdx.x, xcd = bid & 7, idx = bid >> 3;
    const int pm = (xcd & 1) * 8 + (idx & 7), j = (xcd >> 1) * 4 + (idx >> 3);
    const int lane = threadIdx.x & 63, wid = __builtin_amdgcn_readfirstlane(threadIdx.x >> 6), wr = wid >> 2, wc = wid & 3;
    const unsigned lds0 = (unsigned)(uintptr_t)smem;
    const LAS char* lds = (const LAS char*)smem;
    {
        const char* pc; unsigned len;
        asm volatile("s_getpc_b64 %0\n.Lk1_here:\n\ts_mov_b32 %1, .Lk3_end-.Lk1_here" : "=s"(pc), "=s"(len));
        unsigned off = ((idx * 8 + wid) * 2 + (lane & 1)) * 128; off = off < len ? off : len;
        asm volatile("s_mov_b32 m0, %2\n\ts_nop 0\n\tglobal_load_lds_dword %0, %1" ::"v"(off), "s"(pc), "s"(lds0 + K1_TAB + 9216 + 512));
    }
    K1S s; s.em = (wid & 1) ? 0xccccccccull : 0x33333333ull;
    s.m0a = lds0 + wid * 2048; s.m0b = lds0 + K1_B0 + wid * 3072;
    const int brow = (lane >> 2) & 7, bc = (lane & 3) ^ (2 * (wid & 1)), bk = 8 * wid + brow;
    const int ko2 = KO2, ko1 = KO1;
    unsigned oa = pm * 128 * D_ * 2, ob = 256 * j, oa1 = oa + ko1 * 128, ob1 = ob + FH * 2 + ko1 * 128 * F_, vg = bk * (F_ * 2) + 16 * (bc & 1);
    {
        int r8 = lane >> 3;
        s.va[0] = (16 * wid + r8) * (D_ * 2) + (((lane & 7) ^ (r8 >> 1)) * 16);
        s.va[1] = (16 * wid + 8 + r8) * (D_ * 2) + (((lane & 7) ^ (4 + (r8 >> 1))) * 16) - 1024;
        s.vb[0] = bk * (F_ * 2) + (lane >> 5) * 64 + bc * 16 + 1024; s.vb[1] = s.vb[0] + 128 - 1024;
        s.k1 = s.k2 = ko1; s.st = 128 * F_;
        PIN(s.va[0]); PIN(s.va[1]); PIN(s.vb[0]); PIN(s.vb[1]); PIN(vg);
        asm volatile("" : "+s"(oa1), "+s"(ob1), "+s"(s.m0a), "+s"(s.m0b));
    }
    SB();
    s.sa = (const char*)x + oa1; s.sb = (const char*)w_in - 1024 + ob1;
    k1_issue(s, 0, 0); k1_issue(s, 1, 0); k1_issue(s, 3, 0); k1_issue(s, 4, 0);
    SB();
    s.gp = (const char*)w_in + (4096 + 32 * j + FH * 2) + vg;
    k1_issue(s, 2, 0);
    const char* sa0 = (const char*)x + oa;
    const char* sb0 = (const char*)w_in - 1024 + ob;
    gu32* XN = (gu32*)(ws + WS_XCH) + pm * 16;
    gu32* XE = (gu32*)(ws + WS_XCH + 4096) + pm * 256;
    gu64* XP = (gu64*)(ws + WS_ST) + (size_t)pm * 16 * 128;
    const char* xe_s = ws + WS_XCH + 4096 + (pm * 256 + j * 16) * 4;
    const unsigned le = (lane & 16) ? 16384 - (pm * 16 + j) * 48 + (lane & 3) * 4 : (lane & 15) * 4;
    unsigned eo = ld_sc1_async(xe_s, le);
    if (SPRIO == 1 && wid >= 4) __builtin_amdgcn_s_setprio(1);
    SB();
    unsigned nonce = 0;
    const char* gp2; unsigned st2;
    {
        const u16* p; int rs;
        const u16* sp0 = j < 4 ? wq + 512 + 16 * j : (j == 5 ? wk + 10 : (j == 6 ? wv + 8 : wk));
        const u16* sp1 = j < 4 ? wq + 520 + 16 * j : (j == 5 ? wv : (j == 6 ? wv + 10 : wk + 8));
        const int srs = j < 4 ? D_ : 18;
        if (lane >= 32) { p = wq + 32 * j + 8 * bc; rs = D_; }
        else if (bc < 2) { p = w_in + 2048 + 16 * j + 8 * bc; rs = F_; }
        else { p = bc == 2 ? sp0 : sp1; rs = srs; }
        gp2 = (const char*)(p + (size_t)bk * rs); st2 = 128 * rs; PIN(gp2); PIN(st2);
    }
    const int r16 = lane & 15, g = lane >> 4, cg = wc ^ (2 * wr);
    const int nf3 = cg == 3 ? 8 : (cg == 2 ? 9 : 10 + cg);
#define NF1(n) ((n) == 2 ? 8 : 2 * wc + ((n) ^ wr))
#define NF2(n) ((n) == 0 ? nf3 : 2 * cg + (((n) - 1) ^ wr))
    const LAS char* pb2[3];
    {
        int q = r16 >> 2, p = lane & 3;
#pragma unroll
        for (int ks = 0; ks < 2; ++ks) { s.pa[ks] = lds + (r16 * 128 + (((4 * ks + g) ^ (r16 >> 1)) * 16) + wr * 8192); s.px[ks] = s.pa[ks] + wc * 2048; PIN(s.pa[ks]); PIN(s.px[ks]); }
#pragma unroll
        for (int n = 0; n < 3; ++n) {
            int nf = NF1(n); s.pb[n] = lds + (K1_B0 + g * 3072 + q * 64 + 8 * p + (nf >> 1) * 512 + (((nf ^ g) & 1) * 32)); PIN(s.pb[n]);
            nf = NF2(n); pb2[n] = lds + (K1_B0 + g * 3072 + q * 64 + 8 * p + (nf >> 1) * 512 + (((nf ^ g) & 1) * 32)); PIN(pb2[n]);
        }
    }
    f32x4 acc1[4][2] = {}; f32x4 sacc = {}; f32x4 cso[2] = {};
    f32x4 rs = {}, rq = {};
#pragma unroll
    for (int m = 0; m < 4; ++m) { PIN(acc1[m][0]); PIN(acc1[m][1]); }
    PIN(sacc); PIN(cso[0]); PIN(cso[1]); PIN(rs); PIN(rq);
    const u32x4 onesv = {0x3f803f80u, 0x3f803f80u, 0x3f803f80u, 0x3f803f80u}; bf16x8 ones = __builtin_bit_cast(bf16x8, onesv); PIN(ones);
    K1F f0, f1;
    SB();
#pragma unroll
    for (int i = 0; i < 5; ++i) k1_issue(s, i, 1);
    SB();
    if (SPRIO == 2 && wid >= 4) __builtin_amdgcn_s_setprio(1);
    const int w3p = bid < 240 ? bid : 239;
    unsigned w3vo[2], w3so[2]; const char* w3sp; const LAS char* w3rd[2];
    {
        const int jj = w3p / 30, pc = w3p - jj * 30;
        w3sp = (pc < 24 ? (const char*)w_out : (const char*)wo - 2304 * 1152) + jj * 144;
#pragma unroll
        for (int i = 0; i < 2; ++i) {
            unsigned c_ = threadIdx.x + 512 * i; c_ = c_ < 864 ? c_ : 863;
            const unsigned row = (c_ * 7282) >> 16, k_ = pc * 96 + row, jg = (k_ * 29128) >> 22, l_ = k_ - jg * 144;
            const unsigned src = pc < 24 ? (l_ < 128 ? 128 * jg + l_ : 1920 + 16 * jg + l_) : k_;
            w3vo[i] = src * 1152 + (c_ - row * 9) * 16; PIN(w3vo[i]);
            const int u = i ? (wid < 7 ? wid + 8 : 14) : wid, k = u / 5, n = u - 5 * k;
            w3rd[i] = lds + (K1_X0 + (8 * g + (r16 >> 2)) * 144 + 8 * (lane & 3) + k * 4608 + n * 32); PIN(w3rd[i]);
            w3so[i] = w3p * 13824 + k * 4608 + n * 1024 + (n < 4 ? lane : g * 8 + r16) * 16;
            if (n == 4 && r16 >= 8) w3so[i] = 240 * 13824 + bid * 512 + (g * 8 + r16 - 8) * 16;
            PIN(w3so[i]);
        }
        asm volatile("" : "+s"(w3sp));
    }
    unsigned wspo = (pm * 128 + (idx >> 3) * 32 + wid * 4 + g) * (S_ * 2) + r16 * WSTR + WOFF; PIN(wspo);
    LAS float* redp = (LAS float*)(lds + K1_TAB + 4096) + ((wr * 64 + r16) * 5 + wc) * 2 + (g & 1); PIN(redp);
    LAS float* reds = (LAS float*)(lds + K1_TAB + 4096) + ((wr * 64 + wc * 16 + r16) * 5 + 4) * 2 + (g & 1); PIN(reds);
    SB();
#pragma unroll
    for (int i = 0; i < 3; ++i) k1_issue(s, i, 2);
    __builtin_amdgcn_s_setprio(0);
    SB();
    wait_vm_bar<8>();
    SB();
    f0.b[0] = ldsB(s.pb[0]);
    f0.a[0] = ldsA(s.pa[0]); f0.a[1] = ldsA(s.pa[0] + 2048); f0.a[2] = ldsA(s.pa[0] + 4096); f0.a[3] = ldsA(s.pa[0] + 6144);
    f0.b[2] = ldsB(s.pb[2]); f0.x = ldsA(s.px[0]); f0.b[1] = ldsB(s.pb[1]);
    SB();
    PIN(eo);
    if (wid == HW) {
        nonce = 1;
        while (__ballot(eo == nonce) != 0ull) ++nonce;
        if (lane == 0) __hip_atomic_store(XN + j, nonce, RLX_AGENT);
        __hip_atomic_store(XP + j * 128 + lane, ~0ull, RLX_AGENT); __hip_atomic_store(XP + j * 128 + 64 + lane, ~0ull, RLX_AGENT);
    }
#define P1_A(c) k1_p1<1, 2, (c), ((c) + 2) % 3, true>(s, f0, f1, acc1, sacc, cso, rs, rq, ones); wait_vm_bar<5>(); SB()
#define P1_B(c, RX) k1_p1<0, 1, ((c) + 1) % 3, (c), RX>(s, f1, f0, acc1, sacc, cso, rs, rq, ones)
#pragma unroll 1
    for (int it = 0; it < 2; ++it) { P1_A(0); P1_B(0, true); P1_A(1); P1_B(1, true); P1_A(2); P1_B(2, true); }
    const char* xn_s = ws + WS_XCH + pm * 64; const unsigned l4 = (lane & 15) * 4;
    P1_A(0);
    unsigned echoed = ld_sc1_async(xn_s, l4);
    unsigned alln[4];
    asm volatile("s_cmp_lg_u32 %6, 0\n\ts_cbranch_scc1 .Lld%=\n\t" XN_LOADS ".Lld%=:" : "=&v"(alln[0]), "=&v"(alln[1]), "=&v"(alln[2]), "=&v"(alln[3])
                 : "v"(lane * 4), "s"(ws + WS_XCH), "s"(bid) : "memory", "scc");
    s.k1 = s.k2 = ko2; s.sa = sa0 + ko2 * 128; s.sb = sb0 + (size_t)ko2 * 128 * F_; s.gp = gp2; s.st = st2; s.em = ~0ull; SB();
    P1_B(0, true);
    P1_A(1);
    PIN(echoed);
    if (wid == HW && lane < 16) __hip_atomic_store(XE + lane * 16 + j, echoed, RLX_AGENT);
    PIN(alln[0]); PIN(alln[1]); PIN(alln[2]); PIN(alln[3]);
    if (bid == 0 && wid == HW - 1) epoch_reply(ws, lane, alln, __builtin_amdgcn_s_memrealtime() | (1ull << 63));
    SB();
    P1_B(1, true);
    P1_A(2);
    s.pb[0] = pb2[0]; s.pb[1] = pb2[1]; s.pb[2] = pb2[2];
    P1_B(2, false);
    float* CS = (float*)(smem + K1_TAB);
    f32x4* RS = (f32x4*)(smem + K1_TAB + 2048);
    float* red = (float*)(smem + K1_TAB + 4096);
    f32x2* ZS = (f32x2*)(smem + K1_TAB + 9216);
    volatile LAS unsigned* HS = (volatile LAS unsigned*)(lds + K1_TAB + 1792);
    const float c0 = -2.0f * 1.4426950408889634f * 0.7978845608028654f, c1 = c0 * 0.044715f;
    f32x4 acc2[4][3] = {}; f32x4 cs2[2] = {};
    auto none = [](int) {};
    auto tabs = [&](int st) {
        if (st == 2) { if (r16 == 0) { *(f32x4*)(CS + NF1(0) * 16 + 4 * g) = cso[0]; *(f32x4*)(CS + 128 + 4 * g) = cso[1]; } }
        if (st == 3) {
            float sq = (r16 & 2) ? ((r16 & 1) ? rq[3] : rq[2]) : ((r16 & 1) ? rq[1] : rq[0]);
            float r0_ = rs[0]; PIN(r0_); PIN(sq);
            float mean = fmul1(r0_, 1.0f / D_), var = fmaxf(ffma1(-mean, mean, fmul1(sq, 1.0f / D_)), 0.f);
            rs[0] = mean; rs[1] = var + 1e-5f; PIN(rs);
        }
        if (st == 5) { rs[2] = __builtin_amdgcn_rsqf(rs[1]); PIN(rs); }
        if (st == 7) { float rstd = rs[2]; if (g == (r16 >> 2)) RS[wr * 64 + wc * 16 + r16] = f32x4{rs[0], fmul1(rstd, c0), fmul1(fmul1(rstd, rstd), fmul1(rstd, c1)), fmul1(rs[1], rstd)}; }
    };
    f32x4 er[5], cs1[3], e[3];
    f32x2 su[5], sq[5];
#define ZU(u) ((u) == 8 ? sacc : acc1[((u) >> 1) & 3][(u) & 1])
#define UM(u) ((u) == 8 ? 4 : (u) >> 1)
#define UN(u) ((u) == 8 ? 2 : (u) & 1)
#define PRE(i, u) { f32x4 y = ZU(u) - cs1[UN(u)] * er[UM(u)][0]; e[i] = ((y * y) * er[UM(u)][2] + er[UM(u)][1]) * y; ZU(u) = y; PIN(ZU(u)); PIN(e[i]); }
#define FIN(i, u, first) { ZU(u) *= e[i]; PIN(ZU(u)); f32x4 v = ZU(u); f32x2 lo = {v[0], v[1]}, hi = {v[2], v[3]}; \
        if (first) { su[UM(u)] = lo + hi; sq[UM(u)] = lo * lo + hi * hi; } else { su[UM(u)] += lo; su[UM(u)] += hi; sq[UM(u)] += lo * lo; sq[UM(u)] += hi * hi; } PIN(su[UM(u)]); PIN(sq[UM(u)]); }
#define RED(m) { float a_ = su[m][0] + su[m][1], b_ = sq[m][0] + sq[m][1]; \
            auto x1 = __builtin_amdgcn_permlane16_swap(__float_as_uint(a_), __float_as_uint(b_), false, false);       \
            float t_ = __uint_as_float(x1[0]) + __uint_as_float(x1[1]);                                               \
            auto x2 = __builtin_amdgcn_permlane32_swap(__float_as_uint(t_), __float_as_uint(t_), false, false); \
            t_ = __uint_as_float(x2[0]) + __uint_as_float(x2[1]);                                                     \
            if ((m) < 4) redp[(m) * 160] = t_; else reds[0] = t_; }
#define T1(op, v) asm volatile(op " %0, %0" : "+v"(v))
#define DE1(v, sd) asm volatile("v_fma_f32 %0, %0, %1, %1" : "+v"(v) : "v"(sd))
#define EPI_E(u0, u1, u2) [&](int st) { \
        if (st < 12) { float t_ = e[st >> 2][st & 3]; T1("v_exp_f32", t_); e[st >> 2][st & 3] = t_; } \
        if (st >= 2) { const int v_ = st - 2; float t_ = e[v_ >> 2][v_ & 3]; DE1(t_, er[v_ < 4 ? UM(u0) : (v_ < 8 ? UM(u1) : UM(u2))][3]); e[v_ >> 2][v_ & 3] = t_; } }
#define EPI_R(m0, m1) [&](int st) { if (st < 12) { float t_ = e[st >> 2][st & 3]; T1("v_rcp_f32", t_); e[st >> 2][st & 3] = t_; } \
        if (st == 12 && (m0) >= 0) er[(m0) & 3] = RS[wr * 64 + (m0) * 16 + r16]; if (st == 13 && (m1) >= 0) er[(m1) & 3] = RS[wr * 64 + (m1) * 16 + r16]; }
#define VSYNC() SB(); asm volatile("s_waitcnt lgkmcnt(0)\n\ts_barrier" ::: "memory"); SB()
#define P2_A(c, GL, E) k1_p2<1, GL ? 2 : 0, (c), ((c) + 2) % 3, true>(s, f0, f1, acc2, cs2, ones, E)
#define P2_B(c, GL, RD, E) k1_p2<0, GL ? 1 : 0, ((c) + 1) % 3, (c), RD>(s, f1, f0, acc2, cs2, ones, E)
    P2_A(0, true, tabs); wait_vm_bar<5>(); SB();
    er[0] = RS[wr * 64 + r16]; er[4] = RS[wr * 64 + wc * 16 + r16];
#pragma unroll
    for (int n = 0; n < 3; ++n) cs1[n] = *(const f32x4*)(CS + NF1(n) * 16 + 4 * g);
    PRE(0, 0) PRE(1, 1) PRE(2, 8) VSYNC();
    P2_B(0, true, true, EPI_E(0, 1, 8));
    P2_A(1, true, EPI_R(1, 2)); wait_vm_bar<5>(); SB();
    unsigned ev = ld_sc1_async(xe_s, le);
    auto w3ld = [&](int i) { SB(); set_m0(lds0 + K1_X0 + i * 8192 + wid * 1024); glds_s<0>(w3vo[i], w3sp); SB(); };
    w3ld(0); FIN(0, 0, true) FIN(1, 1, false) FIN(2, 8, true) PRE(0, 2) PRE(1, 3) PRE(2, 4) w3ld(1); RED(0) RED(4) VSYNC();
    P2_B(1, true, true, EPI_E(2, 3, 4));
    P2_A(2, true, EPI_R(3, -1)); wait_vm_bar<7>(); SB();
    PIN(ev);
    bool hs = false; u64 epoch = 0;
    auto xcheck = [&]() {
        hs = hs || __all((lane & 16) || ev == nonce);
        const unsigned w0 = __builtin_amdgcn_readlane(ev, 16), w1 = __builtin_amdgcn_readlane(ev, 17), w2 = __builtin_amdgcn_readlane(ev, 18), w3 = __builtin_amdgcn_readlane(ev, 19);
        if (w0 == nonce && w3 == (w0 ^ w1 ^ w2 ^ XSALT)) epoch = ((u64)w2 << 32) | w1;
    };
    unsigned nn = ld_sc1_async(xn_s, l4);
    asm volatile("s_mov_b32 m0, %2\n\ts_nop 0\n\tglobal_load_lds_dword %0, %1" ::"v"(wspo), "s"(w_sp), "s"(lds0 + K1_X0 + 16384));
    SB(); FIN(0, 2, true) FIN(1, 3, false) FIN(2, 4, true) PRE(0, 5) PRE(1, 6) PRE(2, 7) SB();
    asm volatile("s_mov_b32 m0, %2\n\ts_nop 0\n\tglobal_load_lds_dword %0, %1" ::"v"(lane * 4), "s"(b_sp + pm * 128), "s"(lds0 + TOPL + 2304));
    SB(); RED(1) VSYNC();
    P2_B(2, true, true, EPI_E(5, 6, 7));
    P2_A(0, true, EPI_R(-1, -1)); wait_vm_bar<8>(); SB();
    {
        const bf16x8 w0 = ldsB2(w3rd[0], w3rd[0] + 576), w1 = ldsB2(w3rd[1], w3rd[1] + 576);
        SB(); FIN(0, 5, false) FIN(1, 6, true) FIN(2, 7, false) SB();
        st16s<0>(ws + WS_W3T, w3so[0], __builtin_bit_cast(u32x4, w0));
        SB(); RED(2) RED(3) SB();
        st16s<0>(ws + WS_W3T, w3so[1], __builtin_bit_cast(u32x4, w1));
        VSYNC();
    }
    f32x2 pu[5];
    auto pub = [&](int st) {
        if (st == 0) {
#pragma unroll
            for (int i = 0; i < 5; ++i) pu[i] = *(const f32x2*)(red + (wid * 16 + r16) * 10 + 2 * i);
        }
        if (st == 4) {
            float a = fadd1(fadd1(fadd1(lone(pu[0][0]), pu[1][0]), fadd1(lone(pu[2][0]), pu[3][0])), pu[4][0]), b = fadd1(fadd1(fadd1(pu[0][1], pu[1][1]), fadd1(pu[2][1], pu[3][1])), pu[4][1]);
            unsigned bb = __float_as_uint(b); if (bb == 0xffffffffu) bb = 0x7fc00000u;
            st8s<0>((const char*)(XP + j * 128 + wid * 16), r16 * 8, u32x2{__float_as_uint(a), bb});
        }
        if (st == 13 && wid == HW) { xcheck(); *HS = hs; }
    };
    P2_B(0, true, true, pub);
    P2_A(1, true, none);
    wait_vm_bar<8>(); SB();
    ev = ld_sc1_async(xe_s, le);
    auto reecho = [&](int st) { if (st == 2) { PIN(nn); if (wid == HW && nn != echoed) { echoed = nn; if (lane < 16) __hip_atomic_store(XE + lane * 16 + j, nn, RLX_AGENT); } } };
    P2_B(1, true, true, reecho);
    const char* xp_s = ws + WS_ST + (size_t)pm * 16384 + wid * 128; const unsigned xp_v = g * 4096 + r16 * 8;
    u32x2 pv[4];
#define PLOAD() asm volatile("global_load_dwordx2 %0, %4, %5 sc1\n\tglobal_load_dwordx2 %1, %4, %5 offset:1024 sc1\n\tglobal_load_dwordx2 %2, %4, %5 offset:2048 sc1\n\tglobal_load_dwordx2 %3, %4, %5 offset:3072 sc1" \
        : "=&v"(pv[0]), "=&v"(pv[1]), "=&v"(pv[2]), "=&v"(pv[3]) : "v"(xp_v), "s"(xp_s) : "memory")
    auto pload = [&](int st) { if (st >= 10) asm volatile("global_load_dwordx2 %0, %1, %2 offset:%3 sc1" : "=&v"(pv[(st - 10) & 3]) : "v"(xp_v), "s"(xp_s), "n"(((st - 10) & 3) * 1024) : "memory"); };
    P2_A(2, true, pload); wait_vm_bar<9>(); SB();
#define PEMPTY() __any(pv[0][1] == 0xffffffffu || pv[1][1] == 0xffffffffu || pv[2][1] == 0xffffffffu || pv[3][1] == 0xffffffffu)
    const unsigned hsf = *HS;
    P2_B(2, true, true, [&](int st) { if (st == 13) { PIN(ev); if (wid == HW) xcheck(); } });
    float za, zb;
    auto zstat = [&](int st) {
        if (st == ZS0) {
            wait_vm<3>();
            PIN(pv[0]); PIN(pv[1]); PIN(pv[2]); PIN(pv[3]);
            if (__builtin_expect(hsf != 1 || PEMPTY(), 0)) {
                const unsigned long long t0 = __builtin_amdgcn_s_memrealtime();
                if (wid == HW && hsf != 1) {
                    do {
                        ev = ld_sc1_async(xe_s, le); wait_vm<0>(); PIN(ev);
                        xcheck();
                        nn = __hip_atomic_load(XN + (lane & 15), RLX_AGENT);
                        if (nn != echoed) { echoed = nn; if (lane < 16) __hip_atomic_store(XE + lane * 16 + j, nn, RLX_AGENT); }
                    } while (!hs && __builtin_amdgcn_s_memrealtime() - t0 < 5000000ull);
                    *HS = 2;
                }
                while (*HS == 0 && __builtin_amdgcn_s_memrealtime() - t0 < 6000000ull) __builtin_amdgcn_s_sleep(2);
                do {
                    PLOAD(); wait_vm<0>(); PIN(pv[0]); PIN(pv[1]); PIN(pv[2]); PIN(pv[3]);
                } while (PEMPTY() && __builtin_amdgcn_s_memrealtime() - t0 < 6000000ull);
            }
        }
#define UF(x) __uint_as_float(x)
        if (st == ZS0 + 2) {
            za = fadd1(fadd1(lone(UF(pv[0][0])), UF(pv[1][0])), fadd1(lone(UF(pv[2][0])), UF(pv[3][0]))); PIN(za);
            zb = fadd1(fadd1(UF(pv[0][1]), UF(pv[1][1])), fadd1(UF(pv[2][1]), UF(pv[3][1]))); PIN(zb);
        }
        if (st == ZS0 + 4) {
            auto a1 = __builtin_amdgcn_permlane16_swap(__float_as_uint(za), __float_as_uint(za), false, false); za = fadd1(UF(a1[0]), UF(a1[1])); PIN(za);
            auto b1 = __builtin_amdgcn_permlane16_swap(__float_as_uint(zb), __float_as_uint(zb), false, false); zb = fadd1(UF(b1[0]), UF(b1[1])); PIN(zb);
        }
        if (st == ZS0 + 6) {
            auto a2 = __builtin_amdgcn_permlane32_swap(__float_as_uint(za), __float_as_uint(za), false, false); za = fadd1(UF(a2[0]), UF(a2[1])); PIN(za);
            auto b2 = __builtin_amdgcn_permlane32_swap(__float_as_uint(zb), __float_as_uint(zb), false, false); zb = fadd1(UF(b2[0]), UF(b2[1])); PIN(zb);
        }
        if (st == ZS0 + 8) {
            float m = fmul1(za, 1.0f / FH), var = fmaxf(ffma1(-m, m, fmul1(zb, 1.0f / FH)), 0.f), r = __builtin_amdgcn_rsqf(var + 1e-5f); PIN(r);
            ZS[wid * 16 + r16] = f32x2{r, fmul1(-m, r)};
        }
    };
    P2_A(0, true, zstat); wait_vm_bar<5>(); SB();
    auto merge = [&](u32x2 A, u32x2 B) {
        auto r0 = __builtin_amdgcn_permlane16_swap(A[0], B[0], false, false); auto r1 = __builtin_amdgcn_permlane16_swap(A[1], B[1], false, false);
        return u32x4{r0[0], r1[0], r0[1], r1[1]};
    };
    auto pk4 = [&](f32x4 v) { return u32x2{cvtpk(v[0], v[1]), cvtpk(v[2], v[3])}; };
    const char* z2b = ws + WS_Z2T + (size_t)(j * 32 + 2 * pm + wr) * 18432;
    f32x2 zs[5]; u32x2 za2, zb2; unsigned vs, v2;
    auto zn = [&](int q) {
        if (q == 0) {
#pragma unroll
            for (int m = 0; m < 5; ++m) zs[m] = ZS[wr * 64 + (m < 4 ? m : wc) * 16 + r16];
        }
        if (q == 1) { vs = (r16 >> 3) * 2304 + (((r16 & 7) ^ (4 * (r16 >> 3))) * 32); PIN(vs); }
        if (q == 2) { v2 = vs + (g & 1) * 4608 + (g >> 1) * 16; vs += g * 8; PIN(v2); PIN(vs); }
#define NRM(v, m, e) { v[e] = ffma1(v[e], zs[m][0], zs[m][1]); v[(e) + 1] = ffma1(v[(e) + 1], zs[m][0], zs[m][1]); PIN(v); }
        if (q >= 3 && q < 3 + 4 * ZSCH) {
            const int p = (q - 3) / ZSCH, t = (q - 3) % ZSCH, n = p >> 1, m = (p & 1) * 2;
            f32x4& A = acc1[m][n]; f32x4& B = acc1[m + 1][n];
            if (ZSCH == 5) {
                if (t == 0) NRM(A, m, 0)
                if (t == 1) { NRM(A, m, 2) za2[0] = cvtpk(A[0], A[1]); PIN(za2); }
                if (t == 2) { NRM(B, m + 1, 0) za2[1] = cvtpk(A[2], A[3]); PIN(za2); }
                if (t == 3) { NRM(B, m + 1, 2) zb2[0] = cvtpk(B[0], B[1]); PIN(zb2); }
                if (t == 4) { zb2[1] = cvtpk(B[2], B[3]); st16s<0>(z2b + NF1(n) * 256 + m * 4608, v2, merge(za2, zb2)); }
            } else {
                if (t == 0) { NRM(A, m, 0) NRM(A, m, 2) }
                if (t == 1) { NRM(B, m + 1, 0) NRM(B, m + 1, 2) }
                if (t == 2) st16s<0>(z2b + NF1(n) * 256 + m * 4608, v2, merge(pk4(A), pk4(B)));
            }
        }
        if (q == 3 + 4 * ZSCH) { NRM(sacc, 4, 0) NRM(sacc, 4, 2) }
        if (q == 4 + 4 * ZSCH) st8s<2048>(z2b + wc * 4608, vs, pk4(sacc));
    };
    P2_B(0, false, true, zn);
    P2_A(1, false, [&](int st) { zn(14 + st); }); wait_vm_bar<5>(); SB();
    P2_B(1, false, true, [&](int st) { if (st == 0 && W2STR) asm volatile("s_mov_b32 m0, %2\n\ts_nop 0\n\tglobal_load_lds_dword %0, %1 offset:512" ::"v"(wspo + r16 * (W2STR - WSTR)), "s"(w_sp), "s"(lds0 + K1_X0 + 16384)); });
    const bool isq = nf3 >= 10 || j < 4;
    float qn[4], qs[4]; unsigned vq;
    auto qpre = [&](int st) {
        if (st == 7) {
#pragma unroll
            for (int m = 0; m < 4; ++m) er[m] = RS[wr * 64 + m * 16 + r16];
        }
        if (st == 8) { vq = ((g & 1) * 2 + (r16 >> 3)) * 9216 + (g >> 1) * 128 + (r16 & 7) * 16; PIN(vq); }
        if (st >= 10) {
            const int m = (st - 10) & 3;
            qn[m] = fmul1(er[m][0], cg == 3 ? 0.f : -1.f); qs[m] = ffma1(er[m][1], cg == 3 ? 0.f : (isq ? QSCALE / c0 : 1.0f / c0), cg == 3 ? 1.f : 0.f); PIN(qn[m]); PIN(qs[m]);
        }
    };
    auto qfin = [&](int st) {
        if (st >= 6) {
            const int m = (st - 6) >> 1, e_ = ((st - 6) & 1) * 2;
            f32x4& v = acc2[m][0];
            v[e_] = fmul1(ffma1(cs2[0][e_], qn[m], v[e_]), qs[m]); v[e_ + 1] = fmul1(ffma1(cs2[0][e_ + 1], qn[m], v[e_ + 1]), qs[m]); PIN(v);
        }
    };
    P2_A(2, false, qpre);
    P2_B(2, false, false, qfin);
    wait_vm<W2STR ? 1 : 0>();
    if (cg == 3) {
    } else if (isq) {
        const char* qb = ws + WS_Q + (pm * 16 + wr * 8) * 9216 + (nf3 >= 10 ? 4 * j + (nf3 - 10) * 2 : 64 + 2 * j) * 128;
        st16s<0>(qb, vq, merge(pk4(acc2[0][0]), pk4(acc2[1][0]))); st16s<0>(qb + 4 * 9216, vq, merge(pk4(acc2[2][0]), pk4(acc2[3][0])));
    } else if (j < 7) {
#pragma unroll
        for (int m = 0; m < 4; ++m) kv_store(ws + WS_KV + (size_t)(pm * 4 + wr * 2 + (m >> 1)) * 3072, m & 1, j, r16, g, acc2[m][0]);
    }
    SB();
    if (r16 == 0) { *(f32x4*)(CS + 192 + NF2(0) * 16 + 4 * g) = cs2[0]; *(f32x4*)(CS + 192 + NF2(1) * 16 + 4 * g) = cs2[1]; }
    f32x4* HX = (f32x4*)(smem + K1_B0) + wr * 256 + lane;
    if (cg == 3) { HX[0] = acc2[0][0]; HX[64] = acc2[1][0]; HX[128] = acc2[2][0]; HX[192] = acc2[3][0]; }
    if (wid < 4) g2_prep(gp, lds, lane, wid);
    volatile LAS u64* EW = (volatile LAS u64*)(lds + TOPL + 8);
    if (wid == HW) {
        if (__builtin_expect(epoch == 0, 0)) {
            const u64 t0 = __builtin_amdgcn_s_memrealtime();
            do { ev = ld_sc1_async(xe_s, le); wait_vm<0>(); PIN(ev); xcheck(); } while (epoch == 0 && __builtin_amdgcn_s_memrealtime() - t0 < 5000000ull);
        }
        *EW = epoch; EW[-1] = 0; EW[1] = 0; EW[2] = 0;
    }
    VSYNC();
    PIN(cs2[1]);
    if (wid == HW && lane == 0) __hip_atomic_store((gu64*)(ws + WS_FJ) + j * 64 + pm, epoch, RLX_AGENT);
    const u64 e_ = *EW;
    auto gel = [&](f32x4 a, f32x4 c, f32x4 rr) {
        f32x4 y = a - c * rr[0];
        f32x2 y0 = gelu_y2(f32x2{y[0], y[1]}, rr[1], rr[2], rr[3]), y1 = gelu_y2(f32x2{y[2], y[3]}, rr[1], rr[2], rr[3]);
        return u32x2{cvtpk(y0[0], y0[1]), cvtpk(y1[0], y1[1])};
    };
    cs1[1] = cs2[1]; cs1[2] = *(const f32x4*)(CS + 192 + NF2(2) * 16 + 4 * g);
    f32x4 h8 = HX[wc * 64], c8 = *(const f32x4*)(CS + 192 + 128 + 4 * g); er[4] = RS[wr * 64 + wc * 16 + r16];
    u32x4 zw[4];
#pragma unroll
    for (int m = 0; m < ZHALF; ++m) { zw[m] = merge(gel(acc2[m][1], cs1[1], er[m]), gel(acc2[m][2], cs1[2], er[m])); PIN(zw[m]); }
    SB();
    wait_vm_bar<0>();
    if (wid == HW) {
        const bool kvp = j >= 4 && j < 7;
        const unsigned fo = lane == 0 ? (unsigned)(WS_FJ - WS_FA) + (j * 64 + pm) * 8 : (lane == 1 ? (unsigned)(WS_FP - WS_FA) + (pm * 16 + j) * 8 : (lane == 2 ? bid * 8
                            : (unsigned)(WS_FJ - WS_FA) + ((lane & 15) * 64 + 16 + pm * 3 + (j - 4)) * 8));
        if (lane == 1 || lane == 2 || (kvp && g == 1)) __hip_atomic_store((gu64*)(ws + WS_FA + fo), epoch, RLX_AGENT);
    }
    if (wid < 4) {
        const char* sa = (const char*)w_sp - 4096 + pm * 128 * S_ * 2;
#pragma unroll
        for (int t = 0; t < 2; ++t) {
            set_m0(lds0 + wid * 4096 + t * K2_SA);
            glds_s<0>(gp.va[0], sa + t * 128); glds_s<1024>(gp.va[1], sa + t * 128); glds_s<2048>(gp.va[2], sa + t * 128); glds_s<3072>(gp.va[3], sa + t * 128);
        }
    }
    SB();
    epoch = ((u64)(unsigned)__builtin_amdgcn_readfirstlane((unsigned)(e_ >> 32)) << 32) | (unsigned)__builtin_amdgcn_readfirstlane((unsigned)e_);
    volatile LAS unsigned* go = (volatile LAS unsigned*)(lds + TOPL + 20);
    LAS char* zl = (LAS char*)lds + Z1L + wr * 1024 + r16 * 16;
#define ZPUT(m) *(LAS u32x4*)(zl + cg * 8192 + (2 * ((g & 1) ^ wr) + (g >> 1)) * 2048 + (m) * 256) = zw[m]
#pragma unroll
    for (int m = 0; m < ZHALF; ++m) ZPUT(m);
    bool issued = wid >= 3;
    u64 fz; int seen = wid != POLLW;
    asm volatile("" : "=v"(fz));
#pragma unroll
    for (int m = ZHALF; m < 5; ++m) {
        if (m > ZHALF && !seen) { wait_vm<0>(); PIN(fz); if (__all(fz == epoch)) { *go = 1; seen = 1; } }
        asm volatile("s_cmp_lg_u32 %3, 0\n\ts_cbranch_scc1 .Lfz%=\n\tglobal_load_dwordx2 %0, %1, %2 sc1\n.Lfz%=:" : "+v"(fz) : "v"(r16 * 8), "s"(ws + WS_FJ + j * 512), "s"(__builtin_amdgcn_readfirstlane(seen)) : "memory", "scc");
        if (m > ZHALF && !issued && __builtin_amdgcn_readfirstlane(*go)) { first_loads(ws, lds0, lane, wid, j); issued = true; }
        SB();
        if (m < 4) { zw[m & 3] = merge(gel(acc2[m & 3][1], cs1[1], er[m & 3]), gel(acc2[m & 3][2], cs1[2], er[m & 3])); ZPUT(m & 3); }
        else *(LAS u32x2*)(zl + 32768 + wc * 256 + (g >> 1) * 2048 + (g & 1) * 8) = gel(h8, c8, er[4]);
        SB();
    }
    if (!seen) { wait_vm<0>(); PIN(fz); if (!__all(fz == epoch)) poll_flags(ws, epoch, lane, ws + WS_FJ + j * 512, r16 * 8); *go = 1; }
    if (!issued) {
        while (*go == 0) __builtin_amdgcn_s_sleep(1);
        first_loads(ws, lds0, lane, wid, j);
    }
    return epoch;
}

DI void wait_fa(char* ws, u64 epoch, int lane) {
    const u64 t0 = __builtin_amdgcn_s_memrealtime();
    for (;;) {
        u32x4 a, b;
        asm volatile("global_load_dwordx4 %0, %2, %3 sc1\n\tglobal_load_dwordx4 %1, %2, %3 offset:1024 sc1\n\ts_waitcnt vmcnt(0)" : "=&v"(a), "=&v"(b) : "v"(lane * 16), "s"(ws + WS_FA) : "memory");
        const unsigned lo = (unsigned)epoch, hi = (unsigned)(epoch >> 32);
        if (__all(a[0] == lo && a[1] == hi && a[2] == lo && a[3] == hi && b[0] == lo && b[1] == hi && b[2] == lo && b[3] == hi)) break;
        if (__builtin_amdgcn_s_memrealtime() - t0 > 5000000ull) break;
        if (blockIdx.x == 0) epoch_service(ws, lane, epoch);
        __builtin_amdgcn_s_sleep(8);
    }
}

constexpr float ATT_BIAS = 72.0f;
DI float xhalf_max(float m) { auto rr = __builtin_amdgcn_permlane32_swap(__float_as_uint(m), __float_as_uint(m), false, false); return fmaxf(__uint_as_float(rr[0]), __uint_as_float(rr[1])); }
DI float lo_bcast(float m) { auto rr = __builtin_amdgcn_permlane32_swap(__float_as_uint(m), __float_as_uint(m), false, false); return __uint_as_float(rr[0]); }
DI float bf_round(float x) { return __uint_as_float(cvtpk(x, 0.f) << 16); }
DI float max16(const f32x16& s) {
    float a = fmaxf(fmaxf(s[0], s[1]), s[2]), b = fmaxf(fmaxf(s[3], s[4]), s[5]);
    a = fmaxf(fmaxf(a, s[6]), s[7]); b = fmaxf(fmaxf(b, s[8]), s[9]); a = fmaxf(fmaxf(a, s[10]), s[11]); b = fmaxf(fmaxf(b, s[12]), s[13]);
    a = fmaxf(fmaxf(a, s[14]), s[15]); return fmaxf(a, b);
}
DI void exp_pack(const f32x16& s, bf16x8& p0, bf16x8& p1) {
    float e[16];
#pragma unroll
    for (int i = 0; i < 16; ++i) e[i] = __builtin_amdgcn_exp2f(s[i]);
    u32x4 a = {cvtpk(e[0], e[1]), cvtpk(e[2], e[3]), cvtpk(e[4], e[5]), cvtpk(e[6], e[7])};
    u32x4 b = {cvtpk(e[8], e[9]), cvtpk(e[10], e[11]), cvtpk(e[12], e[13]), cvtpk(e[14], e[15])};
    p0 = __builtin_bit_cast(bf16x8, a); p1 = __builtin_bit_cast(bf16x8, b);
}

struct KVF { bf16x8 k1, k2, v0, v1; };
struct KVP { const LAS char* k1; const LAS char* k2; const LAS char* v; };
DI void kv_read(KVF& f, const KVP& p, int tb) {
    f.k1 = *(const LAS bf16x8*)(p.k1 + tb); f.k2 = *(const LAS bf16x8*)(p.k2 + tb);
    f.v0 = *(const LAS bf16x8*)(p.v + tb + 1024); f.v1 = *(const LAS bf16x8*)(p.v + tb + 2048);
}
#define k2frag(x) (x)
struct NoHook { DI void operator()(int) const {} };
template <bool RD, class H = NoHook>
DI void half_step(f32x16& sx, bf16x8& px0, bf16x8& px1, f32x16& sy, const KVF kq, const bf16x8& qy1, const bf16x8& qy2,
                  f32x16& oy, const KVF kv, const bf16x8& py0, const bf16x8& py1, KVF& ld, const KVP& p, int tb, H hook = H(), int hs = 0) {
    const f32x16 zero = {};
    unsigned a0, a1, a2, a3, b0, b1, b2, b3; float e0, e1, e2, e3;
    f32x16 t = MFMA32(kq.k1, qy1, zero); SB();
    hook(hs); SB();
    if (RD) ld.k1 = *(const LAS bf16x8*)(p.k1 + tb);
    e0 = EX(sx[0]); e1 = EX(sx[1]); e2 = EX(sx[2]); e3 = EX(sx[3]); PIN(e0); PIN(e1); PIN(e2); PIN(e3); SB();
    sy = MFMA32(kq.k2, qy2, t); SB();
    if (RD) ld.k2 = *(const LAS bf16x8*)(p.k2 + tb);
    a0 = cvtpk(e0, e1); a1 = cvtpk(e2, e3); PIN(a0); PIN(a1);
    e0 = EX(sx[4]); e1 = EX(sx[5]); e2 = EX(sx[6]); e3 = EX(sx[7]); PIN(e0); PIN(e1); PIN(e2); PIN(e3); SB();
    oy = MFMA32(kv.v0, py0, oy); SB();
    hook(hs + 1); SB();
    if (RD) ld.v0 = *(const LAS bf16x8*)(p.v + tb + 1024);
    a2 = cvtpk(e0, e1); a3 = cvtpk(e2, e3); PIN(a2); PIN(a3);
    e0 = EX(sx[8]); e1 = EX(sx[9]); e2 = EX(sx[10]); e3 = EX(sx[11]); PIN(e0); PIN(e1); PIN(e2); PIN(e3); SB();
    oy = MFMA32(kv.v1, py1, oy); SB();
    if (RD) ld.v1 = *(const LAS bf16x8*)(p.v + tb + 2048);
    b0 = cvtpk(e0, e1); b1 = cvtpk(e2, e3); PIN(b0); PIN(b1);
    e0 = EX(sx[12]); e1 = EX(sx[13]); e2 = EX(sx[14]); e3 = EX(sx[15]);
    b2 = cvtpk(e0, e1); b3 = cvtpk(e2, e3); PIN(b2); PIN(b3); SB();
    u32x4 a = {a0, a1, a2, a3}, b = {b0, b1, b2, b3};
    px0 = __builtin_bit_cast(bf16x8, a); px1 = __builtin_bit_cast(bf16x8, b);
}
DI bf16x8 p_scale(bf16x8 p, int sh) {
    u32x4 w = __builtin_bit_cast(u32x4, p);
#pragma unroll
    for (int i = 0; i < 4; ++i) w[i] = cvtpk(__builtin_amdgcn_ldexpf(bflo(w[i]), sh), __builtin_amdgcn_ldexpf(bfhi(w[i]), sh));
    return __builtin_bit_cast(bf16x8, w);
}
template <bool PP> DI void att_rescale(f32x16& o, f32x16& s, bf16x8& p0, bf16x8& p1, float& mref, bf16x8& q2, bool& bad, int c, int h) {
    bool nf = false;
#pragma unroll
    for (int i = 0; i < 11; ++i) nf = nf || (!(fabsf(o[i]) <= 3.0e38f) && (i < 8 || h == 0));
    if (__any(nf)) { bad = true; return; }
    float lb = lo_bcast(o[10]);
    int e = __builtin_amdgcn_frexp_expf(lb);
    float d = (float)(e + (int)ATT_BIAS); d = d < 0 ? 0 : d;
    float nm = bf_round(mref + d); float dd = nm - mref; mref = nm;
    int sh = -(int)dd;
#pragma unroll
    for (int i = 0; i < 16; ++i) { o[i] = __builtin_amdgcn_ldexpf(o[i], sh); if (!PP) { float b_ = s[i] - dd; PIN(b_); s[i] = b_; } }
    if (PP) { p0 = p_scale(p0, sh); p1 = p_scale(p1, sh); }
    u32x4 b = __builtin_bit_cast(u32x4, q2); b[1] = h ? 0u : (__float_as_uint(-nm) >> 16); q2 = __builtin_bit_cast(bf16x8, b);
}

struct G2F { bf16x8 a[4], b[4], sb; };
struct G2S {
    unsigned va[4], vl, vl2;
    const char* sa; const char* sl; unsigned lstep;
    unsigned m0a, m0l[3];
    const LAS char* pa[2][2]; const LAS char* pb[2]; const LAS char* ps[2];
};
template <int KS, int GL, bool RD, int RS, int WS, class H = NoHook> DI void g2_phase(G2S& s, const G2F& f, G2F& n, f32x4 (&acc)[4][4], f32x4 (&sacc)[2], H hook = H()) {
    const LAS char* a0 = s.pa[0][KS] + RS * K2_SA; const LAS char* a1 = s.pa[1][KS] + RS * K2_SA;
    constexpr int BO = RS * K2_SB + KS * 9216;
    const LAS char* b0 = s.pb[0] + BO; const LAS char* b1 = s.pb[1] + BO;
#define MM(m, nn) acc[m][nn] = MFMA16(f.b[nn], f.a[m], acc[m][nn]); PIN(acc[m][nn]); SB(); hook((nn) * 4 + (m)); SB()
    MM(0, 0); if (RD) n.b[0] = ldsB2(b0, b1); SB();
    MM(1, 0); if (RD) n.a[0] = ldsA(a0); SB();
    MM(2, 0); if (RD) n.a[1] = ldsA(a0 + 2048); SB();
    MM(3, 0); if (RD) n.a[2] = ldsA(a1); SB();
    MM(0, 1); if (RD) n.a[3] = ldsA(a1 + 2048); SB();
    MM(1, 1); if (RD) n.b[1] = ldsB2(b0 + 256, b1 + 256); SB();
    MM(2, 1); if (RD) n.b[2] = ldsB2(b0 + 512, b1 + 512); SB();
    MM(3, 1); if (RD) n.b[3] = ldsB2(b0 + 768, b1 + 768); SB();
    MM(0, 2); if (RD) n.sb = ldsB2(s.ps[0] + BO, s.ps[1] + BO); SB();
    MM(1, 2); if (GL == 1) { set_m0(s.m0a + WS * K2_SA); glds_s<0>(s.va[0], s.sa); } if (GL == 2) { glds_sc1<1024>(s.vl, s.sl); } SB();
    MM(2, 2);
    MM(3, 2); if (GL == 1) glds_s<1024>(s.va[1], s.sa); if (GL == 2) glds_sc1<2048>(s.vl, s.sl); SB();
    MM(0, 3);
    MM(1, 3); if (GL == 1) glds_s<2048>(s.va[2], s.sa); if (GL == 2) glds_sc1<3072>(s.vl, s.sl); SB();
    MM(2, 3);
    MM(3, 3); if (GL == 1) { glds_s<3072>(s.va[3], s.sa); s.sa += 128; asm volatile("" : "+s"(s.sa)); } if (GL == 2) { set_m0(s.m0l[WS] + 4096); glds_sc1<0>(s.vl2, s.sl); } SB();
    sacc[0] = MFMA16(f.sb, f.a[0], sacc[0]); PIN(sacc[0]); SB();
    if (GL == 1) { set_m0(s.m0l[WS]); glds_sc1<0>(s.vl, s.sl); } if (GL == 2) { glds_sc1<1024>(s.vl2, s.sl); s.sl += s.lstep; asm volatile("" : "+s"(s.sl)); } SB();
    sacc[1] = MFMA16(f.sb, f.a[1], sacc[1]); PIN(sacc[1]); SB();
#undef MM
}
template <int C, bool G2nd, bool G1st, int VM, bool RDN> DI void g2_iter(G2S& s, G2F& f0, G2F& f1, f32x4 (&acc)[4][4], f32x4 (&sacc)[2]) {
    g2_phase<1, G2nd ? 2 : 0, true, C, (C + 2) % 3>(s, f0, f1, acc, sacc);
    wait_vm_bar<VM>(); SB();
    g2_phase<0, G1st ? 1 : 0, RDN, (C + 1) % 3, C>(s, f1, f0, acc, sacc);
}

constexpr int K3_S = 4608, K3_RW = 4 * K3_S;
struct B5 { bf16x8 b[5]; };
DI unsigned k3_so(int w, int sl) {
    const int r = sl == 1 ? 2 : sl - 2;
    if (sl == 0) return Z1L + w * K3_S;
    return w < 4 ? K2_B0 + r * K2_SB + w * K3_S : (w < 7 ? r * K2_SA + (w - 4) * K3_S : K2_K0 + r * K2_SK);
}
DI unsigned k3_orow(int r) { return r < 6 ? (r >> 1) * K2_SA + 3 * K3_S + 64 + (r & 1) * 1168 : K2_K0 + (r - 6) * K2_SK + K3_S + 64; }
DI void k3_b1(int p, unsigned m0v, unsigned va, const char* sb) {
    if (p == 0) { set_m0(m0v); glds_sc1<0>(va, sb); }
    if (p == 1) glds_sc1<1024>(va, sb);
    if (p == 2) glds_sc1<2048>(va, sb);
    if (p == 3) glds_sc1<3072>(va, sb);
    if (p == 4) asm volatile("s_mov_b32 m0, %2\n\ts_mov_b32 exec_hi, 0\n\tglobal_load_lds_dwordx4 %0, %1 sc1\n\ts_mov_b32 exec_hi, -1" ::"v"(va), "s"(sb + 4096), "s"(m0v + 4096));
}
DI void k3_a(unsigned m0v, unsigned va, const char* sa) {
    set_m0(m0v); glds_sc1<0>(va, sa); glds_sc1<1024>(va, sa); glds_sc1<2048>(va, sa); glds_sc1<3072>(va, sa);
}
DI void k3_b(unsigned m0v, unsigned va, const char* sb) {
#pragma unroll
    for (int p = 0; p < 5; ++p) k3_b1(p, m0v, va, sb);
}
DI void k3_rdb(B5& r, const LAS char* p, const LAS char* p5) {
#pragma unroll
    for (int n = 0; n < 4; ++n) r.b[n] = ldsA(p + n * 1024);
    r.b[4] = ldsA(p5);
}
DI void k3_lb1(int p, B5& r, unsigned va, unsigned va5, const char* sb) {
    if (p < 4) asm volatile("global_load_dwordx4 %0, %1, %2 offset:%3 sc1" : "+v"(r.b[p & 3]) : "v"(va), "s"(sb), "n"((p & 3) * 1024) : "memory");
    else asm volatile("global_load_dwordx4 %0, %1, %2 sc1" : "+v"(r.b[4]) : "v"(va5), "s"(sb) : "memory");
}
#define LB5 "global_load_dwordx4 %0, %5, %7 sc1\n\tglobal_load_dwordx4 %1, %5, %7 offset:1024 sc1\n\tglobal_load_dwordx4 %2, %5, %7 offset:2048 sc1\n\t" \
            "global_load_dwordx4 %3, %5, %7 offset:3072 sc1\n\tglobal_load_dwordx4 %4, %6, %7 sc1\n"
DI void k3_lb(B5& r, unsigned va, unsigned va5, const char* sb) {
    asm volatile(LB5 : "=&v"(r.b[0]), "=&v"(r.b[1]), "=&v"(r.b[2]), "=&v"(r.b[3]), "=&v"(r.b[4]) : "v"(va), "v"(va5), "s"(sb) : "memory");
}
DI void k3_lb_if(B5& r, unsigned va, unsigned va5, const char* sb, int a, int b) {
    asm volatile("s_cmp_lg_u32 %8, %9\n\ts_cbranch_scc1 .Llb%=\n\t" LB5 ".Llb%=:" : "+v"(r.b[0]), "+v"(r.b[1]), "+v"(r.b[2]), "+v"(r.b[3]), "+v"(r.b[4])
                 : "v"(va), "v"(va5), "s"(sb), "s"(a), "n"(b) : "memory", "scc");
}
DI void pin5(B5& r) { PIN(r.b[0]); PIN(r.b[1]); PIN(r.b[2]); PIN(r.b[3]); PIN(r.b[4]); }
template <int N0, int N1, bool FIRST = false> DI void k3_mm(f32x4 (&acc)[4][5], const bf16x8 (&a)[4], const B5& r) {
#pragma unroll
    for (int n = N0; n < N1; ++n)
#pragma unroll
        for (int m = 0; m < 4; ++m) {
            if (FIRST) asm volatile("v_mfma_f32_16x16x32_bf16 %0, %1, %2, 0" : "=&v"(acc[m][n]) : "v"(r.b[n]), "v"(a[m]));
            else asm volatile("v_mfma_f32_16x16x32_bf16 %0, %1, %2, %0" : "+v"(acc[m][n]) : "v"(r.b[n]), "v"(a[m]));
        }
    SB();
}

#define K3MM(d, b_, a_) asm volatile("v_mfma_f32_16x16x32_bf16 %0, %1, %2, %0" : "+v"(d) : "v"(b_), "v"(a_))
#define ILV 1
struct UA { const char* p[4]; };
template <int NX, bool FIRST = false> DI void k3_step(f32x4 (&acc)[4][5], bf16x8 (&a)[4], const B5& r, unsigned va, const UA& ua) {
#pragma unroll
    for (int m = 0; m < 4; ++m) {
#pragma unroll
        for (int n = 0; n < 5; ++n) {
            if (FIRST) asm volatile("v_mfma_f32_16x16x32_bf16 %0, %1, %2, 0" : "=&v"(acc[m][n]) : "v"(r.b[n]), "v"(a[m]));
            else K3MM(acc[m][n], r.b[n], a[m]);
        }
        if (NX >= 0 && ILV) asm volatile("global_load_dwordx4 %0, %1, %2 sc1" : "+v"(a[m]) : "v"(va), "s"(ua.p[m] + (NX < 0 ? 0 : NX) * 16384) : "memory");
    }
    if (NX >= 0 && !ILV) {
#pragma unroll
        for (int m = 0; m < 4; ++m) asm volatile("global_load_dwordx4 %0, %1, %2 sc1" : "+v"(a[m]) : "v"(va), "s"(ua.p[m] + (NX < 0 ? 0 : NX) * 16384) : "memory");
    }
    SB();
}

DI void phase2(G2P& gp, const u16* __restrict__ w_sp, const u16* __restrict__ b_sp, const u16* __restrict__ x, char* __restrict__ ws, u16* __restrict__ out, const u64 epoch, const int wid) {
    extern __shared__ __attribute__((aligned(16))) char smem[];
    int bid = blockIdx.x, xcd = bid & 7, idx = bid >> 3;
    int pm = (xcd & 1) * 8 + (idx & 7), j = (xcd >> 1) * 4 + (idx >> 3);
    unsigned zz = 0; asm volatile("" : "+s"(zz));
    const int lane = __builtin_amdgcn_mbcnt_hi(~0u, __builtin_amdgcn_mbcnt_lo(~0u, zz)) & 63;
    const int tid = wid * 64 + lane;
    const unsigned lds0 = (unsigned)(uintptr_t)smem; const LAS char* lds = (const LAS char*)smem;
    const int rb = ((xcd & 1) ^ 1) * 16 + (xcd >> 1) * 4 + (idx & 3), j3 = idx >> 2;
    volatile LAS unsigned* SY = (volatile LAS unsigned*)(lds + TOPL);
    gu64* FLr = (gu64*)(ws + WS_FL) + rb * 64;
    const unsigned va = lane * 16;
    unsigned va5 = 4096 + ((lane >> 4) * 8 + (lane & 7)) * 16; PIN(va5);
    B5 rb0, rb1, rb2, rb3;
    const int ks0 = wid < 4 ? wid : wid + 42;
    const char* wb = ws + WS_W3T + (size_t)j3 * 90 * 4608 + ks0 * 4608;
    if (wid >= 4) {
        __builtin_amdgcn_s_setprio(1);
        const unsigned qf = lane < 16 ? (unsigned)(WS_FP - WS_FJ) + pm * 128 + lane * 8 : j * 512 + lane * 8;
        u64 f_;
        asm volatile("s_cmp_lg_u32 %3, 4\n\ts_cbranch_scc1 .Lqf%=\n\tglobal_load_dwordx2 %0, %1, %2 sc1\n.Lqf%=:\n\ts_waitcnt lgkmcnt(0)\n\ts_barrier" : "=&v"(f_) : "v"(qf), "s"(ws + WS_FJ), "s"(wid) : "memory", "scc");
        SB();
        const int c = lane & 31, h = lane >> 5, aw = wid - 4;
        const char* kvg = ws + WS_KV;
        bf16x8 q1[2], q2[2]; float mref[2];
        const f32x16 zero = {};
        f32x16 oA = zero, oB = zero;
        int k2o = 1024 + (c >> 4) * 1024 + (c & 15) * 16;
        int vo = c < 8 ? 256 + (h * 8 + c) * 16 : 512 + (h * 16 + (c < 16 ? c - 8 : (c < 18 ? c - 2 : (c == 18 ? 12 : 13)))) * 16;
        {
            const int qb0 = aw == 2 ? TOPL + 64 : K2_K0 + 2 * K2_SK + (aw < 2 ? aw : 2) * 2304, qb1 = aw == 3 ? TOPL + 64 + 2304 - 1024 : qb0;
            const char* qs = ws + WS_Q + (pm * 16 + j) * 9216 + aw * 32;
            unsigned vq = (lane >> 1) * 128 + (lane & 1) * 16; PIN(vq); PIN(k2o); PIN(vo);
            SB();
            if (wid == 4) {
                wait_vm<0>(); PIN(f_);
                if (!__all(f_ == epoch)) poll_flags(ws, epoch, lane, ws + WS_FJ, qf);
            }
            asm volatile("s_barrier" ::: "memory");
            set_m0(lds0 + qb0); glds_sc1<0>(vq, qs); set_m0(lds0 + qb1); glds_sc1<1024>(vq, qs + 3072);
            if (lane < 16) glds_sc1<2048>(vq, qs + 6144);
            SB();
            const LAS char* qo[5];
#pragma unroll
            for (int i = 0; i < 5; ++i) { const int k = 18 * c + (i < 4 ? 8 * h + 2 * i : 16); qo[i] = lds + ((k < 256 ? qb0 : qb1) + (k >> 3) * 32 + (k & 7) * 2); PIN(qo[i]); }
            SB();
            asm volatile("s_barrier" ::: "memory");
            PIN(oA); PIN(oB);
            SB();
            wait_vm<0>();
#pragma unroll
            for (int t = 0; t < 2; ++t) {
                u32x4 a;
#pragma unroll
                for (int i = 0; i < 4; ++i) a[i] = *(const LAS unsigned*)(qo[i] + t * 16);
                q1[t] = __builtin_bit_cast(bf16x8, a);
                const unsigned e = *(const LAS unsigned*)(qo[4] + t * 16);
                u32x4 b = {h ? 0u : e, 0u, 0u, 0u}; q2[t] = __builtin_bit_cast(bf16x8, b);
                PIN(q1[t]); PIN(q2[t]);
            }
        }
        bool bad = false;
        KVP kp = {lds + (K2_K0 + lane * 16), lds + (K2_K0 + k2o), lds + (K2_K0 + vo)}; PIN(kp.k1); PIN(kp.k2); PIN(kp.v);
        asm volatile("s_waitcnt lgkmcnt(0)\n\ts_barrier" ::: "memory");
        if (PFP == 2) asm volatile("s_mov_b32 m0, %2\n\ts_nop 0\n\tglobal_load_lds_dword %0, %1" ::"v"((pm * 128 + j * 8 + aw * 2 + h) * (S_ * 2) + min(c & 3, PFN - 1) * 1024 + 1920), "s"(w_sp), "s"(lds0 + TOPL + 3648));
        KVF X, Y;
        kv_read(X, kp, 0); kv_read(Y, kp, 3072);
#pragma unroll
        for (int t = 0; t < 2; ++t) {
            f32x16 s0 = MFMA32(X.k1, q1[t], zero); s0 = MFMA32(X.k2, q2[t], s0);
            f32x16 s1 = MFMA32(Y.k1, q1[t], zero); s1 = MFMA32(Y.k2, q2[t], s1);
            float m = xhalf_max(fmaxf(max16(s0), max16(s1)));
            mref[t] = bf_round(rintf(m) + ATT_BIAS);
            u32x4 b = __builtin_bit_cast(u32x4, q2[t]); b[1] = h ? 0u : (__float_as_uint(-mref[t]) >> 16); q2[t] = __builtin_bit_cast(bf16x8, b);
        }
        f32x16 sA = MFMA32(X.k1, q1[0], zero); sA = MFMA32(X.k2, q2[0], sA);
        f32x16 sB = zero;
        bf16x8 pA0, pA1, pB0 = {}, pB1 = {};
        half_step<false>(sA, pA0, pA1, sB, X, q1[1], q2[1], oB, X, pB0, pB1, Y, kp, 0);
        half_step<false>(sB, pB0, pB1, sA, Y, q1[0], q2[0], oA, X, pA0, pA1, Y, kp, 0);
#define A_IV(sl, CHK, H, hs) { const int tb = ((sl) % 3) * K2_SK; \
            half_step<true>(sA, pA0, pA1, sB, Y, q1[1], q2[1], oB, X, pB0, pB1, X, kp, tb, H, hs); \
            half_step<false>(sB, pB0, pB1, sA, X, q1[0], q2[0], oA, Y, pA0, pA1, X, kp, tb, H, hs + 2); \
            if (CHK) { mB = __builtin_amdgcn_ballot_w64(!(oB[10] <= 0x1p20f)); SB(); }                  \
            half_step<true>(sA, pA0, pA1, sB, X, q1[1], q2[1], oB, Y, pB0, pB1, Y, kp, tb + 3072, H, hs + 4); \
            if (CHK) { mA = __builtin_amdgcn_ballot_w64(!(oA[10] <= 0x1p20f)); SB(); } \
            half_step<false>(sB, pB0, pB1, sA, Y, q1[0], q2[0], oA, X, pA0, pA1, Y, kp, tb, H, hs + 6); }
#define A_BAR() asm volatile("s_waitcnt lgkmcnt(0)\n\ts_barrier" ::: "memory")
        unsigned long long mA, mB;
#define UNDEF5(r) asm volatile("" : "=v"(r.b[0]), "=v"(r.b[1]), "=v"(r.b[2]), "=v"(r.b[3]), "=v"(r.b[4]))
        u32x4 fa0, fa1;
        asm volatile("global_load_dwordx4 %0, %2, %3 sc1\n\tglobal_load_dwordx4 %1, %2, %3 offset:1024 sc1" : "=&v"(fa0), "=&v"(fa1) : "v"(va), "s"(ws + WS_FA) : "memory");
        const NoHook nh;
        if (PFP == 1) asm volatile("s_mov_b32 m0, %2\n\ts_nop 0\n\tglobal_load_lds_dword %0, %1" ::"v"((pm * 128 + j * 8 + aw * 2 + h) * (S_ * 2) + min(c & 3, PFN - 1) * 1024 + 1920), "s"(w_sp), "s"(lds0 + TOPL + 3648));
        const char* pf = ws + WS_W3T + (size_t)j3 * 90 * 4608 + 16 * 4608 + min(((idx & 3) * 4 + aw) * 192, 2664 - 192) * 128;
        auto wl = [&](int i) {
            if (i < 20) k3_lb1(i % 5, i < 5 ? rb0 : (i < 10 ? rb1 : (i < 15 ? rb2 : rb3)), va, va5, wb + (i / 5) * 18432);
            else if (i < 23 && PFW)
                asm volatile("s_mov_b32 m0, %2\n\ts_nop 0\n\tglobal_load_lds_dword %0, %1 sc1" ::"v"(va * 8), "s"(pf + (i - 20) * 8192), "s"(lds0 + TOPL + 64 + wid * 256));
        };
#define A_RS() if (__builtin_expect((mA | mB) != 0, 0)) { \
                if (mA) att_rescale<false>(oA, sA, pA0, pA1, mref[0], q2[0], bad, c, h); \
                if (mB) att_rescale<true>(oB, sB, pB0, pB1, mref[1], q2[1], bad, c, h); \
            }
#define A_IT(H) A_BAR(); A_IV(1, false, H, 0); A_BAR(); A_IV(2, false, H, 8); A_BAR(); A_IV(0, true, H, 16); A_RS()
        CAL(AOFF);
#pragma unroll 1
        for (int t = 0; t < TFA; ++t) { A_IT(nh); }
        {
            wait_vm<0>(); PIN(fa0); PIN(fa1);
            const unsigned lo = (unsigned)epoch, hi = (unsigned)(epoch >> 32);
            if (__builtin_expect(!__all(fa0[0] == lo && fa0[1] == hi && fa0[2] == lo && fa0[3] == hi && fa1[0] == lo && fa1[1] == hi && fa1[2] == lo && fa1[3] == hi), 0)) wait_fa(ws, epoch, lane);
            UNDEF5(rb0); UNDEF5(rb1); UNDEF5(rb2); UNDEF5(rb3);
            SB();
            A_IT(wl);
        }
#pragma unroll 1
        for (int t = TFA + 1; t < 9; ++t) { A_IT(nh); }
        A_BAR(); A_IV(1, false, nh, 0); A_BAR();
        wait_vm<0>(); pin5(rb0); pin5(rb1); pin5(rb2); pin5(rb3);
        auto w9 = [&](int i) { if (i < 5) k3_b1(i, lds0 + k3_so(wid, 3), va, wb + 9 * 18432); };
        A_IV(2, false, nh, 0);
        kp.k1 -= K2_K0 - 2 * K2_SA; kp.k2 -= K2_K0 - 2 * K2_SA; kp.v -= K2_K0 - 2 * K2_SA; PIN(kp.k1); PIN(kp.k2); PIN(kp.v);
        A_IV(3, true, w9, 0); A_RS() A_IV(4, false, nh, 0);
        half_step<false>(sA, pA0, pA1, sB, Y, q1[1], q2[1], oB, X, pB0, pB1, X, kp, 0);
        exp_pack(sB, pB0, pB1);
        oA = MFMA32(Y.v0, pA0, oA); oA = MFMA32(Y.v1, pA1, oA);
        oB = MFMA32(Y.v0, pB0, oB); oB = MFMA32(Y.v1, pB1, oB);
        {
            float sa_ = fabsf(oA[10]) * 0x1p27f, sb_ = fabsf(oB[10]) * 0x1p27f;
#pragma unroll
            for (int i = 0; i < 10; ++i) { sa_ += fabsf(oA[i]); PIN(sa_); sb_ += fabsf(oB[i]); PIN(sb_); }
            if (__builtin_amdgcn_ballot_w64(!(sa_ + sb_ <= 3.0e38f))) bad = true;
        }
        f32x16 o[2] = {oA, oB};
        if (__builtin_expect(bad, 0)) {
#pragma unroll 1
            for (int t = 0; t < 2; ++t) {
                bf16x8 qa = t ? q1[1] : q1[0]; u32x4 qb = __builtin_bit_cast(u32x4, t ? q2[1] : q2[0]); qb[1] = 0;
                f32x16 oo = zero; float m = -3.0e38f;
                for (int kt = 0; kt < 64; ++kt) {
                    const char* tb = kvg + (size_t)kt * 3072;
                    bf16x8 k1, v0, v1; u32x4 k2v;
                    asm volatile("global_load_dwordx4 %0, %4, %7 sc1\n\tglobal_load_dwordx4 %1, %5, %7 sc1\n\tglobal_load_dwordx4 %2, %6, %7 offset:1024 sc1\n\t"
                                 "global_load_dwordx4 %3, %6, %7 offset:2048 sc1\n\ts_waitcnt vmcnt(0)"
                                 : "=&v"(k1), "=&v"(k2v), "=&v"(v0), "=&v"(v1) : "v"(lane * 16), "v"((unsigned)(kp.k2 - lds) - 2 * K2_SA), "v"((unsigned)(kp.v - lds) - 2 * K2_SA), "s"(tb) : "memory");
                    f32x16 s = MFMA32(k1, qa, zero); s = MFMA32(__builtin_bit_cast(bf16x8, k2v), __builtin_bit_cast(bf16x8, qb), s);
                    float nm = fmaxf(m, xhalf_max(max16(s)));
                    float sc = __builtin_amdgcn_exp2f(m - nm); m = nm;
#pragma unroll
                    for (int i = 0; i < 16; ++i) { float a_ = oo[i] * sc, b_ = s[i] - nm; PIN(a_); PIN(b_); oo[i] = a_; s[i] = b_; }
                    bf16x8 p0, p1; exp_pack(s, p0, p1);
                    oo = MFMA32(v0, p0, oo); oo = MFMA32(v1, p1, oo);
                }
                if (t) o[1] = oo; else o[0] = oo;
            }
        }
#pragma unroll
        for (int t = 0; t < 2; ++t) {
            float inv = __builtin_amdgcn_rcpf(lo_bcast(o[t][10])); PIN(inv);
            LAS unsigned* ob = (LAS unsigned*)(lds + k3_orow(aw * 2 + t) + 36 * c + 8 * h);
#define ON(i) cvtpk(fmul1(o[t][i], inv), fmul1(o[t][(i) + 1], inv))
            ob[0] = ON(0); ob[1] = ON(2); ob[4] = ON(4); ob[5] = ON(6);
            if (h == 0) ob[8] = ON(8);
        }
        asm volatile("s_waitcnt lgkmcnt(0)" ::: "memory");
        if (lane == 0) __hip_atomic_fetch_add((LAS unsigned*)SY, 1u, __ATOMIC_RELAXED, __HIP_MEMORY_SCOPE_WORKGROUP);
        __builtin_amdgcn_s_setprio(APRIO);
    } else {
        const int wr = wid >> 1, wc = wid & 1;
        G2S s;
        s.vl = lane * 16; s.vl2 = lane * 16 + 4096;
        s.lstep = wid < 3 ? 18432 : 6144;
        unsigned ol = (wid < 3 ? (unsigned)WS_Z2T + j * 32 * 18432 + wid * 6144 : (unsigned)WS_KV - 3 * 6144), oa = pm * 128 * S_ * 2;
        s.m0a = lds0 + wid * 4096; s.m0l[0] = lds0 + (wid < 3 ? K2_B0 + wid * 6144 : K2_K0); s.m0l[1] = s.m0l[0] + s.lstep; s.m0l[2] = s.m0l[1] + s.lstep;
        PIN(s.vl); PIN(s.vl2);
        s.sl = ws + ol + 2 * s.lstep;
#pragma unroll
        for (int i = 0; i < 4; ++i) { s.va[i] = gp.va[i]; s.pa[i >> 1][i & 1] = gp.pa[i >> 1][i & 1]; }
        s.pb[0] = gp.pb[0]; s.pb[1] = gp.pb[1]; s.ps[0] = gp.ps[0]; s.ps[1] = gp.ps[1];
        s.sa = (const char*)w_sp - 4096 + oa + 256;
        G2F f0, f1;
        f32x4 (&acc)[4][4] = gp.acc; f32x4 (&sacc)[2] = gp.sacc;
        SB();
        if (wid == 3) wait_vm<0>();
        wait_vm_bar<6>();
        const unsigned m0l2 = s.m0l[2];
        if (wid == 3) s.m0l[2] = s.m0l[0];
        f0.b[0] = ldsB2(s.pb[0], s.pb[1]);
        f0.a[0] = ldsA(s.pa[0][0]); f0.a[1] = ldsA(s.pa[0][0] + 2048); f0.a[2] = ldsA(s.pa[1][0]); f0.a[3] = ldsA(s.pa[1][0] + 2048);
        f0.b[1] = ldsB2(s.pb[0] + 256, s.pb[1] + 256); f0.b[2] = ldsB2(s.pb[0] + 512, s.pb[1] + 512); f0.b[3] = ldsB2(s.pb[0] + 768, s.pb[1] + 768); f0.sb = ldsB2(s.ps[0], s.ps[1]);
        SB();
        set_m0(s.m0a + 2 * K2_SA); glds_s<0>(s.va[0], s.sa); glds_s<1024>(s.va[1], s.sa); glds_s<2048>(s.va[2], s.sa); glds_s<3072>(s.va[3], s.sa); s.sa += 128;
        set_m0(s.m0l[2]); glds_sc1<0>(s.vl, s.sl);
        SB();
        CAL(GOFF);
#pragma unroll 1
        for (int t = 0; t < 9; ++t) {
            g2_iter<0, true, true, 10, true>(s, f0, f1, acc, sacc); s.m0l[2] = m0l2; g2_iter<1, true, true, 10, true>(s, f0, f1, acc, sacc); g2_iter<2, true, true, 10, true>(s, f0, f1, acc, sacc);
        }
        asm volatile("" : "=v"(rb0.b[0]), "=v"(rb0.b[1]), "=v"(rb0.b[2]), "=v"(rb0.b[3]), "=v"(rb0.b[4]), "=v"(rb1.b[0]), "=v"(rb1.b[1]), "=v"(rb1.b[2]), "=v"(rb1.b[3]), "=v"(rb1.b[4]));
        g2_phase<1, 2, true, 0, 2>(s, f0, f1, acc, sacc);
        wait_vm_bar<10>(); SB();
        g2_phase<0, 1, true, 1, 0>(s, f1, f0, acc, sacc, [&](int st) { if (st < 10 && !(st & 1)) k3_lb1(st >> 1, rb0, va, va5, wb); });
        g2_phase<1, 2, true, 1, 0>(s, f0, f1, acc, sacc);
        wait_vm_bar<15>(); SB();
        g2_phase<0, 1, true, 2, 1>(s, f1, f0, acc, sacc, [&](int st) { if (st < 10 && !(st & 1)) k3_lb1(st >> 1, rb1, va, va5, wb + 18432); });
        const int c = lane & 15, g = lane >> 4;
        const LAS char* z1b = lds + Z1L + (g >> 1) * 2048 + (wr * 64 + c) * 16 + (g & 1) * 8;
        u32x2 z1r[4][4], z1s[2];
#pragma unroll
        for (int m = 0; m < 4; ++m) {
            const int mo = (m ^ (2 * wc)) * 16;
#pragma unroll
            for (int n = 0; n < 4; ++n) z1r[m][n] = *(const LAS u32x2*)(z1b + (wc * 4 + n) * 4096 + mo * 16);
            if (m < 2) z1s[m] = *(const LAS u32x2*)(z1b + 8 * 4096 + mo * 16);
        }
        SB();
#define PREB(i) [&](int st) { if (st < 5) k3_b1(st, lds0 + k3_so(wid, i), va, wb + ((i) + 6) * 18432); else if (st < 10) k3_b1(st - 5, lds0 + k3_so(wid + 4, i), va, wb + ((i) + 6) * 18432 + 46 * 4608); }
        g2_phase<1, 2, true, 2, 1>(s, f0, f1, acc, sacc);
        wait_vm_bar<15>(); SB();
        {
            const int p = wid < 3 ? wid : 2;
            const char* ks = ws + WS_KV + (29 + p) * 6144; const unsigned km = lds0 + (p ? 2 * K2_SA + (p - 1) * 6144 : K2_K0 + 2 * K2_SK);
            g2_phase<0, 0, true, 0, 2>(s, f1, f0, acc, sacc, [&](int st) {
                if (st < 4) k3_b1(st, km, va, ks); else if (st == 4) { set_m0(km + 4096); glds_sc1<0>(s.vl2, ks); } else if (st == 5) glds_sc1<1024>(s.vl2, ks);
                else if (st < 11) k3_b1(st - 6, lds0 + k3_so(wid, 1), va, wb + 7 * 18432); });
        }
        g2_phase<1, 0, true, 0, 2>(s, f0, f1, acc, sacc, PREB(0));
        wait_vm_bar<21>(); SB();
        g2_phase<0, 0, true, 1, 0>(s, f1, f0, acc, sacc, PREB(2));
        unsigned vu[3];
#pragma unroll
        for (int i = 0; i < 3; ++i) {
            const int cgl = 18 * j + (i < 2 ? 2 * (wc * 4 + 2 * i + (g & 1)) : 16) + (g >> 1);
            vu[i] = (cgl >> 2) * 4096 + (cgl & 3) * 256 + c * 16 + (i == 2 ? (g & 1) * 1024 : 0); PIN(vu[i]);
        }
        const char* ub0 = ws + WS_UO + (size_t)(pm * 2 + wr) * 90 * 4096 + wc * 2048; const char* ub1 = ub0 + 2048 - wc * 4096;
        SB();
        g2_phase<1, 0, true, 1, 0>(s, f0, f1, acc, sacc);
        wait_vm_bar<0>(); SB();
        __builtin_amdgcn_s_setprio(2);
        auto fin = [&](f32x4 v, u32x2 z) { return u32x2{cvtpk(fmul1(bflo(z[0]), v[0]), fmul1(bfhi(z[0]), v[1])), cvtpk(fmul1(bflo(z[1]), v[2]), fmul1(bfhi(z[1]), v[3]))}; };
        auto mrg = [&](u32x2 A, u32x2 B) {
            auto r0 = __builtin_amdgcn_permlane16_swap(A[0], B[0], false, false); auto r1 = __builtin_amdgcn_permlane16_swap(A[1], B[1], false, false);
            return u32x4{r0[0], r1[0], r0[1], r1[1]};
        };
#define M2(m, n) acc[m][n] = MFMA16(f1.b[n], f1.a[m], acc[m][n]); acc[m][(n) + 1] = MFMA16(f1.b[(n) + 1], f1.a[m], acc[m][(n) + 1]); PIN(acc[m][n]); PIN(acc[m][(n) + 1]); SB()
#define PUT(m, n) st16s<((m) & 1) * 1024>((m) < 2 ? ub0 : ub1, vu[(n) >> 1], mrg(fin(acc[m][n], z1r[m][n]), fin(acc[m][(n) + 1], z1r[m][(n) + 1]))); SB()
        M2(0, 0); M2(1, 0); M2(2, 0); PUT(0, 0);
        M2(3, 0); PUT(1, 0);
        sacc[0] = MFMA16(f1.sb, f1.a[0], sacc[0]); sacc[1] = MFMA16(f1.sb, f1.a[1], sacc[1]); PIN(sacc[0]); PIN(sacc[1]); SB(); PUT(2, 0);
        M2(0, 2); PUT(3, 0);
        M2(1, 2); st16s<0>(ub0, vu[2], mrg(fin(sacc[0], z1s[0]), fin(sacc[1], z1s[1]))); SB();
        M2(2, 2); PUT(0, 2);
        M2(3, 2); PUT(1, 2);
        PUT(2, 2); PUT(3, 2);
        SB(); k3_b(lds0 + k3_so(wid, 3), va, wb + 9 * 18432);
        wait_vm<5>();
        if (lane == 0) __hip_atomic_store((gu64*)(ws + WS_FL) + (pm * 2 + wr) * 64 + j * 2 + wc, epoch, RLX_AGENT);
        pin5(rb0); pin5(rb1);
        asm volatile("" : "=v"(rb2.b[0]), "=v"(rb2.b[1]), "=v"(rb2.b[2]), "=v"(rb2.b[3]), "=v"(rb2.b[4]), "=v"(rb3.b[0]), "=v"(rb3.b[1]), "=v"(rb3.b[2]), "=v"(rb3.b[3]), "=v"(rb3.b[4]));
        __builtin_amdgcn_s_setprio(GPRIO);
    }
    {
        UA ua;
#pragma unroll
        for (int i = 0; i < 4; ++i) ua.p[i] = ws + WS_UO + (size_t)rb * 90 * 4096 + ks0 * 4096 + ((wid + 1 + i) & 3) * 1024;
        auto relane = [&]() { unsigned t_ = va; PIN(t_); return (int)(t_ >> 4); };
        unsigned so[4];
#pragma unroll
        for (int i = 0; i < 4; ++i) so[i] = __builtin_amdgcn_readfirstlane(k3_so(wid, i));
#define LG0() asm volatile("s_waitcnt lgkmcnt(0)" ::: "memory"); SB()
#define LB(d, i) k3_lb(d, va, va5, wb + (i) * 18432)
#define RB(d, sl) { asm volatile("" : "+s"(so[sl])); k3_rdb(d, lds + va + so[sl], lds + va5 + so[sl]); }
        B5 rb4, rb5; bf16x8 a0[4], a1[4], a2[4], a3[4], a4[4];
        f32x4 ac[4][5];
        const size_t xo = (size_t)(rb * 64 + (wid & 3) * 16) * D_ + 72 * j3;
        bool opub = true;
#define OPUB() if (relane() == 0) __hip_atomic_store((gu64*)(ws + WS_FL) + (pm * 2 + (j >> 3)) * 64 + 32 + (j & 7) * 4 + (wid - 4), epoch, RLX_AGENT)
        UNDEF5(rb4); UNDEF5(rb5);
        if (wid < 4) {
            k3_lb_if(rb2, va, va5, wb + 2 * 18432, 0, 0); k3_lb_if(rb3, va, va5, wb + 3 * 18432, 0, 0);
            k3_lb_if(rb4, va, va5, wb + 4 * 18432, 0, 0); k3_lb_if(rb5, va, va5, wb + 5 * 18432, 0, 0);
        } else {
            const int orow = pm * 128 + j * 8, ck = (wid - 4) * 18 + (lane >> 3);
            const char* ob = ws + WS_UO + ((size_t)((orow >> 6) * 90 + 72) * 4 + ((orow >> 4) & 3)) * 1024 + (orow & 15) * 16;
            unsigned vo_ = (ck >> 2) * 4096 + (ck & 3) * 256 + (lane & 7) * 16; PIN(vo_);
            const LAS char* ol = lds + (k3_orow(lane & 7) + ck * 16); PIN(ol);
            SB();
            while (SY[0] < 4) __builtin_amdgcn_s_sleep(1);
            const u32x4 o0 = *(const LAS u32x4*)ol, o1 = *(const LAS u32x4*)(ol + 128), o2 = *(const LAS u32x4*)(ol + 256);
            opub = __builtin_amdgcn_readfirstlane(SY[1]) == 0;
            st16s<0>(ob, vo_, o0); st16s<0>(ob + 8192, vo_, o1);
            if (lane < 16) st16s<0>(ob + 16384, vo_, o2);
            if (opub) { wait_vm<0>(); OPUB(); }
        }
        SB();
        if (wid == PW2) {
            const unsigned long long t0 = __builtin_amdgcn_s_memrealtime();
            u64 f = __hip_atomic_load(FLr + (lane & 31), RLX_AGENT);
            while (!__all(f == epoch) && __builtin_amdgcn_s_memrealtime() - t0 < 5000000ull) {
                __builtin_amdgcn_s_sleep(POLLSLEEP);
                f = __hip_atomic_load(FLr + (lane & 31), RLX_AGENT);
            }
            SY[1] = 1;
        }
        while (SY[1] == 0) __builtin_amdgcn_s_sleep(1);

#define LA4 "global_load_dwordx4 %0, %4, %5 sc1\n\tglobal_load_dwordx4 %1, %4, %6 sc1\n\tglobal_load_dwordx4 %2, %4, %7 sc1\n\tglobal_load_dwordx4 %3, %4, %8 sc1\n"
#define LAO(d, o_) : "=&v"(d[0]), "=&v"(d[1]), "=&v"(d[2]), "=&v"(d[3]) : "v"(va), "s"(ua.p[0] + (o_)), "s"(ua.p[1] + (o_)), "s"(ua.p[2] + (o_)), "s"(ua.p[3] + (o_))
#define LA(d, i) asm volatile(LA4 LAO(d, (i) * 16384) : "memory")
#define PIN4(d) PIN(d[0]); PIN(d[1]); PIN(d[2]); PIN(d[3])
#define GO(d, rb_, W, NX, ...) wait_vm<W>(); PIN4(d); SB(); k3_step<NX, ##__VA_ARGS__>(ac, d, rb_, va, ua)
        LA(a0, 0); LA(a1, 1);
        const bool ext = wid < 2;
#define GO2(d, rb_, WE, WL, NX, ...) if (wid < 4) wait_vm<WE>(); GO(d, rb_, WL, NX, ##__VA_ARGS__)
        wait_vm<4>();
        if (!opub) OPUB();
        GO(a0, rb0, 5, 2, true); k3_lb_if(rb4, va, va5, wb + 4 * 18432, wid >> 2, 1);
        pin5(rb2); pin5(rb3);
        GO2(a1, rb1, 4, 9, 3); k3_lb_if(rb5, va, va5, wb + 5 * 18432, wid >> 2, 1);
        GO2(a0, rb2, 4, 14, 4);
        if (wid >= 4) {
            asm volatile("s_mov_b32 m0, %2\n\ts_nop 0\n\tglobal_load_lds_dword %0, %1 sc1" ::"v"(relane() * 4), "s"(FLr + 32), "s"(lds0 + TOPL + 64 + wid * 256));
            k3_b(lds0 + so[1], va, wb + 7 * 18432);
        }
        GO2(a1, rb3, 4, 15, 5); LB(rb0, 10);
        if (wid >= 4) {
            wait_vm<14>();
            const int l2 = relane();
            const unsigned fo = *(const volatile LAS unsigned*)(lds + TOPL + 64 + wid * 256 + l2 * 4);
            if (__builtin_expect(!__all(fo == ((l2 & 1) ? (unsigned)(epoch >> 32) : (unsigned)epoch)), 0)) {
                const unsigned long long t0 = __builtin_amdgcn_s_memrealtime();
                u64 f2; do { f2 = __hip_atomic_load(FLr + 32 + (l2 & 31), RLX_AGENT); } while (!__all(f2 == epoch) && __builtin_amdgcn_s_memrealtime() - t0 < 5000000ull);
            }
        }
        if (wid < 4) wait_vm<9>();
        pin5(rb4); pin5(rb5);
        GO(a0, rb4, 14, 6); k3_lb_if(rb1, va, va5, wb + 11 * 18432, wid >> 1, 0); RB(rb2, 0); SB();
        GO(a1, rb5, 9, 7); LA(a2, 8); RB(rb3, 1); SB();
        GO(a0, rb2, 8, 9); LA(a3, 10); RB(rb4, 2); SB();
        asm volatile("s_cmp_lg_u32 %9, 0\n\ts_cbranch_scc1 .Lla%=\n\t" LA4 ".Lla%=:" LAO(a4, 11 * 16384), "s"(wid >> 1) : "memory", "scc");
        GO(a1, rb3, 12, -1); RB(rb5, 3); SB();
        wait_vm<0>(); PIN4(a0); PIN4(a2); PIN4(a3); PIN4(a4); pin5(rb0); pin5(rb1);
        if (ext) k3_step<-1>(ac, a4, rb1, va, ua);
        const int l3 = relane(), r16 = l3 & 15, g = l3 >> 4;
        LAS char* pw = (LAS char*)lds + va;
        u32x2 xv[5];
        if (wid < 4) {
#pragma unroll
            for (int n = 0; n < 5; ++n) xv[n] = *(const u32x2*)(x + xo + r16 * D_ + n * 16 + (n < 4 ? g : (g & 1)) * 4);
        }
        SB();
#pragma unroll
        for (int m = 0; m < 4; ++m)
#pragma unroll
            for (int q = 0; q < 15; ++q) {
                if (q < 5) K3MM(ac[m][q], rb4.b[q], a2[m]); else if (q < 10) K3MM(ac[m][q - 5], rb0.b[q - 5], a3[m]); else K3MM(ac[m][q - 10], rb5.b[q - 10], a0[m]);
                SB();
                if (m > 0 && q % 3 == 0) { const int n = q / 3; if (n < 4 || g < 2) *(LAS f32x4*)(pw + so[m - 1] + n * 1024) = ac[m - 1][n]; SB(); }
            }
        asm volatile("s_nop 7\n\ts_nop 7\n\ts_waitcnt lgkmcnt(0)" ::: "memory");

        const LAS char* pr = lds + va;
#define PART(k, n) *(const LAS f32x4*)(pr + k3_so((wid & 4) + ((wid + 3 - (k)) & 3), k) + (n) * 1024)
        if (wid < 4) {
            __builtin_amdgcn_s_setprio(EPRIO);
            if (l3 == 0) __hip_atomic_fetch_add((LAS unsigned*)SY + 4, 1u, __ATOMIC_RELAXED, __HIP_MEMORY_SCOPE_WORKGROUP);
#pragma unroll
            for (int n = 0; n < 5; ++n) {
                ac[3][n] = f32x4{fadd1(ac[3][n][0], bflo(xv[n][0])), fadd1(ac[3][n][1], bfhi(xv[n][0])), fadd1(ac[3][n][2], bflo(xv[n][1])), fadd1(ac[3][n][3], bfhi(xv[n][1]))}; PIN(ac[3][n]);
            }
            while (SY[4] < 4) __builtin_amdgcn_s_sleep(1);
            SB();
#pragma unroll
            for (int n = 0; n < 5; ++n) {
                const f32x4 p0 = PART(0, n), p1 = PART(1, n), p2 = PART(2, n);
#pragma unroll
                for (int e = 0; e < 4; ++e) ac[3][n][e] = fadd1(fadd1(ac[3][n][e], p0[e]), fadd1(p1[e], p2[e]));
            }
#pragma unroll
            for (int n = 0; n < 5; ++n) if (n < 4 || g < 2) *(LAS f32x4*)(pw + so[3] + n * 1024) = ac[3][n];
        }
        SB();
        asm volatile("s_waitcnt lgkmcnt(0)" ::: "memory");
        asm volatile("s_barrier" ::: "memory");
        if (wid >= 4) {
            u32x2 o[5];
#pragma unroll
            for (int n = 0; n < 5; ++n) {
                const f32x4 r = ((ac[3][n] + PART(0, n)) + (PART(1, n) + PART(2, n))) + *(const LAS f32x4*)(pr + k3_so(wid - 4, 3) + n * 1024);
                o[n] = u32x2{cvtpk(r[0], r[1]), cvtpk(r[2], r[3])};
            }
            u16* op = out + xo + r16 * D_;
#pragma unroll
            for (int n = 0; n < 4; n += 2) {
                auto r0 = __builtin_amdgcn_permlane16_swap(o[n][0], o[n + 1][0], false, false); auto r1 = __builtin_amdgcn_permlane16_swap(o[n][1], o[n + 1][1], false, false);
                *(u32x4*)(op + (n + (g & 1)) * 16 + (g >> 1) * 8) = u32x4{r0[0], r1[0], r0[1], r1[1]};
            }
            if (g < 2) *(u32x2*)(op + 64 + g * 4) = o[4];
        }
    }
}
__global__ __launch_bounds__(512, 2) void fused_kernel(const u16* __restrict__ x, const u16* __restrict__ wq, const u16* __restrict__ wk, const u16* __restrict__ wv,
                                                       const u16* __restrict__ w_in, const u16* __restrict__ w_sp, const u16* __restrict__ b_sp, const u16* __restrict__ w_out,
                                                       const u16* __restrict__ wo, char* __restrict__ ws, u16* __restrict__ out) {
    G2P gp;
    const u64 epoch = phase1(gp, x, wq, wk, wv, w_in, w_sp, b_sp, w_out, wo, ws);
    phase2(gp, w_sp, b_sp, x, ws, out, epoch, __builtin_amdgcn_readfirstlane(threadIdx.x >> 6));
}
__global__ void text_end_marker() { asm volatile(".Lk3_end:"); }

extern "C" void kernel_launch(void* const* d_in, const int* in_sizes, int n_in, void* d_out, int out_size, void* d_ws, size_t ws_size, hipStream_t stream) {
    const u16* x = (const u16*)d_in[0]; const u16* wq = (const u16*)d_in[1]; const u16* wk = (const u16*)d_in[2]; const u16* wv = (const u16*)d_in[3];
    const u16* wo = (const u16*)d_in[4]; const u16* w_in = (const u16*)d_in[6]; const u16* w_sp = (const u16*)d_in[7]; const u16* b_sp = (const u16*)d_in[8];
    const u16* w_out = (const u16*)d_in[9]; char* ws = (char*)d_ws;
    (void)hipFuncSetAttribute((const void*)fused_kernel, hipFuncAttributeMaxDynamicSharedMemorySize, LDS_ALL);
    fused_kernel<<<256, 512, LDS_ALL, stream>>>(x, wq, wk, wv, w_in, w_sp, b_sp, w_out, wo, ws, (u16*)d_out);
}
```
